```python
import torch

def _softmax(x, axis=-1):
    return torch.softmax(x, dim=axis)

def _gelu(x):
    return torch.nn.functional.gelu(x, approximate='tanh')

def _silu(x):
    return torch.nn.functional.silu(x)

def _sigmoid(x):
    return torch.sigmoid(x)

def _layernorm(x, axis=-1, eps=1e-05):
    m = x.mean(dim=axis, keepdim=True)
    v = x.var(dim=axis, keepdim=True, unbiased=False)
    return (x - m) / torch.sqrt(v + eps)

def _scatter_add(tgt, idx, src, axis=0):
    return tgt.clone().index_add_(axis, idx, src)

def _segment_sum(src, seg, n):
    out = torch.zeros((n,) + src.shape[1:], dtype=src.dtype)
    return out.index_add_(0, seg, src)

def _pad(x, pw):
    flat = tuple((v for pair in reversed(pw) for v in pair))
    return torch.nn.functional.pad(x, flat)
kernel_name = 'adaLN_cond__mha_block_cached__mixer_channel_mlp__prompt'

def setup_inputs():

    _w = lambda a: (lambda t: t.to(torch.bfloat16) if t.is_floating_point() else t)(torch.as_tensor(a))

    def _A_setup():
        B, Sq, D = (1, 2048, 1024)
        H, Sk, Dh = (32, 2048, 128)
        return {'x': _w((lambda *s: torch.randn(*s, dtype=torch.bfloat16) * 0.05)(B, Sq, D)), 'k_cache': _w((lambda *s: torch.randn(*s, dtype=torch.bfloat16) * 3.2)(B, H, Sk, Dh)), 'v_cache': _w((lambda *s: torch.randn(*s, dtype=torch.bfloat16) * 0.4)(B, H, Sk, Dh)), 'wq': _w((lambda *s: torch.randn(*s, dtype=torch.bfloat16) * 3.2)(D, H * Dh) * D ** (-0.5)), 'wo': _w((lambda *s: torch.randn(*s, dtype=torch.bfloat16) * 0.4)(H * Dh, D) * (H * Dh) ** (-0.5))}
    _A_in = _A_setup()

    def _B_setup():
        B, S, D, F = (1, 2048, 1024, 3584)
        return {'x': _w((lambda *s: torch.randn(*s, dtype=torch.bfloat16) * 0.05)(B, S, D)), 'w1': _w((lambda *s: torch.randn(*s, dtype=torch.bfloat16) * 3.2)(D, F) * D ** (-0.5)), 'b1': _w((lambda *s: torch.randn(*s, dtype=torch.bfloat16) * 1.6)(F)), 'w2': _w((lambda *s: torch.randn(*s, dtype=torch.bfloat16) * 0.1)(F, D) * F ** (-0.5)), 'b2': _w((lambda *s: torch.randn(*s, dtype=torch.bfloat16) * 0.2)(D))}
    _B_in = _B_setup()
    return {'A_x': _A_in['x'], 'A_k_cache': _A_in['k_cache'], 'A_v_cache': _A_in['v_cache'], 'A_wq': _A_in['wq'], 'A_wo': _A_in['wo'], 'B_x': _B_in['x'], 'B_w1': _B_in['w1'], 'B_b1': _B_in['b1'], 'B_w2': _B_in['w2'], 'B_b2': _B_in['b2'], 'c': _w((lambda *s: torch.randn(*s, dtype=torch.bfloat16) * 0.1)(1, 1024) * 8.0), 'w_mod': _w((lambda *s: torch.randn(*s, dtype=torch.bfloat16) * 0.1)(1024, 6 * 1024) * 1024 ** (-0.5) * 8.0)}

def reference(A_x, A_k_cache, A_v_cache, A_wq, A_wo, B_x, B_w1, B_b1, B_w2, B_b2, c, w_mod):

    def _A_ref(x, k_cache, v_cache, wq, wo):
        B, Sq, D = x.shape
        H, Dh = (32, 128)
        q = (lambda x, *d: x.permute(*d))((x @ wq).reshape(B, Sq, H, Dh), 0, 2, 1, 3)
        scores = torch.einsum('bhqd,bhkd->bhqk', q, k_cache) * Dh ** (-0.5)
        p = _softmax(scores, axis=-1)
        o = torch.einsum('bhqk,bhkd->bhqd', p, v_cache)
        return (lambda x, *d: x.permute(*d))(o, 0, 2, 1, 3).reshape(B, Sq, H * Dh) @ wo

    def _B_ref(x, w1, b1, w2, b2):
        y = _layernorm(x, axis=-1)
        h = _gelu(torch.einsum('bsd,df->bsf', y, w1) + b1)
        return torch.einsum('bsf,fd->bsd', h, w2) + b2
    x = A_x
    mod = (_silu(c) @ w_mod).reshape(c.shape[0], 6, 1, -1)
    sa, ca, ga, sb, cb, gb = (mod[:, 0].clone(), mod[:, 1].clone(), mod[:, 2].clone(), mod[:, 3].clone(), mod[:, 4].clone(), mod[:, 5].clone())
    h = _layernorm(x, axis=-1) * (1 + ca) + sa
    x = x + ga * _A_ref(h, A_k_cache, A_v_cache, A_wq, A_wo)
    h = _layernorm(x, axis=-1) * (1 + cb) + sb
    x = x + gb * _B_ref(h, B_w1, B_b1, B_w2, B_b2)
    return x
```

```cpp
#include <hip/hip_runtime.h>
#include <hip/hip_bf16.h>
typedef unsigned short u16;
typedef __attribute__((ext_vector_type(8))) short bf16x8;
typedef __attribute__((ext_vector_type(4))) float f32x4;
typedef __attribute__((ext_vector_type(16))) float f32x16;
__device__ __forceinline__ float bf2f(u16 x){ return __uint_as_float(((unsigned)x)<<16); }
__device__ __forceinline__ u16 f2bf(float f){ __hip_bfloat16 b = __float2bfloat16(f); return *reinterpret_cast<u16*>(&b); }
__device__ __forceinline__ float rbf(float f){ return bf2f(f2bf(f)); }

__global__ void k_mod(const u16* c, const u16* wmod, float* mod){
  __shared__ float s[1024]; __shared__ float red[256];
  for(int i=threadIdx.x;i<1024;i+=256){ float v=bf2f(c[i]); s[i]=rbf(v/(1.f+__expf(-v))); }
  __syncthreads();
  int col=blockIdx.x*64+(threadIdx.x&63), kg=threadIdx.x>>6;
  float a=0; for(int k=kg*256;k<kg*256+256;++k) a+=s[k]*bf2f(wmod[(size_t)k*6144+col]);
  red[threadIdx.x]=a; __syncthreads();
  if(threadIdx.x<64) mod[col]=rbf(red[threadIdx.x]+red[threadIdx.x+64]+red[threadIdx.x+128]+red[threadIdx.x+192]);
}
__device__ float blk_sum(float v, float* red){
  for(int o=32;o>0;o>>=1) v+=__shfl_xor(v,o);
  __syncthreads();
  if((threadIdx.x&63)==0) red[threadIdx.x>>6]=v;
  __syncthreads();
  return red[0]+red[1]+red[2]+red[3];
}
__global__ void k_ln(const u16* x, const float* sh, const float* sc, u16* out, int twice){
  __shared__ float red[4];
  int r=blockIdx.x; float v[4];
  for(int i=0;i<4;++i) v[i]=bf2f(x[(size_t)r*1024+threadIdx.x*4+i]);
  float m=blk_sum(v[0]+v[1]+v[2]+v[3],red)/1024.f;
  float q=0; for(int i=0;i<4;++i) q+=(v[i]-m)*(v[i]-m);
  float var=blk_sum(q,red)/1024.f; float rs=rsqrtf(var+1e-5f);
  for(int i=0;i<4;++i){ int cidx=threadIdx.x*4+i; v[i]=(v[i]-m)*rs*rbf(1.f+sc[cidx])+sh[cidx]; }
  if(twice){
    float m2=blk_sum(v[0]+v[1]+v[2]+v[3],red)/1024.f;
    float q2=0; for(int i=0;i<4;++i) q2+=(v[i]-m2)*(v[i]-m2);
    float var2=blk_sum(q2,red)/1024.f; float rs2=rsqrtf(var2+1e-5f);
    for(int i=0;i<4;++i) v[i]=(v[i]-m2)*rs2;
  }
  for(int i=0;i<4;++i) out[(size_t)r*1024+threadIdx.x*4+i]=f2bf(v[i]);
}
template<int MODE>
__global__ __launch_bounds__(256) void k_gemm(const u16* A,const u16* B,u16* C,int M,int N,int K,const u16* bias,const float* gate,const u16* res){
  __shared__ __attribute__((aligned(16))) u16 As[64][40]; __shared__ __attribute__((aligned(16))) u16 Bs[64][40];
  int bm=blockIdx.y*64,bn=blockIdx.x*64,t=threadIdx.x,w=t>>6,l=t&63,wr=w>>1,wc=w&1;
  f32x4 acc[2][2]={};
  for(int k0=0;k0<K;k0+=32){
    { int r=t>>2,c=(t&3)*8; *(bf16x8*)&As[r][c]=*(const bf16x8*)&A[(size_t)(bm+r)*K+k0+c]; }
    { int kr=t>>3,c=(t&7)*8; bf16x8 v=*(const bf16x8*)&B[(size_t)(k0+kr)*N+bn+c]; for(int j=0;j<8;++j) Bs[c+j][kr]=v[j]; }
    __syncthreads();
    for(int m=0;m<2;++m)for(int n=0;n<2;++n){
      bf16x8 a=*(bf16x8*)&As[wr*32+m*16+(l&15)][(l>>4)*8]; bf16x8 b=*(bf16x8*)&Bs[wc*32+n*16+(l&15)][(l>>4)*8];
      acc[m][n]=__builtin_amdgcn_mfma_f32_16x16x32_bf16(a,b,acc[m][n],0,0,0);
    }
    __syncthreads();
  }
  for(int m=0;m<2;++m)for(int n=0;n<2;++n)for(int j=0;j<4;++j){
    int r=bm+wr*32+m*16+(l>>4)*4+j, c=bn+wc*32+n*16+(l&15); float v=acc[m][n][j];
    if(MODE==1) v=bf2f(res[(size_t)r*N+c])+gate[c]*v;
    if(MODE==2){ v+=bf2f(bias[c]); float u=0.7978845608f*(v+0.044715f*v*v*v); v=0.5f*v*(1.f+tanhf(u)); }
    if(MODE==3) v=bf2f(res[(size_t)r*N+c])+gate[c]*(v+bf2f(bias[c]));
    C[(size_t)r*N+c]=f2bf(v);
  }
}
__global__ __launch_bounds__(64) void k_attn(const u16* Q,const u16* Kc,const u16* Vc,u16* O){
  int h=blockIdx.y,q0=blockIdx.x*32,l=threadIdx.x,r=l&31,hi=l>>5;
  bf16x8 qf[8]; for(int s=0;s<8;++s) qf[s]=*(const bf16x8*)&Q[(size_t)(q0+r)*4096+h*128+s*16+hi*8];
  f32x16 o[4]={}; float m=-1e30f,lsum=0; const float C=0.088388347648318440f*1.4426950408889634f;
  const u16* Kh=Kc+(size_t)h*2048*128; const u16* Vh=Vc+(size_t)h*2048*128;
  for(int t=0;t<64;++t){
    f32x16 s={};
    for(int d=0;d<8;++d){ bf16x8 kf=*(const bf16x8*)&Kh[(size_t)(t*32+r)*128+d*16+hi*8]; s=__builtin_amdgcn_mfma_f32_32x32x16_bf16(kf,qf[d],s,0,0,0);}
    float mx=s[0]; for(int i=1;i<16;++i) mx=fmaxf(mx,s[i]); mx=fmaxf(mx,__shfl_xor(mx,32));
    float mn=fmaxf(m,mx), al=exp2f((m-mn)*C); m=mn; float ps=0;
    for(int i=0;i<16;++i){ s[i]=exp2f((s[i]-mn)*C); ps+=s[i]; }
    ps+=__shfl_xor(ps,32); lsum=lsum*al+ps;
    for(int db=0;db<4;++db) for(int i=0;i<16;++i) o[db][i]*=al;
    for(int ks=0;ks<2;++ks){
      bf16x8 pf; for(int j=0;j<8;++j) pf[j]=(short)f2bf(s[8*ks+j]);
      for(int db=0;db<4;++db){
        bf16x8 vf; for(int j=0;j<8;++j){ int key=t*32+16*ks+8*(j>>2)+4*hi+(j&3); vf[j]=(short)Vh[(size_t)key*128+db*32+r]; }
        o[db]=__builtin_amdgcn_mfma_f32_32x32x16_bf16(vf,pf,o[db],0,0,0);
      }
    }
  }
  float il=1.f/lsum;
  for(int db=0;db<4;++db) for(int i=0;i<16;++i){ int d=db*32+(i&3)+8*(i>>2)+4*hi; O[(size_t)(q0+r)*4096+h*128+d]=f2bf(o[db][i]*il); }
}
extern "C" void kernel_launch(void* const* d_in, const int* in_sizes, int n_in, void* d_out, int out_size, void* d_ws, size_t ws_size, hipStream_t stream){
  const u16* x=(const u16*)d_in[0]; const u16* kc=(const u16*)d_in[1]; const u16* vc=(const u16*)d_in[2];
  const u16* wq=(const u16*)d_in[3]; const u16* wo=(const u16*)d_in[4];
  const u16* w1=(const u16*)d_in[6]; const u16* b1=(const u16*)d_in[7]; const u16* w2=(const u16*)d_in[8]; const u16* b2=(const u16*)d_in[9];
  const u16* c=(const u16*)d_in[10]; const u16* wmod=(const u16*)d_in[11];
  char* ws=(char*)d_ws;
  float* mod=(float*)ws; u16* h=(u16*)(ws+(1<<20)); u16* q=(u16*)(ws+(8<<20)); u16* o=(u16*)(ws+(32<<20)); u16* x2=(u16*)(ws+(56<<20)); u16* y=(u16*)(ws+(64<<20)); u16* hm=(u16*)(ws+(72<<20));
  k_mod<<<96,256,0,stream>>>(c,wmod,mod);
  k_ln<<<2048,256,0,stream>>>(x,mod,mod+1024,h,0);
  k_gemm<0><<<dim3(64,32),256,0,stream>>>(h,wq,q,2048,4096,1024,nullptr,nullptr,nullptr);
  k_attn<<<dim3(64,32),64,0,stream>>>(q,kc,vc,o);
  k_gemm<1><<<dim3(16,32),256,0,stream>>>(o,wo,x2,2048,1024,4096,nullptr,mod+2048,x);
  k_ln<<<2048,256,0,stream>>>(x2,mod+3072,mod+4096,y,1);
  k_gemm<2><<<dim3(56,32),256,0,stream>>>(y,w1,hm,2048,3584,1024,b1,nullptr,nullptr);
  k_gemm<3><<<dim3(16,32),256,0,stream>>>(hm,w2,(u16*)d_out,2048,1024,3584,b2,mod+5120,x2);
}
```

```cpp
#include <hip/hip_runtime.h>
#include <hip/hip_bf16.h>
typedef unsigned short u16;
typedef unsigned long long u64;
typedef __attribute__((ext_vector_type(8))) short bf16x8;
typedef __attribute__((ext_vector_type(2))) float f32x2;
typedef __attribute__((ext_vector_type(4))) float f32x4;
typedef __attribute__((ext_vector_type(32))) float f32x32;
typedef __attribute__((ext_vector_type(2))) unsigned u32x2;
typedef __attribute__((ext_vector_type(4))) unsigned u32x4;
typedef __attribute__((ext_vector_type(16))) unsigned u32x16;
typedef __attribute__((ext_vector_type(2))) __bf16 bf16x2;
#define DI __device__ __forceinline__
#define SPINMAX (1u << 20)
DI float bf2f(u16 x) { return __uint_as_float(((unsigned)x) << 16); }
DI unsigned pk2(float lo, float hi) { f32x2 v = {lo, hi}; return __builtin_bit_cast(unsigned, __builtin_convertvector(v, bf16x2)); }
DI float lo16(unsigned u) { return __uint_as_float(u << 16); }
DI float hi16(unsigned u) { return __uint_as_float(u & 0xffff0000u); }
DI float rbf(float f) { return lo16(pk2(f, 0.f)); }
template <int CTRL> DI float dppadd(float v) { return v + __uint_as_float(__builtin_amdgcn_update_dpp(0u, __float_as_uint(v), CTRL, 0xf, 0xf, true)); }
DI float gsum(float v) {
  auto a = __builtin_amdgcn_permlane16_swap(__float_as_uint(v), __float_as_uint(v), false, false); v = __uint_as_float(a[0]) + __uint_as_float(a[1]);
  auto b = __builtin_amdgcn_permlane32_swap(__float_as_uint(v), __float_as_uint(v), false, false); return __uint_as_float(b[0]) + __uint_as_float(b[1]);
}
DI float wsum(float v) { v = dppadd<0xB1>(v); v = dppadd<0x4E>(v); v = dppadd<0x141>(v); v = dppadd<0x140>(v); return gsum(v); }
DI u32x4 mksrd(const void* p, unsigned n) { u64 a = (u64)p; u32x4 r = {(unsigned)a, (unsigned)(a >> 32) & 0xffffu, n, 0x20000u}; return r; }
DI void wg_barrier() { asm volatile("s_waitcnt lgkmcnt(0)\n\ts_barrier" ::: "memory"); }
DI unsigned fresh_lane() { unsigned l; asm volatile("v_mbcnt_lo_u32_b32 %0, -1, 0\n\tv_mbcnt_hi_u32_b32 %0, -1, %0" : "=v"(l)); return l; }
DI u32x4 nosent(u32x4 v) { return __builtin_elementwise_min(v, (u32x4){0xfffffffeu, 0xfffffffeu, 0xfffffffeu, 0xfffffffeu}); }
DI bool has_sent(u32x4 a, u32x4 b, u32x4 c, u32x4 d) {
  const u32x4 m = __builtin_elementwise_max(__builtin_elementwise_max(a, b), __builtin_elementwise_max(c, d));
  return __any(max(max(m[0], m[1]), max(m[2], m[3])) == 0xffffffffu);
}
DI void poll_load4(u32x4 (&u)[4], const char* base, unsigned off) {
  for (unsigned spins = 0; spins < SPINMAX; ++spins) {
    asm volatile("global_load_dwordx4 %0, %4, %5 sc1\n\tglobal_load_dwordx4 %1, %4, %5 offset:1024 sc1\n\tglobal_load_dwordx4 %2, %4, %5 offset:2048 sc1\n\t"
                 "global_load_dwordx4 %3, %4, %5 offset:3072 sc1\n\ts_waitcnt vmcnt(0)" : "=&v"(u[0]), "=&v"(u[1]), "=&v"(u[2]), "=&v"(u[3]) : "v"(off), "s"(base) : "memory");
    if (!has_sent(u[0], u[1], u[2], u[3])) break;
  }
}
#define CODE_ADDR(var, sym) asm volatile("s_getpc_b64 vcc\n\ts_add_u32 vcc_lo, vcc_lo, " sym "@rel32@lo+4\n\ts_addc_u32 vcc_hi, vcc_hi, " sym "@rel32@hi+12\n\ts_mov_b64 %0, vcc" : "=s"(var) :: "scc", "vcc")

DI u32x4 code_srd(unsigned bid, unsigned wave) { u64 cb, ce; CODE_ADDR(cb, "code_first"); CODE_ADDR(ce, "code_last");
  const unsigned n = (unsigned)(ce - cb), o = min(((bid >> 3) * 4 + wave) * 1024u, n); return mksrd((const char*)cb + o, (n - o) & ~15u); }
constexpr unsigned HS = 32768 + 1280, OS = 131072 + 1280, MS = 114688 + 1280;

DI float swapadd32(float a, float b) { auto r = __builtin_amdgcn_permlane32_swap(__float_as_uint(a), __float_as_uint(b), false, false); return __uint_as_float(r[0]) + __uint_as_float(r[1]); }
DI float swapadd16(float a, float b) { auto r = __builtin_amdgcn_permlane16_swap(__float_as_uint(a), __float_as_uint(b), false, false); return __uint_as_float(r[0]) + __uint_as_float(r[1]); }

struct Par { const u16 *x, *kc, *vc, *wq, *wo, *w1, *b1, *w2, *b2, *wmod, *c; char* ws; u16* out; };
constexpr unsigned O_MOD = 0, O_SIL = 32768, O_CNT = 131072, O_EP = 262144, O_GR = 327680, O_H = 1u << 20, O_Z = 11u << 20, O_O = 16u << 20, O_SCR = 34u << 20;
#define PAR const __attribute__((address_space(4))) Par* P = (const __attribute__((address_space(4))) Par*)__builtin_amdgcn_kernarg_segment_ptr(); unsigned bid = blockIdx.x, wave = wv, tid; asm volatile("" : "+s"(P), "+s"(bid), "+s"(wave)); asm volatile("v_mbcnt_lo_u32_b32 %0, -1, 0\n\tv_mbcnt_hi_u32_b32 %0, -1, %0" : "=v"(tid)); tid += wave * 64; char* ws = P->ws
template <int NW> DI void edge_signal(unsigned tid, char* ws, unsigned grp, unsigned gn, unsigned sig) {
  asm volatile("s_waitcnt vmcnt(%0) lgkmcnt(0)\n\ts_barrier" :: "n"(NW) : "memory");
  if (tid < 64) { unsigned* c = (unsigned*)(ws + O_CNT); bool last = true;
    if (gn > 1) { unsigned o = 0; if (tid == 0) o = __hip_atomic_fetch_add(c + (320 + grp) * 32, 1u, __ATOMIC_RELAXED, __HIP_MEMORY_SCOPE_AGENT); last = __builtin_amdgcn_readfirstlane(o) == gn - 1; }
    if (last && tid < 8) __hip_atomic_fetch_add(c + (sig * 8 + tid) * 32, 1u, __ATOMIC_RELAXED, __HIP_MEMORY_SCOPE_AGENT); }
}
DI void edge_wait(unsigned tid, unsigned bid, char* ws, unsigned wt, unsigned need) {
  if (tid < 64) { unsigned* c = (unsigned*)(ws + O_CNT); unsigned spins = 0;
    while (__hip_atomic_load(c + (wt * 8 + (bid & 7)) * 32, __ATOMIC_RELAXED, __HIP_MEMORY_SCOPE_AGENT) < need && ++spins < SPINMAX * 4) __builtin_amdgcn_s_sleep(4); }
  asm volatile("s_barrier" ::: "memory");
}

typedef __attribute__((ext_vector_type(8))) float f32x8;
DI void phase_front(char* lds, unsigned wv) {
  PAR; const u16* wq = P->wq;
  const int w = wave, l = tid & 63, r = bid >> 3, kh = r & 1, cg = (bid & 7) * 16 + (r >> 1), rot = (r >> 1) & 3;
  const u32x4 cs_ = code_srd(bid, wave);
  const unsigned cpf = __builtin_amdgcn_raw_buffer_load_b32(__builtin_amdgcn_make_buffer_rsrc((void*)((u64)cs_[0] | (u64)cs_[1] << 32), 0, cs_[2], 0x20000), l * 16, 0, 0);
  __builtin_amdgcn_sched_barrier(0);
  u16 cv[4]; u32x4 wm[4], u[2][2]; int kr[4];
#pragma unroll
  for (int i = 0; i < 4; ++i) { kr[i] = kh * 512 + ((i + rot) & 3) * 128 + w * 32 + (l >> 1); cv[i] = P->c[kr[i]]; }
#pragma unroll
  for (int i = 0; i < 4; ++i) wm[i] = *((const u32x4*)(P->wmod + (size_t)kr[i] * 6144 + cg * 16 + (l & 1) * 8));
  unsigned old = 0;
  if (tid < 2) old = __hip_atomic_exchange((unsigned*)(ws + O_CNT) + (bid * 2 + tid) * 32, 0u, __ATOMIC_RELAXED, __HIP_MEMORY_SCOPE_AGENT);
  const unsigned tag = __hip_atomic_load((const unsigned*)(ws + O_EP), __ATOMIC_RELAXED, __HIP_MEMORY_SCOPE_AGENT) + 1u;
#pragma unroll
  for (int e = 0; e < 4; ++e) u[e >> 1][e & 1] = *((const u32x4*)(P->x + (size_t)(bid * 8 + w * 2 + (e >> 1)) * 1024 + (e & 1) * 512 + l * 8));
  u32x4 pfv[3];
#pragma unroll
  for (int i = 0; i < 3; ++i) pfv[i] = *((const u32x4*)(wq + (size_t)(i * 128 + r * 4 + w) * 4096 + (bid & 7) * 512 + l * 8));
  __builtin_amdgcn_sched_barrier(0);
  float* red = (float*)(lds + 32768); float* gs = red + 64;
  f32x2 a[4] = {{0, 0}, {0, 0}, {0, 0}, {0, 0}}; float sl[4];
#pragma unroll
  for (int i = 0; i < 4; ++i) { const float c0 = bf2f(cv[i]); sl[i] = rbf(c0 * __builtin_amdgcn_rcpf(1.f + __expf(-c0))); }
#pragma unroll
  for (int i = 0; i < 4; ++i)
#pragma unroll
    for (int e = 0; e < 4; ++e) a[e] += (f32x2){lo16(wm[i][e]), hi16(wm[i][e])} * sl[i];
  { const float b0 = swapadd32(a[0][0], a[2][0]), b1 = swapadd32(a[0][1], a[2][1]), b2 = swapadd32(a[1][0], a[3][0]), b3 = swapadd32(a[1][1], a[3][1]);
    const float d0 = dppadd<0x128>(dppadd<0x124>(dppadd<0x4E>(swapadd16(b0, b2)))), d1 = dppadd<0x128>(dppadd<0x124>(dppadd<0x4E>(swapadd16(b1, b3))));
    if (!(l & 14)) *(f32x2*)(red + w * 16 + (l & 1) * 8 + 2 * (l >> 4)) = (f32x2){d0, d1}; }
  asm volatile("" :: "v"(old) : "memory");
  wg_barrier();
  const auto rw = __builtin_amdgcn_make_buffer_rsrc(ws, 0, O_H + 128 * HS, 0x20000);
  if (tid < 16) __builtin_amdgcn_raw_buffer_store_b64((u32x2){__float_as_uint((red[l] + red[16 + l]) + (red[32 + l] + red[48 + l])), tag}, rw, O_GR + (cg * 2 + kh) * 128 + l * 8, 0, 16);
  if ((bid & 23) == 0 && !(l & 1)) {
#pragma unroll
    for (int i = 0; i < 4; ++i) __builtin_amdgcn_raw_buffer_store_b32(__float_as_uint(sl[i]), rw, O_SIL + (bid >> 5) * 4096 + kr[i] * 4, 0, 16); }
  __builtin_amdgcn_sched_barrier(0);
  f32x2 v[2][8];
#pragma unroll
  for (int q = 0; q < 2; ++q) {
#pragma unroll
    for (int e = 0; e < 8; ++e) v[q][e] = (f32x2){lo16(u[q][e >> 2][e & 3]), hi16(u[q][e >> 2][e & 3])};
    const f32x2 s2 = ((v[q][0] + v[q][1]) + (v[q][2] + v[q][3])) + ((v[q][4] + v[q][5]) + (v[q][6] + v[q][7]));
    const float m = wsum(s2[0] + s2[1]) * (1.f / 1024.f);
    f32x2 q2 = {0, 0};
#pragma unroll
    for (int e = 0; e < 8; ++e) { const f32x2 d = v[q][e] - m; q2 += d * d; }
    const float var = wsum(q2[0] + q2[1]) * (1.f / 1024.f);
    const float mb = rbf(m), rs = 1.f / rbf(sqrtf(rbf(rbf(var) + 1e-5f)));
#pragma unroll
    for (int e = 0; e < 8; ++e) { const f32x2 d = v[q][e] - mb; const unsigned p = pk2(d[0], d[1]); const f32x2 n = (f32x2){lo16(p), hi16(p)} * rs; u[q][e >> 2][e & 3] = pk2(n[0], n[1]); }
#pragma unroll
    for (int hf = 0; hf < 2; ++hf) *(u32x4*)(lds + (w * 2 + q) * 2064 + hf * 1024 + l * 16) = u[q][hf]; }
  u32x4 g[8]; const unsigned off = O_GR + w * 8192 + 4096 + l * 16;
  asm volatile("" :: "v"(cpf));
#pragma unroll
  for (int i = 0; i < 3; ++i) asm volatile("" :: "v"(pfv[i]));
      asm volatile("global_load_dwordx4 %0, %8, %9 offset:-4096 sc1\n\tglobal_load_dwordx4 %1, %8, %9 offset:-3072 sc1\n\tglobal_load_dwordx4 %2, %8, %9 offset:-2048 sc1\n\t"
                   "global_load_dwordx4 %3, %8, %9 offset:-1024 sc1\n\tglobal_load_dwordx4 %4, %8, %9 sc1\n\tglobal_load_dwordx4 %5, %8, %9 offset:1024 sc1\n\t"
                   "global_load_dwordx4 %6, %8, %9 offset:2048 sc1\n\tglobal_load_dwordx4 %7, %8, %9 offset:3072 sc1"
                   : "=&v"(g[0]), "=&v"(g[1]), "=&v"(g[2]), "=&v"(g[3]), "=&v"(g[4]), "=&v"(g[5]), "=&v"(g[6]), "=&v"(g[7]) : "v"(off), "s"(ws) : "memory");
  wg_barrier();
  unsigned ho = O_H + (bid >> 1) * HS + ((tid >> 3) * 16 + (bid & 1) * 8 + (tid & 7)) * 16; asm volatile("" : "+v"(ho));
  f32x2 nv[4][4];
#pragma unroll
  for (int it = 0; it < 4; ++it) { const int uu = tid + 256 * it, i = uu & 7, gg = (uu >> 3) & 3, cc = uu >> 5;
#pragma unroll
    for (int hf = 0; hf < 2; ++hf) { const u32x2 p = *(const u32x2*)(lds + i * 2064 + 64 * cc + 32 * hf + 8 * gg);
#pragma unroll
      for (int e = 0; e < 2; ++e) { nv[it][2 * hf + e] = (f32x2){lo16(p[e]), hi16(p[e])}; asm volatile("" : "+v"(nv[it][2 * hf + e])); } } }
  {
    for (unsigned spins = 0; spins < SPINMAX; ++spins) {
      if (spins == 0) asm volatile("s_waitcnt vmcnt(0)" : "+v"(g[0]), "+v"(g[1]), "+v"(g[2]), "+v"(g[3]), "+v"(g[4]), "+v"(g[5]), "+v"(g[6]), "+v"(g[7]) :: "memory"); else
      asm volatile("global_load_dwordx4 %0, %8, %9 offset:-4096 sc1\n\tglobal_load_dwordx4 %1, %8, %9 offset:-3072 sc1\n\tglobal_load_dwordx4 %2, %8, %9 offset:-2048 sc1\n\t"
                   "global_load_dwordx4 %3, %8, %9 offset:-1024 sc1\n\tglobal_load_dwordx4 %4, %8, %9 sc1\n\tglobal_load_dwordx4 %5, %8, %9 offset:1024 sc1\n\t"
                   "global_load_dwordx4 %6, %8, %9 offset:2048 sc1\n\tglobal_load_dwordx4 %7, %8, %9 offset:3072 sc1\n\ts_waitcnt vmcnt(0)"
                   : "=&v"(g[0]), "=&v"(g[1]), "=&v"(g[2]), "=&v"(g[3]), "=&v"(g[4]), "=&v"(g[5]), "=&v"(g[6]), "=&v"(g[7]) : "v"(off), "s"(ws) : "memory");
      unsigned mx = tag, mn = tag;
#pragma unroll
      for (int e = 0; e < 8; ++e) { mx = max(mx, max(g[e][1], g[e][3])); mn = min(mn, min(g[e][1], g[e][3])); }
      if (!__any(mx != mn)) break;
    }
    const float one = w >= 2 ? 1.f : 0.f;
#pragma unroll
    for (int e = 0; e < 8; ++e) { unsigned p = pk2(dppadd<0x128>(__uint_as_float(g[e][0])), dppadd<0x128>(__uint_as_float(g[e][2])));
      p = pk2(one + lo16(p), one + hi16(p));
      *(f32x2*)(gs + w * 512 + e * 64 + (l >> 4) * 16 + (l & 7) * 2) = (f32x2){lo16(p), hi16(p)} * 0.12751743082f; } }
  wg_barrier();
  f32x4 ss[8], gv[8];
#pragma unroll
  for (int e = 0; e < 8; ++e) { const float* p = gs + (tid & 0xe0) + ((tid >> 1) & 12) + (e >> 1) * 256 + (e & 1) * 16; ss[e] = *(const f32x4*)p; gv[e] = *(const f32x4*)(p + 1024); }
  __builtin_amdgcn_sched_barrier(0);
#pragma unroll
  for (int it = 0; it < 4; ++it) { u32x4 o;
#pragma unroll
    for (int e = 0; e < 4; ++e) { const int k = 2 * it + (e >> 1), r2 = (e & 1) * 2; const f32x2 h = nv[it][e] * (f32x2){gv[k][r2], gv[k][r2 + 1]} + (f32x2){ss[k][r2], ss[k][r2 + 1]}; o[e] = pk2(h[0], h[1]); }
    __builtin_amdgcn_raw_buffer_store_b128(o, rw, ho + it * 8192, 0, 16); }
}

DI void attn_slow(const u16* __restrict__ hft, const u16* __restrict__ wq, const u16* __restrict__ Kh, const u16* __restrict__ Vh, u16* __restrict__ oft, int lane) {
  const int i = lane & 15, g = lane >> 4;
  for (int j = 0; j < 4; ++j) {
    f32x4 acc[8];
#pragma unroll
    for (int nb = 0; nb < 8; ++nb) acc[nb] = (f32x4){0, 0, 0, 0};
    for (int c = 0; c < 32; ++c) {
      const bf16x8 xf = __builtin_bit_cast(bf16x8, __builtin_amdgcn_raw_buffer_load_b128(__builtin_amdgcn_make_buffer_rsrc((void*)hft, 0, 4 * HS, 0x20000), j * HS + (c * 64 + lane) * 16, 0, 16));
#pragma unroll
      for (int nb = 0; nb < 8; ++nb) { bf16x8 wf; const int d = 32 * (nb >> 1) + 8 * (i >> 2) + 4 * (nb & 1) + (i & 3);
#pragma unroll
        for (int e = 0; e < 8; ++e) wf[e] = (short)wq[(size_t)(32 * c + 16 * (e >> 2) + 4 * g + (e & 3)) * 4096 + d];
        acc[nb] = __builtin_amdgcn_mfma_f32_16x16x32_bf16(wf, xf, acc[nb], 0, 0, 0); }
    }
    bf16x8 qf[4];
#pragma unroll
    for (int ds = 0; ds < 4; ++ds) { u32x4 u = {pk2(acc[2 * ds][0], acc[2 * ds][1]), pk2(acc[2 * ds][2], acc[2 * ds][3]), pk2(acc[2 * ds + 1][0], acc[2 * ds + 1][1]), pk2(acc[2 * ds + 1][2], acc[2 * ds + 1][3])};
      qf[ds] = __builtin_bit_cast(bf16x8, u); }
#pragma unroll
    for (int nb = 0; nb < 8; ++nb) acc[nb] = (f32x4){0, 0, 0, 0};
    float m = -INFINITY, l = 0.f;
    for (int G = 0; G < 64; ++G) {
      f32x4 s[2];
#pragma unroll
      for (int kb = 0; kb < 2; ++kb) { s[kb] = (f32x4){0, 0, 0, 0};
#pragma unroll
        for (int ds = 0; ds < 4; ++ds) s[kb] = __builtin_amdgcn_mfma_f32_16x16x32_bf16(*(const bf16x8*)(Kh + (size_t)(32 * G + 16 * kb + i) * 128 + 32 * ds + 8 * g), qf[ds], s[kb], 0, 0, 0); }
      float mx = fmaxf(fmaxf(fmaxf(s[0][0], s[0][1]), fmaxf(s[0][2], s[0][3])), fmaxf(fmaxf(s[1][0], s[1][1]), fmaxf(s[1][2], s[1][3])));
      mx = fmaxf(mx, __shfl_xor(mx, 16)); mx = fmaxf(mx, __shfl_xor(mx, 32));
      const float mn = fmaxf(m, mx), al = exp2f(m - mn); m = mn;
      float ps = 0;
#pragma unroll
      for (int kb = 0; kb < 2; ++kb)
#pragma unroll
        for (int r = 0; r < 4; ++r) { s[kb][r] = exp2f(s[kb][r] - mn); ps += s[kb][r]; }
      ps += __shfl_xor(ps, 16); ps += __shfl_xor(ps, 32); l = l * al + ps;
      u32x4 pu = {pk2(s[0][0], s[0][1]), pk2(s[0][2], s[0][3]), pk2(s[1][0], s[1][1]), pk2(s[1][2], s[1][3])};
      const bf16x8 pf = __builtin_bit_cast(bf16x8, pu);
#pragma unroll
      for (int db = 0; db < 8; ++db) { bf16x8 vf;
#pragma unroll
        for (int e = 0; e < 8; ++e) vf[e] = (short)Vh[(size_t)(32 * G + 16 * (e >> 2) + 4 * g + (e & 3)) * 128 + 16 * db + i];
        acc[db] = __builtin_amdgcn_mfma_f32_16x16x32_bf16(vf, pf, acc[db] * al, 0, 0, 0); }
    }
    const float il = 1.f / l;
#pragma unroll
    for (int c = 0; c < 4; ++c) { u32x4 u = {pk2(acc[2 * c][0] * il, acc[2 * c][1] * il), pk2(acc[2 * c][2] * il, acc[2 * c][3] * il), pk2(acc[2 * c + 1][0] * il, acc[2 * c + 1][1] * il), pk2(acc[2 * c + 1][2] * il, acc[2 * c + 1][3] * il)};
      __builtin_amdgcn_raw_buffer_store_b128(u, __builtin_amdgcn_make_buffer_rsrc(oft, 0, 3 * OS + 4096, 0x20000), j * OS + (c * 64 + lane) * 16, 0, 16); }
  }
}

DI void phase_attn(char* lds, unsigned wv) {
  PAR; const u16 *wq = P->wq, *kc = P->kc, *vc = P->vc, *wo = P->wo, *w1 = P->w1, *w2 = P->w2, *wmod = P->wmod;
  const u16* hft = (const u16*)(ws + O_H); u16* oft = (u16*)(ws + O_O); char* scr = ws + O_SCR; const float* sil = (const float*)(ws + O_SIL); float* mod = (float*)(ws + O_MOD);
  const unsigned b = bid, head = (b & 7) * 4 + ((b >> 3) & 3), qb = b >> 5;
  edge_signal<0>(tid, ws, 0, 1, qb);
  u32x16 va; u32x4 vb, so; f32x8 vc8;
  asm volatile(
"v_mbcnt_lo_u32_b32 v116, -1, 0\n"
"v_mbcnt_hi_u32_b32 v116, -1, v116\n"
"v_bfe_u32 v117, v116, 2, 2\n"
"v_lshrrev_b32 v119, 4, v116\n"
"v_and_b32 v118, 3, v116\n"
"s_mov_b32 s60, 0\n"
"s_lshl_b32 s61, s70, 17\n"
"s_lshl_b32 s62, s70, 12\n"
"s_add_u32 s62, s62, 32768\n"
"v_lshlrev_b32 v122, 4, v118\n"
"v_lshl_add_u32 v122, v119, 6, v122\n"
"v_xor_b32 v123, 32, v122\n"
"v_lshl_add_u32 v104, v117, 13, v122\n"
"v_lshl_add_u32 v105, v117, 13, v123\n"
"v_add_u32 v105, 31744, v105\n"
"v_lshl_add_u32 v106, v117, 13, v122\n"
"v_add_u32 v106, 63488, v106\n"
"v_lshl_add_u32 v107, v117, 13, v123\n"
"v_add_u32 v107, 95232, v107\n"
"v_lshlrev_b32 v100, 4, v116\n"
"v_add_u32 v101, 34048, v100\n"
"v_add_u32 v102, 68096, v100\n"
"v_add_u32 v103, 102144, v100\n"
"v_lshlrev_b32 v120, 3, v118\n"
"v_lshl_add_u32 v120, v117, 6, v120\n"
"v_lshl_add_u32 v120, v119, 10, v120\n"
"v_and_b32 v121, 1, v119\n"
"v_lshl_add_u32 v120, v121, 5, v120\n"
"v_lshl_add_u32 v108, v118, 3, v120\n"
"v_lshlrev_b32 v109, 5, v121\n"
"v_sub_u32 v108, v108, v109\n"
"v_xor_b32 v108, v108, v109\n"
"v_add_u32 v108, 32768, v108\n"
"v_add_u32 v109, 8, v108\n"
    : "={v[100:115]}"(va), "={v[116:119]}"(vb), "={v[120:127]}"(vc8), "={s[60:63]}"(so) : "{s70}"(wave) : "scc");
  const unsigned rb0 = qb * 16 + wave * 4;
  const u32x4 sx = mksrd(hft + rb0 * (HS / 2), 4 * HS), sw = mksrd(wq + head * 128, 1023 * 8192 + 256);
  asm volatile(
"s_add_u32 m0, s62, 0\n"
"s_nop 0\n"
"buffer_load_dwordx4 v104, s[44:47], s61 offen lds\n"
"buffer_load_dwordx4 v105, s[44:47], s61 offen offset:1024 lds\n"
"buffer_load_dwordx4 v106, s[44:47], s61 offen offset:2048 lds\n"
"buffer_load_dwordx4 v107, s[44:47], s61 offen offset:3072 lds\n"
"s_add_u32 s61, s61, 524288\n"
"s_add_u32 m0, s62, 16384\n"
"s_nop 0\n"
"buffer_load_dwordx4 v104, s[44:47], s61 offen lds\n"
"buffer_load_dwordx4 v105, s[44:47], s61 offen offset:1024 lds\n"
"buffer_load_dwordx4 v106, s[44:47], s61 offen offset:2048 lds\n"
"buffer_load_dwordx4 v107, s[44:47], s61 offen offset:3072 lds\n"
"s_add_u32 s61, s61, 524288\n"
    : "+{s[60:63]}"(so) : "{v[100:115]}"(va), "{s[44:47]}"(sw) : "scc", "memory");
  const u16* kp = kc + (size_t)head * 2048 * 128; const u16* vp = vc + (size_t)head * 2048 * 128;
  u16* op = oft + rb0 * (OS / 2) + head * 2048;
  const unsigned gw = b * 4 + wave;
  const char* pp; unsigned pn, po;
  if (gw < 357u) { po = gw * 23552u; pp = (const char*)wo; pn = 8388608u; }
  else if (gw < 669u) { po = (gw - 357u) * 23552u; pp = (const char*)w1; pn = 7340032u; }
  else { po = (gw - 669u) * 23552u; pp = (const char*)w2; pn = 7340032u; }
  po = po < pn ? po : pn; pp += po; pn -= po; pn = pn < 23552u ? pn : 23552u;
  char* rp = scr + (8 << 20) + gw * 9216u;
  const unsigned col0 = 2048 + 16 * ((b & 7) * 32 + (b >> 3));
  unsigned bad;
  constexpr int NB = (8 - 3) * 4;
  if (wave && NB) {
    const u32x4 sq = mksrd(wq + (3 * 128 + (b >> 3) * NB) * 4096 + (b & 7) * 512, (NB - 1) * 8192 + 1024);
#pragma unroll
    for (int i = 0; i < (NB + 2) / 3; ++i)
      asm volatile("s_mov_b32 m0, %0\n\ts_nop 0\n\tbuffer_load_dwordx4 %1, %2, %3 offen lds" :: "s"(wave * 1024), "v"((tid & 63) * 16), "s"(sq), "s"((i * 3 + wave - 1) * 8192) : "memory");
  }
  edge_wait(tid, bid, ws, qb, 32);
  asm volatile(
"buffer_load_dwordx4 a[128:131], v100, s[40:43], s60 offen sc1\n"
"buffer_load_dwordx4 a[132:135], v101, s[40:43], s60 offen sc1\n"
"buffer_load_dwordx4 a[136:139], v102, s[40:43], s60 offen sc1\n"
"buffer_load_dwordx4 a[140:143], v103, s[40:43], s60 offen sc1\n"
"buffer_load_dwordx4 a[144:147], v100, s[40:43], s60 offen offset:1024 sc1\n"
"buffer_load_dwordx4 a[148:151], v101, s[40:43], s60 offen offset:1024 sc1\n"
"buffer_load_dwordx4 a[152:155], v102, s[40:43], s60 offen offset:1024 sc1\n"
"buffer_load_dwordx4 a[156:159], v103, s[40:43], s60 offen offset:1024 sc1\n"
"s_waitcnt vmcnt(12)\n"
"s_barrier\n"
"ds_read_b64_tr_b16 a[64:65], v108 offset:0\n"
"ds_read_b64_tr_b16 a[66:67], v108 offset:4096\n"
"ds_read_b64_tr_b16 a[68:69], v109 offset:0\n"
"ds_read_b64_tr_b16 a[70:71], v109 offset:4096\n"
"ds_read_b64_tr_b16 a[72:73], v108 offset:256\n"
"ds_read_b64_tr_b16 a[74:75], v108 offset:4352\n"
"ds_read_b64_tr_b16 a[76:77], v109 offset:256\n"
"ds_read_b64_tr_b16 a[78:79], v109 offset:4352\n"
"s_waitcnt vmcnt(7) lgkmcnt(6)\n"
"v_mfma_f32_16x16x32_bf16 v[128:131], a[64:67], a[128:131], 0\n"
"ds_read_b64_tr_b16 a[80:81], v108 offset:512\n"
"v_mov_b32 v13, v100\n"
"s_waitcnt lgkmcnt(5)\n"
"v_mfma_f32_16x16x32_bf16 v[132:135], a[68:71], a[128:131], 0\n"
"buffer_load_dwordx4 a[160:163], v100, s[40:43], s60 offen offset:2048 sc1\n"
"s_lshl_b32 s68, s70, 12\n"
"s_waitcnt vmcnt(7)\n"
"v_mfma_f32_16x16x32_bf16 v[160:163], a[64:67], a[132:135], 0\n"
"ds_read_b64_tr_b16 a[82:83], v108 offset:4608\n"
"v_add_u32 v11, 65536, v120\n"
"v_mfma_f32_16x16x32_bf16 v[164:167], a[68:71], a[132:135], 0\n"
"ds_read_b64_tr_b16 a[84:85], v109 offset:512\n"
"v_xor_b32 v12, 32, v11\n"
"s_waitcnt vmcnt(6)\n"
"v_mfma_f32_16x16x32_bf16 v[192:195], a[64:67], a[136:139], 0\n"
"ds_read_b64_tr_b16 a[86:87], v109 offset:4608\n"
"v_lshl_add_u32 v8, v117, 8, v122\n"
"v_mfma_f32_16x16x32_bf16 v[196:199], a[68:71], a[136:139], 0\n"
"buffer_load_dwordx4 a[164:167], v101, s[40:43], s60 offen offset:2048 sc1\n"
"v_lshl_add_u32 v9, v117, 8, v123\n"
"s_waitcnt vmcnt(6)\n"
"v_mfma_f32_16x16x32_bf16 v[224:227], a[64:67], a[140:143], 0\n"
"v_and_b32 v121, 1, v117\n"
"v_lshlrev_b32 v121, 1, v121\n"
"v_mfma_f32_16x16x32_bf16 v[228:231], a[68:71], a[140:143], 0\n"
"v_xor_b32 v121, v121, v119\n"
"v_lshlrev_b32 v121, 4, v121\n"
"s_waitcnt lgkmcnt(4)\n"
"v_mfma_f32_16x16x32_bf16 v[136:139], a[72:75], a[128:131], 0\n"
"ds_read_b64_tr_b16 a[88:89], v108 offset:768\n"
"v_lshl_add_u32 v121, v118, 6, v121\n"
"v_mfma_f32_16x16x32_bf16 v[140:143], a[76:79], a[128:131], 0\n"
"buffer_load_dwordx4 a[168:171], v102, s[40:43], s60 offen offset:2048 sc1\n"
"v_lshl_add_u32 v10, v117, 10, v121\n"
"v_mfma_f32_16x16x32_bf16 v[168:171], a[72:75], a[132:135], 0\n"
"ds_read_b64_tr_b16 a[90:91], v108 offset:4864\n"
"v_lshrrev_b32 v121, 1, v116\n"
"v_mfma_f32_16x16x32_bf16 v[172:175], a[76:79], a[132:135], 0\n"
"ds_read_b64_tr_b16 a[92:93], v109 offset:768\n"
"v_lshlrev_b32 v15, 2, v121\n"
"v_mfma_f32_16x16x32_bf16 v[200:203], a[72:75], a[136:139], 0\n"
"ds_read_b64_tr_b16 a[94:95], v109 offset:4864\n"
"v_mul_u32_u24 v14, 12288, v121\n"
"v_mfma_f32_16x16x32_bf16 v[204:207], a[76:79], a[136:139], 0\n"
"buffer_load_dwordx4 a[172:175], v103, s[40:43], s60 offen offset:2048 sc1\n"
"v_and_b32 v121, 1, v116\n"
"v_mfma_f32_16x16x32_bf16 v[232:235], a[72:75], a[140:143], 0\n"
"v_lshl_add_u32 v14, v121, 4, v14\n"
"s_mov_b64 s[48:49], %[k]\n"
"v_mfma_f32_16x16x32_bf16 v[236:239], a[76:79], a[140:143], 0\n"
"s_and_b32 s49, s49, 0xffff\n"
"s_mov_b32 s50, 524288\n"
"s_waitcnt lgkmcnt(4)\n"
"v_mfma_f32_16x16x32_bf16 v[144:147], a[80:83], a[128:131], 0\n"
"ds_read_b64_tr_b16 a[64:65], v108 offset:8192\n"
"s_mov_b32 s51, 0x20000\n"
"v_mfma_f32_16x16x32_bf16 v[148:151], a[84:87], a[128:131], 0\n"
"buffer_load_dwordx4 a[176:179], v100, s[40:43], s60 offen offset:3072 sc1\n"
"s_mov_b64 s[52:53], %[v]\n"
"v_mfma_f32_16x16x32_bf16 v[176:179], a[80:83], a[132:135], 0\n"
"ds_read_b64_tr_b16 a[66:67], v108 offset:12288\n"
"s_and_b32 s53, s53, 0xffff\n"
"v_mfma_f32_16x16x32_bf16 v[180:183], a[84:87], a[132:135], 0\n"
"ds_read_b64_tr_b16 a[68:69], v109 offset:8192\n"
"s_mov_b32 s54, 524288\n"
"v_mfma_f32_16x16x32_bf16 v[208:211], a[80:83], a[136:139], 0\n"
"ds_read_b64_tr_b16 a[70:71], v109 offset:12288\n"
"s_mov_b32 s55, 0x20000\n"
"v_mfma_f32_16x16x32_bf16 v[212:215], a[84:87], a[136:139], 0\n"
"buffer_load_dwordx4 a[180:183], v101, s[40:43], s60 offen offset:3072 sc1\n"
"s_mov_b64 s[56:57], %[o]\n"
"v_mfma_f32_16x16x32_bf16 v[240:243], a[80:83], a[140:143], 0\n"
"s_and_b32 s57, s57, 0xffff\n"
"s_mov_b32 s58, 401152\n"
"v_mfma_f32_16x16x32_bf16 v[244:247], a[84:87], a[140:143], 0\n"
"s_mov_b32 s59, 0x20000\n"
"s_mov_b64 s[76:77], %[p]\n"
"s_waitcnt lgkmcnt(4)\n"
"v_mfma_f32_16x16x32_bf16 v[152:155], a[88:91], a[128:131], 0\n"
"ds_read_b64_tr_b16 a[72:73], v108 offset:8448\n"
"s_and_b32 s77, s77, 0xffff\n"
"v_mfma_f32_16x16x32_bf16 v[156:159], a[92:95], a[128:131], 0\n"
"buffer_load_dwordx4 a[184:187], v102, s[40:43], s60 offen offset:3072 sc1\n"
"s_mov_b32 s78, %[pn]\n"
"v_mfma_f32_16x16x32_bf16 v[184:187], a[88:91], a[132:135], 0\n"
"ds_read_b64_tr_b16 a[74:75], v108 offset:12544\n"
"s_mov_b32 s79, 0x20000\n"
"v_mfma_f32_16x16x32_bf16 v[188:191], a[92:95], a[132:135], 0\n"
"ds_read_b64_tr_b16 a[76:77], v109 offset:8448\n"
"s_mov_b64 s[36:37], %[r]\n"
"v_mfma_f32_16x16x32_bf16 v[216:219], a[88:91], a[136:139], 0\n"
"ds_read_b64_tr_b16 a[78:79], v109 offset:12544\n"
"s_and_b32 s37, s37, 0xffff\n"
"v_mfma_f32_16x16x32_bf16 v[220:223], a[92:95], a[136:139], 0\n"
"buffer_load_dwordx4 a[188:191], v103, s[40:43], s60 offen offset:3072 sc1\n"
"s_add_u32 s60, s60, 4096\n"
"v_mfma_f32_16x16x32_bf16 v[248:251], a[88:91], a[140:143], 0\n"
"s_mov_b32 s38, 9216\n"
"s_mov_b32 s39, 0x20000\n"
"v_mfma_f32_16x16x32_bf16 v[252:255], a[92:95], a[140:143], 0\n"
"s_mov_b64 s[32:33], %[gw]\n"
"s_and_b32 s33, s33, 0xffff\n"
"s_waitcnt vmcnt(11) lgkmcnt(4)\n"
"v_mfma_f32_16x16x32_bf16 v[128:131], a[64:67], a[144:147], v[128:131]\n"
"ds_read_b64_tr_b16 a[80:81], v108 offset:8704\n"
"s_mov_b32 s34, 3133472\n"
"v_mfma_f32_16x16x32_bf16 v[132:135], a[68:71], a[144:147], v[132:135]\n"
"buffer_load_dwordx4 a[192:195], v100, s[40:43], s60 offen sc1\n"
"s_waitcnt vmcnt(11)\n"
"v_mfma_f32_16x16x32_bf16 v[160:163], a[64:67], a[148:151], v[160:163]\n"
"ds_read_b64_tr_b16 a[82:83], v108 offset:12800\n"
"v_mfma_f32_16x16x32_bf16 v[164:167], a[68:71], a[148:151], v[164:167]\n"
"ds_read_b64_tr_b16 a[84:85], v109 offset:8704\n"
"s_waitcnt vmcnt(10)\n"
"v_mfma_f32_16x16x32_bf16 v[192:195], a[64:67], a[152:155], v[192:195]\n"
"ds_read_b64_tr_b16 a[86:87], v109 offset:12800\n"
"v_mfma_f32_16x16x32_bf16 v[196:199], a[68:71], a[152:155], v[196:199]\n"
"buffer_load_dwordx4 a[196:199], v101, s[40:43], s60 offen sc1\n"
"s_waitcnt vmcnt(10)\n"
"v_mfma_f32_16x16x32_bf16 v[224:227], a[64:67], a[156:159], v[224:227]\n"
"v_mfma_f32_16x16x32_bf16 v[228:231], a[68:71], a[156:159], v[228:231]\n"
"s_waitcnt lgkmcnt(4)\n"
"v_mfma_f32_16x16x32_bf16 v[136:139], a[72:75], a[144:147], v[136:139]\n"
"ds_read_b64_tr_b16 a[88:89], v108 offset:8960\n"
"s_add_u32 m0, s62, 32768\n"
"v_mfma_f32_16x16x32_bf16 v[140:143], a[76:79], a[144:147], v[140:143]\n"
"buffer_load_dwordx4 v104, s[44:47], s61 offen lds\n"
"v_mfma_f32_16x16x32_bf16 v[168:171], a[72:75], a[148:151], v[168:171]\n"
"ds_read_b64_tr_b16 a[90:91], v108 offset:13056\n"
"v_mfma_f32_16x16x32_bf16 v[172:175], a[76:79], a[148:151], v[172:175]\n"
"ds_read_b64_tr_b16 a[92:93], v109 offset:8960\n"
"v_mfma_f32_16x16x32_bf16 v[200:203], a[72:75], a[152:155], v[200:203]\n"
"ds_read_b64_tr_b16 a[94:95], v109 offset:13056\n"
"v_mfma_f32_16x16x32_bf16 v[204:207], a[76:79], a[152:155], v[204:207]\n"
"buffer_load_dwordx4 a[200:203], v102, s[40:43], s60 offen sc1\n"
"v_mfma_f32_16x16x32_bf16 v[232:235], a[72:75], a[156:159], v[232:235]\n"
"v_mfma_f32_16x16x32_bf16 v[236:239], a[76:79], a[156:159], v[236:239]\n"
"s_barrier\n"
"s_waitcnt lgkmcnt(4)\n"
"v_mfma_f32_16x16x32_bf16 v[144:147], a[80:83], a[144:147], v[144:147]\n"
"ds_read_b64_tr_b16 a[64:65], v108 offset:16384\n"
"s_mov_b32 s35, 0x20000\n"
"v_mfma_f32_16x16x32_bf16 v[148:151], a[84:87], a[144:147], v[148:151]\n"
"buffer_load_dwordx4 a[204:207], v103, s[40:43], s60 offen sc1\n"
"s_mov_b64 s[28:29], %[gs]\n"
"v_mfma_f32_16x16x32_bf16 v[176:179], a[80:83], a[148:151], v[176:179]\n"
"ds_read_b64_tr_b16 a[66:67], v108 offset:20480\n"
"s_and_b32 s29, s29, 0xffff\n"
"v_mfma_f32_16x16x32_bf16 v[180:183], a[84:87], a[148:151], v[180:183]\n"
"ds_read_b64_tr_b16 a[68:69], v108 offset:24576\n"
"s_mov_b32 s30, 1024\n"
"v_mfma_f32_16x16x32_bf16 v[208:211], a[80:83], a[152:155], v[208:211]\n"
"ds_read_b64_tr_b16 a[70:71], v108 offset:28672\n"
"s_mov_b32 s31, 0x20000\n"
"v_mfma_f32_16x16x32_bf16 v[212:215], a[84:87], a[152:155], v[212:215]\n"
"buffer_load_dwordx4 v105, s[44:47], s61 offen offset:1024 lds\n"
"s_mov_b32 s24, 0\n"
"v_mfma_f32_16x16x32_bf16 v[240:243], a[80:83], a[156:159], v[240:243]\n"
"s_mov_b32 s25, 0\n"
"s_mov_b32 s26, 0\n"
"v_mfma_f32_16x16x32_bf16 v[244:247], a[84:87], a[156:159], v[244:247]\n"
"s_mov_b32 s27, 0x07060302\n"
"s_mov_b32 s65, s68\n"
"s_waitcnt lgkmcnt(4)\n"
"v_mfma_f32_16x16x32_bf16 v[152:155], a[88:91], a[144:147], v[152:155]\n"
"ds_read_b64_tr_b16 a[72:73], v109 offset:16384\n"
"s_add_u32 s66, s68, 65536\n"
"v_mfma_f32_16x16x32_bf16 v[156:159], a[92:95], a[144:147], v[156:159]\n"
"buffer_load_dwordx4 a[208:211], v100, s[40:43], s60 offen offset:1024 sc1\n"
"s_mov_b32 s64, s68\n"
"v_mfma_f32_16x16x32_bf16 v[184:187], a[88:91], a[148:151], v[184:187]\n"
"ds_read_b64_tr_b16 a[74:75], v109 offset:20480\n"
"s_mov_b32 s71, 0\n"
"v_mfma_f32_16x16x32_bf16 v[188:191], a[92:95], a[148:151], v[188:191]\n"
"ds_read_b64_tr_b16 a[76:77], v109 offset:24576\n"
"s_mov_b32 s67, 0\n"
"v_mfma_f32_16x16x32_bf16 v[216:219], a[88:91], a[152:155], v[216:219]\n"
"ds_read_b64_tr_b16 a[78:79], v109 offset:28672\n"
"v_min_u32 v120, 1, v118\n"
"v_mfma_f32_16x16x32_bf16 v[220:223], a[92:95], a[152:155], v[220:223]\n"
"buffer_load_dwordx4 a[212:215], v101, s[40:43], s60 offen offset:1024 sc1\n"
"v_add_u32 v120, -1, v120\n"
"v_mfma_f32_16x16x32_bf16 v[248:251], a[88:91], a[156:159], v[248:251]\n"
"v_and_b32 v96, 0x3f803f80, v120\n"
"v_and_b32 v97, 0x3f803f80, v120\n"
"v_mfma_f32_16x16x32_bf16 v[252:255], a[92:95], a[156:159], v[252:255]\n"
"v_and_b32 v98, 0x3f803f80, v120\n"
"v_and_b32 v99, 0x3f803f80, v120\n"
"s_waitcnt vmcnt(10) lgkmcnt(4)\n"
"v_mfma_f32_16x16x32_bf16 v[128:131], a[64:67], a[160:163], v[128:131]\n"
"ds_read_b64_tr_b16 a[80:81], v108 offset:16640\n"
"v_mfma_f32_16x16x32_bf16 v[128:131], a[68:71], a[176:179], v[128:131]\n"
"buffer_load_dwordx4 v106, s[44:47], s61 offen offset:2048 lds\n"
"v_mfma_f32_16x16x32_bf16 v[160:163], a[64:67], a[164:167], v[160:163]\n"
"ds_read_b64_tr_b16 a[82:83], v108 offset:20736\n"
"v_mfma_f32_16x16x32_bf16 v[160:163], a[68:71], a[180:183], v[160:163]\n"
"ds_read_b64_tr_b16 a[84:85], v108 offset:24832\n"
"v_mfma_f32_16x16x32_bf16 v[192:195], a[64:67], a[168:171], v[192:195]\n"
"ds_read_b64_tr_b16 a[86:87], v108 offset:28928\n"
"s_waitcnt vmcnt(9)\n"
"v_mfma_f32_16x16x32_bf16 v[192:195], a[68:71], a[184:187], v[192:195]\n"
"buffer_load_dwordx4 a[216:219], v102, s[40:43], s60 offen offset:1024 sc1\n"
"v_mfma_f32_16x16x32_bf16 v[224:227], a[64:67], a[172:175], v[224:227]\n"
"v_mfma_f32_16x16x32_bf16 v[224:227], a[68:71], a[188:191], v[224:227]\n"
"s_waitcnt lgkmcnt(4)\n"
"v_mfma_f32_16x16x32_bf16 v[132:135], a[72:75], a[160:163], v[132:135]\n"
"ds_read_b64_tr_b16 a[88:89], v109 offset:16640\n"
"v_mfma_f32_16x16x32_bf16 v[132:135], a[76:79], a[176:179], v[132:135]\n"
"buffer_load_dwordx4 a[220:223], v103, s[40:43], s60 offen offset:1024 sc1\n"
"v_mfma_f32_16x16x32_bf16 v[164:167], a[72:75], a[164:167], v[164:167]\n"
"ds_read_b64_tr_b16 a[90:91], v109 offset:20736\n"
"v_mfma_f32_16x16x32_bf16 v[164:167], a[76:79], a[180:183], v[164:167]\n"
"ds_read_b64_tr_b16 a[92:93], v109 offset:24832\n"
"v_mfma_f32_16x16x32_bf16 v[196:199], a[72:75], a[168:171], v[196:199]\n"
"ds_read_b64_tr_b16 a[94:95], v109 offset:28928\n"
"v_mfma_f32_16x16x32_bf16 v[196:199], a[76:79], a[184:187], v[196:199]\n"
"buffer_load_dwordx4 v107, s[44:47], s61 offen offset:3072 lds\n"
"s_add_u32 s61, s61, 524288\n"
"v_mfma_f32_16x16x32_bf16 v[228:231], a[72:75], a[172:175], v[228:231]\n"
"v_mfma_f32_16x16x32_bf16 v[228:231], a[76:79], a[188:191], v[228:231]\n"
"s_waitcnt lgkmcnt(4)\n"
"v_mfma_f32_16x16x32_bf16 v[136:139], a[80:83], a[160:163], v[136:139]\n"
"ds_read_b64_tr_b16 a[64:65], v108 offset:16896\n"
"v_mfma_f32_16x16x32_bf16 v[136:139], a[84:87], a[176:179], v[136:139]\n"
"buffer_load_dwordx4 a[224:227], v100, s[40:43], s60 offen offset:2048 sc1\n"
"v_mfma_f32_16x16x32_bf16 v[168:171], a[80:83], a[164:167], v[168:171]\n"
"ds_read_b64_tr_b16 a[66:67], v108 offset:20992\n"
"v_mfma_f32_16x16x32_bf16 v[168:171], a[84:87], a[180:183], v[168:171]\n"
"ds_read_b64_tr_b16 a[68:69], v108 offset:25088\n"
"v_mfma_f32_16x16x32_bf16 v[200:203], a[80:83], a[168:171], v[200:203]\n"
"ds_read_b64_tr_b16 a[70:71], v108 offset:29184\n"
"v_mfma_f32_16x16x32_bf16 v[200:203], a[84:87], a[184:187], v[200:203]\n"
"buffer_load_dwordx4 a[228:231], v101, s[40:43], s60 offen offset:2048 sc1\n"
"v_mfma_f32_16x16x32_bf16 v[232:235], a[80:83], a[172:175], v[232:235]\n"
"v_mfma_f32_16x16x32_bf16 v[232:235], a[84:87], a[188:191], v[232:235]\n"
"s_waitcnt lgkmcnt(4)\n"
"v_mfma_f32_16x16x32_bf16 v[140:143], a[88:91], a[160:163], v[140:143]\n"
"ds_read_b64_tr_b16 a[72:73], v109 offset:16896\n"
"s_add_u32 m0, s62, 49152\n"
"v_mfma_f32_16x16x32_bf16 v[140:143], a[92:95], a[176:179], v[140:143]\n"
"buffer_load_dwordx4 v104, s[44:47], s61 offen lds\n"
"v_mfma_f32_16x16x32_bf16 v[172:175], a[88:91], a[164:167], v[172:175]\n"
"ds_read_b64_tr_b16 a[74:75], v109 offset:20992\n"
"v_mfma_f32_16x16x32_bf16 v[172:175], a[92:95], a[180:183], v[172:175]\n"
"ds_read_b64_tr_b16 a[76:77], v109 offset:25088\n"
"v_mfma_f32_16x16x32_bf16 v[204:207], a[88:91], a[168:171], v[204:207]\n"
"ds_read_b64_tr_b16 a[78:79], v109 offset:29184\n"
"v_mfma_f32_16x16x32_bf16 v[204:207], a[92:95], a[184:187], v[204:207]\n"
"buffer_load_dwordx4 a[232:235], v102, s[40:43], s60 offen offset:2048 sc1\n"
"v_mfma_f32_16x16x32_bf16 v[236:239], a[88:91], a[172:175], v[236:239]\n"
"v_mfma_f32_16x16x32_bf16 v[236:239], a[92:95], a[188:191], v[236:239]\n"
"s_waitcnt lgkmcnt(4)\n"
"v_mfma_f32_16x16x32_bf16 v[144:147], a[64:67], a[160:163], v[144:147]\n"
"ds_read_b64_tr_b16 a[80:81], v108 offset:17152\n"
"v_mfma_f32_16x16x32_bf16 v[144:147], a[68:71], a[176:179], v[144:147]\n"
"buffer_load_dwordx4 a[236:239], v103, s[40:43], s60 offen offset:2048 sc1\n"
"v_mfma_f32_16x16x32_bf16 v[176:179], a[64:67], a[164:167], v[176:179]\n"
"ds_read_b64_tr_b16 a[82:83], v108 offset:21248\n"
"v_mfma_f32_16x16x32_bf16 v[176:179], a[68:71], a[180:183], v[176:179]\n"
"ds_read_b64_tr_b16 a[84:85], v108 offset:25344\n"
"v_mfma_f32_16x16x32_bf16 v[208:211], a[64:67], a[168:171], v[208:211]\n"
"ds_read_b64_tr_b16 a[86:87], v108 offset:29440\n"
"v_mfma_f32_16x16x32_bf16 v[208:211], a[68:71], a[184:187], v[208:211]\n"
"buffer_load_dwordx4 v105, s[44:47], s61 offen offset:1024 lds\n"
"v_mfma_f32_16x16x32_bf16 v[240:243], a[64:67], a[172:175], v[240:243]\n"
"v_mfma_f32_16x16x32_bf16 v[240:243], a[68:71], a[188:191], v[240:243]\n"
"s_waitcnt lgkmcnt(4)\n"
"v_mfma_f32_16x16x32_bf16 v[148:151], a[72:75], a[160:163], v[148:151]\n"
"ds_read_b64_tr_b16 a[88:89], v109 offset:17152\n"
"v_mfma_f32_16x16x32_bf16 v[148:151], a[76:79], a[176:179], v[148:151]\n"
"buffer_load_dwordx4 a[240:243], v100, s[40:43], s60 offen offset:3072 sc1\n"
"v_mfma_f32_16x16x32_bf16 v[180:183], a[72:75], a[164:167], v[180:183]\n"
"ds_read_b64_tr_b16 a[90:91], v109 offset:21248\n"
"v_mfma_f32_16x16x32_bf16 v[180:183], a[76:79], a[180:183], v[180:183]\n"
"ds_read_b64_tr_b16 a[92:93], v109 offset:25344\n"
"v_mfma_f32_16x16x32_bf16 v[212:215], a[72:75], a[168:171], v[212:215]\n"
"ds_read_b64_tr_b16 a[94:95], v109 offset:29440\n"
"v_mfma_f32_16x16x32_bf16 v[212:215], a[76:79], a[184:187], v[212:215]\n"
"buffer_load_dwordx4 a[244:247], v101, s[40:43], s60 offen offset:3072 sc1\n"
"v_mfma_f32_16x16x32_bf16 v[244:247], a[72:75], a[172:175], v[244:247]\n"
"v_mfma_f32_16x16x32_bf16 v[244:247], a[76:79], a[188:191], v[244:247]\n"
"s_waitcnt vmcnt(8) lgkmcnt(4)\n"
"s_barrier\n"
"v_mfma_f32_16x16x32_bf16 v[152:155], a[80:83], a[160:163], v[152:155]\n"
"ds_read_b64_tr_b16 a[64:65], v108 offset:32768\n"
"v_mfma_f32_16x16x32_bf16 v[152:155], a[84:87], a[176:179], v[152:155]\n"
"buffer_load_dwordx4 v106, s[44:47], s61 offen offset:2048 lds\n"
"v_mfma_f32_16x16x32_bf16 v[184:187], a[80:83], a[164:167], v[184:187]\n"
"ds_read_b64_tr_b16 a[66:67], v108 offset:36864\n"
"v_mfma_f32_16x16x32_bf16 v[184:187], a[84:87], a[180:183], v[184:187]\n"
"ds_read_b64_tr_b16 a[68:69], v108 offset:40960\n"
"v_mfma_f32_16x16x32_bf16 v[216:219], a[80:83], a[168:171], v[216:219]\n"
"ds_read_b64_tr_b16 a[70:71], v108 offset:45056\n"
"v_mfma_f32_16x16x32_bf16 v[216:219], a[84:87], a[184:187], v[216:219]\n"
"buffer_load_dwordx4 a[248:251], v102, s[40:43], s60 offen offset:3072 sc1\n"
"v_mfma_f32_16x16x32_bf16 v[248:251], a[80:83], a[172:175], v[248:251]\n"
"v_mfma_f32_16x16x32_bf16 v[248:251], a[84:87], a[188:191], v[248:251]\n"
"s_waitcnt lgkmcnt(4)\n"
"v_mfma_f32_16x16x32_bf16 v[156:159], a[88:91], a[160:163], v[156:159]\n"
"ds_read_b64_tr_b16 a[72:73], v109 offset:32768\n"
"v_mfma_f32_16x16x32_bf16 v[156:159], a[92:95], a[176:179], v[156:159]\n"
"buffer_load_dwordx4 a[252:255], v103, s[40:43], s60 offen offset:3072 sc1\n"
"s_add_u32 s60, s60, 4096\n"
"v_mfma_f32_16x16x32_bf16 v[188:191], a[88:91], a[164:167], v[188:191]\n"
"ds_read_b64_tr_b16 a[74:75], v109 offset:36864\n"
"v_mfma_f32_16x16x32_bf16 v[188:191], a[92:95], a[180:183], v[188:191]\n"
"ds_read_b64_tr_b16 a[76:77], v109 offset:40960\n"
"v_mfma_f32_16x16x32_bf16 v[220:223], a[88:91], a[168:171], v[220:223]\n"
"ds_read_b64_tr_b16 a[78:79], v109 offset:45056\n"
"v_mfma_f32_16x16x32_bf16 v[220:223], a[92:95], a[184:187], v[220:223]\n"
"buffer_load_dwordx4 v107, s[44:47], s61 offen offset:3072 lds\n"
"s_add_u32 s61, s61, 524288\n"
"v_mfma_f32_16x16x32_bf16 v[252:255], a[88:91], a[172:175], v[252:255]\n"
"v_mfma_f32_16x16x32_bf16 v[252:255], a[92:95], a[188:191], v[252:255]\n"
"s_waitcnt lgkmcnt(4)\n"
"v_mfma_f32_16x16x32_bf16 v[128:131], a[64:67], a[192:195], v[128:131]\n"
"ds_read_b64_tr_b16 a[80:81], v108 offset:33024\n"
"v_mfma_f32_16x16x32_bf16 v[128:131], a[68:71], a[208:211], v[128:131]\n"
"buffer_load_dwordx4 a[128:131], v100, s[40:43], s60 offen sc1\n"
"v_mfma_f32_16x16x32_bf16 v[160:163], a[64:67], a[196:199], v[160:163]\n"
"ds_read_b64_tr_b16 a[82:83], v108 offset:37120\n"
"v_mfma_f32_16x16x32_bf16 v[160:163], a[68:71], a[212:215], v[160:163]\n"
"ds_read_b64_tr_b16 a[84:85], v108 offset:41216\n"
"v_mfma_f32_16x16x32_bf16 v[192:195], a[64:67], a[200:203], v[192:195]\n"
"ds_read_b64_tr_b16 a[86:87], v108 offset:45312\n"
"v_mfma_f32_16x16x32_bf16 v[192:195], a[68:71], a[216:219], v[192:195]\n"
"buffer_load_dwordx4 a[132:135], v101, s[40:43], s60 offen sc1\n"
"v_mfma_f32_16x16x32_bf16 v[224:227], a[64:67], a[204:207], v[224:227]\n"
"v_mfma_f32_16x16x32_bf16 v[224:227], a[68:71], a[220:223], v[224:227]\n"
"s_waitcnt lgkmcnt(4)\n"
"v_mfma_f32_16x16x32_bf16 v[132:135], a[72:75], a[192:195], v[132:135]\n"
"ds_read_b64_tr_b16 a[88:89], v109 offset:33024\n"
"s_add_u32 m0, s62, 0\n"
"v_mfma_f32_16x16x32_bf16 v[132:135], a[76:79], a[208:211], v[132:135]\n"
"buffer_load_dwordx4 v104, s[44:47], s61 offen lds\n"
"v_mfma_f32_16x16x32_bf16 v[164:167], a[72:75], a[196:199], v[164:167]\n"
"ds_read_b64_tr_b16 a[90:91], v109 offset:37120\n"
"v_mfma_f32_16x16x32_bf16 v[164:167], a[76:79], a[212:215], v[164:167]\n"
"ds_read_b64_tr_b16 a[92:93], v109 offset:41216\n"
"v_mfma_f32_16x16x32_bf16 v[196:199], a[72:75], a[200:203], v[196:199]\n"
"ds_read_b64_tr_b16 a[94:95], v109 offset:45312\n"
"v_mfma_f32_16x16x32_bf16 v[196:199], a[76:79], a[216:219], v[196:199]\n"
"buffer_load_dwordx4 a[136:139], v102, s[40:43], s60 offen sc1\n"
"v_mfma_f32_16x16x32_bf16 v[228:231], a[72:75], a[204:207], v[228:231]\n"
"v_mfma_f32_16x16x32_bf16 v[228:231], a[76:79], a[220:223], v[228:231]\n"
"s_waitcnt lgkmcnt(4)\n"
"v_mfma_f32_16x16x32_bf16 v[136:139], a[80:83], a[192:195], v[136:139]\n"
"ds_read_b64_tr_b16 a[64:65], v108 offset:33280\n"
"v_mfma_f32_16x16x32_bf16 v[136:139], a[84:87], a[208:211], v[136:139]\n"
"buffer_load_dwordx4 a[140:143], v103, s[40:43], s60 offen sc1\n"
"v_mfma_f32_16x16x32_bf16 v[168:171], a[80:83], a[196:199], v[168:171]\n"
"ds_read_b64_tr_b16 a[66:67], v108 offset:37376\n"
"v_mfma_f32_16x16x32_bf16 v[168:171], a[84:87], a[212:215], v[168:171]\n"
"ds_read_b64_tr_b16 a[68:69], v108 offset:41472\n"
"v_mfma_f32_16x16x32_bf16 v[200:203], a[80:83], a[200:203], v[200:203]\n"
"ds_read_b64_tr_b16 a[70:71], v108 offset:45568\n"
"v_mfma_f32_16x16x32_bf16 v[200:203], a[84:87], a[216:219], v[200:203]\n"
"buffer_load_dwordx4 v105, s[44:47], s61 offen offset:1024 lds\n"
"v_mfma_f32_16x16x32_bf16 v[232:235], a[80:83], a[204:207], v[232:235]\n"
"v_mfma_f32_16x16x32_bf16 v[232:235], a[84:87], a[220:223], v[232:235]\n"
"s_waitcnt lgkmcnt(4)\n"
"v_mfma_f32_16x16x32_bf16 v[140:143], a[88:91], a[192:195], v[140:143]\n"
"ds_read_b64_tr_b16 a[72:73], v109 offset:33280\n"
"v_mfma_f32_16x16x32_bf16 v[140:143], a[92:95], a[208:211], v[140:143]\n"
"buffer_load_dwordx4 a[144:147], v100, s[40:43], s60 offen offset:1024 sc1\n"
"v_mfma_f32_16x16x32_bf16 v[172:175], a[88:91], a[196:199], v[172:175]\n"
"ds_read_b64_tr_b16 a[74:75], v109 offset:37376\n"
"v_mfma_f32_16x16x32_bf16 v[172:175], a[92:95], a[212:215], v[172:175]\n"
"ds_read_b64_tr_b16 a[76:77], v109 offset:41472\n"
"v_mfma_f32_16x16x32_bf16 v[204:207], a[88:91], a[200:203], v[204:207]\n"
"ds_read_b64_tr_b16 a[78:79], v109 offset:45568\n"
"v_mfma_f32_16x16x32_bf16 v[204:207], a[92:95], a[216:219], v[204:207]\n"
"buffer_load_dwordx4 a[148:151], v101, s[40:43], s60 offen offset:1024 sc1\n"
"v_mfma_f32_16x16x32_bf16 v[236:239], a[88:91], a[204:207], v[236:239]\n"
"v_mfma_f32_16x16x32_bf16 v[236:239], a[92:95], a[220:223], v[236:239]\n"
"s_waitcnt lgkmcnt(4)\n"
"v_mfma_f32_16x16x32_bf16 v[144:147], a[64:67], a[192:195], v[144:147]\n"
"ds_read_b64_tr_b16 a[80:81], v108 offset:33536\n"
"v_mfma_f32_16x16x32_bf16 v[144:147], a[68:71], a[208:211], v[144:147]\n"
"buffer_load_dwordx4 v106, s[44:47], s61 offen offset:2048 lds\n"
"v_mfma_f32_16x16x32_bf16 v[176:179], a[64:67], a[196:199], v[176:179]\n"
"ds_read_b64_tr_b16 a[82:83], v108 offset:37632\n"
"v_mfma_f32_16x16x32_bf16 v[176:179], a[68:71], a[212:215], v[176:179]\n"
"ds_read_b64_tr_b16 a[84:85], v108 offset:41728\n"
"v_mfma_f32_16x16x32_bf16 v[208:211], a[64:67], a[200:203], v[208:211]\n"
"ds_read_b64_tr_b16 a[86:87], v108 offset:45824\n"
"v_mfma_f32_16x16x32_bf16 v[208:211], a[68:71], a[216:219], v[208:211]\n"
"buffer_load_dwordx4 a[152:155], v102, s[40:43], s60 offen offset:1024 sc1\n"
"v_mfma_f32_16x16x32_bf16 v[240:243], a[64:67], a[204:207], v[240:243]\n"
"v_mfma_f32_16x16x32_bf16 v[240:243], a[68:71], a[220:223], v[240:243]\n"
"s_waitcnt lgkmcnt(4)\n"
"v_mfma_f32_16x16x32_bf16 v[148:151], a[72:75], a[192:195], v[148:151]\n"
"ds_read_b64_tr_b16 a[88:89], v109 offset:33536\n"
"v_mfma_f32_16x16x32_bf16 v[148:151], a[76:79], a[208:211], v[148:151]\n"
"buffer_load_dwordx4 a[156:159], v103, s[40:43], s60 offen offset:1024 sc1\n"
"v_mfma_f32_16x16x32_bf16 v[180:183], a[72:75], a[196:199], v[180:183]\n"
"ds_read_b64_tr_b16 a[90:91], v109 offset:37632\n"
"v_mfma_f32_16x16x32_bf16 v[180:183], a[76:79], a[212:215], v[180:183]\n"
"ds_read_b64_tr_b16 a[92:93], v109 offset:41728\n"
"v_mfma_f32_16x16x32_bf16 v[212:215], a[72:75], a[200:203], v[212:215]\n"
"ds_read_b64_tr_b16 a[94:95], v109 offset:45824\n"
"v_mfma_f32_16x16x32_bf16 v[212:215], a[76:79], a[216:219], v[212:215]\n"
"buffer_load_dwordx4 v107, s[44:47], s61 offen offset:3072 lds\n"
"s_add_u32 s61, s61, 524288\n"
"v_mfma_f32_16x16x32_bf16 v[244:247], a[72:75], a[204:207], v[244:247]\n"
"v_mfma_f32_16x16x32_bf16 v[244:247], a[76:79], a[220:223], v[244:247]\n"
"s_waitcnt vmcnt(12) lgkmcnt(4)\n"
"s_barrier\n"
"s_mov_b32 s63, 1\n"
"LW_%=:\n"
"v_mfma_f32_16x16x32_bf16 v[152:155], a[80:83], a[192:195], v[152:155]\n"
"ds_read_b64_tr_b16 a[64:65], v108 offset:49152\n"
"v_mfma_f32_16x16x32_bf16 v[152:155], a[84:87], a[208:211], v[152:155]\n"
"ds_read_b64_tr_b16 a[66:67], v108 offset:53248\n"
"v_mfma_f32_16x16x32_bf16 v[184:187], a[80:83], a[196:199], v[184:187]\n"
"buffer_load_dwordx4 a[160:163], v100, s[40:43], s60 offen offset:2048 sc1\n"
"v_mfma_f32_16x16x32_bf16 v[184:187], a[84:87], a[212:215], v[184:187]\n"
"ds_read_b64_tr_b16 a[68:69], v108 offset:57344\n"
"v_mfma_f32_16x16x32_bf16 v[216:219], a[80:83], a[200:203], v[216:219]\n"
"ds_read_b64_tr_b16 a[70:71], v108 offset:61440\n"
"v_mfma_f32_16x16x32_bf16 v[216:219], a[84:87], a[216:219], v[216:219]\n"
"v_mfma_f32_16x16x32_bf16 v[248:251], a[80:83], a[204:207], v[248:251]\n"
"v_mfma_f32_16x16x32_bf16 v[248:251], a[84:87], a[220:223], v[248:251]\n"
"buffer_load_dwordx4 a[164:167], v101, s[40:43], s60 offen offset:2048 sc1\n"
"s_waitcnt lgkmcnt(4)\n"
"v_mfma_f32_16x16x32_bf16 v[156:159], a[88:91], a[192:195], v[156:159]\n"
"ds_read_b64_tr_b16 a[72:73], v109 offset:49152\n"
"v_mfma_f32_16x16x32_bf16 v[156:159], a[92:95], a[208:211], v[156:159]\n"
"ds_read_b64_tr_b16 a[74:75], v109 offset:53248\n"
"v_mfma_f32_16x16x32_bf16 v[188:191], a[88:91], a[196:199], v[188:191]\n"
"ds_read_b64_tr_b16 a[76:77], v109 offset:57344\n"
"v_mfma_f32_16x16x32_bf16 v[188:191], a[92:95], a[212:215], v[188:191]\n"
"ds_read_b64_tr_b16 a[78:79], v109 offset:61440\n"
"s_add_u32 m0, s62, 16384\n"
"v_mfma_f32_16x16x32_bf16 v[220:223], a[88:91], a[200:203], v[220:223]\n"
"buffer_load_dwordx4 v104, s[44:47], s61 offen lds\n"
"v_mfma_f32_16x16x32_bf16 v[220:223], a[92:95], a[216:219], v[220:223]\n"
"v_mfma_f32_16x16x32_bf16 v[252:255], a[88:91], a[204:207], v[252:255]\n"
"v_mfma_f32_16x16x32_bf16 v[252:255], a[92:95], a[220:223], v[252:255]\n"
"s_waitcnt lgkmcnt(4)\n"
"v_mfma_f32_16x16x32_bf16 v[128:131], a[64:67], a[224:227], v[128:131]\n"
"ds_read_b64_tr_b16 a[80:81], v108 offset:49408\n"
"v_mfma_f32_16x16x32_bf16 v[128:131], a[68:71], a[240:243], v[128:131]\n"
"buffer_load_dwordx4 a[168:171], v102, s[40:43], s60 offen offset:2048 sc1\n"
"v_mfma_f32_16x16x32_bf16 v[160:163], a[64:67], a[228:231], v[160:163]\n"
"ds_read_b64_tr_b16 a[82:83], v108 offset:53504\n"
"v_mfma_f32_16x16x32_bf16 v[160:163], a[68:71], a[244:247], v[160:163]\n"
"ds_read_b64_tr_b16 a[84:85], v108 offset:57600\n"
"v_mfma_f32_16x16x32_bf16 v[192:195], a[64:67], a[232:235], v[192:195]\n"
"ds_read_b64_tr_b16 a[86:87], v108 offset:61696\n"
"v_mfma_f32_16x16x32_bf16 v[192:195], a[68:71], a[248:251], v[192:195]\n"
"v_mfma_f32_16x16x32_bf16 v[224:227], a[64:67], a[236:239], v[224:227]\n"
"buffer_load_dwordx4 a[172:175], v103, s[40:43], s60 offen offset:2048 sc1\n"
"v_mfma_f32_16x16x32_bf16 v[224:227], a[68:71], a[252:255], v[224:227]\n"
"s_waitcnt lgkmcnt(4)\n"
"v_mfma_f32_16x16x32_bf16 v[132:135], a[72:75], a[224:227], v[132:135]\n"
"ds_read_b64_tr_b16 a[88:89], v109 offset:49408\n"
"v_mfma_f32_16x16x32_bf16 v[132:135], a[76:79], a[240:243], v[132:135]\n"
"ds_read_b64_tr_b16 a[90:91], v109 offset:53504\n"
"v_mfma_f32_16x16x32_bf16 v[164:167], a[72:75], a[228:231], v[164:167]\n"
"ds_read_b64_tr_b16 a[92:93], v109 offset:57600\n"
"v_mfma_f32_16x16x32_bf16 v[164:167], a[76:79], a[244:247], v[164:167]\n"
"ds_read_b64_tr_b16 a[94:95], v109 offset:61696\n"
"v_mfma_f32_16x16x32_bf16 v[196:199], a[72:75], a[232:235], v[196:199]\n"
"buffer_load_dwordx4 v105, s[44:47], s61 offen offset:1024 lds\n"
"v_mfma_f32_16x16x32_bf16 v[196:199], a[76:79], a[248:251], v[196:199]\n"
"v_mfma_f32_16x16x32_bf16 v[228:231], a[72:75], a[236:239], v[228:231]\n"
"v_mfma_f32_16x16x32_bf16 v[228:231], a[76:79], a[252:255], v[228:231]\n"
"s_waitcnt lgkmcnt(4)\n"
"v_mfma_f32_16x16x32_bf16 v[136:139], a[80:83], a[224:227], v[136:139]\n"
"ds_read_b64_tr_b16 a[64:65], v108 offset:49664\n"
"v_mfma_f32_16x16x32_bf16 v[136:139], a[84:87], a[240:243], v[136:139]\n"
"buffer_load_dwordx4 a[176:179], v100, s[40:43], s60 offen offset:3072 sc1\n"
"v_mfma_f32_16x16x32_bf16 v[168:171], a[80:83], a[228:231], v[168:171]\n"
"ds_read_b64_tr_b16 a[66:67], v108 offset:53760\n"
"v_mfma_f32_16x16x32_bf16 v[168:171], a[84:87], a[244:247], v[168:171]\n"
"ds_read_b64_tr_b16 a[68:69], v108 offset:57856\n"
"v_mfma_f32_16x16x32_bf16 v[200:203], a[80:83], a[232:235], v[200:203]\n"
"ds_read_b64_tr_b16 a[70:71], v108 offset:61952\n"
"v_mfma_f32_16x16x32_bf16 v[200:203], a[84:87], a[248:251], v[200:203]\n"
"v_mfma_f32_16x16x32_bf16 v[232:235], a[80:83], a[236:239], v[232:235]\n"
"buffer_load_dwordx4 a[180:183], v101, s[40:43], s60 offen offset:3072 sc1\n"
"v_mfma_f32_16x16x32_bf16 v[232:235], a[84:87], a[252:255], v[232:235]\n"
"s_waitcnt lgkmcnt(4)\n"
"v_mfma_f32_16x16x32_bf16 v[140:143], a[88:91], a[224:227], v[140:143]\n"
"ds_read_b64_tr_b16 a[72:73], v109 offset:49664\n"
"v_mfma_f32_16x16x32_bf16 v[140:143], a[92:95], a[240:243], v[140:143]\n"
"ds_read_b64_tr_b16 a[74:75], v109 offset:53760\n"
"v_mfma_f32_16x16x32_bf16 v[172:175], a[88:91], a[228:231], v[172:175]\n"
"ds_read_b64_tr_b16 a[76:77], v109 offset:57856\n"
"v_mfma_f32_16x16x32_bf16 v[172:175], a[92:95], a[244:247], v[172:175]\n"
"buffer_load_dwordx4 v106, s[44:47], s61 offen offset:2048 lds\n"
"v_mfma_f32_16x16x32_bf16 v[204:207], a[88:91], a[232:235], v[204:207]\n"
"ds_read_b64_tr_b16 a[78:79], v109 offset:61952\n"
"v_mfma_f32_16x16x32_bf16 v[204:207], a[92:95], a[248:251], v[204:207]\n"
"v_mfma_f32_16x16x32_bf16 v[236:239], a[88:91], a[236:239], v[236:239]\n"
"v_mfma_f32_16x16x32_bf16 v[236:239], a[92:95], a[252:255], v[236:239]\n"
"s_waitcnt lgkmcnt(4)\n"
"v_mfma_f32_16x16x32_bf16 v[144:147], a[64:67], a[224:227], v[144:147]\n"
"buffer_load_dwordx4 a[184:187], v102, s[40:43], s60 offen offset:3072 sc1\n"
"v_mfma_f32_16x16x32_bf16 v[144:147], a[68:71], a[240:243], v[144:147]\n"
"ds_read_b64_tr_b16 a[80:81], v108 offset:49920\n"
"v_mfma_f32_16x16x32_bf16 v[176:179], a[64:67], a[228:231], v[176:179]\n"
"ds_read_b64_tr_b16 a[82:83], v108 offset:54016\n"
"v_mfma_f32_16x16x32_bf16 v[176:179], a[68:71], a[244:247], v[176:179]\n"
"ds_read_b64_tr_b16 a[84:85], v108 offset:58112\n"
"v_mfma_f32_16x16x32_bf16 v[208:211], a[64:67], a[232:235], v[208:211]\n"
"ds_read_b64_tr_b16 a[86:87], v108 offset:62208\n"
"v_mfma_f32_16x16x32_bf16 v[208:211], a[68:71], a[248:251], v[208:211]\n"
"buffer_load_dwordx4 a[188:191], v103, s[40:43], s60 offen offset:3072 sc1\n"
"s_add_u32 s60, s60, 4096\n"
"v_mfma_f32_16x16x32_bf16 v[240:243], a[64:67], a[236:239], v[240:243]\n"
"v_mfma_f32_16x16x32_bf16 v[240:243], a[68:71], a[252:255], v[240:243]\n"
"s_waitcnt lgkmcnt(4)\n"
"v_mfma_f32_16x16x32_bf16 v[148:151], a[72:75], a[224:227], v[148:151]\n"
"ds_read_b64_tr_b16 a[88:89], v109 offset:49920\n"
"v_mfma_f32_16x16x32_bf16 v[148:151], a[76:79], a[240:243], v[148:151]\n"
"ds_read_b64_tr_b16 a[90:91], v109 offset:54016\n"
"v_mfma_f32_16x16x32_bf16 v[180:183], a[72:75], a[228:231], v[180:183]\n"
"ds_read_b64_tr_b16 a[92:93], v109 offset:58112\n"
"v_mfma_f32_16x16x32_bf16 v[180:183], a[76:79], a[244:247], v[180:183]\n"
"buffer_load_dwordx4 v107, s[44:47], s61 offen offset:3072 lds\n"
"s_add_u32 s61, s61, 524288\n"
"v_mfma_f32_16x16x32_bf16 v[212:215], a[72:75], a[232:235], v[212:215]\n"
"ds_read_b64_tr_b16 a[94:95], v109 offset:62208\n"
"v_mfma_f32_16x16x32_bf16 v[212:215], a[76:79], a[248:251], v[212:215]\n"
"v_mfma_f32_16x16x32_bf16 v[244:247], a[72:75], a[236:239], v[244:247]\n"
"v_mfma_f32_16x16x32_bf16 v[244:247], a[76:79], a[252:255], v[244:247]\n"
"s_waitcnt vmcnt(12) lgkmcnt(4)\n"
"s_barrier\n"
"v_mfma_f32_16x16x32_bf16 v[152:155], a[80:83], a[224:227], v[152:155]\n"
"ds_read_b64_tr_b16 a[64:65], v108 offset:0\n"
"v_mfma_f32_16x16x32_bf16 v[152:155], a[84:87], a[240:243], v[152:155]\n"
"ds_read_b64_tr_b16 a[66:67], v108 offset:4096\n"
"v_mfma_f32_16x16x32_bf16 v[184:187], a[80:83], a[228:231], v[184:187]\n"
"buffer_load_dwordx4 a[192:195], v100, s[40:43], s60 offen sc1\n"
"v_mfma_f32_16x16x32_bf16 v[184:187], a[84:87], a[244:247], v[184:187]\n"
"ds_read_b64_tr_b16 a[68:69], v108 offset:8192\n"
"v_mfma_f32_16x16x32_bf16 v[216:219], a[80:83], a[232:235], v[216:219]\n"
"ds_read_b64_tr_b16 a[70:71], v108 offset:12288\n"
"v_mfma_f32_16x16x32_bf16 v[216:219], a[84:87], a[248:251], v[216:219]\n"
"v_mfma_f32_16x16x32_bf16 v[248:251], a[80:83], a[236:239], v[248:251]\n"
"v_mfma_f32_16x16x32_bf16 v[248:251], a[84:87], a[252:255], v[248:251]\n"
"buffer_load_dwordx4 a[196:199], v101, s[40:43], s60 offen sc1\n"
"s_waitcnt lgkmcnt(4)\n"
"v_mfma_f32_16x16x32_bf16 v[156:159], a[88:91], a[224:227], v[156:159]\n"
"ds_read_b64_tr_b16 a[72:73], v109 offset:0\n"
"v_mfma_f32_16x16x32_bf16 v[156:159], a[92:95], a[240:243], v[156:159]\n"
"ds_read_b64_tr_b16 a[74:75], v109 offset:4096\n"
"v_mfma_f32_16x16x32_bf16 v[188:191], a[88:91], a[228:231], v[188:191]\n"
"ds_read_b64_tr_b16 a[76:77], v109 offset:8192\n"
"v_mfma_f32_16x16x32_bf16 v[188:191], a[92:95], a[244:247], v[188:191]\n"
"ds_read_b64_tr_b16 a[78:79], v109 offset:12288\n"
"s_add_u32 m0, s62, 32768\n"
"v_mfma_f32_16x16x32_bf16 v[220:223], a[88:91], a[232:235], v[220:223]\n"
"buffer_load_dwordx4 v104, s[44:47], s61 offen lds\n"
"v_mfma_f32_16x16x32_bf16 v[220:223], a[92:95], a[248:251], v[220:223]\n"
"v_mfma_f32_16x16x32_bf16 v[252:255], a[88:91], a[236:239], v[252:255]\n"
"v_mfma_f32_16x16x32_bf16 v[252:255], a[92:95], a[252:255], v[252:255]\n"
"s_waitcnt lgkmcnt(4)\n"
"v_mfma_f32_16x16x32_bf16 v[128:131], a[64:67], a[128:131], v[128:131]\n"
"ds_read_b64_tr_b16 a[80:81], v108 offset:256\n"
"v_mfma_f32_16x16x32_bf16 v[128:131], a[68:71], a[144:147], v[128:131]\n"
"buffer_load_dwordx4 a[200:203], v102, s[40:43], s60 offen sc1\n"
"v_mfma_f32_16x16x32_bf16 v[160:163], a[64:67], a[132:135], v[160:163]\n"
"ds_read_b64_tr_b16 a[82:83], v108 offset:4352\n"
"v_mfma_f32_16x16x32_bf16 v[160:163], a[68:71], a[148:151], v[160:163]\n"
"ds_read_b64_tr_b16 a[84:85], v108 offset:8448\n"
"v_mfma_f32_16x16x32_bf16 v[192:195], a[64:67], a[136:139], v[192:195]\n"
"ds_read_b64_tr_b16 a[86:87], v108 offset:12544\n"
"v_mfma_f32_16x16x32_bf16 v[192:195], a[68:71], a[152:155], v[192:195]\n"
"v_mfma_f32_16x16x32_bf16 v[224:227], a[64:67], a[140:143], v[224:227]\n"
"buffer_load_dwordx4 a[204:207], v103, s[40:43], s60 offen sc1\n"
"v_mfma_f32_16x16x32_bf16 v[224:227], a[68:71], a[156:159], v[224:227]\n"
"s_waitcnt lgkmcnt(4)\n"
"v_mfma_f32_16x16x32_bf16 v[132:135], a[72:75], a[128:131], v[132:135]\n"
"ds_read_b64_tr_b16 a[88:89], v109 offset:256\n"
"v_mfma_f32_16x16x32_bf16 v[132:135], a[76:79], a[144:147], v[132:135]\n"
"ds_read_b64_tr_b16 a[90:91], v109 offset:4352\n"
"v_mfma_f32_16x16x32_bf16 v[164:167], a[72:75], a[132:135], v[164:167]\n"
"ds_read_b64_tr_b16 a[92:93], v109 offset:8448\n"
"v_mfma_f32_16x16x32_bf16 v[164:167], a[76:79], a[148:151], v[164:167]\n"
"ds_read_b64_tr_b16 a[94:95], v109 offset:12544\n"
"v_mfma_f32_16x16x32_bf16 v[196:199], a[72:75], a[136:139], v[196:199]\n"
"buffer_load_dwordx4 v105, s[44:47], s61 offen offset:1024 lds\n"
"v_mfma_f32_16x16x32_bf16 v[196:199], a[76:79], a[152:155], v[196:199]\n"
"v_mfma_f32_16x16x32_bf16 v[228:231], a[72:75], a[140:143], v[228:231]\n"
"v_mfma_f32_16x16x32_bf16 v[228:231], a[76:79], a[156:159], v[228:231]\n"
"s_waitcnt lgkmcnt(4)\n"
"v_mfma_f32_16x16x32_bf16 v[136:139], a[80:83], a[128:131], v[136:139]\n"
"ds_read_b64_tr_b16 a[64:65], v108 offset:512\n"
"v_mfma_f32_16x16x32_bf16 v[136:139], a[84:87], a[144:147], v[136:139]\n"
"buffer_load_dwordx4 a[208:211], v100, s[40:43], s60 offen offset:1024 sc1\n"
"v_mfma_f32_16x16x32_bf16 v[168:171], a[80:83], a[132:135], v[168:171]\n"
"ds_read_b64_tr_b16 a[66:67], v108 offset:4608\n"
"v_mfma_f32_16x16x32_bf16 v[168:171], a[84:87], a[148:151], v[168:171]\n"
"ds_read_b64_tr_b16 a[68:69], v108 offset:8704\n"
"v_mfma_f32_16x16x32_bf16 v[200:203], a[80:83], a[136:139], v[200:203]\n"
"ds_read_b64_tr_b16 a[70:71], v108 offset:12800\n"
"v_mfma_f32_16x16x32_bf16 v[200:203], a[84:87], a[152:155], v[200:203]\n"
"v_mfma_f32_16x16x32_bf16 v[232:235], a[80:83], a[140:143], v[232:235]\n"
"buffer_load_dwordx4 a[212:215], v101, s[40:43], s60 offen offset:1024 sc1\n"
"v_mfma_f32_16x16x32_bf16 v[232:235], a[84:87], a[156:159], v[232:235]\n"
"s_waitcnt lgkmcnt(4)\n"
"v_mfma_f32_16x16x32_bf16 v[140:143], a[88:91], a[128:131], v[140:143]\n"
"ds_read_b64_tr_b16 a[72:73], v109 offset:512\n"
"v_mfma_f32_16x16x32_bf16 v[140:143], a[92:95], a[144:147], v[140:143]\n"
"ds_read_b64_tr_b16 a[74:75], v109 offset:4608\n"
"v_mfma_f32_16x16x32_bf16 v[172:175], a[88:91], a[132:135], v[172:175]\n"
"ds_read_b64_tr_b16 a[76:77], v109 offset:8704\n"
"v_mfma_f32_16x16x32_bf16 v[172:175], a[92:95], a[148:151], v[172:175]\n"
"buffer_load_dwordx4 v106, s[44:47], s61 offen offset:2048 lds\n"
"v_mfma_f32_16x16x32_bf16 v[204:207], a[88:91], a[136:139], v[204:207]\n"
"ds_read_b64_tr_b16 a[78:79], v109 offset:12800\n"
"v_mfma_f32_16x16x32_bf16 v[204:207], a[92:95], a[152:155], v[204:207]\n"
"v_mfma_f32_16x16x32_bf16 v[236:239], a[88:91], a[140:143], v[236:239]\n"
"v_mfma_f32_16x16x32_bf16 v[236:239], a[92:95], a[156:159], v[236:239]\n"
"s_waitcnt lgkmcnt(4)\n"
"v_mfma_f32_16x16x32_bf16 v[144:147], a[64:67], a[128:131], v[144:147]\n"
"buffer_load_dwordx4 a[216:219], v102, s[40:43], s60 offen offset:1024 sc1\n"
"v_mfma_f32_16x16x32_bf16 v[144:147], a[68:71], a[144:147], v[144:147]\n"
"ds_read_b64_tr_b16 a[80:81], v108 offset:768\n"
"v_mfma_f32_16x16x32_bf16 v[176:179], a[64:67], a[132:135], v[176:179]\n"
"ds_read_b64_tr_b16 a[82:83], v108 offset:4864\n"
"v_mfma_f32_16x16x32_bf16 v[176:179], a[68:71], a[148:151], v[176:179]\n"
"ds_read_b64_tr_b16 a[84:85], v108 offset:8960\n"
"v_mfma_f32_16x16x32_bf16 v[208:211], a[64:67], a[136:139], v[208:211]\n"
"ds_read_b64_tr_b16 a[86:87], v108 offset:13056\n"
"v_mfma_f32_16x16x32_bf16 v[208:211], a[68:71], a[152:155], v[208:211]\n"
"buffer_load_dwordx4 a[220:223], v103, s[40:43], s60 offen offset:1024 sc1\n"
"v_mfma_f32_16x16x32_bf16 v[240:243], a[64:67], a[140:143], v[240:243]\n"
"v_mfma_f32_16x16x32_bf16 v[240:243], a[68:71], a[156:159], v[240:243]\n"
"s_waitcnt lgkmcnt(4)\n"
"v_mfma_f32_16x16x32_bf16 v[148:151], a[72:75], a[128:131], v[148:151]\n"
"ds_read_b64_tr_b16 a[88:89], v109 offset:768\n"
"v_mfma_f32_16x16x32_bf16 v[148:151], a[76:79], a[144:147], v[148:151]\n"
"ds_read_b64_tr_b16 a[90:91], v109 offset:4864\n"
"v_mfma_f32_16x16x32_bf16 v[180:183], a[72:75], a[132:135], v[180:183]\n"
"ds_read_b64_tr_b16 a[92:93], v109 offset:8960\n"
"v_mfma_f32_16x16x32_bf16 v[180:183], a[76:79], a[148:151], v[180:183]\n"
"buffer_load_dwordx4 v107, s[44:47], s61 offen offset:3072 lds\n"
"s_add_u32 s61, s61, 524288\n"
"v_mfma_f32_16x16x32_bf16 v[212:215], a[72:75], a[136:139], v[212:215]\n"
"ds_read_b64_tr_b16 a[94:95], v109 offset:13056\n"
"v_mfma_f32_16x16x32_bf16 v[212:215], a[76:79], a[152:155], v[212:215]\n"
"v_mfma_f32_16x16x32_bf16 v[244:247], a[72:75], a[140:143], v[244:247]\n"
"v_mfma_f32_16x16x32_bf16 v[244:247], a[76:79], a[156:159], v[244:247]\n"
"s_waitcnt vmcnt(12) lgkmcnt(4)\n"
"s_barrier\n"
"v_mfma_f32_16x16x32_bf16 v[152:155], a[80:83], a[128:131], v[152:155]\n"
"ds_read_b64_tr_b16 a[64:65], v108 offset:16384\n"
"v_mfma_f32_16x16x32_bf16 v[152:155], a[84:87], a[144:147], v[152:155]\n"
"ds_read_b64_tr_b16 a[66:67], v108 offset:20480\n"
"v_mfma_f32_16x16x32_bf16 v[184:187], a[80:83], a[132:135], v[184:187]\n"
"buffer_load_dwordx4 a[224:227], v100, s[40:43], s60 offen offset:2048 sc1\n"
"v_mfma_f32_16x16x32_bf16 v[184:187], a[84:87], a[148:151], v[184:187]\n"
"ds_read_b64_tr_b16 a[68:69], v108 offset:24576\n"
"v_mfma_f32_16x16x32_bf16 v[216:219], a[80:83], a[136:139], v[216:219]\n"
"ds_read_b64_tr_b16 a[70:71], v108 offset:28672\n"
"v_mfma_f32_16x16x32_bf16 v[216:219], a[84:87], a[152:155], v[216:219]\n"
"v_mfma_f32_16x16x32_bf16 v[248:251], a[80:83], a[140:143], v[248:251]\n"
"v_mfma_f32_16x16x32_bf16 v[248:251], a[84:87], a[156:159], v[248:251]\n"
"buffer_load_dwordx4 a[228:231], v101, s[40:43], s60 offen offset:2048 sc1\n"
"s_waitcnt lgkmcnt(4)\n"
"v_mfma_f32_16x16x32_bf16 v[156:159], a[88:91], a[128:131], v[156:159]\n"
"ds_read_b64_tr_b16 a[72:73], v109 offset:16384\n"
"v_mfma_f32_16x16x32_bf16 v[156:159], a[92:95], a[144:147], v[156:159]\n"
"ds_read_b64_tr_b16 a[74:75], v109 offset:20480\n"
"v_mfma_f32_16x16x32_bf16 v[188:191], a[88:91], a[132:135], v[188:191]\n"
"ds_read_b64_tr_b16 a[76:77], v109 offset:24576\n"
"v_mfma_f32_16x16x32_bf16 v[188:191], a[92:95], a[148:151], v[188:191]\n"
"ds_read_b64_tr_b16 a[78:79], v109 offset:28672\n"
"s_add_u32 m0, s62, 49152\n"
"v_mfma_f32_16x16x32_bf16 v[220:223], a[88:91], a[136:139], v[220:223]\n"
"buffer_load_dwordx4 v104, s[44:47], s61 offen lds\n"
"v_mfma_f32_16x16x32_bf16 v[220:223], a[92:95], a[152:155], v[220:223]\n"
"v_mfma_f32_16x16x32_bf16 v[252:255], a[88:91], a[140:143], v[252:255]\n"
"v_mfma_f32_16x16x32_bf16 v[252:255], a[92:95], a[156:159], v[252:255]\n"
"s_waitcnt lgkmcnt(4)\n"
"v_mfma_f32_16x16x32_bf16 v[128:131], a[64:67], a[160:163], v[128:131]\n"
"ds_read_b64_tr_b16 a[80:81], v108 offset:16640\n"
"v_mfma_f32_16x16x32_bf16 v[128:131], a[68:71], a[176:179], v[128:131]\n"
"buffer_load_dwordx4 a[232:235], v102, s[40:43], s60 offen offset:2048 sc1\n"
"v_mfma_f32_16x16x32_bf16 v[160:163], a[64:67], a[164:167], v[160:163]\n"
"ds_read_b64_tr_b16 a[82:83], v108 offset:20736\n"
"v_mfma_f32_16x16x32_bf16 v[160:163], a[68:71], a[180:183], v[160:163]\n"
"ds_read_b64_tr_b16 a[84:85], v108 offset:24832\n"
"v_mfma_f32_16x16x32_bf16 v[192:195], a[64:67], a[168:171], v[192:195]\n"
"ds_read_b64_tr_b16 a[86:87], v108 offset:28928\n"
"v_mfma_f32_16x16x32_bf16 v[192:195], a[68:71], a[184:187], v[192:195]\n"
"v_mfma_f32_16x16x32_bf16 v[224:227], a[64:67], a[172:175], v[224:227]\n"
"buffer_load_dwordx4 a[236:239], v103, s[40:43], s60 offen offset:2048 sc1\n"
"v_mfma_f32_16x16x32_bf16 v[224:227], a[68:71], a[188:191], v[224:227]\n"
"s_waitcnt lgkmcnt(4)\n"
"v_mfma_f32_16x16x32_bf16 v[132:135], a[72:75], a[160:163], v[132:135]\n"
"ds_read_b64_tr_b16 a[88:89], v109 offset:16640\n"
"v_mfma_f32_16x16x32_bf16 v[132:135], a[76:79], a[176:179], v[132:135]\n"
"ds_read_b64_tr_b16 a[90:91], v109 offset:20736\n"
"v_mfma_f32_16x16x32_bf16 v[164:167], a[72:75], a[164:167], v[164:167]\n"
"ds_read_b64_tr_b16 a[92:93], v109 offset:24832\n"
"v_mfma_f32_16x16x32_bf16 v[164:167], a[76:79], a[180:183], v[164:167]\n"
"ds_read_b64_tr_b16 a[94:95], v109 offset:28928\n"
"v_mfma_f32_16x16x32_bf16 v[196:199], a[72:75], a[168:171], v[196:199]\n"
"buffer_load_dwordx4 v105, s[44:47], s61 offen offset:1024 lds\n"
"v_mfma_f32_16x16x32_bf16 v[196:199], a[76:79], a[184:187], v[196:199]\n"
"v_mfma_f32_16x16x32_bf16 v[228:231], a[72:75], a[172:175], v[228:231]\n"
"v_mfma_f32_16x16x32_bf16 v[228:231], a[76:79], a[188:191], v[228:231]\n"
"s_waitcnt lgkmcnt(4)\n"
"v_mfma_f32_16x16x32_bf16 v[136:139], a[80:83], a[160:163], v[136:139]\n"
"ds_read_b64_tr_b16 a[64:65], v108 offset:16896\n"
"v_mfma_f32_16x16x32_bf16 v[136:139], a[84:87], a[176:179], v[136:139]\n"
"buffer_load_dwordx4 a[240:243], v100, s[40:43], s60 offen offset:3072 sc1\n"
"v_mfma_f32_16x16x32_bf16 v[168:171], a[80:83], a[164:167], v[168:171]\n"
"ds_read_b64_tr_b16 a[66:67], v108 offset:20992\n"
"v_mfma_f32_16x16x32_bf16 v[168:171], a[84:87], a[180:183], v[168:171]\n"
"ds_read_b64_tr_b16 a[68:69], v108 offset:25088\n"
"v_mfma_f32_16x16x32_bf16 v[200:203], a[80:83], a[168:171], v[200:203]\n"
"ds_read_b64_tr_b16 a[70:71], v108 offset:29184\n"
"v_mfma_f32_16x16x32_bf16 v[200:203], a[84:87], a[184:187], v[200:203]\n"
"v_mfma_f32_16x16x32_bf16 v[232:235], a[80:83], a[172:175], v[232:235]\n"
"buffer_load_dwordx4 a[244:247], v101, s[40:43], s60 offen offset:3072 sc1\n"
"v_mfma_f32_16x16x32_bf16 v[232:235], a[84:87], a[188:191], v[232:235]\n"
"s_waitcnt lgkmcnt(4)\n"
"v_mfma_f32_16x16x32_bf16 v[140:143], a[88:91], a[160:163], v[140:143]\n"
"ds_read_b64_tr_b16 a[72:73], v109 offset:16896\n"
"v_mfma_f32_16x16x32_bf16 v[140:143], a[92:95], a[176:179], v[140:143]\n"
"ds_read_b64_tr_b16 a[74:75], v109 offset:20992\n"
"v_mfma_f32_16x16x32_bf16 v[172:175], a[88:91], a[164:167], v[172:175]\n"
"ds_read_b64_tr_b16 a[76:77], v109 offset:25088\n"
"v_mfma_f32_16x16x32_bf16 v[172:175], a[92:95], a[180:183], v[172:175]\n"
"buffer_load_dwordx4 v106, s[44:47], s61 offen offset:2048 lds\n"
"v_mfma_f32_16x16x32_bf16 v[204:207], a[88:91], a[168:171], v[204:207]\n"
"ds_read_b64_tr_b16 a[78:79], v109 offset:29184\n"
"v_mfma_f32_16x16x32_bf16 v[204:207], a[92:95], a[184:187], v[204:207]\n"
"v_mfma_f32_16x16x32_bf16 v[236:239], a[88:91], a[172:175], v[236:239]\n"
"v_mfma_f32_16x16x32_bf16 v[236:239], a[92:95], a[188:191], v[236:239]\n"
"s_waitcnt lgkmcnt(4)\n"
"v_mfma_f32_16x16x32_bf16 v[144:147], a[64:67], a[160:163], v[144:147]\n"
"buffer_load_dwordx4 a[248:251], v102, s[40:43], s60 offen offset:3072 sc1\n"
"v_mfma_f32_16x16x32_bf16 v[144:147], a[68:71], a[176:179], v[144:147]\n"
"ds_read_b64_tr_b16 a[80:81], v108 offset:17152\n"
"v_mfma_f32_16x16x32_bf16 v[176:179], a[64:67], a[164:167], v[176:179]\n"
"ds_read_b64_tr_b16 a[82:83], v108 offset:21248\n"
"v_mfma_f32_16x16x32_bf16 v[176:179], a[68:71], a[180:183], v[176:179]\n"
"ds_read_b64_tr_b16 a[84:85], v108 offset:25344\n"
"v_mfma_f32_16x16x32_bf16 v[208:211], a[64:67], a[168:171], v[208:211]\n"
"ds_read_b64_tr_b16 a[86:87], v108 offset:29440\n"
"v_mfma_f32_16x16x32_bf16 v[208:211], a[68:71], a[184:187], v[208:211]\n"
"buffer_load_dwordx4 a[252:255], v103, s[40:43], s60 offen offset:3072 sc1\n"
"s_add_u32 s60, s60, 4096\n"
"v_mfma_f32_16x16x32_bf16 v[240:243], a[64:67], a[172:175], v[240:243]\n"
"v_mfma_f32_16x16x32_bf16 v[240:243], a[68:71], a[188:191], v[240:243]\n"
"s_waitcnt lgkmcnt(4)\n"
"v_mfma_f32_16x16x32_bf16 v[148:151], a[72:75], a[160:163], v[148:151]\n"
"ds_read_b64_tr_b16 a[88:89], v109 offset:17152\n"
"v_mfma_f32_16x16x32_bf16 v[148:151], a[76:79], a[176:179], v[148:151]\n"
"ds_read_b64_tr_b16 a[90:91], v109 offset:21248\n"
"v_mfma_f32_16x16x32_bf16 v[180:183], a[72:75], a[164:167], v[180:183]\n"
"ds_read_b64_tr_b16 a[92:93], v109 offset:25344\n"
"v_mfma_f32_16x16x32_bf16 v[180:183], a[76:79], a[180:183], v[180:183]\n"
"buffer_load_dwordx4 v107, s[44:47], s61 offen offset:3072 lds\n"
"s_add_u32 s61, s61, 524288\n"
"v_mfma_f32_16x16x32_bf16 v[212:215], a[72:75], a[168:171], v[212:215]\n"
"ds_read_b64_tr_b16 a[94:95], v109 offset:29440\n"
"v_mfma_f32_16x16x32_bf16 v[212:215], a[76:79], a[184:187], v[212:215]\n"
"v_mfma_f32_16x16x32_bf16 v[244:247], a[72:75], a[172:175], v[244:247]\n"
"v_mfma_f32_16x16x32_bf16 v[244:247], a[76:79], a[188:191], v[244:247]\n"
"s_waitcnt vmcnt(12) lgkmcnt(4)\n"
"s_barrier\n"
"v_mfma_f32_16x16x32_bf16 v[152:155], a[80:83], a[160:163], v[152:155]\n"
"ds_read_b64_tr_b16 a[64:65], v108 offset:32768\n"
"v_mfma_f32_16x16x32_bf16 v[152:155], a[84:87], a[176:179], v[152:155]\n"
"ds_read_b64_tr_b16 a[66:67], v108 offset:36864\n"
"v_mfma_f32_16x16x32_bf16 v[184:187], a[80:83], a[164:167], v[184:187]\n"
"buffer_load_dwordx4 a[128:131], v100, s[40:43], s60 offen sc1\n"
"v_mfma_f32_16x16x32_bf16 v[184:187], a[84:87], a[180:183], v[184:187]\n"
"ds_read_b64_tr_b16 a[68:69], v108 offset:40960\n"
"v_mfma_f32_16x16x32_bf16 v[216:219], a[80:83], a[168:171], v[216:219]\n"
"ds_read_b64_tr_b16 a[70:71], v108 offset:45056\n"
"v_mfma_f32_16x16x32_bf16 v[216:219], a[84:87], a[184:187], v[216:219]\n"
"v_mfma_f32_16x16x32_bf16 v[248:251], a[80:83], a[172:175], v[248:251]\n"
"v_mfma_f32_16x16x32_bf16 v[248:251], a[84:87], a[188:191], v[248:251]\n"
"buffer_load_dwordx4 a[132:135], v101, s[40:43], s60 offen sc1\n"
"s_waitcnt lgkmcnt(4)\n"
"v_mfma_f32_16x16x32_bf16 v[156:159], a[88:91], a[160:163], v[156:159]\n"
"ds_read_b64_tr_b16 a[72:73], v109 offset:32768\n"
"v_mfma_f32_16x16x32_bf16 v[156:159], a[92:95], a[176:179], v[156:159]\n"
"ds_read_b64_tr_b16 a[74:75], v109 offset:36864\n"
"v_mfma_f32_16x16x32_bf16 v[188:191], a[88:91], a[164:167], v[188:191]\n"
"ds_read_b64_tr_b16 a[76:77], v109 offset:40960\n"
"v_mfma_f32_16x16x32_bf16 v[188:191], a[92:95], a[180:183], v[188:191]\n"
"ds_read_b64_tr_b16 a[78:79], v109 offset:45056\n"
"s_add_u32 m0, s62, 0\n"
"v_mfma_f32_16x16x32_bf16 v[220:223], a[88:91], a[168:171], v[220:223]\n"
"buffer_load_dwordx4 v104, s[44:47], s61 offen lds\n"
"v_mfma_f32_16x16x32_bf16 v[220:223], a[92:95], a[184:187], v[220:223]\n"
"v_mfma_f32_16x16x32_bf16 v[252:255], a[88:91], a[172:175], v[252:255]\n"
"v_mfma_f32_16x16x32_bf16 v[252:255], a[92:95], a[188:191], v[252:255]\n"
"s_waitcnt lgkmcnt(4)\n"
"v_mfma_f32_16x16x32_bf16 v[128:131], a[64:67], a[192:195], v[128:131]\n"
"ds_read_b64_tr_b16 a[80:81], v108 offset:33024\n"
"v_mfma_f32_16x16x32_bf16 v[128:131], a[68:71], a[208:211], v[128:131]\n"
"buffer_load_dwordx4 a[136:139], v102, s[40:43], s60 offen sc1\n"
"v_mfma_f32_16x16x32_bf16 v[160:163], a[64:67], a[196:199], v[160:163]\n"
"ds_read_b64_tr_b16 a[82:83], v108 offset:37120\n"
"v_mfma_f32_16x16x32_bf16 v[160:163], a[68:71], a[212:215], v[160:163]\n"
"ds_read_b64_tr_b16 a[84:85], v108 offset:41216\n"
"v_mfma_f32_16x16x32_bf16 v[192:195], a[64:67], a[200:203], v[192:195]\n"
"ds_read_b64_tr_b16 a[86:87], v108 offset:45312\n"
"v_mfma_f32_16x16x32_bf16 v[192:195], a[68:71], a[216:219], v[192:195]\n"
"v_mfma_f32_16x16x32_bf16 v[224:227], a[64:67], a[204:207], v[224:227]\n"
"buffer_load_dwordx4 a[140:143], v103, s[40:43], s60 offen sc1\n"
"v_mfma_f32_16x16x32_bf16 v[224:227], a[68:71], a[220:223], v[224:227]\n"
"s_waitcnt lgkmcnt(4)\n"
"v_mfma_f32_16x16x32_bf16 v[132:135], a[72:75], a[192:195], v[132:135]\n"
"ds_read_b64_tr_b16 a[88:89], v109 offset:33024\n"
"v_mfma_f32_16x16x32_bf16 v[132:135], a[76:79], a[208:211], v[132:135]\n"
"ds_read_b64_tr_b16 a[90:91], v109 offset:37120\n"
"v_mfma_f32_16x16x32_bf16 v[164:167], a[72:75], a[196:199], v[164:167]\n"
"ds_read_b64_tr_b16 a[92:93], v109 offset:41216\n"
"v_mfma_f32_16x16x32_bf16 v[164:167], a[76:79], a[212:215], v[164:167]\n"
"ds_read_b64_tr_b16 a[94:95], v109 offset:45312\n"
"v_mfma_f32_16x16x32_bf16 v[196:199], a[72:75], a[200:203], v[196:199]\n"
"buffer_load_dwordx4 v105, s[44:47], s61 offen offset:1024 lds\n"
"v_mfma_f32_16x16x32_bf16 v[196:199], a[76:79], a[216:219], v[196:199]\n"
"v_mfma_f32_16x16x32_bf16 v[228:231], a[72:75], a[204:207], v[228:231]\n"
"v_mfma_f32_16x16x32_bf16 v[228:231], a[76:79], a[220:223], v[228:231]\n"
"s_waitcnt lgkmcnt(4)\n"
"v_mfma_f32_16x16x32_bf16 v[136:139], a[80:83], a[192:195], v[136:139]\n"
"ds_read_b64_tr_b16 a[64:65], v108 offset:33280\n"
"v_mfma_f32_16x16x32_bf16 v[136:139], a[84:87], a[208:211], v[136:139]\n"
"buffer_load_dwordx4 a[144:147], v100, s[40:43], s60 offen offset:1024 sc1\n"
"v_mfma_f32_16x16x32_bf16 v[168:171], a[80:83], a[196:199], v[168:171]\n"
"ds_read_b64_tr_b16 a[66:67], v108 offset:37376\n"
"v_mfma_f32_16x16x32_bf16 v[168:171], a[84:87], a[212:215], v[168:171]\n"
"ds_read_b64_tr_b16 a[68:69], v108 offset:41472\n"
"v_mfma_f32_16x16x32_bf16 v[200:203], a[80:83], a[200:203], v[200:203]\n"
"ds_read_b64_tr_b16 a[70:71], v108 offset:45568\n"
"v_mfma_f32_16x16x32_bf16 v[200:203], a[84:87], a[216:219], v[200:203]\n"
"v_mfma_f32_16x16x32_bf16 v[232:235], a[80:83], a[204:207], v[232:235]\n"
"buffer_load_dwordx4 a[148:151], v101, s[40:43], s60 offen offset:1024 sc1\n"
"v_mfma_f32_16x16x32_bf16 v[232:235], a[84:87], a[220:223], v[232:235]\n"
"s_waitcnt lgkmcnt(4)\n"
"v_mfma_f32_16x16x32_bf16 v[140:143], a[88:91], a[192:195], v[140:143]\n"
"ds_read_b64_tr_b16 a[72:73], v109 offset:33280\n"
"v_mfma_f32_16x16x32_bf16 v[140:143], a[92:95], a[208:211], v[140:143]\n"
"ds_read_b64_tr_b16 a[74:75], v109 offset:37376\n"
"v_mfma_f32_16x16x32_bf16 v[172:175], a[88:91], a[196:199], v[172:175]\n"
"ds_read_b64_tr_b16 a[76:77], v109 offset:41472\n"
"v_mfma_f32_16x16x32_bf16 v[172:175], a[92:95], a[212:215], v[172:175]\n"
"buffer_load_dwordx4 v106, s[44:47], s61 offen offset:2048 lds\n"
"v_mfma_f32_16x16x32_bf16 v[204:207], a[88:91], a[200:203], v[204:207]\n"
"ds_read_b64_tr_b16 a[78:79], v109 offset:45568\n"
"v_mfma_f32_16x16x32_bf16 v[204:207], a[92:95], a[216:219], v[204:207]\n"
"v_mfma_f32_16x16x32_bf16 v[236:239], a[88:91], a[204:207], v[236:239]\n"
"v_mfma_f32_16x16x32_bf16 v[236:239], a[92:95], a[220:223], v[236:239]\n"
"s_waitcnt lgkmcnt(4)\n"
"v_mfma_f32_16x16x32_bf16 v[144:147], a[64:67], a[192:195], v[144:147]\n"
"buffer_load_dwordx4 a[152:155], v102, s[40:43], s60 offen offset:1024 sc1\n"
"v_mfma_f32_16x16x32_bf16 v[144:147], a[68:71], a[208:211], v[144:147]\n"
"ds_read_b64_tr_b16 a[80:81], v108 offset:33536\n"
"v_mfma_f32_16x16x32_bf16 v[176:179], a[64:67], a[196:199], v[176:179]\n"
"ds_read_b64_tr_b16 a[82:83], v108 offset:37632\n"
"v_mfma_f32_16x16x32_bf16 v[176:179], a[68:71], a[212:215], v[176:179]\n"
"ds_read_b64_tr_b16 a[84:85], v108 offset:41728\n"
"v_mfma_f32_16x16x32_bf16 v[208:211], a[64:67], a[200:203], v[208:211]\n"
"ds_read_b64_tr_b16 a[86:87], v108 offset:45824\n"
"v_mfma_f32_16x16x32_bf16 v[208:211], a[68:71], a[216:219], v[208:211]\n"
"buffer_load_dwordx4 a[156:159], v103, s[40:43], s60 offen offset:1024 sc1\n"
"v_mfma_f32_16x16x32_bf16 v[240:243], a[64:67], a[204:207], v[240:243]\n"
"v_mfma_f32_16x16x32_bf16 v[240:243], a[68:71], a[220:223], v[240:243]\n"
"s_waitcnt lgkmcnt(4)\n"
"v_mfma_f32_16x16x32_bf16 v[148:151], a[72:75], a[192:195], v[148:151]\n"
"ds_read_b64_tr_b16 a[88:89], v109 offset:33536\n"
"v_mfma_f32_16x16x32_bf16 v[148:151], a[76:79], a[208:211], v[148:151]\n"
"ds_read_b64_tr_b16 a[90:91], v109 offset:37632\n"
"v_mfma_f32_16x16x32_bf16 v[180:183], a[72:75], a[196:199], v[180:183]\n"
"ds_read_b64_tr_b16 a[92:93], v109 offset:41728\n"
"v_mfma_f32_16x16x32_bf16 v[180:183], a[76:79], a[212:215], v[180:183]\n"
"buffer_load_dwordx4 v107, s[44:47], s61 offen offset:3072 lds\n"
"s_add_u32 s61, s61, 524288\n"
"v_mfma_f32_16x16x32_bf16 v[212:215], a[72:75], a[200:203], v[212:215]\n"
"ds_read_b64_tr_b16 a[94:95], v109 offset:45824\n"
"v_mfma_f32_16x16x32_bf16 v[212:215], a[76:79], a[216:219], v[212:215]\n"
"v_mfma_f32_16x16x32_bf16 v[244:247], a[72:75], a[204:207], v[244:247]\n"
"s_sub_u32 s63, s63, 1\n"
"v_mfma_f32_16x16x32_bf16 v[244:247], a[76:79], a[220:223], v[244:247]\n"
"s_waitcnt vmcnt(12) lgkmcnt(4)\n"
"s_barrier\n"
"s_cbranch_scc0 LW_%=\n"
"v_mfma_f32_16x16x32_bf16 v[152:155], a[80:83], a[192:195], v[152:155]\n"
"ds_read_b64_tr_b16 a[64:65], v108 offset:49152\n"
"v_mfma_f32_16x16x32_bf16 v[152:155], a[84:87], a[208:211], v[152:155]\n"
"ds_read_b64_tr_b16 a[66:67], v108 offset:53248\n"
"v_mfma_f32_16x16x32_bf16 v[184:187], a[80:83], a[196:199], v[184:187]\n"
"buffer_load_dwordx4 a[160:163], v100, s[40:43], s60 offen offset:2048 sc1\n"
"v_mfma_f32_16x16x32_bf16 v[184:187], a[84:87], a[212:215], v[184:187]\n"
"ds_read_b64_tr_b16 a[68:69], v108 offset:57344\n"
"v_mfma_f32_16x16x32_bf16 v[216:219], a[80:83], a[200:203], v[216:219]\n"
"ds_read_b64_tr_b16 a[70:71], v108 offset:61440\n"
"v_mfma_f32_16x16x32_bf16 v[216:219], a[84:87], a[216:219], v[216:219]\n"
"v_mfma_f32_16x16x32_bf16 v[248:251], a[80:83], a[204:207], v[248:251]\n"
"v_mfma_f32_16x16x32_bf16 v[248:251], a[84:87], a[220:223], v[248:251]\n"
"buffer_load_dwordx4 a[164:167], v101, s[40:43], s60 offen offset:2048 sc1\n"
"s_waitcnt lgkmcnt(4)\n"
"v_mfma_f32_16x16x32_bf16 v[156:159], a[88:91], a[192:195], v[156:159]\n"
"ds_read_b64_tr_b16 a[72:73], v109 offset:49152\n"
"v_mfma_f32_16x16x32_bf16 v[156:159], a[92:95], a[208:211], v[156:159]\n"
"ds_read_b64_tr_b16 a[74:75], v109 offset:53248\n"
"v_mfma_f32_16x16x32_bf16 v[188:191], a[88:91], a[196:199], v[188:191]\n"
"ds_read_b64_tr_b16 a[76:77], v109 offset:57344\n"
"v_mfma_f32_16x16x32_bf16 v[188:191], a[92:95], a[212:215], v[188:191]\n"
"ds_read_b64_tr_b16 a[78:79], v109 offset:61440\n"
"s_add_u32 m0, s62, 16384\n"
"v_mfma_f32_16x16x32_bf16 v[220:223], a[88:91], a[200:203], v[220:223]\n"
"buffer_load_dwordx4 v104, s[44:47], s61 offen lds\n"
"v_mfma_f32_16x16x32_bf16 v[220:223], a[92:95], a[216:219], v[220:223]\n"
"v_mfma_f32_16x16x32_bf16 v[252:255], a[88:91], a[204:207], v[252:255]\n"
"v_mfma_f32_16x16x32_bf16 v[252:255], a[92:95], a[220:223], v[252:255]\n"
"s_waitcnt lgkmcnt(4)\n"
"v_mfma_f32_16x16x32_bf16 v[128:131], a[64:67], a[224:227], v[128:131]\n"
"ds_read_b64_tr_b16 a[80:81], v108 offset:49408\n"
"v_mfma_f32_16x16x32_bf16 v[128:131], a[68:71], a[240:243], v[128:131]\n"
"buffer_load_dwordx4 a[168:171], v102, s[40:43], s60 offen offset:2048 sc1\n"
"v_mfma_f32_16x16x32_bf16 v[160:163], a[64:67], a[228:231], v[160:163]\n"
"ds_read_b64_tr_b16 a[82:83], v108 offset:53504\n"
"v_mfma_f32_16x16x32_bf16 v[160:163], a[68:71], a[244:247], v[160:163]\n"
"ds_read_b64_tr_b16 a[84:85], v108 offset:57600\n"
"v_mfma_f32_16x16x32_bf16 v[192:195], a[64:67], a[232:235], v[192:195]\n"
"ds_read_b64_tr_b16 a[86:87], v108 offset:61696\n"
"v_mfma_f32_16x16x32_bf16 v[192:195], a[68:71], a[248:251], v[192:195]\n"
"v_mfma_f32_16x16x32_bf16 v[224:227], a[64:67], a[236:239], v[224:227]\n"
"buffer_load_dwordx4 a[172:175], v103, s[40:43], s60 offen offset:2048 sc1\n"
"v_mfma_f32_16x16x32_bf16 v[224:227], a[68:71], a[252:255], v[224:227]\n"
"s_waitcnt lgkmcnt(4)\n"
"v_mfma_f32_16x16x32_bf16 v[132:135], a[72:75], a[224:227], v[132:135]\n"
"ds_read_b64_tr_b16 a[88:89], v109 offset:49408\n"
"v_mfma_f32_16x16x32_bf16 v[132:135], a[76:79], a[240:243], v[132:135]\n"
"ds_read_b64_tr_b16 a[90:91], v109 offset:53504\n"
"v_mfma_f32_16x16x32_bf16 v[164:167], a[72:75], a[228:231], v[164:167]\n"
"ds_read_b64_tr_b16 a[92:93], v109 offset:57600\n"
"v_mfma_f32_16x16x32_bf16 v[164:167], a[76:79], a[244:247], v[164:167]\n"
"ds_read_b64_tr_b16 a[94:95], v109 offset:61696\n"
"v_mfma_f32_16x16x32_bf16 v[196:199], a[72:75], a[232:235], v[196:199]\n"
"buffer_load_dwordx4 v105, s[44:47], s61 offen offset:1024 lds\n"
"v_mfma_f32_16x16x32_bf16 v[196:199], a[76:79], a[248:251], v[196:199]\n"
"v_mfma_f32_16x16x32_bf16 v[228:231], a[72:75], a[236:239], v[228:231]\n"
"v_mfma_f32_16x16x32_bf16 v[228:231], a[76:79], a[252:255], v[228:231]\n"
"s_waitcnt lgkmcnt(4)\n"
"v_mfma_f32_16x16x32_bf16 v[136:139], a[80:83], a[224:227], v[136:139]\n"
"ds_read_b64_tr_b16 a[64:65], v108 offset:49664\n"
"v_mfma_f32_16x16x32_bf16 v[136:139], a[84:87], a[240:243], v[136:139]\n"
"buffer_load_dwordx4 a[176:179], v100, s[40:43], s60 offen offset:3072 sc1\n"
"v_mfma_f32_16x16x32_bf16 v[168:171], a[80:83], a[228:231], v[168:171]\n"
"ds_read_b64_tr_b16 a[66:67], v108 offset:53760\n"
"v_mfma_f32_16x16x32_bf16 v[168:171], a[84:87], a[244:247], v[168:171]\n"
"ds_read_b64_tr_b16 a[68:69], v108 offset:57856\n"
"v_mfma_f32_16x16x32_bf16 v[200:203], a[80:83], a[232:235], v[200:203]\n"
"ds_read_b64_tr_b16 a[70:71], v108 offset:61952\n"
"v_mfma_f32_16x16x32_bf16 v[200:203], a[84:87], a[248:251], v[200:203]\n"
"v_mfma_f32_16x16x32_bf16 v[232:235], a[80:83], a[236:239], v[232:235]\n"
"buffer_load_dwordx4 a[180:183], v101, s[40:43], s60 offen offset:3072 sc1\n"
"v_mfma_f32_16x16x32_bf16 v[232:235], a[84:87], a[252:255], v[232:235]\n"
"s_waitcnt lgkmcnt(4)\n"
"v_mfma_f32_16x16x32_bf16 v[140:143], a[88:91], a[224:227], v[140:143]\n"
"ds_read_b64_tr_b16 a[72:73], v109 offset:49664\n"
"v_mfma_f32_16x16x32_bf16 v[140:143], a[92:95], a[240:243], v[140:143]\n"
"ds_read_b64_tr_b16 a[74:75], v109 offset:53760\n"
"v_mfma_f32_16x16x32_bf16 v[172:175], a[88:91], a[228:231], v[172:175]\n"
"ds_read_b64_tr_b16 a[76:77], v109 offset:57856\n"
"v_mfma_f32_16x16x32_bf16 v[172:175], a[92:95], a[244:247], v[172:175]\n"
"buffer_load_dwordx4 v106, s[44:47], s61 offen offset:2048 lds\n"
"v_mfma_f32_16x16x32_bf16 v[204:207], a[88:91], a[232:235], v[204:207]\n"
"ds_read_b64_tr_b16 a[78:79], v109 offset:61952\n"
"v_mfma_f32_16x16x32_bf16 v[204:207], a[92:95], a[248:251], v[204:207]\n"
"v_mfma_f32_16x16x32_bf16 v[236:239], a[88:91], a[236:239], v[236:239]\n"
"v_mfma_f32_16x16x32_bf16 v[236:239], a[92:95], a[252:255], v[236:239]\n"
"s_waitcnt lgkmcnt(4)\n"
"v_mfma_f32_16x16x32_bf16 v[144:147], a[64:67], a[224:227], v[144:147]\n"
"buffer_load_dwordx4 a[184:187], v102, s[40:43], s60 offen offset:3072 sc1\n"
"v_mfma_f32_16x16x32_bf16 v[144:147], a[68:71], a[240:243], v[144:147]\n"
"ds_read_b64_tr_b16 a[80:81], v108 offset:49920\n"
"v_mfma_f32_16x16x32_bf16 v[176:179], a[64:67], a[228:231], v[176:179]\n"
"ds_read_b64_tr_b16 a[82:83], v108 offset:54016\n"
"v_mfma_f32_16x16x32_bf16 v[176:179], a[68:71], a[244:247], v[176:179]\n"
"ds_read_b64_tr_b16 a[84:85], v108 offset:58112\n"
"v_mfma_f32_16x16x32_bf16 v[208:211], a[64:67], a[232:235], v[208:211]\n"
"ds_read_b64_tr_b16 a[86:87], v108 offset:62208\n"
"v_mfma_f32_16x16x32_bf16 v[208:211], a[68:71], a[248:251], v[208:211]\n"
"buffer_load_dwordx4 a[188:191], v103, s[40:43], s60 offen offset:3072 sc1\n"
"s_add_u32 s60, s60, 4096\n"
"v_mfma_f32_16x16x32_bf16 v[240:243], a[64:67], a[236:239], v[240:243]\n"
"v_mfma_f32_16x16x32_bf16 v[240:243], a[68:71], a[252:255], v[240:243]\n"
"s_waitcnt lgkmcnt(4)\n"
"v_mfma_f32_16x16x32_bf16 v[148:151], a[72:75], a[224:227], v[148:151]\n"
"ds_read_b64_tr_b16 a[88:89], v109 offset:49920\n"
"v_mfma_f32_16x16x32_bf16 v[148:151], a[76:79], a[240:243], v[148:151]\n"
"ds_read_b64_tr_b16 a[90:91], v109 offset:54016\n"
"v_mfma_f32_16x16x32_bf16 v[180:183], a[72:75], a[228:231], v[180:183]\n"
"ds_read_b64_tr_b16 a[92:93], v109 offset:58112\n"
"v_mfma_f32_16x16x32_bf16 v[180:183], a[76:79], a[244:247], v[180:183]\n"
"buffer_load_dwordx4 v107, s[44:47], s61 offen offset:3072 lds\n"
"s_add_u32 s61, s61, 524288\n"
"v_mfma_f32_16x16x32_bf16 v[212:215], a[72:75], a[232:235], v[212:215]\n"
"ds_read_b64_tr_b16 a[94:95], v109 offset:62208\n"
"v_mfma_f32_16x16x32_bf16 v[212:215], a[76:79], a[248:251], v[212:215]\n"
"v_mfma_f32_16x16x32_bf16 v[244:247], a[72:75], a[236:239], v[244:247]\n"
"v_mfma_f32_16x16x32_bf16 v[244:247], a[76:79], a[252:255], v[244:247]\n"
"s_waitcnt vmcnt(12) lgkmcnt(4)\n"
"s_barrier\n"
"v_mfma_f32_16x16x32_bf16 v[152:155], a[80:83], a[224:227], v[152:155]\n"
"ds_read_b64_tr_b16 a[64:65], v108 offset:0\n"
"v_mfma_f32_16x16x32_bf16 v[152:155], a[84:87], a[240:243], v[152:155]\n"
"ds_read_b64_tr_b16 a[66:67], v108 offset:4096\n"
"v_mfma_f32_16x16x32_bf16 v[184:187], a[80:83], a[228:231], v[184:187]\n"
"buffer_load_dwordx4 a[192:195], v100, s[40:43], s60 offen sc1\n"
"v_mfma_f32_16x16x32_bf16 v[184:187], a[84:87], a[244:247], v[184:187]\n"
"ds_read_b64_tr_b16 a[68:69], v108 offset:8192\n"
"v_mfma_f32_16x16x32_bf16 v[216:219], a[80:83], a[232:235], v[216:219]\n"
"ds_read_b64_tr_b16 a[70:71], v108 offset:12288\n"
"v_mfma_f32_16x16x32_bf16 v[216:219], a[84:87], a[248:251], v[216:219]\n"
"v_mfma_f32_16x16x32_bf16 v[248:251], a[80:83], a[236:239], v[248:251]\n"
"v_mfma_f32_16x16x32_bf16 v[248:251], a[84:87], a[252:255], v[248:251]\n"
"buffer_load_dwordx4 a[196:199], v101, s[40:43], s60 offen sc1\n"
"s_waitcnt lgkmcnt(4)\n"
"v_mfma_f32_16x16x32_bf16 v[156:159], a[88:91], a[224:227], v[156:159]\n"
"ds_read_b64_tr_b16 a[72:73], v109 offset:0\n"
"v_mfma_f32_16x16x32_bf16 v[156:159], a[92:95], a[240:243], v[156:159]\n"
"ds_read_b64_tr_b16 a[74:75], v109 offset:4096\n"
"v_mfma_f32_16x16x32_bf16 v[188:191], a[88:91], a[228:231], v[188:191]\n"
"ds_read_b64_tr_b16 a[76:77], v109 offset:8192\n"
"v_mfma_f32_16x16x32_bf16 v[188:191], a[92:95], a[244:247], v[188:191]\n"
"ds_read_b64_tr_b16 a[78:79], v109 offset:12288\n"
"s_add_u32 m0, s62, 32768\n"
"v_mfma_f32_16x16x32_bf16 v[220:223], a[88:91], a[232:235], v[220:223]\n"
"buffer_load_dwordx4 v104, s[44:47], s61 offen lds\n"
"v_mfma_f32_16x16x32_bf16 v[220:223], a[92:95], a[248:251], v[220:223]\n"
"v_mfma_f32_16x16x32_bf16 v[252:255], a[88:91], a[236:239], v[252:255]\n"
"v_mfma_f32_16x16x32_bf16 v[252:255], a[92:95], a[252:255], v[252:255]\n"
"s_waitcnt lgkmcnt(4)\n"
"v_mfma_f32_16x16x32_bf16 v[128:131], a[64:67], a[128:131], v[128:131]\n"
"ds_read_b64_tr_b16 a[80:81], v108 offset:256\n"
"v_mfma_f32_16x16x32_bf16 v[128:131], a[68:71], a[144:147], v[128:131]\n"
"buffer_load_dwordx4 a[200:203], v102, s[40:43], s60 offen sc1\n"
"v_mfma_f32_16x16x32_bf16 v[160:163], a[64:67], a[132:135], v[160:163]\n"
"ds_read_b64_tr_b16 a[82:83], v108 offset:4352\n"
"v_mfma_f32_16x16x32_bf16 v[160:163], a[68:71], a[148:151], v[160:163]\n"
"ds_read_b64_tr_b16 a[84:85], v108 offset:8448\n"
"v_mfma_f32_16x16x32_bf16 v[192:195], a[64:67], a[136:139], v[192:195]\n"
"ds_read_b64_tr_b16 a[86:87], v108 offset:12544\n"
"v_mfma_f32_16x16x32_bf16 v[192:195], a[68:71], a[152:155], v[192:195]\n"
"v_mfma_f32_16x16x32_bf16 v[224:227], a[64:67], a[140:143], v[224:227]\n"
"buffer_load_dwordx4 a[204:207], v103, s[40:43], s60 offen sc1\n"
"v_mfma_f32_16x16x32_bf16 v[224:227], a[68:71], a[156:159], v[224:227]\n"
"s_waitcnt lgkmcnt(4)\n"
"v_mfma_f32_16x16x32_bf16 v[132:135], a[72:75], a[128:131], v[132:135]\n"
"ds_read_b64_tr_b16 a[88:89], v109 offset:256\n"
"v_mfma_f32_16x16x32_bf16 v[132:135], a[76:79], a[144:147], v[132:135]\n"
"ds_read_b64_tr_b16 a[90:91], v109 offset:4352\n"
"v_mfma_f32_16x16x32_bf16 v[164:167], a[72:75], a[132:135], v[164:167]\n"
"ds_read_b64_tr_b16 a[92:93], v109 offset:8448\n"
"v_mfma_f32_16x16x32_bf16 v[164:167], a[76:79], a[148:151], v[164:167]\n"
"ds_read_b64_tr_b16 a[94:95], v109 offset:12544\n"
"v_mfma_f32_16x16x32_bf16 v[196:199], a[72:75], a[136:139], v[196:199]\n"
"buffer_load_dwordx4 v105, s[44:47], s61 offen offset:1024 lds\n"
"v_mfma_f32_16x16x32_bf16 v[196:199], a[76:79], a[152:155], v[196:199]\n"
"v_mfma_f32_16x16x32_bf16 v[228:231], a[72:75], a[140:143], v[228:231]\n"
"v_mfma_f32_16x16x32_bf16 v[228:231], a[76:79], a[156:159], v[228:231]\n"
"s_waitcnt lgkmcnt(4)\n"
"v_mfma_f32_16x16x32_bf16 v[136:139], a[80:83], a[128:131], v[136:139]\n"
"ds_read_b64_tr_b16 a[64:65], v108 offset:512\n"
"v_mfma_f32_16x16x32_bf16 v[136:139], a[84:87], a[144:147], v[136:139]\n"
"buffer_load_dwordx4 a[208:211], v100, s[40:43], s60 offen offset:1024 sc1\n"
"v_mfma_f32_16x16x32_bf16 v[168:171], a[80:83], a[132:135], v[168:171]\n"
"ds_read_b64_tr_b16 a[66:67], v108 offset:4608\n"
"v_mfma_f32_16x16x32_bf16 v[168:171], a[84:87], a[148:151], v[168:171]\n"
"ds_read_b64_tr_b16 a[68:69], v108 offset:8704\n"
"v_mfma_f32_16x16x32_bf16 v[200:203], a[80:83], a[136:139], v[200:203]\n"
"ds_read_b64_tr_b16 a[70:71], v108 offset:12800\n"
"v_mfma_f32_16x16x32_bf16 v[200:203], a[84:87], a[152:155], v[200:203]\n"
"v_mfma_f32_16x16x32_bf16 v[232:235], a[80:83], a[140:143], v[232:235]\n"
"buffer_load_dwordx4 a[212:215], v101, s[40:43], s60 offen offset:1024 sc1\n"
"v_mfma_f32_16x16x32_bf16 v[232:235], a[84:87], a[156:159], v[232:235]\n"
"s_waitcnt lgkmcnt(4)\n"
"v_mfma_f32_16x16x32_bf16 v[140:143], a[88:91], a[128:131], v[140:143]\n"
"ds_read_b64_tr_b16 a[72:73], v109 offset:512\n"
"v_mfma_f32_16x16x32_bf16 v[140:143], a[92:95], a[144:147], v[140:143]\n"
"ds_read_b64_tr_b16 a[74:75], v109 offset:4608\n"
"v_mfma_f32_16x16x32_bf16 v[172:175], a[88:91], a[132:135], v[172:175]\n"
"ds_read_b64_tr_b16 a[76:77], v109 offset:8704\n"
"v_mfma_f32_16x16x32_bf16 v[172:175], a[92:95], a[148:151], v[172:175]\n"
"buffer_load_dwordx4 v106, s[44:47], s61 offen offset:2048 lds\n"
"v_mfma_f32_16x16x32_bf16 v[204:207], a[88:91], a[136:139], v[204:207]\n"
"ds_read_b64_tr_b16 a[78:79], v109 offset:12800\n"
"v_mfma_f32_16x16x32_bf16 v[204:207], a[92:95], a[152:155], v[204:207]\n"
"v_mfma_f32_16x16x32_bf16 v[236:239], a[88:91], a[140:143], v[236:239]\n"
"v_mfma_f32_16x16x32_bf16 v[236:239], a[92:95], a[156:159], v[236:239]\n"
"s_waitcnt lgkmcnt(4)\n"
"v_mfma_f32_16x16x32_bf16 v[144:147], a[64:67], a[128:131], v[144:147]\n"
"buffer_load_dwordx4 a[216:219], v102, s[40:43], s60 offen offset:1024 sc1\n"
"v_mfma_f32_16x16x32_bf16 v[144:147], a[68:71], a[144:147], v[144:147]\n"
"ds_read_b64_tr_b16 a[80:81], v108 offset:768\n"
"v_mfma_f32_16x16x32_bf16 v[176:179], a[64:67], a[132:135], v[176:179]\n"
"ds_read_b64_tr_b16 a[82:83], v108 offset:4864\n"
"v_mfma_f32_16x16x32_bf16 v[176:179], a[68:71], a[148:151], v[176:179]\n"
"ds_read_b64_tr_b16 a[84:85], v108 offset:8960\n"
"v_mfma_f32_16x16x32_bf16 v[208:211], a[64:67], a[136:139], v[208:211]\n"
"ds_read_b64_tr_b16 a[86:87], v108 offset:13056\n"
"v_mfma_f32_16x16x32_bf16 v[208:211], a[68:71], a[152:155], v[208:211]\n"
"buffer_load_dwordx4 a[220:223], v103, s[40:43], s60 offen offset:1024 sc1\n"
"v_mfma_f32_16x16x32_bf16 v[240:243], a[64:67], a[140:143], v[240:243]\n"
"v_mfma_f32_16x16x32_bf16 v[240:243], a[68:71], a[156:159], v[240:243]\n"
"s_waitcnt lgkmcnt(4)\n"
"v_mfma_f32_16x16x32_bf16 v[148:151], a[72:75], a[128:131], v[148:151]\n"
"ds_read_b64_tr_b16 a[88:89], v109 offset:768\n"
"v_mfma_f32_16x16x32_bf16 v[148:151], a[76:79], a[144:147], v[148:151]\n"
"ds_read_b64_tr_b16 a[90:91], v109 offset:4864\n"
"v_mfma_f32_16x16x32_bf16 v[180:183], a[72:75], a[132:135], v[180:183]\n"
"ds_read_b64_tr_b16 a[92:93], v109 offset:8960\n"
"v_mfma_f32_16x16x32_bf16 v[180:183], a[76:79], a[148:151], v[180:183]\n"
"buffer_load_dwordx4 v107, s[44:47], s61 offen offset:3072 lds\n"
"s_add_u32 s61, s61, 524288\n"
"v_mfma_f32_16x16x32_bf16 v[212:215], a[72:75], a[136:139], v[212:215]\n"
"ds_read_b64_tr_b16 a[94:95], v109 offset:13056\n"
"v_mfma_f32_16x16x32_bf16 v[212:215], a[76:79], a[152:155], v[212:215]\n"
"v_mfma_f32_16x16x32_bf16 v[244:247], a[72:75], a[140:143], v[244:247]\n"
"v_mfma_f32_16x16x32_bf16 v[244:247], a[76:79], a[156:159], v[244:247]\n"
"s_waitcnt vmcnt(12) lgkmcnt(4)\n"
"s_barrier\n"
"v_mfma_f32_16x16x32_bf16 v[152:155], a[80:83], a[128:131], v[152:155]\n"
"ds_read_b64_tr_b16 a[64:65], v108 offset:16384\n"
"v_mfma_f32_16x16x32_bf16 v[152:155], a[84:87], a[144:147], v[152:155]\n"
"ds_read_b64_tr_b16 a[66:67], v108 offset:20480\n"
"v_mfma_f32_16x16x32_bf16 v[184:187], a[80:83], a[132:135], v[184:187]\n"
"buffer_load_dwordx4 a[224:227], v100, s[40:43], s60 offen offset:2048 sc1\n"
"v_mfma_f32_16x16x32_bf16 v[184:187], a[84:87], a[148:151], v[184:187]\n"
"ds_read_b64_tr_b16 a[68:69], v108 offset:24576\n"
"v_mfma_f32_16x16x32_bf16 v[216:219], a[80:83], a[136:139], v[216:219]\n"
"ds_read_b64_tr_b16 a[70:71], v108 offset:28672\n"
"v_mfma_f32_16x16x32_bf16 v[216:219], a[84:87], a[152:155], v[216:219]\n"
"v_mfma_f32_16x16x32_bf16 v[248:251], a[80:83], a[140:143], v[248:251]\n"
"v_mfma_f32_16x16x32_bf16 v[248:251], a[84:87], a[156:159], v[248:251]\n"
"buffer_load_dwordx4 a[228:231], v101, s[40:43], s60 offen offset:2048 sc1\n"
"s_waitcnt lgkmcnt(4)\n"
"v_mfma_f32_16x16x32_bf16 v[156:159], a[88:91], a[128:131], v[156:159]\n"
"ds_read_b64_tr_b16 a[72:73], v109 offset:16384\n"
"v_mfma_f32_16x16x32_bf16 v[156:159], a[92:95], a[144:147], v[156:159]\n"
"ds_read_b64_tr_b16 a[74:75], v109 offset:20480\n"
"v_mfma_f32_16x16x32_bf16 v[188:191], a[88:91], a[132:135], v[188:191]\n"
"ds_read_b64_tr_b16 a[76:77], v109 offset:24576\n"
"v_mfma_f32_16x16x32_bf16 v[188:191], a[92:95], a[148:151], v[188:191]\n"
"ds_read_b64_tr_b16 a[78:79], v109 offset:28672\n"
"s_add_u32 m0, s62, 49152\n"
"v_mfma_f32_16x16x32_bf16 v[220:223], a[88:91], a[136:139], v[220:223]\n"
"buffer_load_dwordx4 v104, s[44:47], s61 offen lds\n"
"v_mfma_f32_16x16x32_bf16 v[220:223], a[92:95], a[152:155], v[220:223]\n"
"v_mfma_f32_16x16x32_bf16 v[252:255], a[88:91], a[140:143], v[252:255]\n"
"v_mfma_f32_16x16x32_bf16 v[252:255], a[92:95], a[156:159], v[252:255]\n"
"s_waitcnt lgkmcnt(4)\n"
"v_mfma_f32_16x16x32_bf16 v[128:131], a[64:67], a[160:163], v[128:131]\n"
"ds_read_b64_tr_b16 a[80:81], v108 offset:16640\n"
"v_mfma_f32_16x16x32_bf16 v[128:131], a[68:71], a[176:179], v[128:131]\n"
"buffer_load_dwordx4 a[232:235], v102, s[40:43], s60 offen offset:2048 sc1\n"
"v_mfma_f32_16x16x32_bf16 v[160:163], a[64:67], a[164:167], v[160:163]\n"
"ds_read_b64_tr_b16 a[82:83], v108 offset:20736\n"
"v_mfma_f32_16x16x32_bf16 v[160:163], a[68:71], a[180:183], v[160:163]\n"
"ds_read_b64_tr_b16 a[84:85], v108 offset:24832\n"
"v_mfma_f32_16x16x32_bf16 v[192:195], a[64:67], a[168:171], v[192:195]\n"
"ds_read_b64_tr_b16 a[86:87], v108 offset:28928\n"
"v_mfma_f32_16x16x32_bf16 v[192:195], a[68:71], a[184:187], v[192:195]\n"
"v_mfma_f32_16x16x32_bf16 v[224:227], a[64:67], a[172:175], v[224:227]\n"
"buffer_load_dwordx4 a[236:239], v103, s[40:43], s60 offen offset:2048 sc1\n"
"v_mfma_f32_16x16x32_bf16 v[224:227], a[68:71], a[188:191], v[224:227]\n"
"s_waitcnt lgkmcnt(4)\n"
"v_mfma_f32_16x16x32_bf16 v[132:135], a[72:75], a[160:163], v[132:135]\n"
"ds_read_b64_tr_b16 a[88:89], v109 offset:16640\n"
"v_mfma_f32_16x16x32_bf16 v[132:135], a[76:79], a[176:179], v[132:135]\n"
"ds_read_b64_tr_b16 a[90:91], v109 offset:20736\n"
"v_mfma_f32_16x16x32_bf16 v[164:167], a[72:75], a[164:167], v[164:167]\n"
"ds_read_b64_tr_b16 a[92:93], v109 offset:24832\n"
"v_mfma_f32_16x16x32_bf16 v[164:167], a[76:79], a[180:183], v[164:167]\n"
"ds_read_b64_tr_b16 a[94:95], v109 offset:28928\n"
"v_mfma_f32_16x16x32_bf16 v[196:199], a[72:75], a[168:171], v[196:199]\n"
"buffer_load_dwordx4 v105, s[44:47], s61 offen offset:1024 lds\n"
"v_mfma_f32_16x16x32_bf16 v[196:199], a[76:79], a[184:187], v[196:199]\n"
"v_mfma_f32_16x16x32_bf16 v[228:231], a[72:75], a[172:175], v[228:231]\n"
"v_mfma_f32_16x16x32_bf16 v[228:231], a[76:79], a[188:191], v[228:231]\n"
"s_waitcnt lgkmcnt(4)\n"
"v_mfma_f32_16x16x32_bf16 v[136:139], a[80:83], a[160:163], v[136:139]\n"
"ds_read_b64_tr_b16 a[64:65], v108 offset:16896\n"
"v_mfma_f32_16x16x32_bf16 v[136:139], a[84:87], a[176:179], v[136:139]\n"
"buffer_load_dwordx4 a[240:243], v100, s[40:43], s60 offen offset:3072 sc1\n"
"v_mfma_f32_16x16x32_bf16 v[168:171], a[80:83], a[164:167], v[168:171]\n"
"ds_read_b64_tr_b16 a[66:67], v108 offset:20992\n"
"v_mfma_f32_16x16x32_bf16 v[168:171], a[84:87], a[180:183], v[168:171]\n"
"ds_read_b64_tr_b16 a[68:69], v108 offset:25088\n"
"v_mfma_f32_16x16x32_bf16 v[200:203], a[80:83], a[168:171], v[200:203]\n"
"ds_read_b64_tr_b16 a[70:71], v108 offset:29184\n"
"v_mfma_f32_16x16x32_bf16 v[200:203], a[84:87], a[184:187], v[200:203]\n"
"v_mfma_f32_16x16x32_bf16 v[232:235], a[80:83], a[172:175], v[232:235]\n"
"buffer_load_dwordx4 a[244:247], v101, s[40:43], s60 offen offset:3072 sc1\n"
"v_mfma_f32_16x16x32_bf16 v[232:235], a[84:87], a[188:191], v[232:235]\n"
"s_waitcnt lgkmcnt(4)\n"
"v_mfma_f32_16x16x32_bf16 v[140:143], a[88:91], a[160:163], v[140:143]\n"
"ds_read_b64_tr_b16 a[72:73], v109 offset:16896\n"
"v_mfma_f32_16x16x32_bf16 v[140:143], a[92:95], a[176:179], v[140:143]\n"
"ds_read_b64_tr_b16 a[74:75], v109 offset:20992\n"
"v_mfma_f32_16x16x32_bf16 v[172:175], a[88:91], a[164:167], v[172:175]\n"
"ds_read_b64_tr_b16 a[76:77], v109 offset:25088\n"
"v_mfma_f32_16x16x32_bf16 v[172:175], a[92:95], a[180:183], v[172:175]\n"
"buffer_load_dwordx4 v106, s[44:47], s61 offen offset:2048 lds\n"
"v_mfma_f32_16x16x32_bf16 v[204:207], a[88:91], a[168:171], v[204:207]\n"
"ds_read_b64_tr_b16 a[78:79], v109 offset:29184\n"
"v_mfma_f32_16x16x32_bf16 v[204:207], a[92:95], a[184:187], v[204:207]\n"
"v_mfma_f32_16x16x32_bf16 v[236:239], a[88:91], a[172:175], v[236:239]\n"
"v_mfma_f32_16x16x32_bf16 v[236:239], a[92:95], a[188:191], v[236:239]\n"
"s_waitcnt lgkmcnt(4)\n"
"v_mfma_f32_16x16x32_bf16 v[144:147], a[64:67], a[160:163], v[144:147]\n"
"buffer_load_dwordx4 a[248:251], v102, s[40:43], s60 offen offset:3072 sc1\n"
"v_mfma_f32_16x16x32_bf16 v[144:147], a[68:71], a[176:179], v[144:147]\n"
"ds_read_b64_tr_b16 a[80:81], v108 offset:17152\n"
"v_mfma_f32_16x16x32_bf16 v[176:179], a[64:67], a[164:167], v[176:179]\n"
"ds_read_b64_tr_b16 a[82:83], v108 offset:21248\n"
"v_mfma_f32_16x16x32_bf16 v[176:179], a[68:71], a[180:183], v[176:179]\n"
"ds_read_b64_tr_b16 a[84:85], v108 offset:25344\n"
"v_mfma_f32_16x16x32_bf16 v[208:211], a[64:67], a[168:171], v[208:211]\n"
"ds_read_b64_tr_b16 a[86:87], v108 offset:29440\n"
"v_mfma_f32_16x16x32_bf16 v[208:211], a[68:71], a[184:187], v[208:211]\n"
"buffer_load_dwordx4 a[252:255], v103, s[40:43], s60 offen offset:3072 sc1\n"
"s_add_u32 s60, s60, 4096\n"
"v_mfma_f32_16x16x32_bf16 v[240:243], a[64:67], a[172:175], v[240:243]\n"
"v_mfma_f32_16x16x32_bf16 v[240:243], a[68:71], a[188:191], v[240:243]\n"
"s_waitcnt lgkmcnt(4)\n"
"v_mfma_f32_16x16x32_bf16 v[148:151], a[72:75], a[160:163], v[148:151]\n"
"ds_read_b64_tr_b16 a[88:89], v109 offset:17152\n"
"v_mfma_f32_16x16x32_bf16 v[148:151], a[76:79], a[176:179], v[148:151]\n"
"ds_read_b64_tr_b16 a[90:91], v109 offset:21248\n"
"v_mfma_f32_16x16x32_bf16 v[180:183], a[72:75], a[164:167], v[180:183]\n"
"ds_read_b64_tr_b16 a[92:93], v109 offset:25344\n"
"v_mfma_f32_16x16x32_bf16 v[180:183], a[76:79], a[180:183], v[180:183]\n"
"buffer_load_dwordx4 v107, s[44:47], s61 offen offset:3072 lds\n"
"s_add_u32 s61, s61, 524288\n"
"v_mfma_f32_16x16x32_bf16 v[212:215], a[72:75], a[168:171], v[212:215]\n"
"ds_read_b64_tr_b16 a[94:95], v109 offset:29440\n"
"v_mfma_f32_16x16x32_bf16 v[212:215], a[76:79], a[184:187], v[212:215]\n"
"v_mfma_f32_16x16x32_bf16 v[244:247], a[72:75], a[172:175], v[244:247]\n"
"v_mfma_f32_16x16x32_bf16 v[244:247], a[76:79], a[188:191], v[244:247]\n"
"s_waitcnt vmcnt(12) lgkmcnt(4)\n"
"s_barrier\n"
"v_mfma_f32_16x16x32_bf16 v[152:155], a[80:83], a[160:163], v[152:155]\n"
"ds_read_b64_tr_b16 a[64:65], v108 offset:32768\n"
"v_mfma_f32_16x16x32_bf16 v[152:155], a[84:87], a[176:179], v[152:155]\n"
"buffer_load_dwordx4 v[124:127], v13, %[code], 0 offen\n"
"v_mfma_f32_16x16x32_bf16 v[184:187], a[80:83], a[164:167], v[184:187]\n"
"ds_read_b64_tr_b16 a[66:67], v108 offset:36864\n"
"v_mfma_f32_16x16x32_bf16 v[184:187], a[84:87], a[180:183], v[184:187]\n"
"ds_read_b64_tr_b16 a[68:69], v108 offset:40960\n"
"v_mfma_f32_16x16x32_bf16 v[216:219], a[80:83], a[168:171], v[216:219]\n"
"ds_read_b64_tr_b16 a[70:71], v108 offset:45056\n"
"s_add_u32 m0, s65, 0\n"
"v_mfma_f32_16x16x32_bf16 v[216:219], a[84:87], a[184:187], v[216:219]\n"
"buffer_load_dwordx4 v8, s[48:51], s64 offen lds\n"
"v_mfma_f32_16x16x32_bf16 v[248:251], a[80:83], a[172:175], v[248:251]\n"
"v_mfma_f32_16x16x32_bf16 v[248:251], a[84:87], a[188:191], v[248:251]\n"
"s_waitcnt lgkmcnt(4)\n"
"v_mfma_f32_16x16x32_bf16 v[156:159], a[88:91], a[160:163], v[156:159]\n"
"ds_read_b64_tr_b16 a[72:73], v109 offset:32768\n"
"v_mfma_f32_16x16x32_bf16 v[156:159], a[92:95], a[176:179], v[156:159]\n"
"buffer_load_dwordx4 v9, s[48:51], s64 offen offset:1024 lds\n"
"v_mfma_f32_16x16x32_bf16 v[188:191], a[88:91], a[164:167], v[188:191]\n"
"ds_read_b64_tr_b16 a[74:75], v109 offset:36864\n"
"v_mfma_f32_16x16x32_bf16 v[188:191], a[92:95], a[180:183], v[188:191]\n"
"ds_read_b64_tr_b16 a[76:77], v109 offset:40960\n"
"v_mfma_f32_16x16x32_bf16 v[220:223], a[88:91], a[168:171], v[220:223]\n"
"ds_read_b64_tr_b16 a[78:79], v109 offset:45056\n"
"v_mfma_f32_16x16x32_bf16 v[220:223], a[92:95], a[184:187], v[220:223]\n"
"buffer_load_dwordx4 v8, s[48:51], s64 offen offset:2048 lds\n"
"v_mfma_f32_16x16x32_bf16 v[252:255], a[88:91], a[172:175], v[252:255]\n"
"v_mfma_f32_16x16x32_bf16 v[252:255], a[92:95], a[188:191], v[252:255]\n"
"s_waitcnt lgkmcnt(4)\n"
"v_mfma_f32_16x16x32_bf16 v[128:131], a[64:67], a[192:195], v[128:131]\n"
"ds_read_b64_tr_b16 a[80:81], v108 offset:33024\n"
"v_mfma_f32_16x16x32_bf16 v[128:131], a[68:71], a[208:211], v[128:131]\n"
"buffer_load_dwordx4 v9, s[48:51], s64 offen offset:3072 lds\n"
"v_mfma_f32_16x16x32_bf16 v[160:163], a[64:67], a[196:199], v[160:163]\n"
"ds_read_b64_tr_b16 a[82:83], v108 offset:37120\n"
"v_mfma_f32_16x16x32_bf16 v[160:163], a[68:71], a[212:215], v[160:163]\n"
"ds_read_b64_tr_b16 a[84:85], v108 offset:41216\n"
"v_mfma_f32_16x16x32_bf16 v[192:195], a[64:67], a[200:203], v[192:195]\n"
"ds_read_b64_tr_b16 a[86:87], v108 offset:45312\n"
"s_add_u32 m0, s66, 32768\n"
"v_mfma_f32_16x16x32_bf16 v[192:195], a[68:71], a[216:219], v[192:195]\n"
"buffer_load_dwordx4 v8, s[52:55], s64 offen lds\n"
"v_mfma_f32_16x16x32_bf16 v[224:227], a[64:67], a[204:207], v[224:227]\n"
"v_mfma_f32_16x16x32_bf16 v[224:227], a[68:71], a[220:223], v[224:227]\n"
"s_waitcnt lgkmcnt(4)\n"
"v_mfma_f32_16x16x32_bf16 v[132:135], a[72:75], a[192:195], v[132:135]\n"
"ds_read_b64_tr_b16 a[88:89], v109 offset:33024\n"
"v_mfma_f32_16x16x32_bf16 v[132:135], a[76:79], a[208:211], v[132:135]\n"
"buffer_load_dwordx4 v9, s[52:55], s64 offen offset:1024 lds\n"
"v_mfma_f32_16x16x32_bf16 v[164:167], a[72:75], a[196:199], v[164:167]\n"
"ds_read_b64_tr_b16 a[90:91], v109 offset:37120\n"
"v_mfma_f32_16x16x32_bf16 v[164:167], a[76:79], a[212:215], v[164:167]\n"
"ds_read_b64_tr_b16 a[92:93], v109 offset:41216\n"
"v_mfma_f32_16x16x32_bf16 v[196:199], a[72:75], a[200:203], v[196:199]\n"
"ds_read_b64_tr_b16 a[94:95], v109 offset:45312\n"
"v_mfma_f32_16x16x32_bf16 v[196:199], a[76:79], a[216:219], v[196:199]\n"
"buffer_load_dwordx4 v8, s[52:55], s64 offen offset:2048 lds\n"
"v_mfma_f32_16x16x32_bf16 v[228:231], a[72:75], a[204:207], v[228:231]\n"
"v_mfma_f32_16x16x32_bf16 v[228:231], a[76:79], a[220:223], v[228:231]\n"
"s_waitcnt lgkmcnt(4)\n"
"v_mfma_f32_16x16x32_bf16 v[136:139], a[80:83], a[192:195], v[136:139]\n"
"ds_read_b64_tr_b16 a[64:65], v108 offset:33280\n"
"v_mfma_f32_16x16x32_bf16 v[136:139], a[84:87], a[208:211], v[136:139]\n"
"buffer_load_dwordx4 v9, s[52:55], s64 offen offset:3072 lds\n"
"s_add_u32 s64, s64, 16384\n"
"v_mfma_f32_16x16x32_bf16 v[168:171], a[80:83], a[196:199], v[168:171]\n"
"ds_read_b64_tr_b16 a[66:67], v108 offset:37376\n"
"v_mfma_f32_16x16x32_bf16 v[168:171], a[84:87], a[212:215], v[168:171]\n"
"ds_read_b64_tr_b16 a[68:69], v108 offset:41472\n"
"v_mfma_f32_16x16x32_bf16 v[200:203], a[80:83], a[200:203], v[200:203]\n"
"ds_read_b64_tr_b16 a[70:71], v108 offset:45568\n"
"v_mfma_f32_16x16x32_bf16 v[200:203], a[84:87], a[216:219], v[200:203]\n"
"v_mfma_f32_16x16x32_bf16 v[232:235], a[80:83], a[204:207], v[232:235]\n"
"v_mfma_f32_16x16x32_bf16 v[232:235], a[84:87], a[220:223], v[232:235]\n"
"s_waitcnt lgkmcnt(4)\n"
"v_mfma_f32_16x16x32_bf16 v[140:143], a[88:91], a[192:195], v[140:143]\n"
"ds_read_b64_tr_b16 a[72:73], v109 offset:33280\n"
"v_mfma_f32_16x16x32_bf16 v[140:143], a[92:95], a[208:211], v[140:143]\n"
"ds_read_b64_tr_b16 a[74:75], v109 offset:37376\n"
"v_mfma_f32_16x16x32_bf16 v[172:175], a[88:91], a[196:199], v[172:175]\n"
"ds_read_b64_tr_b16 a[76:77], v109 offset:41472\n"
"v_mfma_f32_16x16x32_bf16 v[172:175], a[92:95], a[212:215], v[172:175]\n"
"ds_read_b64_tr_b16 a[78:79], v109 offset:45568\n"
"v_mfma_f32_16x16x32_bf16 v[204:207], a[88:91], a[200:203], v[204:207]\n"
"v_mfma_f32_16x16x32_bf16 v[204:207], a[92:95], a[216:219], v[204:207]\n"
"v_mfma_f32_16x16x32_bf16 v[236:239], a[88:91], a[204:207], v[236:239]\n"
"v_mfma_f32_16x16x32_bf16 v[236:239], a[92:95], a[220:223], v[236:239]\n"
"s_waitcnt vmcnt(9) lgkmcnt(4)\n"
"s_barrier\n"
"v_mfma_f32_16x16x32_bf16 v[144:147], a[64:67], a[192:195], v[144:147]\n"
"ds_read_b64_tr_b16 a[80:81], v108 offset:33536\n"
"ds_read_b64_tr_b16 a[172:173], v109 offset:57856\n"
"v_mfma_f32_16x16x32_bf16 v[144:147], a[68:71], a[208:211], v[144:147]\n"
"ds_read_b64_tr_b16 a[82:83], v108 offset:37632\n"
"s_add_u32 m0, s65, 16384\n"
"v_mfma_f32_16x16x32_bf16 v[176:179], a[64:67], a[196:199], v[176:179]\n"
"buffer_load_dwordx4 v8, s[48:51], s64 offen lds\n"
"ds_read_b64_tr_b16 a[174:175], v109 offset:61952\n"
"v_mfma_f32_16x16x32_bf16 v[176:179], a[68:71], a[212:215], v[176:179]\n"
"ds_read_b64_tr_b16 a[84:85], v108 offset:41728\n"
"ds_read_b64_tr_b16 a[180:181], v108 offset:58112\n"
"v_mfma_f32_16x16x32_bf16 v[208:211], a[64:67], a[200:203], v[208:211]\n"
"ds_read_b64_tr_b16 a[86:87], v108 offset:45824\n"
"ds_read_b64_tr_b16 a[182:183], v108 offset:62208\n"
"v_mfma_f32_16x16x32_bf16 v[208:211], a[68:71], a[216:219], v[208:211]\n"
"ds_read_b64_tr_b16 a[128:129], v108 offset:49152\n"
"ds_read_b64_tr_b16 a[188:189], v109 offset:58112\n"
"v_mfma_f32_16x16x32_bf16 v[240:243], a[64:67], a[204:207], v[240:243]\n"
"ds_read_b64_tr_b16 a[130:131], v108 offset:53248\n"
"ds_read_b64_tr_b16 a[190:191], v109 offset:62208\n"
"v_mfma_f32_16x16x32_bf16 v[240:243], a[68:71], a[220:223], v[240:243]\n"
"ds_read_b64_tr_b16 a[132:133], v108 offset:57344\n"
"s_waitcnt lgkmcnt(13)\n"
"v_mfma_f32_16x16x32_bf16 v[148:151], a[72:75], a[192:195], v[148:151]\n"
"ds_read_b64_tr_b16 a[88:89], v109 offset:33536\n"
"v_mfma_f32_16x16x32_bf16 v[148:151], a[76:79], a[208:211], v[148:151]\n"
"ds_read_b64_tr_b16 a[90:91], v109 offset:37632\n"
"v_mfma_f32_16x16x32_bf16 v[180:183], a[72:75], a[196:199], v[180:183]\n"
"buffer_load_dwordx4 v9, s[48:51], s64 offen offset:1024 lds\n"
"v_mfma_f32_16x16x32_bf16 v[180:183], a[76:79], a[212:215], v[180:183]\n"
"ds_read_b64_tr_b16 a[92:93], v109 offset:41728\n"
"v_mfma_f32_16x16x32_bf16 v[212:215], a[72:75], a[200:203], v[212:215]\n"
"ds_read_b64_tr_b16 a[94:95], v109 offset:45824\n"
"v_mfma_f32_16x16x32_bf16 v[212:215], a[76:79], a[216:219], v[212:215]\n"
"ds_read_b64_tr_b16 a[134:135], v108 offset:61440\n"
"v_mfma_f32_16x16x32_bf16 v[244:247], a[72:75], a[204:207], v[244:247]\n"
"ds_read_b64_tr_b16 a[136:137], v109 offset:49152\n"
"v_mfma_f32_16x16x32_bf16 v[244:247], a[76:79], a[220:223], v[244:247]\n"
"ds_read_b64_tr_b16 a[138:139], v109 offset:53248\n"
"s_waitcnt lgkmcnt(7)\n"
"v_mfma_f32_16x16x32_bf16 v[152:155], a[80:83], a[192:195], v[152:155]\n"
"ds_read_b64_tr_b16 a[140:141], v109 offset:57344\n"
"v_mfma_f32_16x16x32_bf16 v[152:155], a[84:87], a[208:211], v[152:155]\n"
"ds_read_b64_tr_b16 a[142:143], v109 offset:61440\n"
"v_mfma_f32_16x16x32_bf16 v[184:187], a[80:83], a[196:199], v[184:187]\n"
"buffer_load_dwordx4 v8, s[48:51], s64 offen offset:2048 lds\n"
"v_mfma_f32_16x16x32_bf16 v[184:187], a[84:87], a[212:215], v[184:187]\n"
"ds_read_b64_tr_b16 a[144:145], v108 offset:49408\n"
"v_mfma_f32_16x16x32_bf16 v[216:219], a[80:83], a[200:203], v[216:219]\n"
"ds_read_b64_tr_b16 a[146:147], v108 offset:53504\n"
"v_mfma_f32_16x16x32_bf16 v[216:219], a[84:87], a[216:219], v[216:219]\n"
"ds_read_b64_tr_b16 a[148:149], v108 offset:57600\n"
"v_mfma_f32_16x16x32_bf16 v[248:251], a[80:83], a[204:207], v[248:251]\n"
"ds_read_b64_tr_b16 a[150:151], v108 offset:61696\n"
"v_mfma_f32_16x16x32_bf16 v[248:251], a[84:87], a[220:223], v[248:251]\n"
"ds_read_b64_tr_b16 a[152:153], v109 offset:49408\n"
"s_waitcnt lgkmcnt(7)\n"
"v_mfma_f32_16x16x32_bf16 v[156:159], a[88:91], a[192:195], v[156:159]\n"
"ds_read_b64_tr_b16 a[154:155], v109 offset:53504\n"
"v_mfma_f32_16x16x32_bf16 v[156:159], a[92:95], a[208:211], v[156:159]\n"
"ds_read_b64_tr_b16 a[156:157], v109 offset:57600\n"
"v_mfma_f32_16x16x32_bf16 v[188:191], a[88:91], a[196:199], v[188:191]\n"
"buffer_load_dwordx4 v9, s[48:51], s64 offen offset:3072 lds\n"
"v_mfma_f32_16x16x32_bf16 v[188:191], a[92:95], a[212:215], v[188:191]\n"
"ds_read_b64_tr_b16 a[158:159], v109 offset:61696\n"
"v_mfma_f32_16x16x32_bf16 v[220:223], a[88:91], a[200:203], v[220:223]\n"
"ds_read_b64_tr_b16 a[160:161], v108 offset:49664\n"
"v_mfma_f32_16x16x32_bf16 v[220:223], a[92:95], a[216:219], v[220:223]\n"
"ds_read_b64_tr_b16 a[162:163], v108 offset:53760\n"
"v_mfma_f32_16x16x32_bf16 v[252:255], a[88:91], a[204:207], v[252:255]\n"
"ds_read_b64_tr_b16 a[164:165], v108 offset:57856\n"
"v_mfma_f32_16x16x32_bf16 v[252:255], a[92:95], a[220:223], v[252:255]\n"
"ds_read_b64_tr_b16 a[166:167], v108 offset:61952\n"
"v_mfma_f32_16x16x32_bf16 v[128:131], a[128:131], a[224:227], v[128:131]\n"
"ds_read_b64_tr_b16 a[168:169], v109 offset:49664\n"
"v_mfma_f32_16x16x32_bf16 v[128:131], a[132:135], a[240:243], v[128:131]\n"
"ds_read_b64_tr_b16 a[170:171], v109 offset:53760\n"
"s_add_u32 m0, s66, 49152\n"
"v_mfma_f32_16x16x32_bf16 v[132:135], a[136:139], a[224:227], v[132:135]\n"
"buffer_load_dwordx4 v8, s[52:55], s64 offen lds\n"
"s_waitcnt lgkmcnt(7)\n"
"v_mfma_f32_16x16x32_bf16 v[132:135], a[140:143], a[240:243], v[132:135]\n"
"ds_read_b64_tr_b16 a[176:177], v108 offset:49920\n"
"v_mfma_f32_16x16x32_bf16 v[136:139], a[144:147], a[224:227], v[136:139]\n"
"ds_read_b64_tr_b16 a[178:179], v108 offset:54016\n"
"v_mfma_f32_16x16x32_bf16 v[136:139], a[148:151], a[240:243], v[136:139]\n"
"ds_read_b64_tr_b16 a[184:185], v109 offset:49920\n"
"v_mfma_f32_16x16x32_bf16 v[140:143], a[152:155], a[224:227], v[140:143]\n"
"ds_read_b64_tr_b16 a[186:187], v109 offset:54016\n"
"s_waitcnt lgkmcnt(7)\n"
"v_mfma_f32_16x16x32_bf16 v[140:143], a[156:159], a[240:243], v[140:143]\n"
"v_mfma_f32_16x16x32_bf16 v[144:147], a[160:163], a[224:227], v[144:147]\n"
"s_waitcnt lgkmcnt(5)\n"
"v_mfma_f32_16x16x32_bf16 v[144:147], a[164:167], a[240:243], v[144:147]\n"
"v_cvt_pk_bf16_f32 v128, v128, v129\n"
"v_cvt_pk_bf16_f32 v129, v130, v131\n"
"s_waitcnt lgkmcnt(4)\n"
"v_mfma_f32_16x16x32_bf16 v[148:151], a[168:171], a[224:227], v[148:151]\n"
"buffer_load_dwordx4 v9, s[52:55], s64 offen offset:1024 lds\n"
"v_cvt_pk_bf16_f32 v130, v132, v133\n"
"v_mfma_f32_16x16x32_bf16 v[148:151], a[172:175], a[240:243], v[148:151]\n"
"v_cvt_pk_bf16_f32 v131, v134, v135\n"
"v_accvgpr_write_b32 a0, v128\n"
"s_waitcnt lgkmcnt(2)\n"
"v_mfma_f32_16x16x32_bf16 v[152:155], a[176:179], a[224:227], v[152:155]\n"
"v_accvgpr_write_b32 a1, v129\n"
"v_accvgpr_write_b32 a2, v130\n"
"v_mfma_f32_16x16x32_bf16 v[152:155], a[180:183], a[240:243], v[152:155]\n"
"v_accvgpr_write_b32 a3, v131\n"
"v_cvt_pk_bf16_f32 v136, v136, v137\n"
"s_waitcnt lgkmcnt(0)\n"
"v_mfma_f32_16x16x32_bf16 v[156:159], a[184:187], a[224:227], v[156:159]\n"
"v_cvt_pk_bf16_f32 v137, v138, v139\n"
"v_cvt_pk_bf16_f32 v138, v140, v141\n"
"v_mfma_f32_16x16x32_bf16 v[156:159], a[188:191], a[240:243], v[156:159]\n"
"v_cvt_pk_bf16_f32 v139, v142, v143\n"
"v_accvgpr_write_b32 a4, v136\n"
"v_mfma_f32_16x16x32_bf16 v[160:163], a[128:131], a[228:231], v[160:163]\n"
"v_accvgpr_write_b32 a5, v137\n"
"v_accvgpr_write_b32 a6, v138\n"
"v_mfma_f32_16x16x32_bf16 v[160:163], a[132:135], a[244:247], v[160:163]\n"
"v_accvgpr_write_b32 a7, v139\n"
"v_cvt_pk_bf16_f32 v144, v144, v145\n"
"v_mfma_f32_16x16x32_bf16 v[164:167], a[136:139], a[228:231], v[164:167]\n"
"buffer_load_dwordx4 v8, s[52:55], s64 offen offset:2048 lds\n"
"v_cvt_pk_bf16_f32 v145, v146, v147\n"
"v_mfma_f32_16x16x32_bf16 v[164:167], a[140:143], a[244:247], v[164:167]\n"
"v_cvt_pk_bf16_f32 v146, v148, v149\n"
"v_cvt_pk_bf16_f32 v147, v150, v151\n"
"v_mfma_f32_16x16x32_bf16 v[168:171], a[144:147], a[228:231], v[168:171]\n"
"v_accvgpr_write_b32 a8, v144\n"
"v_accvgpr_write_b32 a9, v145\n"
"v_mfma_f32_16x16x32_bf16 v[168:171], a[148:151], a[244:247], v[168:171]\n"
"v_accvgpr_write_b32 a10, v146\n"
"v_accvgpr_write_b32 a11, v147\n"
"v_mfma_f32_16x16x32_bf16 v[172:175], a[152:155], a[228:231], v[172:175]\n"
"v_cvt_pk_bf16_f32 v152, v152, v153\n"
"v_cvt_pk_bf16_f32 v153, v154, v155\n"
"v_mfma_f32_16x16x32_bf16 v[172:175], a[156:159], a[244:247], v[172:175]\n"
"v_cvt_pk_bf16_f32 v154, v156, v157\n"
"v_cvt_pk_bf16_f32 v155, v158, v159\n"
"v_mfma_f32_16x16x32_bf16 v[176:179], a[160:163], a[228:231], v[176:179]\n"
"v_accvgpr_write_b32 a12, v152\n"
"v_accvgpr_write_b32 a13, v153\n"
"v_mfma_f32_16x16x32_bf16 v[176:179], a[164:167], a[244:247], v[176:179]\n"
"v_accvgpr_write_b32 a14, v154\n"
"v_accvgpr_write_b32 a15, v155\n"
"v_mfma_f32_16x16x32_bf16 v[180:183], a[168:171], a[228:231], v[180:183]\n"
"buffer_load_dwordx4 v9, s[52:55], s64 offen offset:3072 lds\n"
"s_add_u32 s64, s64, 16384\n"
"v_mfma_f32_16x16x32_bf16 v[180:183], a[172:175], a[244:247], v[180:183]\n"
"v_cvt_pk_bf16_f32 v160, v160, v161\n"
"v_cvt_pk_bf16_f32 v161, v162, v163\n"
"v_mfma_f32_16x16x32_bf16 v[184:187], a[176:179], a[228:231], v[184:187]\n"
"v_cvt_pk_bf16_f32 v162, v164, v165\n"
"v_cvt_pk_bf16_f32 v163, v166, v167\n"
"v_mfma_f32_16x16x32_bf16 v[184:187], a[180:183], a[244:247], v[184:187]\n"
"v_accvgpr_write_b32 a16, v160\n"
"v_accvgpr_write_b32 a17, v161\n"
"v_mfma_f32_16x16x32_bf16 v[188:191], a[184:187], a[228:231], v[188:191]\n"
"s_waitcnt vmcnt(8)\n"
"s_barrier\n"
"v_mfma_f32_16x16x32_bf16 v[188:191], a[188:191], a[244:247], v[188:191]\n"
"ds_read_b128 a[64:67], v10 offset:0\n"
"v_accvgpr_write_b32 a18, v162\n"
"v_mfma_f32_16x16x32_bf16 v[192:195], a[128:131], a[232:235], v[192:195]\n"
"v_accvgpr_write_b32 a19, v163\n"
"v_cvt_pk_bf16_f32 v168, v168, v169\n"
"v_mfma_f32_16x16x32_bf16 v[192:195], a[132:135], a[248:251], v[192:195]\n"
"ds_read_b128 a[68:71], v10 offset:256\n"
"v_cvt_pk_bf16_f32 v169, v170, v171\n"
"v_mfma_f32_16x16x32_bf16 v[196:199], a[136:139], a[232:235], v[196:199]\n"
"v_cvt_pk_bf16_f32 v170, v172, v173\n"
"v_cvt_pk_bf16_f32 v171, v174, v175\n"
"v_mfma_f32_16x16x32_bf16 v[196:199], a[140:143], a[248:251], v[196:199]\n"
"ds_read_b128 a[72:75], v10 offset:512\n"
"v_accvgpr_write_b32 a20, v168\n"
"v_mfma_f32_16x16x32_bf16 v[200:203], a[144:147], a[232:235], v[200:203]\n"
"v_accvgpr_write_b32 a21, v169\n"
"v_accvgpr_write_b32 a22, v170\n"
"v_mfma_f32_16x16x32_bf16 v[200:203], a[148:151], a[248:251], v[200:203]\n"
"ds_read_b128 a[76:79], v10 offset:768\n"
"v_accvgpr_write_b32 a23, v171\n"
"v_mfma_f32_16x16x32_bf16 v[204:207], a[152:155], a[232:235], v[204:207]\n"
"v_cvt_pk_bf16_f32 v176, v176, v177\n"
"v_cvt_pk_bf16_f32 v177, v178, v179\n"
"v_mfma_f32_16x16x32_bf16 v[204:207], a[156:159], a[248:251], v[204:207]\n"
"ds_read_b128 a[80:83], v10 offset:4096\n"
"v_cvt_pk_bf16_f32 v178, v180, v181\n"
"v_mfma_f32_16x16x32_bf16 v[208:211], a[160:163], a[232:235], v[208:211]\n"
"v_cvt_pk_bf16_f32 v179, v182, v183\n"
"v_accvgpr_write_b32 a24, v176\n"
"v_mfma_f32_16x16x32_bf16 v[208:211], a[164:167], a[248:251], v[208:211]\n"
"ds_read_b128 a[84:87], v10 offset:4352\n"
"v_accvgpr_write_b32 a25, v177\n"
"v_mfma_f32_16x16x32_bf16 v[212:215], a[168:171], a[232:235], v[212:215]\n"
"v_accvgpr_write_b32 a26, v178\n"
"v_accvgpr_write_b32 a27, v179\n"
"v_mfma_f32_16x16x32_bf16 v[212:215], a[172:175], a[248:251], v[212:215]\n"
"ds_read_b128 a[88:91], v10 offset:4608\n"
"v_cvt_pk_bf16_f32 v184, v184, v185\n"
"v_mfma_f32_16x16x32_bf16 v[216:219], a[176:179], a[232:235], v[216:219]\n"
"v_cvt_pk_bf16_f32 v185, v186, v187\n"
"v_cvt_pk_bf16_f32 v186, v188, v189\n"
"v_mfma_f32_16x16x32_bf16 v[216:219], a[180:183], a[248:251], v[216:219]\n"
"ds_read_b128 a[92:95], v10 offset:4864\n"
"v_cvt_pk_bf16_f32 v187, v190, v191\n"
"v_mfma_f32_16x16x32_bf16 v[220:223], a[184:187], a[232:235], v[220:223]\n"
"v_accvgpr_write_b32 a28, v184\n"
"v_accvgpr_write_b32 a29, v185\n"
"v_mfma_f32_16x16x32_bf16 v[220:223], a[188:191], a[248:251], v[220:223]\n"
"ds_read_b128 a[96:99], v10 offset:8192\n"
"v_accvgpr_write_b32 a30, v186\n"
"v_mfma_f32_16x16x32_bf16 v[224:227], a[128:131], a[236:239], v[224:227]\n"
"v_accvgpr_write_b32 a31, v187\n"
"v_cvt_pk_bf16_f32 v192, v192, v193\n"
"v_mfma_f32_16x16x32_bf16 v[224:227], a[132:135], a[252:255], v[224:227]\n"
"ds_read_b128 a[100:103], v10 offset:8448\n"
"v_cvt_pk_bf16_f32 v193, v194, v195\n"
"v_mfma_f32_16x16x32_bf16 v[228:231], a[136:139], a[236:239], v[228:231]\n"
"v_cvt_pk_bf16_f32 v194, v196, v197\n"
"v_cvt_pk_bf16_f32 v195, v198, v199\n"
"v_mfma_f32_16x16x32_bf16 v[228:231], a[140:143], a[252:255], v[228:231]\n"
"ds_read_b128 a[104:107], v10 offset:8704\n"
"v_accvgpr_write_b32 a32, v192\n"
"v_mfma_f32_16x16x32_bf16 v[232:235], a[144:147], a[236:239], v[232:235]\n"
"v_accvgpr_write_b32 a33, v193\n"
"v_accvgpr_write_b32 a34, v194\n"
"v_mfma_f32_16x16x32_bf16 v[232:235], a[148:151], a[252:255], v[232:235]\n"
"ds_read_b128 a[108:111], v10 offset:8960\n"
"v_accvgpr_write_b32 a35, v195\n"
"v_mfma_f32_16x16x32_bf16 v[236:239], a[152:155], a[236:239], v[236:239]\n"
"v_cvt_pk_bf16_f32 v200, v200, v201\n"
"v_cvt_pk_bf16_f32 v201, v202, v203\n"
"v_mfma_f32_16x16x32_bf16 v[236:239], a[156:159], a[252:255], v[236:239]\n"
"ds_read_b128 a[112:115], v10 offset:12288\n"
"v_cvt_pk_bf16_f32 v202, v204, v205\n"
"v_mfma_f32_16x16x32_bf16 v[240:243], a[160:163], a[236:239], v[240:243]\n"
"v_cvt_pk_bf16_f32 v203, v206, v207\n"
"v_accvgpr_write_b32 a36, v200\n"
"v_mfma_f32_16x16x32_bf16 v[240:243], a[164:167], a[252:255], v[240:243]\n"
"ds_read_b128 a[116:119], v10 offset:12544\n"
"v_accvgpr_write_b32 a37, v201\n"
"v_mfma_f32_16x16x32_bf16 v[244:247], a[168:171], a[236:239], v[244:247]\n"
"v_accvgpr_write_b32 a38, v202\n"
"v_accvgpr_write_b32 a39, v203\n"
"v_mfma_f32_16x16x32_bf16 v[244:247], a[172:175], a[252:255], v[244:247]\n"
"ds_read_b128 a[120:123], v10 offset:12800\n"
"v_cvt_pk_bf16_f32 v208, v208, v209\n"
"v_mfma_f32_16x16x32_bf16 v[248:251], a[176:179], a[236:239], v[248:251]\n"
"v_cvt_pk_bf16_f32 v209, v210, v211\n"
"v_cvt_pk_bf16_f32 v210, v212, v213\n"
"v_mfma_f32_16x16x32_bf16 v[248:251], a[180:183], a[252:255], v[248:251]\n"
"ds_read_b128 a[124:127], v10 offset:13056\n"
"v_cvt_pk_bf16_f32 v211, v214, v215\n"
"v_mfma_f32_16x16x32_bf16 v[252:255], a[184:187], a[236:239], v[252:255]\n"
"v_accvgpr_write_b32 a40, v208\n"
"v_accvgpr_write_b32 a41, v209\n"
"v_mfma_f32_16x16x32_bf16 v[252:255], a[188:191], a[252:255], v[252:255]\n"
"v_accvgpr_write_b32 a42, v210\n"
"v_accvgpr_write_b32 a43, v211\n"
"v_cvt_pk_bf16_f32 v216, v216, v217\n"
"v_cvt_pk_bf16_f32 v217, v218, v219\n"
"v_cvt_pk_bf16_f32 v218, v220, v221\n"
"v_cvt_pk_bf16_f32 v219, v222, v223\n"
"v_accvgpr_write_b32 a44, v216\n"
"v_accvgpr_write_b32 a45, v217\n"
"v_accvgpr_write_b32 a46, v218\n"
"v_accvgpr_write_b32 a47, v219\n"
"s_nop 1\n"
"s_waitcnt lgkmcnt(15)\n"
"v_mfma_f32_16x16x32_bf16 v[16:19], a[64:67], a[0:3], 0\n"
"ds_read_b64_tr_b16 a[128:129], v11 offset:32768\n"
"ds_read_b64_tr_b16 a[130:131], v11 offset:36864\n"
"ds_read_b64_tr_b16 a[154:155], v12 offset:37120\n"
"s_waitcnt lgkmcnt(15)\n"
"v_mfma_f32_16x16x32_bf16 v[16:19], a[68:71], a[4:7], v[16:19]\n"
"ds_read_b64_tr_b16 a[132:133], v11 offset:40960\n"
"ds_read_b64_tr_b16 a[156:157], v12 offset:41216\n"
"s_add_u32 m0, s65, 32768\n"
"v_mfma_f32_16x16x32_bf16 v[16:19], a[72:75], a[8:11], v[16:19]\n"
"ds_read_b64_tr_b16 a[134:135], v11 offset:45056\n"
"ds_read_b64_tr_b16 a[136:137], v12 offset:32768\n"
"ds_read_b64_tr_b16 a[158:159], v12 offset:45312\n"
"v_mfma_f32_16x16x32_bf16 v[16:19], a[76:79], a[12:15], v[16:19]\n"
"buffer_load_dwordx4 v8, s[48:51], s64 offen lds\n"
"s_waitcnt lgkmcnt(15)\n"
"v_mfma_f32_16x16x32_bf16 v[20:23], a[80:83], a[0:3], 0\n"
"buffer_load_dwordx4 v9, s[48:51], s64 offen offset:1024 lds\n"
"v_mfma_f32_16x16x32_bf16 v[20:23], a[84:87], a[4:7], v[20:23]\n"
"buffer_load_dwordx4 v8, s[48:51], s64 offen offset:2048 lds\n"
"v_mfma_f32_16x16x32_bf16 v[20:23], a[88:91], a[8:11], v[20:23]\n"
"ds_read_b64_tr_b16 a[138:139], v12 offset:36864\n"
"ds_read_b64_tr_b16 a[140:141], v12 offset:40960\n"
"v_mfma_f32_16x16x32_bf16 v[20:23], a[92:95], a[12:15], v[20:23]\n"
"buffer_load_dwordx4 v9, s[48:51], s64 offen offset:3072 lds\n"
"s_add_u32 m0, s66, 0\n"
"v_mfma_f32_16x16x32_bf16 v[24:27], a[96:99], a[0:3], 0\n"
"buffer_load_dwordx4 v8, s[52:55], s64 offen lds\n"
"s_waitcnt lgkmcnt(7)\n"
"v_mfma_f32_16x16x32_bf16 v[24:27], a[100:103], a[4:7], v[24:27]\n"
"ds_read_b64_tr_b16 a[142:143], v12 offset:45056\n"
"ds_read_b64_tr_b16 a[144:145], v11 offset:33024\n"
"v_mfma_f32_16x16x32_bf16 v[24:27], a[104:107], a[8:11], v[24:27]\n"
"buffer_load_dwordx4 v9, s[52:55], s64 offen offset:1024 lds\n"
"v_mfma_f32_16x16x32_bf16 v[24:27], a[108:111], a[12:15], v[24:27]\n"
"buffer_load_dwordx4 v[100:103], v13, %[code], 0 offen\n"
"v_mfma_f32_16x16x32_bf16 v[28:31], a[112:115], a[0:3], 0\n"
"ds_read_b64_tr_b16 a[146:147], v11 offset:37120\n"
"ds_read_b64_tr_b16 a[148:149], v11 offset:41216\n"
"v_mfma_f32_16x16x32_bf16 v[28:31], a[116:119], a[4:7], v[28:31]\n"
"ds_read_b64_tr_b16 a[150:151], v11 offset:45312\n"
"ds_read_b64_tr_b16 a[152:153], v12 offset:33024\n"
"v_mfma_f32_16x16x32_bf16 v[28:31], a[120:123], a[8:11], v[28:31]\n"
"s_nop 3\n"
"v_cvt_pk_bf16_f32 v224, v224, v225\n"
"v_mfma_f32_16x16x32_bf16 v[28:31], a[124:127], a[12:15], v[28:31]\n"
"v_cvt_pk_bf16_f32 v225, v226, v227\n"
"v_cvt_pk_bf16_f32 v226, v228, v229\n"
"v_cvt_pk_bf16_f32 v227, v230, v231\n"
"v_accvgpr_write_b32 a48, v224\n"
"v_accvgpr_write_b32 a49, v225\n"
"v_accvgpr_write_b32 a50, v226\n"
"v_accvgpr_write_b32 a51, v227\n"
"v_cvt_pk_bf16_f32 v232, v232, v233\n"
"v_cvt_pk_bf16_f32 v233, v234, v235\n"
"v_cvt_pk_bf16_f32 v234, v236, v237\n"
"v_cvt_pk_bf16_f32 v235, v238, v239\n"
"v_accvgpr_write_b32 a52, v232\n"
"v_accvgpr_write_b32 a53, v233\n"
"v_accvgpr_write_b32 a54, v234\n"
"v_accvgpr_write_b32 a55, v235\n"
"v_cvt_pk_bf16_f32 v240, v240, v241\n"
"v_cvt_pk_bf16_f32 v241, v242, v243\n"
"v_cvt_pk_bf16_f32 v242, v244, v245\n"
"v_cvt_pk_bf16_f32 v243, v246, v247\n"
"v_accvgpr_write_b32 a56, v240\n"
"v_accvgpr_write_b32 a57, v241\n"
"v_accvgpr_write_b32 a58, v242\n"
"v_accvgpr_write_b32 a59, v243\n"
"v_cvt_pk_bf16_f32 v248, v248, v249\n"
"v_cvt_pk_bf16_f32 v249, v250, v251\n"
"v_cvt_pk_bf16_f32 v250, v252, v253\n"
"v_cvt_pk_bf16_f32 v251, v254, v255\n"
"v_accvgpr_write_b32 a60, v248\n"
"v_accvgpr_write_b32 a61, v249\n"
"v_accvgpr_write_b32 a62, v250\n"
"v_accvgpr_write_b32 a63, v251\n"
"v_mfma_f32_16x16x32_bf16 v[32:35], a[64:67], a[16:19], 0\n"
"ds_read_b64_tr_b16 a[160:161], v11 offset:33280\n"
"ds_read_b64_tr_b16 a[162:163], v11 offset:37376\n"
"v_max3_f32 v124, v16, v17, v18\n"
"v_max3_f32 v124, v124, v19, v20\n"
"v_max3_f32 v124, v124, v21, v22\n"
"v_max3_f32 v124, v124, v23, v24\n"
"v_max3_f32 v124, v124, v25, v26\n"
"v_mfma_f32_16x16x32_bf16 v[32:35], a[68:71], a[20:23], v[32:35]\n"
"ds_read_b64_tr_b16 a[164:165], v11 offset:41472\n"
"ds_read_b64_tr_b16 a[166:167], v11 offset:45568\n"
"v_max3_f32 v124, v124, v27, v28\n"
"v_max3_f32 v124, v124, v29, v30\n"
"v_max_f32 v124, v124, v31\n"
"v_mov_b32 v125, v124\n"
"s_nop 1\n"
"v_permlane32_swap_b32 v124, v125\n"
"v_mfma_f32_16x16x32_bf16 v[32:35], a[72:75], a[24:27], v[32:35]\n"
"ds_read_b64_tr_b16 a[168:169], v12 offset:33280\n"
"ds_read_b64_tr_b16 a[170:171], v12 offset:37376\n"
"v_max_f32 v124, v124, v125\n"
"v_mov_b32 v125, v124\n"
"s_nop 1\n"
"v_permlane16_swap_b32 v124, v125\n"
"v_max_f32 v124, v124, v125\n"
"v_sub_f32 v64, 0xc2cc0000, v124\n"
"v_mfma_f32_16x16x32_bf16 v[32:35], a[76:79], a[28:31], v[32:35]\n"
"buffer_load_dwordx4 v8, s[52:55], s64 offen offset:2048 lds\n"
"v_sub_f32 v65, 0xc2cc0000, v124\n"
"v_sub_f32 v66, 0xc2cc0000, v124\n"
"v_sub_f32 v67, 0xc2cc0000, v124\n"
"v_add_f32 v16, v16, v64\n"
"v_add_f32 v17, v17, v64\n"
"v_mfma_f32_16x16x32_bf16 v[36:39], a[80:83], a[16:19], 0\n"
"ds_read_b64_tr_b16 a[172:173], v12 offset:41472\n"
"ds_read_b64_tr_b16 a[174:175], v12 offset:45568\n"
"v_exp_f32 v16, v16\n"
"v_exp_f32 v17, v17\n"
"v_add_f32 v18, v18, v64\n"
"v_mfma_f32_16x16x32_bf16 v[36:39], a[84:87], a[20:23], v[36:39]\n"
"ds_read_b64_tr_b16 a[176:177], v11 offset:33536\n"
"ds_read_b64_tr_b16 a[178:179], v11 offset:37632\n"
"v_add_f32 v19, v19, v64\n"
"v_exp_f32 v18, v18\n"
"v_exp_f32 v19, v19\n"
"v_mfma_f32_16x16x32_bf16 v[36:39], a[88:91], a[24:27], v[36:39]\n"
"ds_read_b64_tr_b16 a[180:181], v11 offset:41728\n"
"ds_read_b64_tr_b16 a[182:183], v11 offset:45824\n"
"v_perm_b32 v48, v17, v16, s27\n"
"v_add_f32 v20, v20, v64\n"
"v_add_f32 v21, v21, v64\n"
"v_exp_f32 v20, v20\n"
"v_mfma_f32_16x16x32_bf16 v[36:39], a[92:95], a[28:31], v[36:39]\n"
"ds_read_b64_tr_b16 a[184:185], v12 offset:33536\n"
"ds_read_b64_tr_b16 a[186:187], v12 offset:37632\n"
"v_exp_f32 v21, v21\n"
"v_perm_b32 v49, v19, v18, s27\n"
"v_add_f32 v22, v22, v64\n"
"v_add_f32 v23, v23, v64\n"
"v_mfma_f32_16x16x32_bf16 v[40:43], a[96:99], a[16:19], 0\n"
"buffer_load_dwordx4 v9, s[52:55], s64 offen offset:3072 lds\n"
"v_exp_f32 v22, v22\n"
"v_exp_f32 v23, v23\n"
"v_perm_b32 v50, v21, v20, s27\n"
"v_mfma_f32_16x16x32_bf16 v[40:43], a[100:103], a[20:23], v[40:43]\n"
"ds_read_b64_tr_b16 a[188:189], v12 offset:41728\n"
"s_add_u32 s64, s64, 16384\n"
"v_add_f32 v24, v24, v64\n"
"v_add_f32 v25, v25, v64\n"
"v_exp_f32 v24, v24\n"
"v_mfma_f32_16x16x32_bf16 v[40:43], a[104:107], a[24:27], v[40:43]\n"
"ds_read_b64_tr_b16 a[190:191], v12 offset:45824\n"
"v_exp_f32 v25, v25\n"
"v_perm_b32 v51, v23, v22, s27\n"
"v_add_f32 v26, v26, v64\n"
"v_add_f32 v27, v27, v64\n"
"v_mfma_f32_16x16x32_bf16 v[40:43], a[108:111], a[28:31], v[40:43]\n"
"v_exp_f32 v26, v26\n"
"v_exp_f32 v27, v27\n"
"v_perm_b32 v52, v25, v24, s27\n"
"v_add_f32 v28, v28, v64\n"
"v_add_f32 v29, v29, v64\n"
"v_mfma_f32_16x16x32_bf16 v[44:47], a[112:115], a[16:19], 0\n"
"v_exp_f32 v28, v28\n"
"v_exp_f32 v29, v29\n"
"v_perm_b32 v53, v27, v26, s27\n"
"v_add_f32 v30, v30, v64\n"
"v_add_f32 v31, v31, v64\n"
"v_mfma_f32_16x16x32_bf16 v[44:47], a[116:119], a[20:23], v[44:47]\n"
"v_exp_f32 v30, v30\n"
"v_exp_f32 v31, v31\n"
"v_perm_b32 v54, v29, v28, s27\n"
"v_perm_b32 v55, v31, v30, s27\n"
"v_mfma_f32_16x16x32_bf16 v[44:47], a[120:123], a[24:27], v[44:47]\n"
"v_mfma_f32_16x16x32_bf16 v[44:47], a[124:127], a[28:31], v[44:47]\n"
"s_waitcnt vmcnt(9)\n"
"s_barrier\n"
"v_mfma_f32_16x16x32_bf16 v[16:19], a[64:67], a[32:35], 0\n"
"ds_read_b64_tr_b16 a[192:193], v11 offset:49152\n"
"v_max3_f32 v124, v32, v33, v34\n"
"v_max3_f32 v124, v124, v35, v36\n"
"v_mfma_f32_16x16x32_bf16 v[16:19], a[68:71], a[36:39], v[16:19]\n"
"ds_read_b64_tr_b16 a[194:195], v11 offset:53248\n"
"s_add_u32 m0, s65, 49152\n"
"v_max3_f32 v124, v124, v37, v38\n"
"v_mfma_f32_16x16x32_bf16 v[16:19], a[72:75], a[40:43], v[16:19]\n"
"ds_read_b64_tr_b16 a[196:197], v11 offset:57344\n"
"v_max3_f32 v124, v124, v39, v40\n"
"v_max3_f32 v124, v124, v41, v42\n"
"v_mfma_f32_16x16x32_bf16 v[16:19], a[76:79], a[44:47], v[16:19]\n"
"buffer_load_dwordx4 v8, s[48:51], s64 offen lds\n"
"v_max3_f32 v124, v124, v43, v44\n"
"v_mfma_f32_16x16x32_bf16 v[128:131], a[128:131], v[48:51], 0\n"
"ds_read_b64_tr_b16 a[198:199], v11 offset:61440\n"
"v_max3_f32 v124, v124, v45, v46\n"
"v_max_f32 v124, v124, v47\n"
"s_waitcnt lgkmcnt(4)\n"
"v_mfma_f32_16x16x32_bf16 v[128:131], a[132:135], v[52:55], v[128:131]\n"
"ds_read_b64_tr_b16 a[200:201], v12 offset:49152\n"
"v_mov_b32 v125, v124\n"
"s_nop 1\n"
"v_permlane32_swap_b32 v124, v125\n"
"v_mfma_f32_16x16x32_bf16 v[132:135], a[136:139], v[48:51], 0\n"
"ds_read_b64_tr_b16 a[202:203], v12 offset:53248\n"
"v_max_f32 v124, v124, v125\n"
"v_mov_b32 v125, v124\n"
"v_mfma_f32_16x16x32_bf16 v[132:135], a[140:143], v[52:55], v[132:135]\n"
"ds_read_b64_tr_b16 a[204:205], v12 offset:57344\n"
"v_permlane16_swap_b32 v124, v125\n"
"v_max_f32 v124, v124, v125\n"
"v_mfma_f32_16x16x32_bf16 v[20:23], a[80:83], a[32:35], 0\n"
"ds_read_b64_tr_b16 a[206:207], v12 offset:61440\n"
"v_sub_f32 v68, 0xc2cc0000, v124\n"
"v_sub_f32 v69, 0xc2cc0000, v124\n"
"v_mfma_f32_16x16x32_bf16 v[20:23], a[84:87], a[36:39], v[20:23]\n"
"ds_read_b64_tr_b16 a[208:209], v11 offset:49408\n"
"v_sub_f32 v70, 0xc2cc0000, v124\n"
"v_sub_f32 v71, 0xc2cc0000, v124\n"
"v_mfma_f32_16x16x32_bf16 v[20:23], a[88:91], a[40:43], v[20:23]\n"
"ds_read_b64_tr_b16 a[210:211], v11 offset:53504\n"
"v_add_f32 v32, v32, v68\n"
"v_add_f32 v33, v33, v68\n"
"v_mfma_f32_16x16x32_bf16 v[20:23], a[92:95], a[44:47], v[20:23]\n"
"ds_read_b64_tr_b16 a[212:213], v11 offset:57600\n"
"v_exp_f32 v32, v32\n"
"v_mfma_f32_16x16x32_bf16 v[136:139], a[144:147], v[48:51], 0\n"
"buffer_load_dwordx4 v9, s[48:51], s64 offen offset:1024 lds\n"
"v_mfma_f32_16x16x32_bf16 v[136:139], a[148:151], v[52:55], v[136:139]\n"
"v_exp_f32 v33, v33\n"
"v_add_f32 v34, v34, v68\n"
"v_mfma_f32_16x16x32_bf16 v[140:143], a[152:155], v[48:51], 0\n"
"v_add_f32 v35, v35, v68\n"
"v_exp_f32 v34, v34\n"
"v_mfma_f32_16x16x32_bf16 v[140:143], a[156:159], v[52:55], v[140:143]\n"
"v_exp_f32 v35, v35\n"
"v_perm_b32 v56, v33, v32, s27\n"
"v_mfma_f32_16x16x32_bf16 v[24:27], a[96:99], a[32:35], 0\n"
"v_add_f32 v36, v36, v68\n"
"v_add_f32 v37, v37, v68\n"
"v_mfma_f32_16x16x32_bf16 v[24:27], a[100:103], a[36:39], v[24:27]\n"
"v_exp_f32 v36, v36\n"
"v_mfma_f32_16x16x32_bf16 v[24:27], a[104:107], a[40:43], v[24:27]\n"
"v_exp_f32 v37, v37\n"
"v_perm_b32 v57, v35, v34, s27\n"
"v_mfma_f32_16x16x32_bf16 v[24:27], a[108:111], a[44:47], v[24:27]\n"
"v_add_f32 v38, v38, v68\n"
"v_add_f32 v39, v39, v68\n"
"v_mfma_f32_16x16x32_bf16 v[144:147], a[160:163], v[48:51], 0\n"
"v_exp_f32 v38, v38\n"
"v_mfma_f32_16x16x32_bf16 v[144:147], a[164:167], v[52:55], v[144:147]\n"
"buffer_load_dwordx4 v8, s[48:51], s64 offen offset:2048 lds\n"
"v_mfma_f32_16x16x32_bf16 v[148:151], a[168:171], v[48:51], 0\n"
"v_exp_f32 v39, v39\n"
"v_perm_b32 v58, v37, v36, s27\n"
"v_mfma_f32_16x16x32_bf16 v[148:151], a[172:175], v[52:55], v[148:151]\n"
"v_add_f32 v40, v40, v68\n"
"v_add_f32 v41, v41, v68\n"
"v_mfma_f32_16x16x32_bf16 v[28:31], a[112:115], a[32:35], 0\n"
"v_exp_f32 v40, v40\n"
"v_mfma_f32_16x16x32_bf16 v[28:31], a[116:119], a[36:39], v[28:31]\n"
"v_exp_f32 v41, v41\n"
"v_perm_b32 v59, v39, v38, s27\n"
"v_mfma_f32_16x16x32_bf16 v[28:31], a[120:123], a[40:43], v[28:31]\n"
"v_add_f32 v42, v42, v68\n"
"v_add_f32 v43, v43, v68\n"
"v_mfma_f32_16x16x32_bf16 v[28:31], a[124:127], a[44:47], v[28:31]\n"
"v_exp_f32 v42, v42\n"
"v_mfma_f32_16x16x32_bf16 v[152:155], a[176:179], v[48:51], 0\n"
"v_exp_f32 v43, v43\n"
"v_perm_b32 v60, v41, v40, s27\n"
"v_mfma_f32_16x16x32_bf16 v[152:155], a[180:183], v[52:55], v[152:155]\n"
"v_add_f32 v44, v44, v68\n"
"v_add_f32 v45, v45, v68\n"
"v_mfma_f32_16x16x32_bf16 v[156:159], a[184:187], v[48:51], 0\n"
"v_exp_f32 v44, v44\n"
"v_mfma_f32_16x16x32_bf16 v[156:159], a[188:191], v[52:55], v[156:159]\n"
"v_exp_f32 v45, v45\n"
"v_perm_b32 v61, v43, v42, s27\n"
"v_mfma_f32_16x16x32_bf16 v[80:83], v[96:99], v[48:51], 0\n"
"v_add_f32 v46, v46, v68\n"
"v_add_f32 v47, v47, v68\n"
"v_mfma_f32_16x16x32_bf16 v[80:83], v[96:99], v[52:55], v[80:83]\n"
"v_exp_f32 v46, v46\n"
"v_exp_f32 v47, v47\n"
"v_perm_b32 v62, v45, v44, s27\n"
"v_perm_b32 v63, v47, v46, s27\n"
"v_mfma_f32_16x16x32_bf16 v[32:35], a[64:67], a[48:51], 0\n"
"ds_read_b128 a[64:67], v10 offset:16384\n"
"v_mfma_f32_16x16x32_bf16 v[32:35], a[68:71], a[52:55], v[32:35]\n"
"ds_read_b128 a[68:71], v10 offset:16640\n"
"v_mfma_f32_16x16x32_bf16 v[32:35], a[72:75], a[56:59], v[32:35]\n"
"ds_read_b128 a[72:75], v10 offset:16896\n"
"v_mfma_f32_16x16x32_bf16 v[32:35], a[76:79], a[60:63], v[32:35]\n"
"ds_read_b128 a[76:79], v10 offset:17152\n"
"v_max3_f32 v124, v16, v17, v18\n"
"v_max3_f32 v124, v124, v19, v20\n"
"v_mfma_f32_16x16x32_bf16 v[160:163], a[128:131], v[56:59], 0\n"
"v_max3_f32 v124, v124, v21, v22\n"
"v_max3_f32 v124, v124, v23, v24\n"
"v_max3_f32 v124, v124, v25, v26\n"
"v_mfma_f32_16x16x32_bf16 v[160:163], a[132:135], v[60:63], v[160:163]\n"
"v_max3_f32 v124, v124, v27, v28\n"
"v_max3_f32 v124, v124, v29, v30\n"
"v_max_f32 v124, v124, v31\n"
"v_mfma_f32_16x16x32_bf16 v[164:167], a[136:139], v[56:59], 0\n"
"v_mov_b32 v125, v124\n"
"s_nop 1\n"
"v_permlane32_swap_b32 v124, v125\n"
"v_max_f32 v124, v124, v125\n"
"v_mfma_f32_16x16x32_bf16 v[164:167], a[140:143], v[60:63], v[164:167]\n"
"v_mov_b32 v125, v124\n"
"s_nop 1\n"
"v_permlane16_swap_b32 v124, v125\n"
"v_max_f32 v124, v124, v125\n"
"v_mfma_f32_16x16x32_bf16 v[36:39], a[80:83], a[48:51], 0\n"
"ds_read_b128 a[80:83], v10 offset:20480\n"
"v_sub_f32 v72, 0xc2cc0000, v124\n"
"v_sub_f32 v73, 0xc2cc0000, v124\n"
"v_mfma_f32_16x16x32_bf16 v[36:39], a[84:87], a[52:55], v[36:39]\n"
"ds_read_b128 a[84:87], v10 offset:20736\n"
"v_sub_f32 v74, 0xc2cc0000, v124\n"
"v_sub_f32 v75, 0xc2cc0000, v124\n"
"v_mfma_f32_16x16x32_bf16 v[36:39], a[88:91], a[56:59], v[36:39]\n"
"ds_read_b128 a[88:91], v10 offset:20992\n"
"v_add_f32 v16, v16, v72\n"
"v_add_f32 v17, v17, v72\n"
"v_mfma_f32_16x16x32_bf16 v[36:39], a[92:95], a[60:63], v[36:39]\n"
"ds_read_b128 a[92:95], v10 offset:21248\n"
"v_exp_f32 v16, v16\n"
"v_mfma_f32_16x16x32_bf16 v[168:171], a[144:147], v[56:59], 0\n"
"v_exp_f32 v17, v17\n"
"v_add_f32 v18, v18, v72\n"
"v_mfma_f32_16x16x32_bf16 v[168:171], a[148:151], v[60:63], v[168:171]\n"
"v_add_f32 v19, v19, v72\n"
"v_exp_f32 v18, v18\n"
"v_mfma_f32_16x16x32_bf16 v[172:175], a[152:155], v[56:59], 0\n"
"v_exp_f32 v19, v19\n"
"v_perm_b32 v48, v17, v16, s27\n"
"v_mfma_f32_16x16x32_bf16 v[172:175], a[156:159], v[60:63], v[172:175]\n"
"v_add_f32 v20, v20, v72\n"
"v_add_f32 v21, v21, v72\n"
"v_mfma_f32_16x16x32_bf16 v[40:43], a[96:99], a[48:51], 0\n"
"ds_read_b128 a[96:99], v10 offset:24576\n"
"v_exp_f32 v20, v20\n"
"v_mfma_f32_16x16x32_bf16 v[40:43], a[100:103], a[52:55], v[40:43]\n"
"ds_read_b128 a[100:103], v10 offset:24832\n"
"v_exp_f32 v21, v21\n"
"v_mfma_f32_16x16x32_bf16 v[40:43], a[104:107], a[56:59], v[40:43]\n"
"ds_read_b128 a[104:107], v10 offset:25088\n"
"v_perm_b32 v49, v19, v18, s27\n"
"v_add_f32 v22, v22, v72\n"
"v_mfma_f32_16x16x32_bf16 v[40:43], a[108:111], a[60:63], v[40:43]\n"
"ds_read_b128 a[108:111], v10 offset:25344\n"
"v_add_f32 v23, v23, v72\n"
"v_mfma_f32_16x16x32_bf16 v[176:179], a[160:163], v[56:59], 0\n"
"v_exp_f32 v22, v22\n"
"v_mfma_f32_16x16x32_bf16 v[176:179], a[164:167], v[60:63], v[176:179]\n"
"v_exp_f32 v23, v23\n"
"v_perm_b32 v50, v21, v20, s27\n"
"v_mfma_f32_16x16x32_bf16 v[180:183], a[168:171], v[56:59], 0\n"
"v_add_f32 v24, v24, v72\n"
"v_add_f32 v25, v25, v72\n"
"v_mfma_f32_16x16x32_bf16 v[180:183], a[172:175], v[60:63], v[180:183]\n"
"v_exp_f32 v24, v24\n"
"v_mfma_f32_16x16x32_bf16 v[44:47], a[112:115], a[48:51], 0\n"
"ds_read_b128 a[112:115], v10 offset:28672\n"
"v_exp_f32 v25, v25\n"
"v_mfma_f32_16x16x32_bf16 v[44:47], a[116:119], a[52:55], v[44:47]\n"
"ds_read_b128 a[116:119], v10 offset:28928\n"
"v_perm_b32 v51, v23, v22, s27\n"
"v_add_f32 v26, v26, v72\n"
"v_mfma_f32_16x16x32_bf16 v[44:47], a[120:123], a[56:59], v[44:47]\n"
"ds_read_b128 a[120:123], v10 offset:29184\n"
"v_add_f32 v27, v27, v72\n"
"v_mfma_f32_16x16x32_bf16 v[44:47], a[124:127], a[60:63], v[44:47]\n"
"ds_read_b128 a[124:127], v10 offset:29440\n"
"v_exp_f32 v26, v26\n"
"v_mfma_f32_16x16x32_bf16 v[184:187], a[176:179], v[56:59], 0\n"
"v_exp_f32 v27, v27\n"
"v_perm_b32 v52, v25, v24, s27\n"
"v_mfma_f32_16x16x32_bf16 v[184:187], a[180:183], v[60:63], v[184:187]\n"
"v_add_f32 v28, v28, v72\n"
"v_add_f32 v29, v29, v72\n"
"v_mfma_f32_16x16x32_bf16 v[188:191], a[184:187], v[56:59], 0\n"
"v_exp_f32 v28, v28\n"
"v_mfma_f32_16x16x32_bf16 v[188:191], a[188:191], v[60:63], v[188:191]\n"
"v_exp_f32 v29, v29\n"
"v_perm_b32 v53, v27, v26, s27\n"
"v_mfma_f32_16x16x32_bf16 v[84:87], v[96:99], v[56:59], 0\n"
"v_add_f32 v30, v30, v72\n"
"v_add_f32 v31, v31, v72\n"
"v_mfma_f32_16x16x32_bf16 v[84:87], v[96:99], v[60:63], v[84:87]\n"
"v_exp_f32 v30, v30\n"
"v_exp_f32 v31, v31\n"
"v_perm_b32 v54, v29, v28, s27\n"
"v_perm_b32 v55, v31, v30, s27\n"
"s_waitcnt lgkmcnt(2)\n"
"v_mfma_f32_16x16x32_bf16 v[16:19], a[64:67], a[0:3], v[64:67]\n"
"ds_read_b64_tr_b16 a[214:215], v11 offset:61696\n"
"v_mfma_f32_16x16x32_bf16 v[16:19], a[68:71], a[4:7], v[16:19]\n"
"ds_read_b64_tr_b16 a[216:217], v12 offset:49408\n"
"v_mfma_f32_16x16x32_bf16 v[16:19], a[72:75], a[8:11], v[16:19]\n"
"ds_read_b64_tr_b16 a[218:219], v12 offset:53504\n"
"v_mfma_f32_16x16x32_bf16 v[16:19], a[76:79], a[12:15], v[16:19]\n"
"buffer_load_dwordx4 v9, s[48:51], s64 offen offset:3072 lds\n"
"v_max3_f32 v124, v32, v33, v34\n"
"v_max3_f32 v124, v124, v35, v36\n"
"v_mfma_f32_16x16x32_bf16 v[192:195], a[128:131], v[48:51], 0\n"
"ds_read_b64_tr_b16 a[220:221], v12 offset:57600\n"
"v_max3_f32 v124, v124, v37, v38\n"
"v_max3_f32 v124, v124, v39, v40\n"
"v_max3_f32 v124, v124, v41, v42\n"
"v_mfma_f32_16x16x32_bf16 v[192:195], a[132:135], v[52:55], v[192:195]\n"
"ds_read_b64_tr_b16 a[222:223], v12 offset:61696\n"
"v_max3_f32 v124, v124, v43, v44\n"
"v_max3_f32 v124, v124, v45, v46\n"
"v_max_f32 v124, v124, v47\n"
"v_mfma_f32_16x16x32_bf16 v[196:199], a[136:139], v[48:51], 0\n"
"ds_read_b64_tr_b16 a[224:225], v11 offset:49664\n"
"s_add_u32 m0, s66, 16384\n"
"v_mov_b32 v125, v124\n"
"s_nop 1\n"
"v_permlane32_swap_b32 v124, v125\n"
"v_mfma_f32_16x16x32_bf16 v[196:199], a[140:143], v[52:55], v[196:199]\n"
"ds_read_b64_tr_b16 a[226:227], v11 offset:53760\n"
"v_max_f32 v124, v124, v125\n"
"v_mov_b32 v125, v124\n"
"s_nop 1\n"
"v_permlane16_swap_b32 v124, v125\n"
"v_mfma_f32_16x16x32_bf16 v[20:23], a[80:83], a[0:3], v[64:67]\n"
"buffer_load_dwordx4 v8, s[52:55], s64 offen lds\n"
"v_max_f32 v124, v124, v125\n"
"v_sub_f32 v76, 0xc2cc0000, v124\n"
"v_mfma_f32_16x16x32_bf16 v[20:23], a[84:87], a[4:7], v[20:23]\n"
"ds_read_b64_tr_b16 a[228:229], v11 offset:57856\n"
"v_sub_f32 v77, 0xc2cc0000, v124\n"
"v_sub_f32 v78, 0xc2cc0000, v124\n"
"v_sub_f32 v79, 0xc2cc0000, v124\n"
"v_mfma_f32_16x16x32_bf16 v[20:23], a[88:91], a[8:11], v[20:23]\n"
"ds_read_b64_tr_b16 a[230:231], v11 offset:61952\n"
"v_add_f32 v32, v32, v76\n"
"v_add_f32 v33, v33, v76\n"
"v_mfma_f32_16x16x32_bf16 v[20:23], a[92:95], a[12:15], v[20:23]\n"
"ds_read_b64_tr_b16 a[232:233], v12 offset:49664\n"
"v_exp_f32 v32, v32\n"
"v_mfma_f32_16x16x32_bf16 v[200:203], a[144:147], v[48:51], 0\n"
"ds_read_b64_tr_b16 a[234:235], v12 offset:53760\n"
"v_exp_f32 v33, v33\n"
"v_add_f32 v34, v34, v76\n"
"v_mfma_f32_16x16x32_bf16 v[200:203], a[148:151], v[52:55], v[200:203]\n"
"v_add_f32 v35, v35, v76\n"
"v_exp_f32 v34, v34\n"
"v_mfma_f32_16x16x32_bf16 v[204:207], a[152:155], v[48:51], 0\n"
"buffer_load_dwordx4 v9, s[52:55], s64 offen offset:1024 lds\n"
"v_exp_f32 v35, v35\n"
"v_mfma_f32_16x16x32_bf16 v[204:207], a[156:159], v[52:55], v[204:207]\n"
"v_perm_b32 v56, v33, v32, s27\n"
"v_add_f32 v36, v36, v76\n"
"v_add_f32 v37, v37, v76\n"
"v_mfma_f32_16x16x32_bf16 v[24:27], a[96:99], a[0:3], v[64:67]\n"
"v_exp_f32 v36, v36\n"
"v_exp_f32 v37, v37\n"
"v_mfma_f32_16x16x32_bf16 v[24:27], a[100:103], a[4:7], v[24:27]\n"
"v_perm_b32 v57, v35, v34, s27\n"
"v_add_f32 v38, v38, v76\n"
"v_add_f32 v39, v39, v76\n"
"v_mfma_f32_16x16x32_bf16 v[24:27], a[104:107], a[8:11], v[24:27]\n"
"v_exp_f32 v38, v38\n"
"v_exp_f32 v39, v39\n"
"v_mfma_f32_16x16x32_bf16 v[24:27], a[108:111], a[12:15], v[24:27]\n"
"buffer_load_dwordx4 v[116:119], v14, s[32:35], s25 offen\n"
"v_perm_b32 v58, v37, v36, s27\n"
"v_add_f32 v40, v40, v76\n"
"v_mfma_f32_16x16x32_bf16 v[208:211], a[160:163], v[48:51], 0\n"
"s_add_u32 s25, s25, 393216\n"
"v_add_f32 v41, v41, v76\n"
"v_exp_f32 v40, v40\n"
"v_mfma_f32_16x16x32_bf16 v[208:211], a[164:167], v[52:55], v[208:211]\n"
"v_exp_f32 v41, v41\n"
"v_perm_b32 v59, v39, v38, s27\n"
"v_add_f32 v42, v42, v76\n"
"v_mfma_f32_16x16x32_bf16 v[212:215], a[168:171], v[48:51], 0\n"
"v_add_f32 v43, v43, v76\n"
"v_exp_f32 v42, v42\n"
"v_mfma_f32_16x16x32_bf16 v[212:215], a[172:175], v[52:55], v[212:215]\n"
"v_exp_f32 v43, v43\n"
"v_perm_b32 v60, v41, v40, s27\n"
"v_add_f32 v44, v44, v76\n"
"v_mfma_f32_16x16x32_bf16 v[28:31], a[112:115], a[0:3], v[64:67]\n"
"v_add_f32 v45, v45, v76\n"
"v_exp_f32 v44, v44\n"
"v_mfma_f32_16x16x32_bf16 v[28:31], a[116:119], a[4:7], v[28:31]\n"
"buffer_load_dword v110, v15, s[28:31], s26 offen sc1\n"
"v_exp_f32 v45, v45\n"
"s_waitcnt lgkmcnt(0)\n"
"v_mfma_f32_16x16x32_bf16 v[28:31], a[120:123], a[8:11], v[28:31]\n"
"s_add_u32 s26, s26, 128\n"
"v_perm_b32 v61, v43, v42, s27\n"
"v_add_f32 v46, v46, v76\n"
"v_add_f32 v47, v47, v76\n"
"v_mfma_f32_16x16x32_bf16 v[28:31], a[124:127], a[12:15], v[28:31]\n"
"v_exp_f32 v46, v46\n"
"v_exp_f32 v47, v47\n"
"v_mfma_f32_16x16x32_bf16 v[216:219], a[176:179], v[48:51], 0\n"
"v_perm_b32 v62, v45, v44, s27\n"
"v_perm_b32 v63, v47, v46, s27\n"
"v_mfma_f32_16x16x32_bf16 v[216:219], a[180:183], v[52:55], v[216:219]\n"
"v_mfma_f32_16x16x32_bf16 v[220:223], a[184:187], v[48:51], 0\n"
"v_mfma_f32_16x16x32_bf16 v[220:223], a[188:191], v[52:55], v[220:223]\n"
"v_mfma_f32_16x16x32_bf16 v[88:91], v[96:99], v[48:51], 0\n"
"v_mfma_f32_16x16x32_bf16 v[88:91], v[96:99], v[52:55], v[88:91]\n"
"v_mfma_f32_16x16x32_bf16 v[32:35], a[64:67], a[16:19], v[68:71]\n"
"v_exp_f32 v16, v16\n"
"v_mfma_f32_16x16x32_bf16 v[32:35], a[68:71], a[20:23], v[32:35]\n"
"ds_read_b64_tr_b16 a[236:237], v12 offset:57856\n"
"v_mov_b32 v112, -1\n"
"v_mfma_f32_16x16x32_bf16 v[32:35], a[72:75], a[24:27], v[32:35]\n"
"v_exp_f32 v17, v17\n"
"v_mfma_f32_16x16x32_bf16 v[32:35], a[76:79], a[28:31], v[32:35]\n"
"buffer_load_dwordx4 v8, s[52:55], s64 offen offset:2048 lds\n"
"v_mfma_f32_16x16x32_bf16 v[224:227], a[128:131], v[56:59], 0\n"
"v_exp_f32 v18, v18\n"
"v_mfma_f32_16x16x32_bf16 v[224:227], a[132:135], v[60:63], v[224:227]\n"
"ds_read_b64_tr_b16 a[238:239], v12 offset:61952\n"
"v_perm_b32 v48, v17, v16, s27\n"
"v_mfma_f32_16x16x32_bf16 v[228:231], a[136:139], v[56:59], 0\n"
"v_exp_f32 v19, v19\n"
"v_mfma_f32_16x16x32_bf16 v[228:231], a[140:143], v[60:63], v[228:231]\n"
"ds_read_b64_tr_b16 a[240:241], v11 offset:49920\n"
"v_perm_b32 v49, v19, v18, s27\n"
"v_mfma_f32_16x16x32_bf16 v[36:39], a[80:83], a[16:19], v[68:71]\n"
"v_exp_f32 v20, v20\n"
"v_mfma_f32_16x16x32_bf16 v[36:39], a[84:87], a[20:23], v[36:39]\n"
"ds_read_b64_tr_b16 a[242:243], v11 offset:54016\n"
"v_mov_b32 v113, -1\n"
"v_mfma_f32_16x16x32_bf16 v[36:39], a[88:91], a[24:27], v[36:39]\n"
"v_exp_f32 v21, v21\n"
"v_mfma_f32_16x16x32_bf16 v[36:39], a[92:95], a[28:31], v[36:39]\n"
"ds_read_b64_tr_b16 a[244:245], v11 offset:58112\n"
"v_perm_b32 v50, v21, v20, s27\n"
"v_mfma_f32_16x16x32_bf16 v[232:235], a[144:147], v[56:59], 0\n"
"v_exp_f32 v22, v22\n"
"v_mfma_f32_16x16x32_bf16 v[232:235], a[148:151], v[60:63], v[232:235]\n"
"buffer_load_dwordx4 v9, s[52:55], s64 offen offset:3072 lds\n"
"v_mfma_f32_16x16x32_bf16 v[236:239], a[152:155], v[56:59], 0\n"
"v_exp_f32 v23, v23\n"
"v_mfma_f32_16x16x32_bf16 v[236:239], a[156:159], v[60:63], v[236:239]\n"
"ds_read_b64_tr_b16 a[246:247], v11 offset:62208\n"
"s_add_u32 s64, s64, 16384\n"
"v_mfma_f32_16x16x32_bf16 v[40:43], a[96:99], a[16:19], v[68:71]\n"
"v_exp_f32 v24, v24\n"
"v_mfma_f32_16x16x32_bf16 v[40:43], a[100:103], a[20:23], v[40:43]\n"
"ds_read_b64_tr_b16 a[248:249], v12 offset:49920\n"
"v_perm_b32 v51, v23, v22, s27\n"
"v_mfma_f32_16x16x32_bf16 v[40:43], a[104:107], a[24:27], v[40:43]\n"
"v_exp_f32 v25, v25\n"
"v_mfma_f32_16x16x32_bf16 v[40:43], a[108:111], a[28:31], v[40:43]\n"
"ds_read_b64_tr_b16 a[250:251], v12 offset:54016\n"
"v_perm_b32 v52, v25, v24, s27\n"
"v_mfma_f32_16x16x32_bf16 v[240:243], a[160:163], v[56:59], 0\n"
"v_exp_f32 v26, v26\n"
"v_mfma_f32_16x16x32_bf16 v[240:243], a[164:167], v[60:63], v[240:243]\n"
"buffer_load_dwordx4 v[100:103], v13, s[76:79], s71 offen\n"
"v_mfma_f32_16x16x32_bf16 v[244:247], a[168:171], v[56:59], 0\n"
"v_exp_f32 v27, v27\n"
"v_mfma_f32_16x16x32_bf16 v[244:247], a[172:175], v[60:63], v[244:247]\n"
"ds_read_b64_tr_b16 a[252:253], v12 offset:58112\n"
"s_add_u32 s71, s71, 1024\n"
"v_mfma_f32_16x16x32_bf16 v[44:47], a[112:115], a[16:19], v[68:71]\n"
"v_exp_f32 v28, v28\n"
"v_mfma_f32_16x16x32_bf16 v[44:47], a[116:119], a[20:23], v[44:47]\n"
"ds_read_b64_tr_b16 a[254:255], v12 offset:62208\n"
"v_perm_b32 v53, v27, v26, s27\n"
"v_mfma_f32_16x16x32_bf16 v[44:47], a[120:123], a[24:27], v[44:47]\n"
"v_exp_f32 v29, v29\n"
"v_mfma_f32_16x16x32_bf16 v[44:47], a[124:127], a[28:31], v[44:47]\n"
"v_perm_b32 v54, v29, v28, s27\n"
"v_mov_b32 v114, -1\n"
"v_mfma_f32_16x16x32_bf16 v[248:251], a[176:179], v[56:59], 0\n"
"v_exp_f32 v30, v30\n"
"v_mfma_f32_16x16x32_bf16 v[248:251], a[180:183], v[60:63], v[248:251]\n"
"v_mov_b32 v115, -1\n"
"v_mfma_f32_16x16x32_bf16 v[252:255], a[184:187], v[56:59], 0\n"
"v_exp_f32 v31, v31\n"
"v_mfma_f32_16x16x32_bf16 v[252:255], a[188:191], v[60:63], v[252:255]\n"
"v_perm_b32 v55, v31, v30, s27\n"
"v_mfma_f32_16x16x32_bf16 v[92:95], v[96:99], v[56:59], 0\n"
"v_mfma_f32_16x16x32_bf16 v[92:95], v[96:99], v[60:63], v[92:95]\n"
"s_waitcnt vmcnt(11)\n"
"s_barrier\n"
"v_mfma_f32_16x16x32_bf16 v[16:19], a[64:67], a[32:35], v[72:75]\n"
"v_exp_f32 v32, v32\n"
"v_mfma_f32_16x16x32_bf16 v[16:19], a[68:71], a[36:39], v[16:19]\n"
"ds_read_b64_tr_b16 a[128:129], v11 offset:0\n"
"s_add_u32 m0, s65, 0\n"
"v_mfma_f32_16x16x32_bf16 v[16:19], a[72:75], a[40:43], v[16:19]\n"
"v_exp_f32 v33, v33\n"
"v_mfma_f32_16x16x32_bf16 v[16:19], a[76:79], a[44:47], v[16:19]\n"
"buffer_load_dwordx4 v8, s[48:51], s64 offen lds\n"
"v_mfma_f32_16x16x32_bf16 v[128:131], a[192:195], v[48:51], v[128:131]\n"
"v_exp_f32 v34, v34\n"
"v_mfma_f32_16x16x32_bf16 v[128:131], a[196:199], v[52:55], v[128:131]\n"
"ds_read_b64_tr_b16 a[130:131], v11 offset:4096\n"
"v_perm_b32 v56, v33, v32, s27\n"
"v_mfma_f32_16x16x32_bf16 v[132:135], a[200:203], v[48:51], v[132:135]\n"
"v_exp_f32 v35, v35\n"
"v_mfma_f32_16x16x32_bf16 v[132:135], a[204:207], v[52:55], v[132:135]\n"
"ds_read_b64_tr_b16 a[132:133], v11 offset:8192\n"
"v_perm_b32 v57, v35, v34, s27\n"
"v_mfma_f32_16x16x32_bf16 v[20:23], a[80:83], a[32:35], v[72:75]\n"
"v_exp_f32 v36, v36\n"
"v_mfma_f32_16x16x32_bf16 v[20:23], a[84:87], a[36:39], v[20:23]\n"
"ds_read_b64_tr_b16 a[134:135], v11 offset:12288\n"
"v_mov_b32 v120, 0\n"
"v_mfma_f32_16x16x32_bf16 v[20:23], a[88:91], a[40:43], v[20:23]\n"
"v_exp_f32 v37, v37\n"
"v_mfma_f32_16x16x32_bf16 v[20:23], a[92:95], a[44:47], v[20:23]\n"
"ds_read_b64_tr_b16 a[136:137], v12 offset:0\n"
"v_perm_b32 v58, v37, v36, s27\n"
"v_mfma_f32_16x16x32_bf16 v[136:139], a[208:211], v[48:51], v[136:139]\n"
"v_exp_f32 v38, v38\n"
"v_mfma_f32_16x16x32_bf16 v[136:139], a[212:215], v[52:55], v[136:139]\n"
"buffer_load_dwordx4 v9, s[48:51], s64 offen offset:1024 lds\n"
"v_mfma_f32_16x16x32_bf16 v[140:143], a[216:219], v[48:51], v[140:143]\n"
"v_exp_f32 v39, v39\n"
"v_mfma_f32_16x16x32_bf16 v[140:143], a[220:223], v[52:55], v[140:143]\n"
"ds_read_b64_tr_b16 a[138:139], v12 offset:4096\n"
"v_perm_b32 v59, v39, v38, s27\n"
"v_mfma_f32_16x16x32_bf16 v[24:27], a[96:99], a[32:35], v[72:75]\n"
"v_exp_f32 v40, v40\n"
"v_mfma_f32_16x16x32_bf16 v[24:27], a[100:103], a[36:39], v[24:27]\n"
"ds_read_b64_tr_b16 a[140:141], v12 offset:8192\n"
"v_mov_b32 v121, 0\n"
"v_mfma_f32_16x16x32_bf16 v[24:27], a[104:107], a[40:43], v[24:27]\n"
"v_exp_f32 v41, v41\n"
"v_mfma_f32_16x16x32_bf16 v[24:27], a[108:111], a[44:47], v[24:27]\n"
"ds_read_b64_tr_b16 a[142:143], v12 offset:12288\n"
"v_perm_b32 v60, v41, v40, s27\n"
"v_mfma_f32_16x16x32_bf16 v[144:147], a[224:227], v[48:51], v[144:147]\n"
"v_exp_f32 v42, v42\n"
"v_mfma_f32_16x16x32_bf16 v[144:147], a[228:231], v[52:55], v[144:147]\n"
"buffer_load_dwordx4 v8, s[48:51], s64 offen offset:2048 lds\n"
"v_mfma_f32_16x16x32_bf16 v[148:151], a[232:235], v[48:51], v[148:151]\n"
"v_exp_f32 v43, v43\n"
"s_waitcnt lgkmcnt(3)\n"
"v_mfma_f32_16x16x32_bf16 v[148:151], a[236:239], v[52:55], v[148:151]\n"
"ds_read_b64_tr_b16 a[144:145], v11 offset:256\n"
"v_perm_b32 v61, v43, v42, s27\n"
"v_mfma_f32_16x16x32_bf16 v[28:31], a[112:115], a[32:35], v[72:75]\n"
"v_exp_f32 v44, v44\n"
"v_mfma_f32_16x16x32_bf16 v[28:31], a[116:119], a[36:39], v[28:31]\n"
"ds_read_b64_tr_b16 a[146:147], v11 offset:4352\n"
"v_mov_b32 v122, 0\n"
"v_mfma_f32_16x16x32_bf16 v[28:31], a[120:123], a[40:43], v[28:31]\n"
"v_exp_f32 v45, v45\n"
"v_mfma_f32_16x16x32_bf16 v[28:31], a[124:127], a[44:47], v[28:31]\n"
"ds_read_b64_tr_b16 a[148:149], v11 offset:8448\n"
"v_perm_b32 v62, v45, v44, s27\n"
"v_mfma_f32_16x16x32_bf16 v[152:155], a[240:243], v[48:51], v[152:155]\n"
"v_exp_f32 v46, v46\n"
"v_mfma_f32_16x16x32_bf16 v[152:155], a[244:247], v[52:55], v[152:155]\n"
"v_mov_b32 v123, 0\n"
"v_mov_b32 v124, 0\n"
"v_mfma_f32_16x16x32_bf16 v[156:159], a[248:251], v[48:51], v[156:159]\n"
"v_exp_f32 v47, v47\n"
"v_mfma_f32_16x16x32_bf16 v[156:159], a[252:255], v[52:55], v[156:159]\n"
"v_perm_b32 v63, v47, v46, s27\n"
"v_mov_b32 v125, 0\n"
"v_mfma_f32_16x16x32_bf16 v[80:83], v[96:99], v[48:51], v[80:83]\n"
"v_mov_b32 v126, 0\n"
"v_mov_b32 v127, 0\n"
"v_mfma_f32_16x16x32_bf16 v[80:83], v[96:99], v[52:55], v[80:83]\n"
"s_mov_b32 s63, 6\n"
".p2align 6\n"
"Lattn_%=:\n"
"v_mfma_f32_16x16x32_bf16 v[32:35], a[64:67], a[48:51], v[76:79]\n"
"v_exp_f32 v16, v16\n"
"v_mfma_f32_16x16x32_bf16 v[32:35], a[68:71], a[52:55], v[32:35]\n"
"ds_read_b128 a[64:67], v10 offset:32768\n"
"v_mfma_f32_16x16x32_bf16 v[32:35], a[72:75], a[56:59], v[32:35]\n"
"v_exp_f32 v17, v17\n"
"v_mfma_f32_16x16x32_bf16 v[32:35], a[76:79], a[60:63], v[32:35]\n"
"ds_read_b128 a[68:71], v10 offset:33024\n"
"v_perm_b32 v48, v17, v16, s27\n"
"v_mfma_f32_16x16x32_bf16 v[160:163], a[192:195], v[56:59], v[160:163]\n"
"v_exp_f32 v18, v18\n"
"v_mfma_f32_16x16x32_bf16 v[160:163], a[196:199], v[60:63], v[160:163]\n"
"ds_read_b128 a[72:75], v10 offset:33280\n"
"v_mfma_f32_16x16x32_bf16 v[164:167], a[200:203], v[56:59], v[164:167]\n"
"v_exp_f32 v19, v19\n"
"v_mfma_f32_16x16x32_bf16 v[164:167], a[204:207], v[60:63], v[164:167]\n"
"ds_read_b128 a[76:79], v10 offset:33536\n"
"v_perm_b32 v49, v19, v18, s27\n"
"v_mfma_f32_16x16x32_bf16 v[36:39], a[80:83], a[48:51], v[76:79]\n"
"v_exp_f32 v20, v20\n"
"v_mfma_f32_16x16x32_bf16 v[36:39], a[84:87], a[52:55], v[36:39]\n"
"ds_read_b128 a[80:83], v10 offset:36864\n"
"v_mfma_f32_16x16x32_bf16 v[36:39], a[88:91], a[56:59], v[36:39]\n"
"v_exp_f32 v21, v21\n"
"v_mfma_f32_16x16x32_bf16 v[36:39], a[92:95], a[60:63], v[36:39]\n"
"ds_read_b128 a[84:87], v10 offset:37120\n"
"v_perm_b32 v50, v21, v20, s27\n"
"v_mfma_f32_16x16x32_bf16 v[168:171], a[208:211], v[56:59], v[168:171]\n"
"v_exp_f32 v22, v22\n"
"v_mfma_f32_16x16x32_bf16 v[168:171], a[212:215], v[60:63], v[168:171]\n"
"ds_read_b128 a[88:91], v10 offset:37376\n"
"v_mfma_f32_16x16x32_bf16 v[172:175], a[216:219], v[56:59], v[172:175]\n"
"v_exp_f32 v23, v23\n"
"v_mfma_f32_16x16x32_bf16 v[172:175], a[220:223], v[60:63], v[172:175]\n"
"ds_read_b128 a[92:95], v10 offset:37632\n"
"v_perm_b32 v51, v23, v22, s27\n"
"v_mfma_f32_16x16x32_bf16 v[40:43], a[96:99], a[48:51], v[76:79]\n"
"v_exp_f32 v24, v24\n"
"v_mfma_f32_16x16x32_bf16 v[40:43], a[100:103], a[52:55], v[40:43]\n"
"ds_read_b128 a[96:99], v10 offset:40960\n"
"v_mfma_f32_16x16x32_bf16 v[40:43], a[104:107], a[56:59], v[40:43]\n"
"v_exp_f32 v25, v25\n"
"v_mfma_f32_16x16x32_bf16 v[40:43], a[108:111], a[60:63], v[40:43]\n"
"ds_read_b128 a[100:103], v10 offset:41216\n"
"v_perm_b32 v52, v25, v24, s27\n"
"v_mfma_f32_16x16x32_bf16 v[176:179], a[224:227], v[56:59], v[176:179]\n"
"v_exp_f32 v26, v26\n"
"v_mfma_f32_16x16x32_bf16 v[176:179], a[228:231], v[60:63], v[176:179]\n"
"ds_read_b128 a[104:107], v10 offset:41472\n"
"v_mfma_f32_16x16x32_bf16 v[180:183], a[232:235], v[56:59], v[180:183]\n"
"v_exp_f32 v27, v27\n"
"v_mfma_f32_16x16x32_bf16 v[180:183], a[236:239], v[60:63], v[180:183]\n"
"ds_read_b128 a[108:111], v10 offset:41728\n"
"v_perm_b32 v53, v27, v26, s27\n"
"v_mfma_f32_16x16x32_bf16 v[44:47], a[112:115], a[48:51], v[76:79]\n"
"v_exp_f32 v28, v28\n"
"v_mfma_f32_16x16x32_bf16 v[44:47], a[116:119], a[52:55], v[44:47]\n"
"ds_read_b128 a[112:115], v10 offset:45056\n"
"v_mfma_f32_16x16x32_bf16 v[44:47], a[120:123], a[56:59], v[44:47]\n"
"v_exp_f32 v29, v29\n"
"v_mfma_f32_16x16x32_bf16 v[44:47], a[124:127], a[60:63], v[44:47]\n"
"ds_read_b128 a[116:119], v10 offset:45312\n"
"v_perm_b32 v54, v29, v28, s27\n"
"v_mfma_f32_16x16x32_bf16 v[184:187], a[240:243], v[56:59], v[184:187]\n"
"v_exp_f32 v30, v30\n"
"v_mfma_f32_16x16x32_bf16 v[184:187], a[244:247], v[60:63], v[184:187]\n"
"ds_read_b128 a[120:123], v10 offset:45568\n"
"v_mfma_f32_16x16x32_bf16 v[188:191], a[248:251], v[56:59], v[188:191]\n"
"v_exp_f32 v31, v31\n"
"v_mfma_f32_16x16x32_bf16 v[188:191], a[252:255], v[60:63], v[188:191]\n"
"ds_read_b128 a[124:127], v10 offset:45824\n"
"v_perm_b32 v55, v31, v30, s27\n"
"v_mfma_f32_16x16x32_bf16 v[84:87], v[96:99], v[56:59], v[84:87]\n"
"v_mfma_f32_16x16x32_bf16 v[84:87], v[96:99], v[60:63], v[84:87]\n"
"s_waitcnt lgkmcnt(3)\n"
"v_mfma_f32_16x16x32_bf16 v[16:19], a[64:67], a[0:3], v[64:67]\n"
"v_exp_f32 v32, v32\n"
"v_mfma_f32_16x16x32_bf16 v[16:19], a[68:71], a[4:7], v[16:19]\n"
"ds_read_b64_tr_b16 a[150:151], v11 offset:12544\n"
"v_mfma_f32_16x16x32_bf16 v[16:19], a[72:75], a[8:11], v[16:19]\n"
"v_exp_f32 v33, v33\n"
"v_mfma_f32_16x16x32_bf16 v[16:19], a[76:79], a[12:15], v[16:19]\n"
"buffer_load_dwordx4 v9, s[48:51], s64 offen offset:3072 lds\n"
"v_mfma_f32_16x16x32_bf16 v[192:195], a[192:195], v[48:51], v[192:195]\n"
"v_exp_f32 v34, v34\n"
"v_mfma_f32_16x16x32_bf16 v[192:195], a[196:199], v[52:55], v[192:195]\n"
"ds_read_b64_tr_b16 a[152:153], v12 offset:256\n"
"v_perm_b32 v56, v33, v32, s27\n"
"v_mfma_f32_16x16x32_bf16 v[196:199], a[200:203], v[48:51], v[196:199]\n"
"v_exp_f32 v35, v35\n"
"v_mfma_f32_16x16x32_bf16 v[196:199], a[204:207], v[52:55], v[196:199]\n"
"ds_read_b64_tr_b16 a[154:155], v12 offset:4352\n"
"s_add_u32 m0, s66, 32768\n"
"v_mfma_f32_16x16x32_bf16 v[20:23], a[80:83], a[0:3], v[64:67]\n"
"v_exp_f32 v36, v36\n"
"v_mfma_f32_16x16x32_bf16 v[20:23], a[84:87], a[4:7], v[20:23]\n"
"buffer_load_dwordx4 v8, s[52:55], s64 offen lds\n"
"v_mfma_f32_16x16x32_bf16 v[20:23], a[88:91], a[8:11], v[20:23]\n"
"v_exp_f32 v37, v37\n"
"v_mfma_f32_16x16x32_bf16 v[20:23], a[92:95], a[12:15], v[20:23]\n"
"ds_read_b64_tr_b16 a[156:157], v12 offset:8448\n"
"v_perm_b32 v57, v35, v34, s27\n"
"v_mfma_f32_16x16x32_bf16 v[200:203], a[208:211], v[48:51], v[200:203]\n"
"v_exp_f32 v38, v38\n"
"v_mfma_f32_16x16x32_bf16 v[200:203], a[212:215], v[52:55], v[200:203]\n"
"ds_read_b64_tr_b16 a[158:159], v12 offset:12544\n"
"v_perm_b32 v58, v37, v36, s27\n"
"v_mfma_f32_16x16x32_bf16 v[204:207], a[216:219], v[48:51], v[204:207]\n"
"v_exp_f32 v39, v39\n"
"v_mfma_f32_16x16x32_bf16 v[204:207], a[220:223], v[52:55], v[204:207]\n"
"buffer_load_dwordx4 v9, s[52:55], s64 offen offset:1024 lds\n"
"v_mfma_f32_16x16x32_bf16 v[24:27], a[96:99], a[0:3], v[64:67]\n"
"v_exp_f32 v40, v40\n"
"v_mfma_f32_16x16x32_bf16 v[24:27], a[100:103], a[4:7], v[24:27]\n"
"ds_read_b64_tr_b16 a[160:161], v11 offset:512\n"
"v_perm_b32 v59, v39, v38, s27\n"
"v_mfma_f32_16x16x32_bf16 v[24:27], a[104:107], a[8:11], v[24:27]\n"
"v_exp_f32 v41, v41\n"
"v_mfma_f32_16x16x32_bf16 v[24:27], a[108:111], a[12:15], v[24:27]\n"
"buffer_load_dwordx4 v[104:107], v14, s[32:35], s25 offen\n"
"v_mfma_f32_16x16x32_bf16 v[208:211], a[224:227], v[48:51], v[208:211]\n"
"v_exp_f32 v42, v42\n"
"v_mfma_f32_16x16x32_bf16 v[208:211], a[228:231], v[52:55], v[208:211]\n"
"ds_read_b64_tr_b16 a[162:163], v11 offset:4608\n"
"s_add_u32 s25, s25, 393216\n"
"v_mfma_f32_16x16x32_bf16 v[212:215], a[232:235], v[48:51], v[212:215]\n"
"v_exp_f32 v43, v43\n"
"v_mfma_f32_16x16x32_bf16 v[212:215], a[236:239], v[52:55], v[212:215]\n"
"ds_read_b64_tr_b16 a[164:165], v11 offset:8704\n"
"v_perm_b32 v60, v41, v40, s27\n"
"v_mfma_f32_16x16x32_bf16 v[28:31], a[112:115], a[0:3], v[64:67]\n"
"v_exp_f32 v44, v44\n"
"s_waitcnt lgkmcnt(3)\n"
"v_mfma_f32_16x16x32_bf16 v[28:31], a[116:119], a[4:7], v[28:31]\n"
"buffer_load_dword v108, v15, s[28:31], s26 offen sc1\n"
"v_mfma_f32_16x16x32_bf16 v[28:31], a[120:123], a[8:11], v[28:31]\n"
"v_exp_f32 v45, v45\n"
"v_mfma_f32_16x16x32_bf16 v[28:31], a[124:127], a[12:15], v[28:31]\n"
"ds_read_b64_tr_b16 a[166:167], v11 offset:12800\n"
"s_add_u32 s26, s26, 128\n"
"v_mfma_f32_16x16x32_bf16 v[216:219], a[240:243], v[48:51], v[216:219]\n"
"v_exp_f32 v46, v46\n"
"v_mfma_f32_16x16x32_bf16 v[216:219], a[244:247], v[52:55], v[216:219]\n"
"ds_read_b64_tr_b16 a[168:169], v12 offset:512\n"
"v_perm_b32 v61, v43, v42, s27\n"
"v_mfma_f32_16x16x32_bf16 v[220:223], a[248:251], v[48:51], v[220:223]\n"
"v_exp_f32 v47, v47\n"
"v_mfma_f32_16x16x32_bf16 v[220:223], a[252:255], v[52:55], v[220:223]\n"
"ds_read_b64_tr_b16 a[170:171], v12 offset:4608\n"
"v_perm_b32 v62, v45, v44, s27\n"
"v_mfma_f32_16x16x32_bf16 v[88:91], v[96:99], v[48:51], v[88:91]\n"
"v_perm_b32 v63, v47, v46, s27\n"
"v_mfma_f32_16x16x32_bf16 v[88:91], v[96:99], v[52:55], v[88:91]\n"
"v_mfma_f32_16x16x32_bf16 v[32:35], a[64:67], a[16:19], v[68:71]\n"
"v_exp_f32 v16, v16\n"
"v_mfma_f32_16x16x32_bf16 v[32:35], a[68:71], a[20:23], v[32:35]\n"
"ds_read_b64_tr_b16 a[172:173], v12 offset:8704\n"
"v_mfma_f32_16x16x32_bf16 v[32:35], a[72:75], a[24:27], v[32:35]\n"
"v_exp_f32 v17, v17\n"
"v_mfma_f32_16x16x32_bf16 v[32:35], a[76:79], a[28:31], v[32:35]\n"
"buffer_load_dwordx4 v8, s[52:55], s64 offen offset:2048 lds\n"
"v_mfma_f32_16x16x32_bf16 v[224:227], a[192:195], v[56:59], v[224:227]\n"
"v_exp_f32 v18, v18\n"
"v_mfma_f32_16x16x32_bf16 v[224:227], a[196:199], v[60:63], v[224:227]\n"
"ds_read_b64_tr_b16 a[174:175], v12 offset:12800\n"
"v_perm_b32 v48, v17, v16, s27\n"
"v_mfma_f32_16x16x32_bf16 v[228:231], a[200:203], v[56:59], v[228:231]\n"
"v_exp_f32 v19, v19\n"
"v_mfma_f32_16x16x32_bf16 v[228:231], a[204:207], v[60:63], v[228:231]\n"
"ds_read_b64_tr_b16 a[176:177], v11 offset:768\n"
"v_perm_b32 v49, v19, v18, s27\n"
"v_mfma_f32_16x16x32_bf16 v[36:39], a[80:83], a[16:19], v[68:71]\n"
"v_exp_f32 v20, v20\n"
"v_mfma_f32_16x16x32_bf16 v[36:39], a[84:87], a[20:23], v[36:39]\n"
"ds_read_b64_tr_b16 a[178:179], v11 offset:4864\n"
"v_mfma_f32_16x16x32_bf16 v[36:39], a[88:91], a[24:27], v[36:39]\n"
"v_exp_f32 v21, v21\n"
"v_mfma_f32_16x16x32_bf16 v[36:39], a[92:95], a[28:31], v[36:39]\n"
"ds_read_b64_tr_b16 a[180:181], v11 offset:8960\n"
"v_perm_b32 v50, v21, v20, s27\n"
"v_mfma_f32_16x16x32_bf16 v[232:235], a[208:211], v[56:59], v[232:235]\n"
"v_exp_f32 v22, v22\n"
"v_mfma_f32_16x16x32_bf16 v[232:235], a[212:215], v[60:63], v[232:235]\n"
"buffer_load_dwordx4 v9, s[52:55], s64 offen offset:3072 lds\n"
"v_mfma_f32_16x16x32_bf16 v[236:239], a[216:219], v[56:59], v[236:239]\n"
"v_exp_f32 v23, v23\n"
"v_mfma_f32_16x16x32_bf16 v[236:239], a[220:223], v[60:63], v[236:239]\n"
"ds_read_b64_tr_b16 a[182:183], v11 offset:13056\n"
"s_add_u32 s64, s64, 16384\n"
"v_mfma_f32_16x16x32_bf16 v[40:43], a[96:99], a[16:19], v[68:71]\n"
"v_exp_f32 v24, v24\n"
"v_mfma_f32_16x16x32_bf16 v[40:43], a[100:103], a[20:23], v[40:43]\n"
"ds_read_b64_tr_b16 a[184:185], v12 offset:768\n"
"v_perm_b32 v51, v23, v22, s27\n"
"v_mfma_f32_16x16x32_bf16 v[40:43], a[104:107], a[24:27], v[40:43]\n"
"v_exp_f32 v25, v25\n"
"v_mfma_f32_16x16x32_bf16 v[40:43], a[108:111], a[28:31], v[40:43]\n"
"ds_read_b64_tr_b16 a[186:187], v12 offset:4864\n"
"v_perm_b32 v52, v25, v24, s27\n"
"v_mfma_f32_16x16x32_bf16 v[240:243], a[224:227], v[56:59], v[240:243]\n"
"v_exp_f32 v26, v26\n"
"v_mfma_f32_16x16x32_bf16 v[240:243], a[228:231], v[60:63], v[240:243]\n"
"buffer_load_dwordx4 v[100:103], v13, s[76:79], s71 offen\n"
"v_mfma_f32_16x16x32_bf16 v[244:247], a[232:235], v[56:59], v[244:247]\n"
"v_exp_f32 v27, v27\n"
"v_mfma_f32_16x16x32_bf16 v[244:247], a[236:239], v[60:63], v[244:247]\n"
"ds_read_b64_tr_b16 a[188:189], v12 offset:8960\n"
"s_add_u32 s71, s71, 1024\n"
"v_mfma_f32_16x16x32_bf16 v[44:47], a[112:115], a[16:19], v[68:71]\n"
"v_exp_f32 v28, v28\n"
"v_mfma_f32_16x16x32_bf16 v[44:47], a[116:119], a[20:23], v[44:47]\n"
"ds_read_b64_tr_b16 a[190:191], v12 offset:13056\n"
"v_perm_b32 v53, v27, v26, s27\n"
"v_mfma_f32_16x16x32_bf16 v[44:47], a[120:123], a[24:27], v[44:47]\n"
"v_exp_f32 v29, v29\n"
"v_mfma_f32_16x16x32_bf16 v[44:47], a[124:127], a[28:31], v[44:47]\n"
"v_perm_b32 v54, v29, v28, s27\n"
"v_mfma_f32_16x16x32_bf16 v[248:251], a[240:243], v[56:59], v[248:251]\n"
"v_exp_f32 v30, v30\n"
"v_mfma_f32_16x16x32_bf16 v[248:251], a[244:247], v[60:63], v[248:251]\n"
"v_mfma_f32_16x16x32_bf16 v[252:255], a[248:251], v[56:59], v[252:255]\n"
"v_exp_f32 v31, v31\n"
"v_mfma_f32_16x16x32_bf16 v[252:255], a[252:255], v[60:63], v[252:255]\n"
"v_perm_b32 v55, v31, v30, s27\n"
"v_mfma_f32_16x16x32_bf16 v[92:95], v[96:99], v[56:59], v[92:95]\n"
"v_mfma_f32_16x16x32_bf16 v[92:95], v[96:99], v[60:63], v[92:95]\n"
"s_waitcnt vmcnt(12)\n"
"s_barrier\n"
"v_mfma_f32_16x16x32_bf16 v[16:19], a[64:67], a[32:35], v[72:75]\n"
"v_exp_f32 v32, v32\n"
"v_mfma_f32_16x16x32_bf16 v[16:19], a[68:71], a[36:39], v[16:19]\n"
"ds_read_b64_tr_b16 a[192:193], v11 offset:16384\n"
"s_add_u32 m0, s65, 16384\n"
"v_mfma_f32_16x16x32_bf16 v[16:19], a[72:75], a[40:43], v[16:19]\n"
"v_exp_f32 v33, v33\n"
"v_mfma_f32_16x16x32_bf16 v[16:19], a[76:79], a[44:47], v[16:19]\n"
"buffer_load_dwordx4 v8, s[48:51], s64 offen lds\n"
"v_mfma_f32_16x16x32_bf16 v[128:131], a[128:131], v[48:51], v[128:131]\n"
"v_exp_f32 v34, v34\n"
"v_mfma_f32_16x16x32_bf16 v[128:131], a[132:135], v[52:55], v[128:131]\n"
"ds_read_b64_tr_b16 a[194:195], v11 offset:20480\n"
"v_perm_b32 v56, v33, v32, s27\n"
"v_mfma_f32_16x16x32_bf16 v[132:135], a[136:139], v[48:51], v[132:135]\n"
"v_exp_f32 v35, v35\n"
"v_mfma_f32_16x16x32_bf16 v[132:135], a[140:143], v[52:55], v[132:135]\n"
"ds_read_b64_tr_b16 a[196:197], v11 offset:24576\n"
"v_perm_b32 v57, v35, v34, s27\n"
"v_mfma_f32_16x16x32_bf16 v[20:23], a[80:83], a[32:35], v[72:75]\n"
"v_exp_f32 v36, v36\n"
"v_mfma_f32_16x16x32_bf16 v[20:23], a[84:87], a[36:39], v[20:23]\n"
"ds_read_b64_tr_b16 a[198:199], v11 offset:28672\n"
"v_mfma_f32_16x16x32_bf16 v[20:23], a[88:91], a[40:43], v[20:23]\n"
"v_exp_f32 v37, v37\n"
"v_mfma_f32_16x16x32_bf16 v[20:23], a[92:95], a[44:47], v[20:23]\n"
"ds_read_b64_tr_b16 a[200:201], v12 offset:16384\n"
"v_perm_b32 v58, v37, v36, s27\n"
"v_mfma_f32_16x16x32_bf16 v[136:139], a[144:147], v[48:51], v[136:139]\n"
"v_exp_f32 v38, v38\n"
"v_mfma_f32_16x16x32_bf16 v[136:139], a[148:151], v[52:55], v[136:139]\n"
"buffer_load_dwordx4 v9, s[48:51], s64 offen offset:1024 lds\n"
"v_mfma_f32_16x16x32_bf16 v[140:143], a[152:155], v[48:51], v[140:143]\n"
"v_exp_f32 v39, v39\n"
"v_mfma_f32_16x16x32_bf16 v[140:143], a[156:159], v[52:55], v[140:143]\n"
"ds_read_b64_tr_b16 a[202:203], v12 offset:20480\n"
"v_perm_b32 v59, v39, v38, s27\n"
"v_mfma_f32_16x16x32_bf16 v[24:27], a[96:99], a[32:35], v[72:75]\n"
"v_exp_f32 v40, v40\n"
"v_mfma_f32_16x16x32_bf16 v[24:27], a[100:103], a[36:39], v[24:27]\n"
"ds_read_b64_tr_b16 a[204:205], v12 offset:24576\n"
"v_mfma_f32_16x16x32_bf16 v[24:27], a[104:107], a[40:43], v[24:27]\n"
"v_exp_f32 v41, v41\n"
"v_mfma_f32_16x16x32_bf16 v[24:27], a[108:111], a[44:47], v[24:27]\n"
"ds_read_b64_tr_b16 a[206:207], v12 offset:28672\n"
"v_perm_b32 v60, v41, v40, s27\n"
"s_waitcnt lgkmcnt(3)\n"
"v_mfma_f32_16x16x32_bf16 v[144:147], a[160:163], v[48:51], v[144:147]\n"
"v_exp_f32 v42, v42\n"
"v_mfma_f32_16x16x32_bf16 v[144:147], a[164:167], v[52:55], v[144:147]\n"
"buffer_load_dwordx4 v8, s[48:51], s64 offen offset:2048 lds\n"
"v_mfma_f32_16x16x32_bf16 v[148:151], a[168:171], v[48:51], v[148:151]\n"
"v_exp_f32 v43, v43\n"
"v_mfma_f32_16x16x32_bf16 v[148:151], a[172:175], v[52:55], v[148:151]\n"
"ds_read_b64_tr_b16 a[208:209], v11 offset:16640\n"
"v_perm_b32 v61, v43, v42, s27\n"
"v_mfma_f32_16x16x32_bf16 v[28:31], a[112:115], a[32:35], v[72:75]\n"
"v_exp_f32 v44, v44\n"
"v_mfma_f32_16x16x32_bf16 v[28:31], a[116:119], a[36:39], v[28:31]\n"
"ds_read_b64_tr_b16 a[210:211], v11 offset:20736\n"
"v_mfma_f32_16x16x32_bf16 v[28:31], a[120:123], a[40:43], v[28:31]\n"
"v_exp_f32 v45, v45\n"
"v_mfma_f32_16x16x32_bf16 v[28:31], a[124:127], a[44:47], v[28:31]\n"
"ds_read_b64_tr_b16 a[212:213], v11 offset:24832\n"
"v_perm_b32 v62, v45, v44, s27\n"
"v_mfma_f32_16x16x32_bf16 v[152:155], a[176:179], v[48:51], v[152:155]\n"
"v_exp_f32 v46, v46\n"
"v_mfma_f32_16x16x32_bf16 v[152:155], a[180:183], v[52:55], v[152:155]\n"
"v_mfma_f32_16x16x32_bf16 v[156:159], a[184:187], v[48:51], v[156:159]\n"
"v_exp_f32 v47, v47\n"
"v_mfma_f32_16x16x32_bf16 v[156:159], a[188:191], v[52:55], v[156:159]\n"
"v_perm_b32 v63, v47, v46, s27\n"
"v_mfma_f32_16x16x32_bf16 v[80:83], v[96:99], v[48:51], v[80:83]\n"
"v_mfma_f32_16x16x32_bf16 v[80:83], v[96:99], v[52:55], v[80:83]\n"
"v_mfma_f32_16x16x32_bf16 v[32:35], a[64:67], a[48:51], v[76:79]\n"
"v_exp_f32 v16, v16\n"
"v_mfma_f32_16x16x32_bf16 v[32:35], a[68:71], a[52:55], v[32:35]\n"
"ds_read_b128 a[64:67], v10 offset:49152\n"
"v_mfma_f32_16x16x32_bf16 v[32:35], a[72:75], a[56:59], v[32:35]\n"
"v_exp_f32 v17, v17\n"
"v_mfma_f32_16x16x32_bf16 v[32:35], a[76:79], a[60:63], v[32:35]\n"
"ds_read_b128 a[68:71], v10 offset:49408\n"
"v_perm_b32 v48, v17, v16, s27\n"
"v_mfma_f32_16x16x32_bf16 v[160:163], a[128:131], v[56:59], v[160:163]\n"
"v_exp_f32 v18, v18\n"
"v_mfma_f32_16x16x32_bf16 v[160:163], a[132:135], v[60:63], v[160:163]\n"
"ds_read_b128 a[72:75], v10 offset:49664\n"
"v_mfma_f32_16x16x32_bf16 v[164:167], a[136:139], v[56:59], v[164:167]\n"
"v_exp_f32 v19, v19\n"
"v_mfma_f32_16x16x32_bf16 v[164:167], a[140:143], v[60:63], v[164:167]\n"
"ds_read_b128 a[76:79], v10 offset:49920\n"
"v_perm_b32 v49, v19, v18, s27\n"
"v_mfma_f32_16x16x32_bf16 v[36:39], a[80:83], a[48:51], v[76:79]\n"
"v_exp_f32 v20, v20\n"
"v_mfma_f32_16x16x32_bf16 v[36:39], a[84:87], a[52:55], v[36:39]\n"
"ds_read_b128 a[80:83], v10 offset:53248\n"
"v_mfma_f32_16x16x32_bf16 v[36:39], a[88:91], a[56:59], v[36:39]\n"
"v_exp_f32 v21, v21\n"
"v_mfma_f32_16x16x32_bf16 v[36:39], a[92:95], a[60:63], v[36:39]\n"
"ds_read_b128 a[84:87], v10 offset:53504\n"
"v_perm_b32 v50, v21, v20, s27\n"
"v_mfma_f32_16x16x32_bf16 v[168:171], a[144:147], v[56:59], v[168:171]\n"
"v_exp_f32 v22, v22\n"
"v_mfma_f32_16x16x32_bf16 v[168:171], a[148:151], v[60:63], v[168:171]\n"
"ds_read_b128 a[88:91], v10 offset:53760\n"
"v_mfma_f32_16x16x32_bf16 v[172:175], a[152:155], v[56:59], v[172:175]\n"
"v_exp_f32 v23, v23\n"
"v_mfma_f32_16x16x32_bf16 v[172:175], a[156:159], v[60:63], v[172:175]\n"
"ds_read_b128 a[92:95], v10 offset:54016\n"
"v_perm_b32 v51, v23, v22, s27\n"
"v_mfma_f32_16x16x32_bf16 v[40:43], a[96:99], a[48:51], v[76:79]\n"
"v_exp_f32 v24, v24\n"
"v_mfma_f32_16x16x32_bf16 v[40:43], a[100:103], a[52:55], v[40:43]\n"
"ds_read_b128 a[96:99], v10 offset:57344\n"
"v_mfma_f32_16x16x32_bf16 v[40:43], a[104:107], a[56:59], v[40:43]\n"
"v_exp_f32 v25, v25\n"
"v_mfma_f32_16x16x32_bf16 v[40:43], a[108:111], a[60:63], v[40:43]\n"
"ds_read_b128 a[100:103], v10 offset:57600\n"
"v_perm_b32 v52, v25, v24, s27\n"
"v_mfma_f32_16x16x32_bf16 v[176:179], a[160:163], v[56:59], v[176:179]\n"
"v_exp_f32 v26, v26\n"
"v_mfma_f32_16x16x32_bf16 v[176:179], a[164:167], v[60:63], v[176:179]\n"
"ds_read_b128 a[104:107], v10 offset:57856\n"
"v_mfma_f32_16x16x32_bf16 v[180:183], a[168:171], v[56:59], v[180:183]\n"
"v_exp_f32 v27, v27\n"
"v_mfma_f32_16x16x32_bf16 v[180:183], a[172:175], v[60:63], v[180:183]\n"
"ds_read_b128 a[108:111], v10 offset:58112\n"
"v_perm_b32 v53, v27, v26, s27\n"
"v_mfma_f32_16x16x32_bf16 v[44:47], a[112:115], a[48:51], v[76:79]\n"
"v_exp_f32 v28, v28\n"
"v_mfma_f32_16x16x32_bf16 v[44:47], a[116:119], a[52:55], v[44:47]\n"
"ds_read_b128 a[112:115], v10 offset:61440\n"
"v_mfma_f32_16x16x32_bf16 v[44:47], a[120:123], a[56:59], v[44:47]\n"
"v_exp_f32 v29, v29\n"
"v_mfma_f32_16x16x32_bf16 v[44:47], a[124:127], a[60:63], v[44:47]\n"
"ds_read_b128 a[116:119], v10 offset:61696\n"
"v_perm_b32 v54, v29, v28, s27\n"
"v_mfma_f32_16x16x32_bf16 v[184:187], a[176:179], v[56:59], v[184:187]\n"
"v_exp_f32 v30, v30\n"
"v_mfma_f32_16x16x32_bf16 v[184:187], a[180:183], v[60:63], v[184:187]\n"
"ds_read_b128 a[120:123], v10 offset:61952\n"
"v_mfma_f32_16x16x32_bf16 v[188:191], a[184:187], v[56:59], v[188:191]\n"
"v_exp_f32 v31, v31\n"
"v_mfma_f32_16x16x32_bf16 v[188:191], a[188:191], v[60:63], v[188:191]\n"
"ds_read_b128 a[124:127], v10 offset:62208\n"
"v_perm_b32 v55, v31, v30, s27\n"
"v_mfma_f32_16x16x32_bf16 v[84:87], v[96:99], v[56:59], v[84:87]\n"
"v_mfma_f32_16x16x32_bf16 v[84:87], v[96:99], v[60:63], v[84:87]\n"
"s_waitcnt lgkmcnt(3)\n"
"v_mfma_f32_16x16x32_bf16 v[16:19], a[64:67], a[0:3], v[64:67]\n"
"v_exp_f32 v32, v32\n"
"v_mfma_f32_16x16x32_bf16 v[16:19], a[68:71], a[4:7], v[16:19]\n"
"ds_read_b64_tr_b16 a[214:215], v11 offset:28928\n"
"v_mfma_f32_16x16x32_bf16 v[16:19], a[72:75], a[8:11], v[16:19]\n"
"v_exp_f32 v33, v33\n"
"v_mfma_f32_16x16x32_bf16 v[16:19], a[76:79], a[12:15], v[16:19]\n"
"buffer_load_dwordx4 v9, s[48:51], s64 offen offset:3072 lds\n"
"v_mfma_f32_16x16x32_bf16 v[192:195], a[128:131], v[48:51], v[192:195]\n"
"v_exp_f32 v34, v34\n"
"v_mfma_f32_16x16x32_bf16 v[192:195], a[132:135], v[52:55], v[192:195]\n"
"ds_read_b64_tr_b16 a[216:217], v12 offset:16640\n"
"v_perm_b32 v56, v33, v32, s27\n"
"v_mfma_f32_16x16x32_bf16 v[196:199], a[136:139], v[48:51], v[196:199]\n"
"v_exp_f32 v35, v35\n"
"v_mfma_f32_16x16x32_bf16 v[196:199], a[140:143], v[52:55], v[196:199]\n"
"ds_read_b64_tr_b16 a[218:219], v12 offset:20736\n"
"v_perm_b32 v57, v35, v34, s27\n"
"v_mfma_f32_16x16x32_bf16 v[20:23], a[80:83], a[0:3], v[64:67]\n"
"v_exp_f32 v36, v36\n"
"v_mfma_f32_16x16x32_bf16 v[20:23], a[84:87], a[4:7], v[20:23]\n"
"ds_read_b64_tr_b16 a[220:221], v12 offset:24832\n"
"s_add_u32 m0, s66, 49152\n"
"v_mfma_f32_16x16x32_bf16 v[20:23], a[88:91], a[8:11], v[20:23]\n"
"v_exp_f32 v37, v37\n"
"v_mfma_f32_16x16x32_bf16 v[20:23], a[92:95], a[12:15], v[20:23]\n"
"buffer_load_dwordx4 v8, s[52:55], s64 offen lds\n"
"v_mfma_f32_16x16x32_bf16 v[200:203], a[144:147], v[48:51], v[200:203]\n"
"v_exp_f32 v38, v38\n"
"v_mfma_f32_16x16x32_bf16 v[200:203], a[148:151], v[52:55], v[200:203]\n"
"ds_read_b64_tr_b16 a[222:223], v12 offset:28928\n"
"v_perm_b32 v58, v37, v36, s27\n"
"v_mfma_f32_16x16x32_bf16 v[204:207], a[152:155], v[48:51], v[204:207]\n"
"v_exp_f32 v39, v39\n"
"v_mfma_f32_16x16x32_bf16 v[204:207], a[156:159], v[52:55], v[204:207]\n"
"ds_read_b64_tr_b16 a[224:225], v11 offset:16896\n"
"v_perm_b32 v59, v39, v38, s27\n"
"v_mfma_f32_16x16x32_bf16 v[24:27], a[96:99], a[0:3], v[64:67]\n"
"v_exp_f32 v40, v40\n"
"v_mfma_f32_16x16x32_bf16 v[24:27], a[100:103], a[4:7], v[24:27]\n"
"buffer_load_dwordx4 v9, s[52:55], s64 offen offset:1024 lds\n"
"v_mfma_f32_16x16x32_bf16 v[24:27], a[104:107], a[8:11], v[24:27]\n"
"v_exp_f32 v41, v41\n"
"v_mfma_f32_16x16x32_bf16 v[24:27], a[108:111], a[12:15], v[24:27]\n"
"ds_read_b64_tr_b16 a[226:227], v11 offset:20992\n"
"v_perm_b32 v60, v41, v40, s27\n"
"v_mfma_f32_16x16x32_bf16 v[208:211], a[160:163], v[48:51], v[208:211]\n"
"v_exp_f32 v42, v42\n"
"v_mfma_f32_16x16x32_bf16 v[208:211], a[164:167], v[52:55], v[208:211]\n"
"ds_read_b64_tr_b16 a[228:229], v11 offset:25088\n"
"v_mfma_f32_16x16x32_bf16 v[212:215], a[168:171], v[48:51], v[212:215]\n"
"v_exp_f32 v43, v43\n"
"v_mfma_f32_16x16x32_bf16 v[212:215], a[172:175], v[52:55], v[212:215]\n"
"ds_read_b64_tr_b16 a[230:231], v11 offset:29184\n"
"v_perm_b32 v61, v43, v42, s27\n"
"v_mfma_f32_16x16x32_bf16 v[28:31], a[112:115], a[0:3], v[64:67]\n"
"v_exp_f32 v44, v44\n"
"s_waitcnt lgkmcnt(3)\n"
"v_mfma_f32_16x16x32_bf16 v[28:31], a[116:119], a[4:7], v[28:31]\n"
"buffer_store_dwordx4 v[112:115], v13, s[36:39], s24 offen sc1\n"
"v_mfma_f32_16x16x32_bf16 v[28:31], a[120:123], a[8:11], v[28:31]\n"
"v_exp_f32 v45, v45\n"
"v_mfma_f32_16x16x32_bf16 v[28:31], a[124:127], a[12:15], v[28:31]\n"
"ds_read_b64_tr_b16 a[232:233], v12 offset:16896\n"
"s_add_u32 s24, s24, 1024\n"
"v_mfma_f32_16x16x32_bf16 v[216:219], a[176:179], v[48:51], v[216:219]\n"
"v_exp_f32 v46, v46\n"
"v_mfma_f32_16x16x32_bf16 v[216:219], a[180:183], v[52:55], v[216:219]\n"
"ds_read_b64_tr_b16 a[234:235], v12 offset:20992\n"
"v_perm_b32 v62, v45, v44, s27\n"
"v_mfma_f32_16x16x32_bf16 v[220:223], a[184:187], v[48:51], v[220:223]\n"
"v_exp_f32 v47, v47\n"
"v_mfma_f32_16x16x32_bf16 v[220:223], a[188:191], v[52:55], v[220:223]\n"
"v_perm_b32 v63, v47, v46, s27\n"
"v_mfma_f32_16x16x32_bf16 v[88:91], v[96:99], v[48:51], v[88:91]\n"
"v_mfma_f32_16x16x32_bf16 v[88:91], v[96:99], v[52:55], v[88:91]\n"
"v_mfma_f32_16x16x32_bf16 v[32:35], a[64:67], a[16:19], v[68:71]\n"
"v_exp_f32 v16, v16\n"
"v_mfma_f32_16x16x32_bf16 v[32:35], a[68:71], a[20:23], v[32:35]\n"
"ds_read_b64_tr_b16 a[236:237], v12 offset:25088\n"
"v_mfma_f32_16x16x32_bf16 v[32:35], a[72:75], a[24:27], v[32:35]\n"
"v_exp_f32 v17, v17\n"
"v_mfma_f32_16x16x32_bf16 v[32:35], a[76:79], a[28:31], v[32:35]\n"
"buffer_load_dwordx4 v8, s[52:55], s64 offen offset:2048 lds\n"
"v_mfma_f32_16x16x32_bf16 v[224:227], a[128:131], v[56:59], v[224:227]\n"
"v_exp_f32 v18, v18\n"
"v_mfma_f32_16x16x32_bf16 v[224:227], a[132:135], v[60:63], v[224:227]\n"
"ds_read_b64_tr_b16 a[238:239], v12 offset:29184\n"
"v_perm_b32 v48, v17, v16, s27\n"
"v_mfma_f32_16x16x32_bf16 v[228:231], a[136:139], v[56:59], v[228:231]\n"
"v_exp_f32 v19, v19\n"
"v_mfma_f32_16x16x32_bf16 v[228:231], a[140:143], v[60:63], v[228:231]\n"
"ds_read_b64_tr_b16 a[240:241], v11 offset:17152\n"
"v_perm_b32 v49, v19, v18, s27\n"
"v_mfma_f32_16x16x32_bf16 v[36:39], a[80:83], a[16:19], v[68:71]\n"
"v_exp_f32 v20, v20\n"
"v_mfma_f32_16x16x32_bf16 v[36:39], a[84:87], a[20:23], v[36:39]\n"
"ds_read_b64_tr_b16 a[242:243], v11 offset:21248\n"
"v_mfma_f32_16x16x32_bf16 v[36:39], a[88:91], a[24:27], v[36:39]\n"
"v_exp_f32 v21, v21\n"
"v_mfma_f32_16x16x32_bf16 v[36:39], a[92:95], a[28:31], v[36:39]\n"
"ds_read_b64_tr_b16 a[244:245], v11 offset:25344\n"
"v_perm_b32 v50, v21, v20, s27\n"
"v_mfma_f32_16x16x32_bf16 v[232:235], a[144:147], v[56:59], v[232:235]\n"
"v_exp_f32 v22, v22\n"
"v_mfma_f32_16x16x32_bf16 v[232:235], a[148:151], v[60:63], v[232:235]\n"
"buffer_load_dwordx4 v9, s[52:55], s64 offen offset:3072 lds\n"
"v_mfma_f32_16x16x32_bf16 v[236:239], a[152:155], v[56:59], v[236:239]\n"
"v_exp_f32 v23, v23\n"
"v_mfma_f32_16x16x32_bf16 v[236:239], a[156:159], v[60:63], v[236:239]\n"
"ds_read_b64_tr_b16 a[246:247], v11 offset:29440\n"
"s_add_u32 s64, s64, 16384\n"
"v_mfma_f32_16x16x32_bf16 v[40:43], a[96:99], a[16:19], v[68:71]\n"
"v_exp_f32 v24, v24\n"
"v_mfma_f32_16x16x32_bf16 v[40:43], a[100:103], a[20:23], v[40:43]\n"
"ds_read_b64_tr_b16 a[248:249], v12 offset:17152\n"
"v_perm_b32 v51, v23, v22, s27\n"
"v_mfma_f32_16x16x32_bf16 v[40:43], a[104:107], a[24:27], v[40:43]\n"
"v_exp_f32 v25, v25\n"
"v_mfma_f32_16x16x32_bf16 v[40:43], a[108:111], a[28:31], v[40:43]\n"
"ds_read_b64_tr_b16 a[250:251], v12 offset:21248\n"
"v_perm_b32 v52, v25, v24, s27\n"
"v_mfma_f32_16x16x32_bf16 v[240:243], a[160:163], v[56:59], v[240:243]\n"
"v_exp_f32 v26, v26\n"
"v_mfma_f32_16x16x32_bf16 v[240:243], a[164:167], v[60:63], v[240:243]\n"
"buffer_load_dwordx4 v[100:103], v13, s[76:79], s71 offen\n"
"v_mfma_f32_16x16x32_bf16 v[244:247], a[168:171], v[56:59], v[244:247]\n"
"v_exp_f32 v27, v27\n"
"v_mfma_f32_16x16x32_bf16 v[244:247], a[172:175], v[60:63], v[244:247]\n"
"ds_read_b64_tr_b16 a[252:253], v12 offset:25344\n"
"s_add_u32 s71, s71, 1024\n"
"v_mfma_f32_16x16x32_bf16 v[44:47], a[112:115], a[16:19], v[68:71]\n"
"v_exp_f32 v28, v28\n"
"v_mfma_f32_16x16x32_bf16 v[44:47], a[116:119], a[20:23], v[44:47]\n"
"ds_read_b64_tr_b16 a[254:255], v12 offset:29440\n"
"v_perm_b32 v53, v27, v26, s27\n"
"v_mfma_f32_16x16x32_bf16 v[44:47], a[120:123], a[24:27], v[44:47]\n"
"v_exp_f32 v29, v29\n"
"v_mfma_f32_16x16x32_bf16 v[44:47], a[124:127], a[28:31], v[44:47]\n"
"v_perm_b32 v54, v29, v28, s27\n"
"v_mfma_f32_16x16x32_bf16 v[248:251], a[176:179], v[56:59], v[248:251]\n"
"v_exp_f32 v30, v30\n"
"v_mfma_f32_16x16x32_bf16 v[248:251], a[180:183], v[60:63], v[248:251]\n"
"v_mfma_f32_16x16x32_bf16 v[252:255], a[184:187], v[56:59], v[252:255]\n"
"v_exp_f32 v31, v31\n"
"v_mfma_f32_16x16x32_bf16 v[252:255], a[188:191], v[60:63], v[252:255]\n"
"v_perm_b32 v55, v31, v30, s27\n"
"v_mfma_f32_16x16x32_bf16 v[92:95], v[96:99], v[56:59], v[92:95]\n"
"v_mfma_f32_16x16x32_bf16 v[92:95], v[96:99], v[60:63], v[92:95]\n"
"s_waitcnt vmcnt(11)\n"
"s_barrier\n"
"v_mfma_f32_16x16x32_bf16 v[16:19], a[64:67], a[32:35], v[72:75]\n"
"v_exp_f32 v32, v32\n"
"v_mfma_f32_16x16x32_bf16 v[16:19], a[68:71], a[36:39], v[16:19]\n"
"ds_read_b64_tr_b16 a[128:129], v11 offset:32768\n"
"s_add_u32 m0, s65, 32768\n"
"v_mfma_f32_16x16x32_bf16 v[16:19], a[72:75], a[40:43], v[16:19]\n"
"v_exp_f32 v33, v33\n"
"v_mfma_f32_16x16x32_bf16 v[16:19], a[76:79], a[44:47], v[16:19]\n"
"buffer_load_dwordx4 v8, s[48:51], s64 offen lds\n"
"v_mfma_f32_16x16x32_bf16 v[128:131], a[192:195], v[48:51], v[128:131]\n"
"v_exp_f32 v34, v34\n"
"v_mfma_f32_16x16x32_bf16 v[128:131], a[196:199], v[52:55], v[128:131]\n"
"ds_read_b64_tr_b16 a[130:131], v11 offset:36864\n"
"v_perm_b32 v56, v33, v32, s27\n"
"v_mfma_f32_16x16x32_bf16 v[132:135], a[200:203], v[48:51], v[132:135]\n"
"v_exp_f32 v35, v35\n"
"v_mfma_f32_16x16x32_bf16 v[132:135], a[204:207], v[52:55], v[132:135]\n"
"ds_read_b64_tr_b16 a[132:133], v11 offset:40960\n"
"v_perm_b32 v57, v35, v34, s27\n"
"v_mfma_f32_16x16x32_bf16 v[20:23], a[80:83], a[32:35], v[72:75]\n"
"v_exp_f32 v36, v36\n"
"v_mfma_f32_16x16x32_bf16 v[20:23], a[84:87], a[36:39], v[20:23]\n"
"ds_read_b64_tr_b16 a[134:135], v11 offset:45056\n"
"v_mfma_f32_16x16x32_bf16 v[20:23], a[88:91], a[40:43], v[20:23]\n"
"v_exp_f32 v37, v37\n"
"v_mfma_f32_16x16x32_bf16 v[20:23], a[92:95], a[44:47], v[20:23]\n"
"ds_read_b64_tr_b16 a[136:137], v12 offset:32768\n"
"v_perm_b32 v58, v37, v36, s27\n"
"v_mfma_f32_16x16x32_bf16 v[136:139], a[208:211], v[48:51], v[136:139]\n"
"v_exp_f32 v38, v38\n"
"v_mfma_f32_16x16x32_bf16 v[136:139], a[212:215], v[52:55], v[136:139]\n"
"buffer_load_dwordx4 v9, s[48:51], s64 offen offset:1024 lds\n"
"v_mfma_f32_16x16x32_bf16 v[140:143], a[216:219], v[48:51], v[140:143]\n"
"v_exp_f32 v39, v39\n"
"v_mfma_f32_16x16x32_bf16 v[140:143], a[220:223], v[52:55], v[140:143]\n"
"ds_read_b64_tr_b16 a[138:139], v12 offset:36864\n"
"v_perm_b32 v59, v39, v38, s27\n"
"v_mfma_f32_16x16x32_bf16 v[24:27], a[96:99], a[32:35], v[72:75]\n"
"v_exp_f32 v40, v40\n"
"v_mfma_f32_16x16x32_bf16 v[24:27], a[100:103], a[36:39], v[24:27]\n"
"ds_read_b64_tr_b16 a[140:141], v12 offset:40960\n"
"v_mfma_f32_16x16x32_bf16 v[24:27], a[104:107], a[40:43], v[24:27]\n"
"v_exp_f32 v41, v41\n"
"v_mfma_f32_16x16x32_bf16 v[24:27], a[108:111], a[44:47], v[24:27]\n"
"ds_read_b64_tr_b16 a[142:143], v12 offset:45056\n"
"v_perm_b32 v60, v41, v40, s27\n"
"s_waitcnt lgkmcnt(3)\n"
"v_mfma_f32_16x16x32_bf16 v[144:147], a[224:227], v[48:51], v[144:147]\n"
"v_exp_f32 v42, v42\n"
"v_mfma_f32_16x16x32_bf16 v[144:147], a[228:231], v[52:55], v[144:147]\n"
"buffer_load_dwordx4 v8, s[48:51], s64 offen offset:2048 lds\n"
"v_mfma_f32_16x16x32_bf16 v[148:151], a[232:235], v[48:51], v[148:151]\n"
"v_exp_f32 v43, v43\n"
"v_mfma_f32_16x16x32_bf16 v[148:151], a[236:239], v[52:55], v[148:151]\n"
"ds_read_b64_tr_b16 a[144:145], v11 offset:33024\n"
"v_perm_b32 v61, v43, v42, s27\n"
"v_mfma_f32_16x16x32_bf16 v[28:31], a[112:115], a[32:35], v[72:75]\n"
"v_exp_f32 v44, v44\n"
"v_mfma_f32_16x16x32_bf16 v[28:31], a[116:119], a[36:39], v[28:31]\n"
"ds_read_b64_tr_b16 a[146:147], v11 offset:37120\n"
"v_mfma_f32_16x16x32_bf16 v[28:31], a[120:123], a[40:43], v[28:31]\n"
"v_exp_f32 v45, v45\n"
"v_mfma_f32_16x16x32_bf16 v[28:31], a[124:127], a[44:47], v[28:31]\n"
"ds_read_b64_tr_b16 a[148:149], v11 offset:41216\n"
"v_perm_b32 v62, v45, v44, s27\n"
"v_mfma_f32_16x16x32_bf16 v[152:155], a[240:243], v[48:51], v[152:155]\n"
"v_exp_f32 v46, v46\n"
"v_mfma_f32_16x16x32_bf16 v[152:155], a[244:247], v[52:55], v[152:155]\n"
"v_mfma_f32_16x16x32_bf16 v[156:159], a[248:251], v[48:51], v[156:159]\n"
"v_exp_f32 v47, v47\n"
"v_mfma_f32_16x16x32_bf16 v[156:159], a[252:255], v[52:55], v[156:159]\n"
"v_perm_b32 v63, v47, v46, s27\n"
"v_mfma_f32_16x16x32_bf16 v[80:83], v[96:99], v[48:51], v[80:83]\n"
"v_mfma_f32_16x16x32_bf16 v[80:83], v[96:99], v[52:55], v[80:83]\n"
"v_mfma_f32_16x16x32_bf16 v[32:35], a[64:67], a[48:51], v[76:79]\n"
"v_exp_f32 v16, v16\n"
"v_mfma_f32_16x16x32_bf16 v[32:35], a[68:71], a[52:55], v[32:35]\n"
"ds_read_b128 a[64:67], v10 offset:0\n"
"v_mfma_f32_16x16x32_bf16 v[32:35], a[72:75], a[56:59], v[32:35]\n"
"v_exp_f32 v17, v17\n"
"v_mfma_f32_16x16x32_bf16 v[32:35], a[76:79], a[60:63], v[32:35]\n"
"ds_read_b128 a[68:71], v10 offset:256\n"
"v_perm_b32 v48, v17, v16, s27\n"
"v_mfma_f32_16x16x32_bf16 v[160:163], a[192:195], v[56:59], v[160:163]\n"
"v_exp_f32 v18, v18\n"
"v_mfma_f32_16x16x32_bf16 v[160:163], a[196:199], v[60:63], v[160:163]\n"
"ds_read_b128 a[72:75], v10 offset:512\n"
"v_mfma_f32_16x16x32_bf16 v[164:167], a[200:203], v[56:59], v[164:167]\n"
"v_exp_f32 v19, v19\n"
"v_mfma_f32_16x16x32_bf16 v[164:167], a[204:207], v[60:63], v[164:167]\n"
"ds_read_b128 a[76:79], v10 offset:768\n"
"v_perm_b32 v49, v19, v18, s27\n"
"v_mfma_f32_16x16x32_bf16 v[36:39], a[80:83], a[48:51], v[76:79]\n"
"v_exp_f32 v20, v20\n"
"v_mfma_f32_16x16x32_bf16 v[36:39], a[84:87], a[52:55], v[36:39]\n"
"ds_read_b128 a[80:83], v10 offset:4096\n"
"v_mfma_f32_16x16x32_bf16 v[36:39], a[88:91], a[56:59], v[36:39]\n"
"v_exp_f32 v21, v21\n"
"v_mfma_f32_16x16x32_bf16 v[36:39], a[92:95], a[60:63], v[36:39]\n"
"ds_read_b128 a[84:87], v10 offset:4352\n"
"v_perm_b32 v50, v21, v20, s27\n"
"v_mfma_f32_16x16x32_bf16 v[168:171], a[208:211], v[56:59], v[168:171]\n"
"v_exp_f32 v22, v22\n"
"v_mfma_f32_16x16x32_bf16 v[168:171], a[212:215], v[60:63], v[168:171]\n"
"ds_read_b128 a[88:91], v10 offset:4608\n"
"v_mfma_f32_16x16x32_bf16 v[172:175], a[216:219], v[56:59], v[172:175]\n"
"v_exp_f32 v23, v23\n"
"v_mfma_f32_16x16x32_bf16 v[172:175], a[220:223], v[60:63], v[172:175]\n"
"ds_read_b128 a[92:95], v10 offset:4864\n"
"v_perm_b32 v51, v23, v22, s27\n"
"v_mfma_f32_16x16x32_bf16 v[40:43], a[96:99], a[48:51], v[76:79]\n"
"v_exp_f32 v24, v24\n"
"v_mfma_f32_16x16x32_bf16 v[40:43], a[100:103], a[52:55], v[40:43]\n"
"ds_read_b128 a[96:99], v10 offset:8192\n"
"v_mfma_f32_16x16x32_bf16 v[40:43], a[104:107], a[56:59], v[40:43]\n"
"v_exp_f32 v25, v25\n"
"v_mfma_f32_16x16x32_bf16 v[40:43], a[108:111], a[60:63], v[40:43]\n"
"ds_read_b128 a[100:103], v10 offset:8448\n"
"v_perm_b32 v52, v25, v24, s27\n"
"v_mfma_f32_16x16x32_bf16 v[176:179], a[224:227], v[56:59], v[176:179]\n"
"v_exp_f32 v26, v26\n"
"v_mfma_f32_16x16x32_bf16 v[176:179], a[228:231], v[60:63], v[176:179]\n"
"ds_read_b128 a[104:107], v10 offset:8704\n"
"v_mfma_f32_16x16x32_bf16 v[180:183], a[232:235], v[56:59], v[180:183]\n"
"v_exp_f32 v27, v27\n"
"v_mfma_f32_16x16x32_bf16 v[180:183], a[236:239], v[60:63], v[180:183]\n"
"ds_read_b128 a[108:111], v10 offset:8960\n"
"v_perm_b32 v53, v27, v26, s27\n"
"v_mfma_f32_16x16x32_bf16 v[44:47], a[112:115], a[48:51], v[76:79]\n"
"v_exp_f32 v28, v28\n"
"v_mfma_f32_16x16x32_bf16 v[44:47], a[116:119], a[52:55], v[44:47]\n"
"ds_read_b128 a[112:115], v10 offset:12288\n"
"v_mfma_f32_16x16x32_bf16 v[44:47], a[120:123], a[56:59], v[44:47]\n"
"v_exp_f32 v29, v29\n"
"v_mfma_f32_16x16x32_bf16 v[44:47], a[124:127], a[60:63], v[44:47]\n"
"ds_read_b128 a[116:119], v10 offset:12544\n"
"v_perm_b32 v54, v29, v28, s27\n"
"v_mfma_f32_16x16x32_bf16 v[184:187], a[240:243], v[56:59], v[184:187]\n"
"v_exp_f32 v30, v30\n"
"v_mfma_f32_16x16x32_bf16 v[184:187], a[244:247], v[60:63], v[184:187]\n"
"ds_read_b128 a[120:123], v10 offset:12800\n"
"v_mfma_f32_16x16x32_bf16 v[188:191], a[248:251], v[56:59], v[188:191]\n"
"v_exp_f32 v31, v31\n"
"v_mfma_f32_16x16x32_bf16 v[188:191], a[252:255], v[60:63], v[188:191]\n"
"ds_read_b128 a[124:127], v10 offset:13056\n"
"v_perm_b32 v55, v31, v30, s27\n"
"v_mfma_f32_16x16x32_bf16 v[84:87], v[96:99], v[56:59], v[84:87]\n"
"v_mfma_f32_16x16x32_bf16 v[84:87], v[96:99], v[60:63], v[84:87]\n"
"s_waitcnt lgkmcnt(3)\n"
"v_mfma_f32_16x16x32_bf16 v[16:19], a[64:67], a[0:3], v[64:67]\n"
"v_exp_f32 v32, v32\n"
"v_mfma_f32_16x16x32_bf16 v[16:19], a[68:71], a[4:7], v[16:19]\n"
"ds_read_b64_tr_b16 a[150:151], v11 offset:45312\n"
"v_mfma_f32_16x16x32_bf16 v[16:19], a[72:75], a[8:11], v[16:19]\n"
"v_exp_f32 v33, v33\n"
"v_mfma_f32_16x16x32_bf16 v[16:19], a[76:79], a[12:15], v[16:19]\n"
"buffer_load_dwordx4 v9, s[48:51], s64 offen offset:3072 lds\n"
"v_mfma_f32_16x16x32_bf16 v[192:195], a[192:195], v[48:51], v[192:195]\n"
"v_exp_f32 v34, v34\n"
"v_mfma_f32_16x16x32_bf16 v[192:195], a[196:199], v[52:55], v[192:195]\n"
"ds_read_b64_tr_b16 a[152:153], v12 offset:33024\n"
"v_perm_b32 v56, v33, v32, s27\n"
"v_mfma_f32_16x16x32_bf16 v[196:199], a[200:203], v[48:51], v[196:199]\n"
"v_exp_f32 v35, v35\n"
"v_mfma_f32_16x16x32_bf16 v[196:199], a[204:207], v[52:55], v[196:199]\n"
"ds_read_b64_tr_b16 a[154:155], v12 offset:37120\n"
"v_perm_b32 v57, v35, v34, s27\n"
"v_mfma_f32_16x16x32_bf16 v[20:23], a[80:83], a[0:3], v[64:67]\n"
"v_exp_f32 v36, v36\n"
"v_mfma_f32_16x16x32_bf16 v[20:23], a[84:87], a[4:7], v[20:23]\n"
"ds_read_b64_tr_b16 a[156:157], v12 offset:41216\n"
"s_add_u32 m0, s66, 0\n"
"v_mfma_f32_16x16x32_bf16 v[20:23], a[88:91], a[8:11], v[20:23]\n"
"v_exp_f32 v37, v37\n"
"v_mfma_f32_16x16x32_bf16 v[20:23], a[92:95], a[12:15], v[20:23]\n"
"buffer_load_dwordx4 v8, s[52:55], s64 offen lds\n"
"v_mfma_f32_16x16x32_bf16 v[200:203], a[208:211], v[48:51], v[200:203]\n"
"v_exp_f32 v38, v38\n"
"v_mfma_f32_16x16x32_bf16 v[200:203], a[212:215], v[52:55], v[200:203]\n"
"ds_read_b64_tr_b16 a[158:159], v12 offset:45312\n"
"v_perm_b32 v58, v37, v36, s27\n"
"v_mfma_f32_16x16x32_bf16 v[204:207], a[216:219], v[48:51], v[204:207]\n"
"v_exp_f32 v39, v39\n"
"v_mfma_f32_16x16x32_bf16 v[204:207], a[220:223], v[52:55], v[204:207]\n"
"ds_read_b64_tr_b16 a[160:161], v11 offset:33280\n"
"v_perm_b32 v59, v39, v38, s27\n"
"v_mfma_f32_16x16x32_bf16 v[24:27], a[96:99], a[0:3], v[64:67]\n"
"v_exp_f32 v40, v40\n"
"v_mfma_f32_16x16x32_bf16 v[24:27], a[100:103], a[4:7], v[24:27]\n"
"buffer_load_dwordx4 v9, s[52:55], s64 offen offset:1024 lds\n"
"v_mfma_f32_16x16x32_bf16 v[24:27], a[104:107], a[8:11], v[24:27]\n"
"v_exp_f32 v41, v41\n"
"v_mfma_f32_16x16x32_bf16 v[24:27], a[108:111], a[12:15], v[24:27]\n"
"ds_read_b64_tr_b16 a[162:163], v11 offset:37376\n"
"v_perm_b32 v60, v41, v40, s27\n"
"v_mfma_f32_16x16x32_bf16 v[208:211], a[224:227], v[48:51], v[208:211]\n"
"v_exp_f32 v42, v42\n"
"v_mfma_f32_16x16x32_bf16 v[208:211], a[228:231], v[52:55], v[208:211]\n"
"ds_read_b64_tr_b16 a[164:165], v11 offset:41472\n"
"v_mfma_f32_16x16x32_bf16 v[212:215], a[232:235], v[48:51], v[212:215]\n"
"v_exp_f32 v43, v43\n"
"v_mfma_f32_16x16x32_bf16 v[212:215], a[236:239], v[52:55], v[212:215]\n"
"ds_read_b64_tr_b16 a[166:167], v11 offset:45568\n"
"v_perm_b32 v61, v43, v42, s27\n"
"v_mfma_f32_16x16x32_bf16 v[28:31], a[112:115], a[0:3], v[64:67]\n"
"v_exp_f32 v44, v44\n"
"s_waitcnt lgkmcnt(3)\n"
"v_mfma_f32_16x16x32_bf16 v[28:31], a[116:119], a[4:7], v[28:31]\n"
"buffer_load_dwordx4 v[100:103], v13, %[code], 0 offen\n"
"v_mfma_f32_16x16x32_bf16 v[28:31], a[120:123], a[8:11], v[28:31]\n"
"v_exp_f32 v45, v45\n"
"v_mfma_f32_16x16x32_bf16 v[28:31], a[124:127], a[12:15], v[28:31]\n"
"ds_read_b64_tr_b16 a[168:169], v12 offset:33280\n"
"v_perm_b32 v62, v45, v44, s27\n"
"v_mfma_f32_16x16x32_bf16 v[216:219], a[240:243], v[48:51], v[216:219]\n"
"v_exp_f32 v46, v46\n"
"v_mfma_f32_16x16x32_bf16 v[216:219], a[244:247], v[52:55], v[216:219]\n"
"ds_read_b64_tr_b16 a[170:171], v12 offset:37376\n"
"v_mfma_f32_16x16x32_bf16 v[220:223], a[248:251], v[48:51], v[220:223]\n"
"v_exp_f32 v47, v47\n"
"v_mfma_f32_16x16x32_bf16 v[220:223], a[252:255], v[52:55], v[220:223]\n"
"v_perm_b32 v63, v47, v46, s27\n"
"v_mfma_f32_16x16x32_bf16 v[88:91], v[96:99], v[48:51], v[88:91]\n"
"v_mfma_f32_16x16x32_bf16 v[88:91], v[96:99], v[52:55], v[88:91]\n"
"v_mfma_f32_16x16x32_bf16 v[32:35], a[64:67], a[16:19], v[68:71]\n"
"v_exp_f32 v16, v16\n"
"v_mfma_f32_16x16x32_bf16 v[32:35], a[68:71], a[20:23], v[32:35]\n"
"ds_read_b64_tr_b16 a[172:173], v12 offset:41472\n"
"v_mfma_f32_16x16x32_bf16 v[32:35], a[72:75], a[24:27], v[32:35]\n"
"v_exp_f32 v17, v17\n"
"v_mfma_f32_16x16x32_bf16 v[32:35], a[76:79], a[28:31], v[32:35]\n"
"buffer_load_dwordx4 v8, s[52:55], s64 offen offset:2048 lds\n"
"v_mfma_f32_16x16x32_bf16 v[224:227], a[192:195], v[56:59], v[224:227]\n"
"v_exp_f32 v18, v18\n"
"v_mfma_f32_16x16x32_bf16 v[224:227], a[196:199], v[60:63], v[224:227]\n"
"ds_read_b64_tr_b16 a[174:175], v12 offset:45568\n"
"v_perm_b32 v48, v17, v16, s27\n"
"v_mfma_f32_16x16x32_bf16 v[228:231], a[200:203], v[56:59], v[228:231]\n"
"v_exp_f32 v19, v19\n"
"v_mfma_f32_16x16x32_bf16 v[228:231], a[204:207], v[60:63], v[228:231]\n"
"ds_read_b64_tr_b16 a[176:177], v11 offset:33536\n"
"v_perm_b32 v49, v19, v18, s27\n"
"v_mfma_f32_16x16x32_bf16 v[36:39], a[80:83], a[16:19], v[68:71]\n"
"v_exp_f32 v20, v20\n"
"v_mfma_f32_16x16x32_bf16 v[36:39], a[84:87], a[20:23], v[36:39]\n"
"ds_read_b64_tr_b16 a[178:179], v11 offset:37632\n"
"v_mfma_f32_16x16x32_bf16 v[36:39], a[88:91], a[24:27], v[36:39]\n"
"v_exp_f32 v21, v21\n"
"v_mfma_f32_16x16x32_bf16 v[36:39], a[92:95], a[28:31], v[36:39]\n"
"ds_read_b64_tr_b16 a[180:181], v11 offset:41728\n"
"v_perm_b32 v50, v21, v20, s27\n"
"v_mfma_f32_16x16x32_bf16 v[232:235], a[208:211], v[56:59], v[232:235]\n"
"v_exp_f32 v22, v22\n"
"v_mfma_f32_16x16x32_bf16 v[232:235], a[212:215], v[60:63], v[232:235]\n"
"buffer_load_dwordx4 v9, s[52:55], s64 offen offset:3072 lds\n"
"v_mfma_f32_16x16x32_bf16 v[236:239], a[216:219], v[56:59], v[236:239]\n"
"v_exp_f32 v23, v23\n"
"v_mfma_f32_16x16x32_bf16 v[236:239], a[220:223], v[60:63], v[236:239]\n"
"ds_read_b64_tr_b16 a[182:183], v11 offset:45824\n"
"s_add_u32 s64, s64, 16384\n"
"v_mfma_f32_16x16x32_bf16 v[40:43], a[96:99], a[16:19], v[68:71]\n"
"v_exp_f32 v24, v24\n"
"v_mfma_f32_16x16x32_bf16 v[40:43], a[100:103], a[20:23], v[40:43]\n"
"ds_read_b64_tr_b16 a[184:185], v12 offset:33536\n"
"v_perm_b32 v51, v23, v22, s27\n"
"v_mfma_f32_16x16x32_bf16 v[40:43], a[104:107], a[24:27], v[40:43]\n"
"v_exp_f32 v25, v25\n"
"v_mfma_f32_16x16x32_bf16 v[40:43], a[108:111], a[28:31], v[40:43]\n"
"ds_read_b64_tr_b16 a[186:187], v12 offset:37632\n"
"v_perm_b32 v52, v25, v24, s27\n"
"v_mfma_f32_16x16x32_bf16 v[240:243], a[224:227], v[56:59], v[240:243]\n"
"v_exp_f32 v26, v26\n"
"v_mfma_f32_16x16x32_bf16 v[240:243], a[228:231], v[60:63], v[240:243]\n"
"buffer_load_dwordx4 v[100:103], v13, s[76:79], s71 offen\n"
"v_mfma_f32_16x16x32_bf16 v[244:247], a[232:235], v[56:59], v[244:247]\n"
"v_exp_f32 v27, v27\n"
"v_mfma_f32_16x16x32_bf16 v[244:247], a[236:239], v[60:63], v[244:247]\n"
"ds_read_b64_tr_b16 a[188:189], v12 offset:41728\n"
"s_add_u32 s71, s71, 1024\n"
"v_mfma_f32_16x16x32_bf16 v[44:47], a[112:115], a[16:19], v[68:71]\n"
"v_exp_f32 v28, v28\n"
"v_mfma_f32_16x16x32_bf16 v[44:47], a[116:119], a[20:23], v[44:47]\n"
"ds_read_b64_tr_b16 a[190:191], v12 offset:45824\n"
"v_perm_b32 v53, v27, v26, s27\n"
"v_mfma_f32_16x16x32_bf16 v[44:47], a[120:123], a[24:27], v[44:47]\n"
"v_exp_f32 v29, v29\n"
"v_mfma_f32_16x16x32_bf16 v[44:47], a[124:127], a[28:31], v[44:47]\n"
"v_perm_b32 v54, v29, v28, s27\n"
"v_mfma_f32_16x16x32_bf16 v[248:251], a[240:243], v[56:59], v[248:251]\n"
"v_exp_f32 v30, v30\n"
"v_mfma_f32_16x16x32_bf16 v[248:251], a[244:247], v[60:63], v[248:251]\n"
"v_mfma_f32_16x16x32_bf16 v[252:255], a[248:251], v[56:59], v[252:255]\n"
"v_exp_f32 v31, v31\n"
"v_mfma_f32_16x16x32_bf16 v[252:255], a[252:255], v[60:63], v[252:255]\n"
"v_perm_b32 v55, v31, v30, s27\n"
"v_mfma_f32_16x16x32_bf16 v[92:95], v[96:99], v[56:59], v[92:95]\n"
"v_mfma_f32_16x16x32_bf16 v[92:95], v[96:99], v[60:63], v[92:95]\n"
"s_waitcnt vmcnt(11)\n"
"s_barrier\n"
"v_mfma_f32_16x16x32_bf16 v[16:19], a[64:67], a[32:35], v[72:75]\n"
"v_exp_f32 v32, v32\n"
"v_mfma_f32_16x16x32_bf16 v[16:19], a[68:71], a[36:39], v[16:19]\n"
"ds_read_b64_tr_b16 a[192:193], v11 offset:49152\n"
"s_add_u32 m0, s65, 49152\n"
"v_mfma_f32_16x16x32_bf16 v[16:19], a[72:75], a[40:43], v[16:19]\n"
"v_exp_f32 v33, v33\n"
"v_mfma_f32_16x16x32_bf16 v[16:19], a[76:79], a[44:47], v[16:19]\n"
"buffer_load_dwordx4 v8, s[48:51], s64 offen lds\n"
"v_mfma_f32_16x16x32_bf16 v[128:131], a[128:131], v[48:51], v[128:131]\n"
"v_exp_f32 v34, v34\n"
"v_mfma_f32_16x16x32_bf16 v[128:131], a[132:135], v[52:55], v[128:131]\n"
"ds_read_b64_tr_b16 a[194:195], v11 offset:53248\n"
"v_perm_b32 v56, v33, v32, s27\n"
"v_mfma_f32_16x16x32_bf16 v[132:135], a[136:139], v[48:51], v[132:135]\n"
"v_exp_f32 v35, v35\n"
"v_mfma_f32_16x16x32_bf16 v[132:135], a[140:143], v[52:55], v[132:135]\n"
"ds_read_b64_tr_b16 a[196:197], v11 offset:57344\n"
"v_perm_b32 v57, v35, v34, s27\n"
"v_mfma_f32_16x16x32_bf16 v[20:23], a[80:83], a[32:35], v[72:75]\n"
"v_exp_f32 v36, v36\n"
"v_mfma_f32_16x16x32_bf16 v[20:23], a[84:87], a[36:39], v[20:23]\n"
"ds_read_b64_tr_b16 a[198:199], v11 offset:61440\n"
"v_lshlrev_b32 v109, 16, v104\n"
"v_mfma_f32_16x16x32_bf16 v[20:23], a[88:91], a[40:43], v[20:23]\n"
"v_exp_f32 v37, v37\n"
"v_mfma_f32_16x16x32_bf16 v[20:23], a[92:95], a[44:47], v[20:23]\n"
"ds_read_b64_tr_b16 a[200:201], v12 offset:49152\n"
"v_perm_b32 v58, v37, v36, s27\n"
"v_mfma_f32_16x16x32_bf16 v[136:139], a[144:147], v[48:51], v[136:139]\n"
"v_exp_f32 v38, v38\n"
"v_mfma_f32_16x16x32_bf16 v[136:139], a[148:151], v[52:55], v[136:139]\n"
"buffer_load_dwordx4 v9, s[48:51], s64 offen offset:1024 lds\n"
"v_mfma_f32_16x16x32_bf16 v[140:143], a[152:155], v[48:51], v[140:143]\n"
"v_exp_f32 v39, v39\n"
"v_mfma_f32_16x16x32_bf16 v[140:143], a[156:159], v[52:55], v[140:143]\n"
"ds_read_b64_tr_b16 a[202:203], v12 offset:53248\n"
"v_perm_b32 v59, v39, v38, s27\n"
"v_mfma_f32_16x16x32_bf16 v[24:27], a[96:99], a[32:35], v[72:75]\n"
"v_exp_f32 v40, v40\n"
"v_mfma_f32_16x16x32_bf16 v[24:27], a[100:103], a[36:39], v[24:27]\n"
"ds_read_b64_tr_b16 a[204:205], v12 offset:57344\n"
"v_fmac_f32 v120, v108, v109\n"
"v_mfma_f32_16x16x32_bf16 v[24:27], a[104:107], a[40:43], v[24:27]\n"
"v_exp_f32 v41, v41\n"
"v_mfma_f32_16x16x32_bf16 v[24:27], a[108:111], a[44:47], v[24:27]\n"
"ds_read_b64_tr_b16 a[206:207], v12 offset:61440\n"
"v_perm_b32 v60, v41, v40, s27\n"
"s_waitcnt lgkmcnt(3)\n"
"v_mfma_f32_16x16x32_bf16 v[144:147], a[160:163], v[48:51], v[144:147]\n"
"v_exp_f32 v42, v42\n"
"v_mfma_f32_16x16x32_bf16 v[144:147], a[164:167], v[52:55], v[144:147]\n"
"buffer_load_dwordx4 v8, s[48:51], s64 offen offset:2048 lds\n"
"v_mfma_f32_16x16x32_bf16 v[148:151], a[168:171], v[48:51], v[148:151]\n"
"v_exp_f32 v43, v43\n"
"v_mfma_f32_16x16x32_bf16 v[148:151], a[172:175], v[52:55], v[148:151]\n"
"ds_read_b64_tr_b16 a[208:209], v11 offset:49408\n"
"v_perm_b32 v61, v43, v42, s27\n"
"v_mfma_f32_16x16x32_bf16 v[28:31], a[112:115], a[32:35], v[72:75]\n"
"v_exp_f32 v44, v44\n"
"v_mfma_f32_16x16x32_bf16 v[28:31], a[116:119], a[36:39], v[28:31]\n"
"ds_read_b64_tr_b16 a[210:211], v11 offset:53504\n"
"v_and_b32 v109, 0xffff0000, v104\n"
"v_mfma_f32_16x16x32_bf16 v[28:31], a[120:123], a[40:43], v[28:31]\n"
"v_exp_f32 v45, v45\n"
"v_mfma_f32_16x16x32_bf16 v[28:31], a[124:127], a[44:47], v[28:31]\n"
"ds_read_b64_tr_b16 a[212:213], v11 offset:57600\n"
"v_perm_b32 v62, v45, v44, s27\n"
"v_mfma_f32_16x16x32_bf16 v[152:155], a[176:179], v[48:51], v[152:155]\n"
"v_exp_f32 v46, v46\n"
"v_mfma_f32_16x16x32_bf16 v[152:155], a[180:183], v[52:55], v[152:155]\n"
"v_fmac_f32 v121, v108, v109\n"
"v_lshlrev_b32 v109, 16, v105\n"
"v_mfma_f32_16x16x32_bf16 v[156:159], a[184:187], v[48:51], v[156:159]\n"
"v_exp_f32 v47, v47\n"
"v_mfma_f32_16x16x32_bf16 v[156:159], a[188:191], v[52:55], v[156:159]\n"
"v_perm_b32 v63, v47, v46, s27\n"
"v_fmac_f32 v122, v108, v109\n"
"v_mfma_f32_16x16x32_bf16 v[80:83], v[96:99], v[48:51], v[80:83]\n"
"v_and_b32 v109, 0xffff0000, v105\n"
"v_fmac_f32 v123, v108, v109\n"
"v_mfma_f32_16x16x32_bf16 v[80:83], v[96:99], v[52:55], v[80:83]\n"
"v_mfma_f32_16x16x32_bf16 v[32:35], a[64:67], a[48:51], v[76:79]\n"
"v_exp_f32 v16, v16\n"
"v_mfma_f32_16x16x32_bf16 v[32:35], a[68:71], a[52:55], v[32:35]\n"
"ds_read_b128 a[64:67], v10 offset:16384\n"
"v_lshlrev_b32 v109, 16, v106\n"
"v_mfma_f32_16x16x32_bf16 v[32:35], a[72:75], a[56:59], v[32:35]\n"
"v_exp_f32 v17, v17\n"
"v_mfma_f32_16x16x32_bf16 v[32:35], a[76:79], a[60:63], v[32:35]\n"
"ds_read_b128 a[68:71], v10 offset:16640\n"
"v_perm_b32 v48, v17, v16, s27\n"
"v_mfma_f32_16x16x32_bf16 v[160:163], a[128:131], v[56:59], v[160:163]\n"
"v_exp_f32 v18, v18\n"
"v_mfma_f32_16x16x32_bf16 v[160:163], a[132:135], v[60:63], v[160:163]\n"
"ds_read_b128 a[72:75], v10 offset:16896\n"
"v_fmac_f32 v124, v108, v109\n"
"v_mfma_f32_16x16x32_bf16 v[164:167], a[136:139], v[56:59], v[164:167]\n"
"v_exp_f32 v19, v19\n"
"v_mfma_f32_16x16x32_bf16 v[164:167], a[140:143], v[60:63], v[164:167]\n"
"ds_read_b128 a[76:79], v10 offset:17152\n"
"v_perm_b32 v49, v19, v18, s27\n"
"v_mfma_f32_16x16x32_bf16 v[36:39], a[80:83], a[48:51], v[76:79]\n"
"v_exp_f32 v20, v20\n"
"v_mfma_f32_16x16x32_bf16 v[36:39], a[84:87], a[52:55], v[36:39]\n"
"ds_read_b128 a[80:83], v10 offset:20480\n"
"v_and_b32 v109, 0xffff0000, v106\n"
"v_mfma_f32_16x16x32_bf16 v[36:39], a[88:91], a[56:59], v[36:39]\n"
"v_exp_f32 v21, v21\n"
"v_mfma_f32_16x16x32_bf16 v[36:39], a[92:95], a[60:63], v[36:39]\n"
"ds_read_b128 a[84:87], v10 offset:20736\n"
"v_perm_b32 v50, v21, v20, s27\n"
"v_mfma_f32_16x16x32_bf16 v[168:171], a[144:147], v[56:59], v[168:171]\n"
"v_exp_f32 v22, v22\n"
"v_mfma_f32_16x16x32_bf16 v[168:171], a[148:151], v[60:63], v[168:171]\n"
"ds_read_b128 a[88:91], v10 offset:20992\n"
"v_fmac_f32 v125, v108, v109\n"
"v_mfma_f32_16x16x32_bf16 v[172:175], a[152:155], v[56:59], v[172:175]\n"
"v_exp_f32 v23, v23\n"
"v_mfma_f32_16x16x32_bf16 v[172:175], a[156:159], v[60:63], v[172:175]\n"
"ds_read_b128 a[92:95], v10 offset:21248\n"
"v_perm_b32 v51, v23, v22, s27\n"
"v_mfma_f32_16x16x32_bf16 v[40:43], a[96:99], a[48:51], v[76:79]\n"
"v_exp_f32 v24, v24\n"
"v_mfma_f32_16x16x32_bf16 v[40:43], a[100:103], a[52:55], v[40:43]\n"
"ds_read_b128 a[96:99], v10 offset:24576\n"
"v_lshlrev_b32 v109, 16, v107\n"
"v_mfma_f32_16x16x32_bf16 v[40:43], a[104:107], a[56:59], v[40:43]\n"
"v_exp_f32 v25, v25\n"
"v_mfma_f32_16x16x32_bf16 v[40:43], a[108:111], a[60:63], v[40:43]\n"
"ds_read_b128 a[100:103], v10 offset:24832\n"
"v_perm_b32 v52, v25, v24, s27\n"
"v_mfma_f32_16x16x32_bf16 v[176:179], a[160:163], v[56:59], v[176:179]\n"
"v_exp_f32 v26, v26\n"
"v_mfma_f32_16x16x32_bf16 v[176:179], a[164:167], v[60:63], v[176:179]\n"
"ds_read_b128 a[104:107], v10 offset:25088\n"
"v_fmac_f32 v126, v108, v109\n"
"v_mfma_f32_16x16x32_bf16 v[180:183], a[168:171], v[56:59], v[180:183]\n"
"v_exp_f32 v27, v27\n"
"v_mfma_f32_16x16x32_bf16 v[180:183], a[172:175], v[60:63], v[180:183]\n"
"ds_read_b128 a[108:111], v10 offset:25344\n"
"v_perm_b32 v53, v27, v26, s27\n"
"v_mfma_f32_16x16x32_bf16 v[44:47], a[112:115], a[48:51], v[76:79]\n"
"v_exp_f32 v28, v28\n"
"v_mfma_f32_16x16x32_bf16 v[44:47], a[116:119], a[52:55], v[44:47]\n"
"ds_read_b128 a[112:115], v10 offset:28672\n"
"v_and_b32 v109, 0xffff0000, v107\n"
"v_mfma_f32_16x16x32_bf16 v[44:47], a[120:123], a[56:59], v[44:47]\n"
"v_exp_f32 v29, v29\n"
"v_mfma_f32_16x16x32_bf16 v[44:47], a[124:127], a[60:63], v[44:47]\n"
"ds_read_b128 a[116:119], v10 offset:28928\n"
"v_perm_b32 v54, v29, v28, s27\n"
"v_mfma_f32_16x16x32_bf16 v[184:187], a[176:179], v[56:59], v[184:187]\n"
"v_exp_f32 v30, v30\n"
"v_mfma_f32_16x16x32_bf16 v[184:187], a[180:183], v[60:63], v[184:187]\n"
"ds_read_b128 a[120:123], v10 offset:29184\n"
"v_fmac_f32 v127, v108, v109\n"
"v_mfma_f32_16x16x32_bf16 v[188:191], a[184:187], v[56:59], v[188:191]\n"
"v_exp_f32 v31, v31\n"
"v_mfma_f32_16x16x32_bf16 v[188:191], a[188:191], v[60:63], v[188:191]\n"
"ds_read_b128 a[124:127], v10 offset:29440\n"
"v_perm_b32 v55, v31, v30, s27\n"
"v_mfma_f32_16x16x32_bf16 v[84:87], v[96:99], v[56:59], v[84:87]\n"
"v_mfma_f32_16x16x32_bf16 v[84:87], v[96:99], v[60:63], v[84:87]\n"
"s_waitcnt lgkmcnt(3)\n"
"v_mfma_f32_16x16x32_bf16 v[16:19], a[64:67], a[0:3], v[64:67]\n"
"v_exp_f32 v32, v32\n"
"v_mfma_f32_16x16x32_bf16 v[16:19], a[68:71], a[4:7], v[16:19]\n"
"ds_read_b64_tr_b16 a[214:215], v11 offset:61696\n"
"v_mfma_f32_16x16x32_bf16 v[16:19], a[72:75], a[8:11], v[16:19]\n"
"v_exp_f32 v33, v33\n"
"v_mfma_f32_16x16x32_bf16 v[16:19], a[76:79], a[12:15], v[16:19]\n"
"buffer_load_dwordx4 v9, s[48:51], s64 offen offset:3072 lds\n"
"v_mfma_f32_16x16x32_bf16 v[192:195], a[128:131], v[48:51], v[192:195]\n"
"v_exp_f32 v34, v34\n"
"v_mfma_f32_16x16x32_bf16 v[192:195], a[132:135], v[52:55], v[192:195]\n"
"ds_read_b64_tr_b16 a[216:217], v12 offset:49408\n"
"v_perm_b32 v56, v33, v32, s27\n"
"v_mfma_f32_16x16x32_bf16 v[196:199], a[136:139], v[48:51], v[196:199]\n"
"v_exp_f32 v35, v35\n"
"v_mfma_f32_16x16x32_bf16 v[196:199], a[140:143], v[52:55], v[196:199]\n"
"ds_read_b64_tr_b16 a[218:219], v12 offset:53504\n"
"v_perm_b32 v57, v35, v34, s27\n"
"v_mfma_f32_16x16x32_bf16 v[20:23], a[80:83], a[0:3], v[64:67]\n"
"v_exp_f32 v36, v36\n"
"v_mfma_f32_16x16x32_bf16 v[20:23], a[84:87], a[4:7], v[20:23]\n"
"ds_read_b64_tr_b16 a[220:221], v12 offset:57600\n"
"s_add_u32 m0, s66, 16384\n"
"v_mfma_f32_16x16x32_bf16 v[20:23], a[88:91], a[8:11], v[20:23]\n"
"v_exp_f32 v37, v37\n"
"v_mfma_f32_16x16x32_bf16 v[20:23], a[92:95], a[12:15], v[20:23]\n"
"buffer_load_dwordx4 v8, s[52:55], s64 offen lds\n"
"v_mfma_f32_16x16x32_bf16 v[200:203], a[144:147], v[48:51], v[200:203]\n"
"v_exp_f32 v38, v38\n"
"v_mfma_f32_16x16x32_bf16 v[200:203], a[148:151], v[52:55], v[200:203]\n"
"ds_read_b64_tr_b16 a[222:223], v12 offset:61696\n"
"v_perm_b32 v58, v37, v36, s27\n"
"v_mfma_f32_16x16x32_bf16 v[204:207], a[152:155], v[48:51], v[204:207]\n"
"v_exp_f32 v39, v39\n"
"v_mfma_f32_16x16x32_bf16 v[204:207], a[156:159], v[52:55], v[204:207]\n"
"ds_read_b64_tr_b16 a[224:225], v11 offset:49664\n"
"v_perm_b32 v59, v39, v38, s27\n"
"v_mfma_f32_16x16x32_bf16 v[24:27], a[96:99], a[0:3], v[64:67]\n"
"v_exp_f32 v40, v40\n"
"v_mfma_f32_16x16x32_bf16 v[24:27], a[100:103], a[4:7], v[24:27]\n"
"buffer_load_dwordx4 v9, s[52:55], s64 offen offset:1024 lds\n"
"v_mfma_f32_16x16x32_bf16 v[24:27], a[104:107], a[8:11], v[24:27]\n"
"v_exp_f32 v41, v41\n"
"v_mfma_f32_16x16x32_bf16 v[24:27], a[108:111], a[12:15], v[24:27]\n"
"ds_read_b64_tr_b16 a[226:227], v11 offset:53760\n"
"v_perm_b32 v60, v41, v40, s27\n"
"v_mfma_f32_16x16x32_bf16 v[208:211], a[160:163], v[48:51], v[208:211]\n"
"v_exp_f32 v42, v42\n"
"v_mfma_f32_16x16x32_bf16 v[208:211], a[164:167], v[52:55], v[208:211]\n"
"ds_read_b64_tr_b16 a[228:229], v11 offset:57856\n"
"v_mfma_f32_16x16x32_bf16 v[212:215], a[168:171], v[48:51], v[212:215]\n"
"v_exp_f32 v43, v43\n"
"v_mfma_f32_16x16x32_bf16 v[212:215], a[172:175], v[52:55], v[212:215]\n"
"ds_read_b64_tr_b16 a[230:231], v11 offset:61952\n"
"v_perm_b32 v61, v43, v42, s27\n"
"v_mfma_f32_16x16x32_bf16 v[28:31], a[112:115], a[0:3], v[64:67]\n"
"v_exp_f32 v44, v44\n"
"s_waitcnt lgkmcnt(3)\n"
"v_mfma_f32_16x16x32_bf16 v[28:31], a[116:119], a[4:7], v[28:31]\n"
"buffer_store_dwordx4 v[112:115], v13, s[36:39], s24 offen sc1\n"
"v_mfma_f32_16x16x32_bf16 v[28:31], a[120:123], a[8:11], v[28:31]\n"
"v_exp_f32 v45, v45\n"
"v_mfma_f32_16x16x32_bf16 v[28:31], a[124:127], a[12:15], v[28:31]\n"
"ds_read_b64_tr_b16 a[232:233], v12 offset:49664\n"
"s_add_u32 s24, s24, 1024\n"
"v_mfma_f32_16x16x32_bf16 v[216:219], a[176:179], v[48:51], v[216:219]\n"
"v_exp_f32 v46, v46\n"
"v_mfma_f32_16x16x32_bf16 v[216:219], a[180:183], v[52:55], v[216:219]\n"
"ds_read_b64_tr_b16 a[234:235], v12 offset:53760\n"
"v_perm_b32 v62, v45, v44, s27\n"
"v_mfma_f32_16x16x32_bf16 v[220:223], a[184:187], v[48:51], v[220:223]\n"
"v_exp_f32 v47, v47\n"
"v_mfma_f32_16x16x32_bf16 v[220:223], a[188:191], v[52:55], v[220:223]\n"
"v_perm_b32 v63, v47, v46, s27\n"
"v_mfma_f32_16x16x32_bf16 v[88:91], v[96:99], v[48:51], v[88:91]\n"
"v_mfma_f32_16x16x32_bf16 v[88:91], v[96:99], v[52:55], v[88:91]\n"
"v_mfma_f32_16x16x32_bf16 v[32:35], a[64:67], a[16:19], v[68:71]\n"
"v_exp_f32 v16, v16\n"
"v_mfma_f32_16x16x32_bf16 v[32:35], a[68:71], a[20:23], v[32:35]\n"
"ds_read_b64_tr_b16 a[236:237], v12 offset:57856\n"
"v_mfma_f32_16x16x32_bf16 v[32:35], a[72:75], a[24:27], v[32:35]\n"
"v_exp_f32 v17, v17\n"
"v_mfma_f32_16x16x32_bf16 v[32:35], a[76:79], a[28:31], v[32:35]\n"
"buffer_load_dwordx4 v8, s[52:55], s64 offen offset:2048 lds\n"
"v_mfma_f32_16x16x32_bf16 v[224:227], a[128:131], v[56:59], v[224:227]\n"
"v_exp_f32 v18, v18\n"
"v_mfma_f32_16x16x32_bf16 v[224:227], a[132:135], v[60:63], v[224:227]\n"
"ds_read_b64_tr_b16 a[238:239], v12 offset:61952\n"
"v_perm_b32 v48, v17, v16, s27\n"
"v_mfma_f32_16x16x32_bf16 v[228:231], a[136:139], v[56:59], v[228:231]\n"
"v_exp_f32 v19, v19\n"
"v_mfma_f32_16x16x32_bf16 v[228:231], a[140:143], v[60:63], v[228:231]\n"
"ds_read_b64_tr_b16 a[240:241], v11 offset:49920\n"
"v_perm_b32 v49, v19, v18, s27\n"
"v_mfma_f32_16x16x32_bf16 v[36:39], a[80:83], a[16:19], v[68:71]\n"
"v_exp_f32 v20, v20\n"
"v_mfma_f32_16x16x32_bf16 v[36:39], a[84:87], a[20:23], v[36:39]\n"
"ds_read_b64_tr_b16 a[242:243], v11 offset:54016\n"
"v_mfma_f32_16x16x32_bf16 v[36:39], a[88:91], a[24:27], v[36:39]\n"
"v_exp_f32 v21, v21\n"
"v_mfma_f32_16x16x32_bf16 v[36:39], a[92:95], a[28:31], v[36:39]\n"
"ds_read_b64_tr_b16 a[244:245], v11 offset:58112\n"
"v_perm_b32 v50, v21, v20, s27\n"
"v_mfma_f32_16x16x32_bf16 v[232:235], a[144:147], v[56:59], v[232:235]\n"
"v_exp_f32 v22, v22\n"
"v_mfma_f32_16x16x32_bf16 v[232:235], a[148:151], v[60:63], v[232:235]\n"
"buffer_load_dwordx4 v9, s[52:55], s64 offen offset:3072 lds\n"
"v_mfma_f32_16x16x32_bf16 v[236:239], a[152:155], v[56:59], v[236:239]\n"
"v_exp_f32 v23, v23\n"
"v_mfma_f32_16x16x32_bf16 v[236:239], a[156:159], v[60:63], v[236:239]\n"
"ds_read_b64_tr_b16 a[246:247], v11 offset:62208\n"
"s_add_u32 s64, s64, 16384\n"
"v_mfma_f32_16x16x32_bf16 v[40:43], a[96:99], a[16:19], v[68:71]\n"
"v_exp_f32 v24, v24\n"
"v_mfma_f32_16x16x32_bf16 v[40:43], a[100:103], a[20:23], v[40:43]\n"
"ds_read_b64_tr_b16 a[248:249], v12 offset:49920\n"
"v_perm_b32 v51, v23, v22, s27\n"
"v_mfma_f32_16x16x32_bf16 v[40:43], a[104:107], a[24:27], v[40:43]\n"
"v_exp_f32 v25, v25\n"
"v_mfma_f32_16x16x32_bf16 v[40:43], a[108:111], a[28:31], v[40:43]\n"
"ds_read_b64_tr_b16 a[250:251], v12 offset:54016\n"
"v_perm_b32 v52, v25, v24, s27\n"
"v_mfma_f32_16x16x32_bf16 v[240:243], a[160:163], v[56:59], v[240:243]\n"
"v_exp_f32 v26, v26\n"
"v_mfma_f32_16x16x32_bf16 v[240:243], a[164:167], v[60:63], v[240:243]\n"
"buffer_load_dwordx4 v[100:103], v13, s[76:79], s71 offen\n"
"v_mfma_f32_16x16x32_bf16 v[244:247], a[168:171], v[56:59], v[244:247]\n"
"v_exp_f32 v27, v27\n"
"v_mfma_f32_16x16x32_bf16 v[244:247], a[172:175], v[60:63], v[244:247]\n"
"ds_read_b64_tr_b16 a[252:253], v12 offset:58112\n"
"s_add_u32 s71, s71, 1024\n"
"v_mfma_f32_16x16x32_bf16 v[44:47], a[112:115], a[16:19], v[68:71]\n"
"v_exp_f32 v28, v28\n"
"v_mfma_f32_16x16x32_bf16 v[44:47], a[116:119], a[20:23], v[44:47]\n"
"ds_read_b64_tr_b16 a[254:255], v12 offset:62208\n"
"v_perm_b32 v53, v27, v26, s27\n"
"v_mfma_f32_16x16x32_bf16 v[44:47], a[120:123], a[24:27], v[44:47]\n"
"v_exp_f32 v29, v29\n"
"v_mfma_f32_16x16x32_bf16 v[44:47], a[124:127], a[28:31], v[44:47]\n"
"v_perm_b32 v54, v29, v28, s27\n"
"v_mfma_f32_16x16x32_bf16 v[248:251], a[176:179], v[56:59], v[248:251]\n"
"v_exp_f32 v30, v30\n"
"v_mfma_f32_16x16x32_bf16 v[248:251], a[180:183], v[60:63], v[248:251]\n"
"v_mfma_f32_16x16x32_bf16 v[252:255], a[184:187], v[56:59], v[252:255]\n"
"v_exp_f32 v31, v31\n"
"v_mfma_f32_16x16x32_bf16 v[252:255], a[188:191], v[60:63], v[252:255]\n"
"v_perm_b32 v55, v31, v30, s27\n"
"v_mfma_f32_16x16x32_bf16 v[92:95], v[96:99], v[56:59], v[92:95]\n"
"v_mfma_f32_16x16x32_bf16 v[92:95], v[96:99], v[60:63], v[92:95]\n"
"s_waitcnt vmcnt(11)\n"
"s_barrier\n"
"v_mfma_f32_16x16x32_bf16 v[16:19], a[64:67], a[32:35], v[72:75]\n"
"v_exp_f32 v32, v32\n"
"v_mfma_f32_16x16x32_bf16 v[16:19], a[68:71], a[36:39], v[16:19]\n"
"ds_read_b64_tr_b16 a[128:129], v11 offset:0\n"
"s_add_u32 m0, s65, 0\n"
"v_mfma_f32_16x16x32_bf16 v[16:19], a[72:75], a[40:43], v[16:19]\n"
"v_exp_f32 v33, v33\n"
"v_mfma_f32_16x16x32_bf16 v[16:19], a[76:79], a[44:47], v[16:19]\n"
"buffer_load_dwordx4 v8, s[48:51], s64 offen lds\n"
"v_mfma_f32_16x16x32_bf16 v[128:131], a[192:195], v[48:51], v[128:131]\n"
"v_exp_f32 v34, v34\n"
"v_mfma_f32_16x16x32_bf16 v[128:131], a[196:199], v[52:55], v[128:131]\n"
"ds_read_b64_tr_b16 a[130:131], v11 offset:4096\n"
"v_perm_b32 v56, v33, v32, s27\n"
"v_mfma_f32_16x16x32_bf16 v[132:135], a[200:203], v[48:51], v[132:135]\n"
"v_exp_f32 v35, v35\n"
"v_mfma_f32_16x16x32_bf16 v[132:135], a[204:207], v[52:55], v[132:135]\n"
"ds_read_b64_tr_b16 a[132:133], v11 offset:8192\n"
"v_perm_b32 v57, v35, v34, s27\n"
"v_mfma_f32_16x16x32_bf16 v[20:23], a[80:83], a[32:35], v[72:75]\n"
"v_exp_f32 v36, v36\n"
"v_mfma_f32_16x16x32_bf16 v[20:23], a[84:87], a[36:39], v[20:23]\n"
"ds_read_b64_tr_b16 a[134:135], v11 offset:12288\n"
"v_mfma_f32_16x16x32_bf16 v[20:23], a[88:91], a[40:43], v[20:23]\n"
"v_exp_f32 v37, v37\n"
"v_mfma_f32_16x16x32_bf16 v[20:23], a[92:95], a[44:47], v[20:23]\n"
"ds_read_b64_tr_b16 a[136:137], v12 offset:0\n"
"v_perm_b32 v58, v37, v36, s27\n"
"v_mfma_f32_16x16x32_bf16 v[136:139], a[208:211], v[48:51], v[136:139]\n"
"v_exp_f32 v38, v38\n"
"v_mfma_f32_16x16x32_bf16 v[136:139], a[212:215], v[52:55], v[136:139]\n"
"buffer_load_dwordx4 v9, s[48:51], s64 offen offset:1024 lds\n"
"v_mfma_f32_16x16x32_bf16 v[140:143], a[216:219], v[48:51], v[140:143]\n"
"v_exp_f32 v39, v39\n"
"v_mfma_f32_16x16x32_bf16 v[140:143], a[220:223], v[52:55], v[140:143]\n"
"ds_read_b64_tr_b16 a[138:139], v12 offset:4096\n"
"v_perm_b32 v59, v39, v38, s27\n"
"v_mfma_f32_16x16x32_bf16 v[24:27], a[96:99], a[32:35], v[72:75]\n"
"v_exp_f32 v40, v40\n"
"v_mfma_f32_16x16x32_bf16 v[24:27], a[100:103], a[36:39], v[24:27]\n"
"ds_read_b64_tr_b16 a[140:141], v12 offset:8192\n"
"v_mfma_f32_16x16x32_bf16 v[24:27], a[104:107], a[40:43], v[24:27]\n"
"v_exp_f32 v41, v41\n"
"v_mfma_f32_16x16x32_bf16 v[24:27], a[108:111], a[44:47], v[24:27]\n"
"ds_read_b64_tr_b16 a[142:143], v12 offset:12288\n"
"v_perm_b32 v60, v41, v40, s27\n"
"s_waitcnt lgkmcnt(3)\n"
"v_mfma_f32_16x16x32_bf16 v[144:147], a[224:227], v[48:51], v[144:147]\n"
"v_exp_f32 v42, v42\n"
"v_mfma_f32_16x16x32_bf16 v[144:147], a[228:231], v[52:55], v[144:147]\n"
"buffer_load_dwordx4 v8, s[48:51], s64 offen offset:2048 lds\n"
"v_mfma_f32_16x16x32_bf16 v[148:151], a[232:235], v[48:51], v[148:151]\n"
"v_exp_f32 v43, v43\n"
"v_mfma_f32_16x16x32_bf16 v[148:151], a[236:239], v[52:55], v[148:151]\n"
"ds_read_b64_tr_b16 a[144:145], v11 offset:256\n"
"v_perm_b32 v61, v43, v42, s27\n"
"v_mfma_f32_16x16x32_bf16 v[28:31], a[112:115], a[32:35], v[72:75]\n"
"v_exp_f32 v44, v44\n"
"v_mfma_f32_16x16x32_bf16 v[28:31], a[116:119], a[36:39], v[28:31]\n"
"ds_read_b64_tr_b16 a[146:147], v11 offset:4352\n"
"v_mfma_f32_16x16x32_bf16 v[28:31], a[120:123], a[40:43], v[28:31]\n"
"v_exp_f32 v45, v45\n"
"v_mfma_f32_16x16x32_bf16 v[28:31], a[124:127], a[44:47], v[28:31]\n"
"ds_read_b64_tr_b16 a[148:149], v11 offset:8448\n"
"v_perm_b32 v62, v45, v44, s27\n"
"v_mfma_f32_16x16x32_bf16 v[152:155], a[240:243], v[48:51], v[152:155]\n"
"v_exp_f32 v46, v46\n"
"v_mfma_f32_16x16x32_bf16 v[152:155], a[244:247], v[52:55], v[152:155]\n"
"v_mfma_f32_16x16x32_bf16 v[156:159], a[248:251], v[48:51], v[156:159]\n"
"v_exp_f32 v47, v47\n"
"v_mfma_f32_16x16x32_bf16 v[156:159], a[252:255], v[52:55], v[156:159]\n"
"v_perm_b32 v63, v47, v46, s27\n"
"v_mfma_f32_16x16x32_bf16 v[80:83], v[96:99], v[48:51], v[80:83]\n"
"s_sub_u32 s63, s63, 1\n"
"v_mfma_f32_16x16x32_bf16 v[80:83], v[96:99], v[52:55], v[80:83]\n"
"s_cbranch_scc0 Lattn_%=\n"
"v_mfma_f32_16x16x32_bf16 v[32:35], a[64:67], a[48:51], v[76:79]\n"
"v_exp_f32 v16, v16\n"
"v_mfma_f32_16x16x32_bf16 v[32:35], a[68:71], a[52:55], v[32:35]\n"
"ds_read_b128 a[64:67], v10 offset:32768\n"
"v_lshlrev_b32 v109, 16, v116\n"
"v_fmac_f32 v120, v110, v109\n"
"v_mfma_f32_16x16x32_bf16 v[32:35], a[72:75], a[56:59], v[32:35]\n"
"v_exp_f32 v17, v17\n"
"v_mfma_f32_16x16x32_bf16 v[32:35], a[76:79], a[60:63], v[32:35]\n"
"ds_read_b128 a[68:71], v10 offset:33024\n"
"v_perm_b32 v48, v17, v16, s27\n"
"v_and_b32 v109, 0xffff0000, v116\n"
"v_mfma_f32_16x16x32_bf16 v[160:163], a[192:195], v[56:59], v[160:163]\n"
"v_exp_f32 v18, v18\n"
"v_mfma_f32_16x16x32_bf16 v[160:163], a[196:199], v[60:63], v[160:163]\n"
"ds_read_b128 a[72:75], v10 offset:33280\n"
"v_fmac_f32 v121, v110, v109\n"
"v_lshlrev_b32 v109, 16, v117\n"
"v_mfma_f32_16x16x32_bf16 v[164:167], a[200:203], v[56:59], v[164:167]\n"
"v_exp_f32 v19, v19\n"
"v_mfma_f32_16x16x32_bf16 v[164:167], a[204:207], v[60:63], v[164:167]\n"
"ds_read_b128 a[76:79], v10 offset:33536\n"
"v_perm_b32 v49, v19, v18, s27\n"
"v_fmac_f32 v122, v110, v109\n"
"v_mfma_f32_16x16x32_bf16 v[36:39], a[80:83], a[48:51], v[76:79]\n"
"v_exp_f32 v20, v20\n"
"v_mfma_f32_16x16x32_bf16 v[36:39], a[84:87], a[52:55], v[36:39]\n"
"ds_read_b128 a[80:83], v10 offset:36864\n"
"v_and_b32 v109, 0xffff0000, v117\n"
"v_fmac_f32 v123, v110, v109\n"
"v_mfma_f32_16x16x32_bf16 v[36:39], a[88:91], a[56:59], v[36:39]\n"
"v_exp_f32 v21, v21\n"
"v_mfma_f32_16x16x32_bf16 v[36:39], a[92:95], a[60:63], v[36:39]\n"
"ds_read_b128 a[84:87], v10 offset:37120\n"
"v_perm_b32 v50, v21, v20, s27\n"
"v_lshlrev_b32 v109, 16, v118\n"
"v_mfma_f32_16x16x32_bf16 v[168:171], a[208:211], v[56:59], v[168:171]\n"
"v_exp_f32 v22, v22\n"
"v_mfma_f32_16x16x32_bf16 v[168:171], a[212:215], v[60:63], v[168:171]\n"
"ds_read_b128 a[88:91], v10 offset:37376\n"
"v_fmac_f32 v124, v110, v109\n"
"v_and_b32 v109, 0xffff0000, v118\n"
"v_mfma_f32_16x16x32_bf16 v[172:175], a[216:219], v[56:59], v[172:175]\n"
"v_exp_f32 v23, v23\n"
"v_mfma_f32_16x16x32_bf16 v[172:175], a[220:223], v[60:63], v[172:175]\n"
"ds_read_b128 a[92:95], v10 offset:37632\n"
"v_perm_b32 v51, v23, v22, s27\n"
"v_fmac_f32 v125, v110, v109\n"
"v_mfma_f32_16x16x32_bf16 v[40:43], a[96:99], a[48:51], v[76:79]\n"
"v_exp_f32 v24, v24\n"
"v_mfma_f32_16x16x32_bf16 v[40:43], a[100:103], a[52:55], v[40:43]\n"
"ds_read_b128 a[96:99], v10 offset:40960\n"
"v_lshlrev_b32 v109, 16, v119\n"
"v_fmac_f32 v126, v110, v109\n"
"v_mfma_f32_16x16x32_bf16 v[40:43], a[104:107], a[56:59], v[40:43]\n"
"v_exp_f32 v25, v25\n"
"v_mfma_f32_16x16x32_bf16 v[40:43], a[108:111], a[60:63], v[40:43]\n"
"ds_read_b128 a[100:103], v10 offset:41216\n"
"v_perm_b32 v52, v25, v24, s27\n"
"v_and_b32 v109, 0xffff0000, v119\n"
"v_mfma_f32_16x16x32_bf16 v[176:179], a[224:227], v[56:59], v[176:179]\n"
"v_exp_f32 v26, v26\n"
"v_mfma_f32_16x16x32_bf16 v[176:179], a[228:231], v[60:63], v[176:179]\n"
"ds_read_b128 a[104:107], v10 offset:41472\n"
"v_fmac_f32 v127, v110, v109\n"
"v_mfma_f32_16x16x32_bf16 v[180:183], a[232:235], v[56:59], v[180:183]\n"
"v_exp_f32 v27, v27\n"
"v_mfma_f32_16x16x32_bf16 v[180:183], a[236:239], v[60:63], v[180:183]\n"
"ds_read_b128 a[108:111], v10 offset:41728\n"
"v_perm_b32 v53, v27, v26, s27\n"
"v_mfma_f32_16x16x32_bf16 v[44:47], a[112:115], a[48:51], v[76:79]\n"
"v_exp_f32 v28, v28\n"
"v_mfma_f32_16x16x32_bf16 v[44:47], a[116:119], a[52:55], v[44:47]\n"
"ds_read_b128 a[112:115], v10 offset:45056\n"
"v_mfma_f32_16x16x32_bf16 v[44:47], a[120:123], a[56:59], v[44:47]\n"
"v_exp_f32 v29, v29\n"
"v_mfma_f32_16x16x32_bf16 v[44:47], a[124:127], a[60:63], v[44:47]\n"
"ds_read_b128 a[116:119], v10 offset:45312\n"
"v_perm_b32 v54, v29, v28, s27\n"
"v_mfma_f32_16x16x32_bf16 v[184:187], a[240:243], v[56:59], v[184:187]\n"
"v_exp_f32 v30, v30\n"
"v_mfma_f32_16x16x32_bf16 v[184:187], a[244:247], v[60:63], v[184:187]\n"
"ds_read_b128 a[120:123], v10 offset:45568\n"
"v_mfma_f32_16x16x32_bf16 v[188:191], a[248:251], v[56:59], v[188:191]\n"
"v_exp_f32 v31, v31\n"
"v_mfma_f32_16x16x32_bf16 v[188:191], a[252:255], v[60:63], v[188:191]\n"
"ds_read_b128 a[124:127], v10 offset:45824\n"
"v_perm_b32 v55, v31, v30, s27\n"
"v_mfma_f32_16x16x32_bf16 v[84:87], v[96:99], v[56:59], v[84:87]\n"
"v_mfma_f32_16x16x32_bf16 v[84:87], v[96:99], v[60:63], v[84:87]\n"
"s_waitcnt vmcnt(4) lgkmcnt(3)\n"
"s_barrier\n"
"v_mfma_f32_16x16x32_bf16 v[16:19], a[64:67], a[0:3], v[64:67]\n"
"v_exp_f32 v32, v32\n"
"v_mfma_f32_16x16x32_bf16 v[16:19], a[68:71], a[4:7], v[16:19]\n"
"ds_read_b128 a[64:67], v10 offset:49152\n"
"ds_read_b64_tr_b16 a[150:151], v11 offset:12544\n"
"v_permlane32_swap_b32 v120, v124\n"
"v_mfma_f32_16x16x32_bf16 v[16:19], a[72:75], a[8:11], v[16:19]\n"
"v_exp_f32 v33, v33\n"
"v_mfma_f32_16x16x32_bf16 v[16:19], a[76:79], a[12:15], v[16:19]\n"
"ds_read_b128 a[68:71], v10 offset:49408\n"
"ds_read_b64_tr_b16 a[152:153], v12 offset:256\n"
"v_permlane32_swap_b32 v121, v125\n"
"v_mfma_f32_16x16x32_bf16 v[192:195], a[192:195], v[48:51], v[192:195]\n"
"v_exp_f32 v34, v34\n"
"v_mfma_f32_16x16x32_bf16 v[192:195], a[196:199], v[52:55], v[192:195]\n"
"ds_read_b128 a[72:75], v10 offset:49664\n"
"ds_read_b64_tr_b16 a[154:155], v12 offset:4352\n"
"v_permlane32_swap_b32 v122, v126\n"
"v_mfma_f32_16x16x32_bf16 v[196:199], a[200:203], v[48:51], v[196:199]\n"
"v_exp_f32 v35, v35\n"
"v_mfma_f32_16x16x32_bf16 v[196:199], a[204:207], v[52:55], v[196:199]\n"
"ds_read_b128 a[76:79], v10 offset:49920\n"
"ds_read_b64_tr_b16 a[156:157], v12 offset:8448\n"
"v_permlane32_swap_b32 v123, v127\n"
"v_mfma_f32_16x16x32_bf16 v[20:23], a[80:83], a[0:3], v[64:67]\n"
"v_exp_f32 v36, v36\n"
"v_mfma_f32_16x16x32_bf16 v[20:23], a[84:87], a[4:7], v[20:23]\n"
"ds_read_b128 a[80:83], v10 offset:53248\n"
"ds_read_b64_tr_b16 a[158:159], v12 offset:12544\n"
"v_add_f32 v120, v120, v124\n"
"v_mfma_f32_16x16x32_bf16 v[20:23], a[88:91], a[8:11], v[20:23]\n"
"v_exp_f32 v37, v37\n"
"v_mfma_f32_16x16x32_bf16 v[20:23], a[92:95], a[12:15], v[20:23]\n"
"ds_read_b128 a[84:87], v10 offset:53504\n"
"ds_read_b64_tr_b16 a[160:161], v11 offset:512\n"
"v_add_f32 v121, v121, v125\n"
"v_mfma_f32_16x16x32_bf16 v[200:203], a[208:211], v[48:51], v[200:203]\n"
"v_exp_f32 v38, v38\n"
"v_mfma_f32_16x16x32_bf16 v[200:203], a[212:215], v[52:55], v[200:203]\n"
"ds_read_b128 a[88:91], v10 offset:53760\n"
"ds_read_b64_tr_b16 a[162:163], v11 offset:4608\n"
"v_add_f32 v122, v122, v126\n"
"v_mfma_f32_16x16x32_bf16 v[204:207], a[216:219], v[48:51], v[204:207]\n"
"v_exp_f32 v39, v39\n"
"v_mfma_f32_16x16x32_bf16 v[204:207], a[220:223], v[52:55], v[204:207]\n"
"ds_read_b128 a[92:95], v10 offset:54016\n"
"ds_read_b64_tr_b16 a[164:165], v11 offset:8704\n"
"v_add_f32 v123, v123, v127\n"
"v_mfma_f32_16x16x32_bf16 v[24:27], a[96:99], a[0:3], v[64:67]\n"
"v_exp_f32 v40, v40\n"
"v_mfma_f32_16x16x32_bf16 v[24:27], a[100:103], a[4:7], v[24:27]\n"
"ds_read_b128 a[96:99], v10 offset:57344\n"
"ds_read_b64_tr_b16 a[166:167], v11 offset:12800\n"
"v_permlane16_swap_b32 v120, v122\n"
"v_mfma_f32_16x16x32_bf16 v[24:27], a[104:107], a[8:11], v[24:27]\n"
"v_exp_f32 v41, v41\n"
"v_mfma_f32_16x16x32_bf16 v[24:27], a[108:111], a[12:15], v[24:27]\n"
"ds_read_b128 a[100:103], v10 offset:57600\n"
"ds_read_b64_tr_b16 a[168:169], v12 offset:512\n"
"v_permlane16_swap_b32 v121, v123\n"
"v_mfma_f32_16x16x32_bf16 v[208:211], a[224:227], v[48:51], v[208:211]\n"
"v_exp_f32 v42, v42\n"
"v_mfma_f32_16x16x32_bf16 v[208:211], a[228:231], v[52:55], v[208:211]\n"
"ds_read_b128 a[104:107], v10 offset:57856\n"
"ds_read_b64_tr_b16 a[170:171], v12 offset:4608\n"
"v_add_f32 v120, v120, v122\n"
"v_mfma_f32_16x16x32_bf16 v[212:215], a[232:235], v[48:51], v[212:215]\n"
"v_exp_f32 v43, v43\n"
"v_mfma_f32_16x16x32_bf16 v[212:215], a[236:239], v[52:55], v[212:215]\n"
"ds_read_b128 a[108:111], v10 offset:58112\n"
"v_perm_b32 v56, v33, v32, s27\n"
"v_add_f32 v121, v121, v123\n"
"v_mfma_f32_16x16x32_bf16 v[28:31], a[112:115], a[0:3], v[64:67]\n"
"v_exp_f32 v44, v44\n"
"s_waitcnt lgkmcnt(7)\n"
"v_mfma_f32_16x16x32_bf16 v[28:31], a[116:119], a[4:7], v[28:31]\n"
"ds_read_b128 a[112:115], v10 offset:61440\n"
"v_perm_b32 v57, v35, v34, s27\n"
"v_mfma_f32_16x16x32_bf16 v[28:31], a[120:123], a[8:11], v[28:31]\n"
"v_exp_f32 v45, v45\n"
"v_mfma_f32_16x16x32_bf16 v[28:31], a[124:127], a[12:15], v[28:31]\n"
"ds_read_b128 a[116:119], v10 offset:61696\n"
"v_perm_b32 v58, v37, v36, s27\n"
"v_mfma_f32_16x16x32_bf16 v[216:219], a[240:243], v[48:51], v[216:219]\n"
"v_exp_f32 v46, v46\n"
"v_mfma_f32_16x16x32_bf16 v[216:219], a[244:247], v[52:55], v[216:219]\n"
"ds_read_b128 a[120:123], v10 offset:61952\n"
"v_perm_b32 v59, v39, v38, s27\n"
"v_mfma_f32_16x16x32_bf16 v[220:223], a[248:251], v[48:51], v[220:223]\n"
"v_exp_f32 v47, v47\n"
"v_mfma_f32_16x16x32_bf16 v[220:223], a[252:255], v[52:55], v[220:223]\n"
"ds_read_b128 a[124:127], v10 offset:62208\n"
"v_perm_b32 v60, v41, v40, s27\n"
"v_mfma_f32_16x16x32_bf16 v[88:91], v[96:99], v[48:51], v[88:91]\n"
"v_perm_b32 v61, v43, v42, s27\n"
"v_perm_b32 v62, v45, v44, s27\n"
"v_mfma_f32_16x16x32_bf16 v[88:91], v[96:99], v[52:55], v[88:91]\n"
"v_perm_b32 v63, v47, v46, s27\n"
"v_mfma_f32_16x16x32_bf16 v[32:35], a[64:67], a[0:3], v[64:67]\n"
"ds_read_b64_tr_b16 a[172:173], v12 offset:8704\n"
"v_exp_f32 v16, v16\n"
"v_mfma_f32_16x16x32_bf16 v[32:35], a[68:71], a[4:7], v[32:35]\n"
"ds_read_b128 a[64:67], v10 offset:32768\n"
"v_lshrrev_b32 v15, 6, v13\n"
"v_and_b32 v15, 3, v15\n"
"v_mfma_f32_16x16x32_bf16 v[32:35], a[72:75], a[8:11], v[32:35]\n"
"ds_read_b64_tr_b16 a[174:175], v12 offset:12800\n"
"v_exp_f32 v17, v17\n"
"v_mfma_f32_16x16x32_bf16 v[32:35], a[76:79], a[12:15], v[32:35]\n"
"ds_read_b128 a[68:71], v10 offset:33024\n"
"v_mul_u32_u24 v15, 1792, v15\n"
"v_add_u32 v14, v8, v15\n"
"v_mfma_f32_16x16x32_bf16 v[224:227], a[192:195], v[56:59], v[224:227]\n"
"ds_read_b64_tr_b16 a[176:177], v11 offset:768\n"
"v_exp_f32 v18, v18\n"
"v_mfma_f32_16x16x32_bf16 v[224:227], a[196:199], v[60:63], v[224:227]\n"
"ds_read_b128 a[72:75], v10 offset:33280\n"
"ds_read_b64_tr_b16 a[192:193], v11 offset:16384\n"
"v_xor_b32 v15, 32, v14\n"
"v_mfma_f32_16x16x32_bf16 v[228:231], a[200:203], v[56:59], v[228:231]\n"
"ds_read_b64_tr_b16 a[178:179], v11 offset:4864\n"
"v_exp_f32 v19, v19\n"
"v_mfma_f32_16x16x32_bf16 v[228:231], a[204:207], v[60:63], v[228:231]\n"
"ds_read_b128 a[76:79], v10 offset:33536\n"
"ds_read_b64_tr_b16 a[194:195], v11 offset:20480\n"
"s_mov_b32 s67, %[o1]\n"
"v_mfma_f32_16x16x32_bf16 v[36:39], a[80:83], a[0:3], v[64:67]\n"
"ds_read_b64_tr_b16 a[180:181], v11 offset:8960\n"
"v_exp_f32 v20, v20\n"
"v_mfma_f32_16x16x32_bf16 v[36:39], a[84:87], a[4:7], v[36:39]\n"
"ds_read_b128 a[80:83], v10 offset:36864\n"
"ds_read_b64_tr_b16 a[196:197], v11 offset:24576\n"
"s_lshl_b32 s24, s70, 13\n"
"v_mfma_f32_16x16x32_bf16 v[36:39], a[88:91], a[8:11], v[36:39]\n"
"ds_read_b64_tr_b16 a[182:183], v11 offset:13056\n"
"v_exp_f32 v21, v21\n"
"v_mfma_f32_16x16x32_bf16 v[36:39], a[92:95], a[12:15], v[36:39]\n"
"ds_read_b128 a[84:87], v10 offset:37120\n"
"ds_read_b64_tr_b16 a[198:199], v11 offset:28672\n"
"ds_read_b64_tr_b16 a[184:185], v12 offset:768\n"
"v_mfma_f32_16x16x32_bf16 v[232:235], a[208:211], v[56:59], v[232:235]\n"
"ds_read_b64_tr_b16 a[186:187], v12 offset:4864\n"
"v_exp_f32 v22, v22\n"
"v_mfma_f32_16x16x32_bf16 v[232:235], a[212:215], v[60:63], v[232:235]\n"
"ds_read_b128 a[88:91], v10 offset:37376\n"
"ds_read_b64_tr_b16 a[200:201], v12 offset:16384\n"
"ds_read_b64_tr_b16 a[188:189], v12 offset:8960\n"
"v_mfma_f32_16x16x32_bf16 v[236:239], a[216:219], v[56:59], v[236:239]\n"
"ds_read_b64_tr_b16 a[190:191], v12 offset:13056\n"
"v_exp_f32 v23, v23\n"
"v_mfma_f32_16x16x32_bf16 v[236:239], a[220:223], v[60:63], v[236:239]\n"
"ds_read_b128 a[92:95], v10 offset:37632\n"
"ds_read_b64_tr_b16 a[202:203], v12 offset:20480\n"
"v_perm_b32 v48, v17, v16, s27\n"
"s_waitcnt lgkmcnt(14)\n"
"v_mfma_f32_16x16x32_bf16 v[40:43], a[96:99], a[0:3], v[64:67]\n"
"v_exp_f32 v24, v24\n"
"v_perm_b32 v49, v19, v18, s27\n"
"v_mfma_f32_16x16x32_bf16 v[40:43], a[100:103], a[4:7], v[40:43]\n"
"ds_read_b128 a[96:99], v10 offset:40960\n"
"ds_read_b64_tr_b16 a[204:205], v12 offset:24576\n"
"v_perm_b32 v50, v21, v20, s27\n"
"v_mfma_f32_16x16x32_bf16 v[40:43], a[104:107], a[8:11], v[40:43]\n"
"v_exp_f32 v25, v25\n"
"v_perm_b32 v51, v23, v22, s27\n"
"v_mfma_f32_16x16x32_bf16 v[40:43], a[108:111], a[12:15], v[40:43]\n"
"ds_read_b128 a[100:103], v10 offset:41216\n"
"ds_read_b64_tr_b16 a[206:207], v12 offset:28672\n"
"v_perm_b32 v52, v25, v24, s27\n"
"v_mfma_f32_16x16x32_bf16 v[240:243], a[224:227], v[56:59], v[240:243]\n"
"v_exp_f32 v26, v26\n"
"s_mov_b64 s[28:29], %[m]\n"
"v_mfma_f32_16x16x32_bf16 v[240:243], a[228:231], v[60:63], v[240:243]\n"
"ds_read_b128 a[104:107], v10 offset:41472\n"
"ds_read_b64_tr_b16 a[208:209], v11 offset:16640\n"
"s_and_b32 s29, s29, 0xffff\n"
"v_mfma_f32_16x16x32_bf16 v[244:247], a[232:235], v[56:59], v[244:247]\n"
"v_exp_f32 v27, v27\n"
"s_mov_b32 s30, 64\n"
"v_mfma_f32_16x16x32_bf16 v[244:247], a[236:239], v[60:63], v[244:247]\n"
"ds_read_b128 a[108:111], v10 offset:41728\n"
"ds_read_b64_tr_b16 a[210:211], v11 offset:20736\n"
"v_perm_b32 v53, v27, v26, s27\n"
"v_mfma_f32_16x16x32_bf16 v[44:47], a[112:115], a[0:3], v[64:67]\n"
"v_exp_f32 v28, v28\n"
"v_mfma_f32_16x16x32_bf16 v[44:47], a[116:119], a[4:7], v[44:47]\n"
"ds_read_b128 a[112:115], v10 offset:45056\n"
"ds_read_b64_tr_b16 a[212:213], v11 offset:24832\n"
"v_mfma_f32_16x16x32_bf16 v[44:47], a[120:123], a[8:11], v[44:47]\n"
"v_exp_f32 v29, v29\n"
"v_mfma_f32_16x16x32_bf16 v[44:47], a[124:127], a[12:15], v[44:47]\n"
"ds_read_b128 a[116:119], v10 offset:45312\n"
"ds_read_b64_tr_b16 a[214:215], v11 offset:28928\n"
"v_perm_b32 v54, v29, v28, s27\n"
"v_mfma_f32_16x16x32_bf16 v[248:251], a[240:243], v[56:59], v[248:251]\n"
"v_exp_f32 v30, v30\n"
"v_mfma_f32_16x16x32_bf16 v[248:251], a[244:247], v[60:63], v[248:251]\n"
"ds_read_b128 a[120:123], v10 offset:45568\n"
"ds_read_b64_tr_b16 a[216:217], v12 offset:16640\n"
"v_mfma_f32_16x16x32_bf16 v[252:255], a[248:251], v[56:59], v[252:255]\n"
"v_exp_f32 v31, v31\n"
"v_mfma_f32_16x16x32_bf16 v[252:255], a[252:255], v[60:63], v[252:255]\n"
"ds_read_b128 a[124:127], v10 offset:45824\n"
"ds_read_b64_tr_b16 a[218:219], v12 offset:20736\n"
"v_perm_b32 v55, v31, v30, s27\n"
"v_mfma_f32_16x16x32_bf16 v[92:95], v[96:99], v[56:59], v[92:95]\n"
"ds_read_b64_tr_b16 a[220:221], v12 offset:24832\n"
"ds_read_b64_tr_b16 a[222:223], v12 offset:28928\n"
"v_mfma_f32_16x16x32_bf16 v[92:95], v[96:99], v[60:63], v[92:95]\n"
"buffer_load_dwordx4 v[100:103], v13, s[76:79], s71 offen\n"
"s_add_u32 s71, s71, 1024\n"
"v_mfma_f32_16x16x32_bf16 v[16:19], a[64:67], a[16:19], v[68:71]\n"
"ds_read_b64_tr_b16 a[250:251], v12 offset:21248\n"
"v_exp_f32 v32, v32\n"
"v_mfma_f32_16x16x32_bf16 v[16:19], a[68:71], a[20:23], v[16:19]\n"
"ds_read_b128 a[64:67], v10 offset:49152\n"
"ds_read_b64_tr_b16 a[224:225], v11 offset:16896\n"
"ds_read_b64_tr_b16 a[252:253], v12 offset:25344\n"
"v_mfma_f32_16x16x32_bf16 v[16:19], a[72:75], a[24:27], v[16:19]\n"
"ds_read_b64_tr_b16 a[254:255], v12 offset:29440\n"
"v_exp_f32 v33, v33\n"
"v_mfma_f32_16x16x32_bf16 v[16:19], a[76:79], a[28:31], v[16:19]\n"
"ds_read_b128 a[68:71], v10 offset:49408\n"
"ds_read_b64_tr_b16 a[226:227], v11 offset:20992\n"
"v_add_f32_dpp v120, v120, v120 quad_perm:[2,3,0,1] row_mask:0xf bank_mask:0xf\n"
"v_mfma_f32_16x16x32_bf16 v[128:131], a[128:131], v[48:51], v[128:131]\n"
"v_exp_f32 v34, v34\n"
"v_mfma_f32_16x16x32_bf16 v[128:131], a[132:135], v[52:55], v[128:131]\n"
"ds_read_b128 a[72:75], v10 offset:49664\n"
"ds_read_b64_tr_b16 a[228:229], v11 offset:25088\n"
"v_add_f32_dpp v121, v121, v121 quad_perm:[2,3,0,1] row_mask:0xf bank_mask:0xf\n"
"v_mfma_f32_16x16x32_bf16 v[132:135], a[136:139], v[48:51], v[132:135]\n"
"v_exp_f32 v35, v35\n"
"v_mfma_f32_16x16x32_bf16 v[132:135], a[140:143], v[52:55], v[132:135]\n"
"ds_read_b128 a[76:79], v10 offset:49920\n"
"ds_read_b64_tr_b16 a[230:231], v11 offset:29184\n"
"v_add_f32_dpp v120, v120, v120 row_ror:4 row_mask:0xf bank_mask:0xf\n"
"s_waitcnt lgkmcnt(11)\n"
"v_mfma_f32_16x16x32_bf16 v[20:23], a[80:83], a[16:19], v[68:71]\n"
"v_exp_f32 v36, v36\n"
"v_mfma_f32_16x16x32_bf16 v[20:23], a[84:87], a[20:23], v[20:23]\n"
"ds_read_b128 a[80:83], v10 offset:53248\n"
"ds_read_b64_tr_b16 a[232:233], v12 offset:16896\n"
"v_add_f32_dpp v121, v121, v121 row_ror:4 row_mask:0xf bank_mask:0xf\n"
"v_mfma_f32_16x16x32_bf16 v[20:23], a[88:91], a[24:27], v[20:23]\n"
"v_exp_f32 v37, v37\n"
"v_mfma_f32_16x16x32_bf16 v[20:23], a[92:95], a[28:31], v[20:23]\n"
"ds_read_b128 a[84:87], v10 offset:53504\n"
"ds_read_b64_tr_b16 a[234:235], v12 offset:20992\n"
"v_add_f32_dpp v120, v120, v120 row_ror:8 row_mask:0xf bank_mask:0xf\n"
"v_mfma_f32_16x16x32_bf16 v[136:139], a[144:147], v[48:51], v[136:139]\n"
"v_exp_f32 v38, v38\n"
"v_mfma_f32_16x16x32_bf16 v[136:139], a[148:151], v[52:55], v[136:139]\n"
"ds_read_b128 a[88:91], v10 offset:53760\n"
"ds_read_b64_tr_b16 a[236:237], v12 offset:25088\n"
"v_add_f32_dpp v121, v121, v121 row_ror:8 row_mask:0xf bank_mask:0xf\n"
"v_mfma_f32_16x16x32_bf16 v[140:143], a[152:155], v[48:51], v[140:143]\n"
"v_exp_f32 v39, v39\n"
"v_mfma_f32_16x16x32_bf16 v[140:143], a[156:159], v[52:55], v[140:143]\n"
"ds_read_b128 a[92:95], v10 offset:54016\n"
"ds_read_b64_tr_b16 a[238:239], v12 offset:29184\n"
"v_lshrrev_b32 v109, 5, v13\n"
"v_mfma_f32_16x16x32_bf16 v[24:27], a[96:99], a[16:19], v[68:71]\n"
"v_exp_f32 v40, v40\n"
"v_mfma_f32_16x16x32_bf16 v[24:27], a[100:103], a[20:23], v[24:27]\n"
"ds_read_b128 a[96:99], v10 offset:57344\n"
"ds_read_b64_tr_b16 a[240:241], v11 offset:17152\n"
"v_and_b32 v109, 24, v109\n"
"v_mfma_f32_16x16x32_bf16 v[24:27], a[104:107], a[24:27], v[24:27]\n"
"v_exp_f32 v41, v41\n"
"v_mfma_f32_16x16x32_bf16 v[24:27], a[108:111], a[28:31], v[24:27]\n"
"ds_read_b128 a[100:103], v10 offset:57600\n"
"ds_read_b64_tr_b16 a[242:243], v11 offset:21248\n"
"v_and_b32 v104, 16, v13\n"
"v_mfma_f32_16x16x32_bf16 v[144:147], a[160:163], v[48:51], v[144:147]\n"
"v_exp_f32 v42, v42\n"
"v_mfma_f32_16x16x32_bf16 v[144:147], a[164:167], v[52:55], v[144:147]\n"
"ds_read_b128 a[104:107], v10 offset:57856\n"
"ds_read_b64_tr_b16 a[244:245], v11 offset:25344\n"
"v_lshl_add_u32 v109, v104, 1, v109\n"
"v_mfma_f32_16x16x32_bf16 v[148:151], a[168:171], v[48:51], v[148:151]\n"
"v_exp_f32 v43, v43\n"
"v_mfma_f32_16x16x32_bf16 v[148:151], a[172:175], v[52:55], v[148:151]\n"
"ds_read_b128 a[108:111], v10 offset:58112\n"
"ds_read_b64_tr_b16 a[246:247], v11 offset:29440\n"
"s_lshl_b32 s69, s70, 6\n"
"v_mfma_f32_16x16x32_bf16 v[28:31], a[112:115], a[16:19], v[68:71]\n"
"v_exp_f32 v44, v44\n"
"v_mfma_f32_16x16x32_bf16 v[28:31], a[116:119], a[20:23], v[28:31]\n"
"ds_read_b128 a[112:115], v10 offset:61440\n"
"ds_read_b64_tr_b16 a[248:249], v12 offset:17152\n"
"s_add_u32 s69, s69, 131072\n"
"v_mfma_f32_16x16x32_bf16 v[28:31], a[120:123], a[24:27], v[28:31]\n"
"v_exp_f32 v45, v45\n"
"v_mfma_f32_16x16x32_bf16 v[28:31], a[124:127], a[28:31], v[28:31]\n"
"ds_read_b128 a[116:119], v10 offset:61696\n"
"v_perm_b32 v56, v33, v32, s27\n"
"v_add_u32 v109, s69, v109\n"
"v_mfma_f32_16x16x32_bf16 v[152:155], a[176:179], v[48:51], v[152:155]\n"
"v_exp_f32 v46, v46\n"
"v_mfma_f32_16x16x32_bf16 v[152:155], a[180:183], v[52:55], v[152:155]\n"
"ds_read_b128 a[120:123], v10 offset:61952\n"
"ds_write_b64 v109, v[120:121] offset:0\n"
"v_perm_b32 v57, v35, v34, s27\n"
"v_mfma_f32_16x16x32_bf16 v[156:159], a[184:187], v[48:51], v[156:159]\n"
"v_exp_f32 v47, v47\n"
"v_mfma_f32_16x16x32_bf16 v[156:159], a[188:191], v[52:55], v[156:159]\n"
"ds_read_b128 a[124:127], v10 offset:62208\n"
"v_perm_b32 v58, v37, v36, s27\n"
"v_perm_b32 v63, v47, v46, s27\n"
"v_mfma_f32_16x16x32_bf16 v[80:83], v[96:99], v[48:51], v[80:83]\n"
"v_perm_b32 v59, v39, v38, s27\n"
"v_perm_b32 v60, v41, v40, s27\n"
"v_mfma_f32_16x16x32_bf16 v[80:83], v[96:99], v[52:55], v[80:83]\n"
"v_perm_b32 v61, v43, v42, s27\n"
"v_perm_b32 v62, v45, v44, s27\n"
"s_waitcnt lgkmcnt(4)\n"
"v_mfma_f32_16x16x32_bf16 v[32:35], a[64:67], a[16:19], v[68:71]\n"
"v_exp_f32 v16, v16\n"
"v_mfma_f32_16x16x32_bf16 v[32:35], a[68:71], a[20:23], v[32:35]\n"
"ds_read_b128 a[64:67], v10 offset:32768\n"
"v_lshrrev_b32 v108, 2, v13\n"
"v_and_b32 v109, 60, v108\n"
"v_mfma_f32_16x16x32_bf16 v[32:35], a[72:75], a[24:27], v[32:35]\n"
"v_exp_f32 v17, v17\n"
"v_mfma_f32_16x16x32_bf16 v[32:35], a[76:79], a[28:31], v[32:35]\n"
"ds_read_b128 a[68:71], v10 offset:33024\n"
"v_perm_b32 v48, v17, v16, s27\n"
"v_add_u32 v109, 131072, v109\n"
"v_mfma_f32_16x16x32_bf16 v[128:131], a[192:195], v[56:59], v[128:131]\n"
"v_exp_f32 v18, v18\n"
"v_mfma_f32_16x16x32_bf16 v[128:131], a[196:199], v[60:63], v[128:131]\n"
"ds_read_b128 a[72:75], v10 offset:33280\n"
"v_add_u32 v116, 0, v13\n"
"v_mfma_f32_16x16x32_bf16 v[132:135], a[200:203], v[56:59], v[132:135]\n"
"v_exp_f32 v19, v19\n"
"v_mfma_f32_16x16x32_bf16 v[132:135], a[204:207], v[60:63], v[132:135]\n"
"ds_read_b128 a[76:79], v10 offset:33536\n"
"v_perm_b32 v49, v19, v18, s27\n"
"v_mfma_f32_16x16x32_bf16 v[36:39], a[80:83], a[16:19], v[68:71]\n"
"v_exp_f32 v20, v20\n"
"v_mfma_f32_16x16x32_bf16 v[36:39], a[84:87], a[20:23], v[36:39]\n"
"ds_read_b128 a[80:83], v10 offset:36864\n"
"s_waitcnt vmcnt(1)\n"
"s_waitcnt lgkmcnt(0)\n"
"s_barrier\n"
"v_mfma_f32_16x16x32_bf16 v[36:39], a[88:91], a[24:27], v[36:39]\n"
"v_exp_f32 v21, v21\n"
"v_mfma_f32_16x16x32_bf16 v[36:39], a[92:95], a[28:31], v[36:39]\n"
"ds_read_b128 a[84:87], v10 offset:37120\n"
"v_perm_b32 v50, v21, v20, s27\n"
"v_mfma_f32_16x16x32_bf16 v[136:139], a[208:211], v[56:59], v[136:139]\n"
"v_exp_f32 v22, v22\n"
"v_mfma_f32_16x16x32_bf16 v[136:139], a[212:215], v[60:63], v[136:139]\n"
"ds_read_b128 a[88:91], v10 offset:37376\n"
"ds_read_b32 v104, v109 offset:0\n"
"v_mfma_f32_16x16x32_bf16 v[140:143], a[216:219], v[56:59], v[140:143]\n"
"v_exp_f32 v23, v23\n"
"v_mfma_f32_16x16x32_bf16 v[140:143], a[220:223], v[60:63], v[140:143]\n"
"ds_read_b128 a[92:95], v10 offset:37632\n"
"v_perm_b32 v51, v23, v22, s27\n"
"v_mfma_f32_16x16x32_bf16 v[40:43], a[96:99], a[16:19], v[68:71]\n"
"v_exp_f32 v24, v24\n"
"v_mfma_f32_16x16x32_bf16 v[40:43], a[100:103], a[20:23], v[40:43]\n"
"ds_read_b128 a[96:99], v10 offset:40960\n"
"ds_read_b32 v105, v109 offset:64\n"
"v_mfma_f32_16x16x32_bf16 v[40:43], a[104:107], a[24:27], v[40:43]\n"
"v_exp_f32 v25, v25\n"
"v_mfma_f32_16x16x32_bf16 v[40:43], a[108:111], a[28:31], v[40:43]\n"
"ds_read_b128 a[100:103], v10 offset:41216\n"
"v_perm_b32 v52, v25, v24, s27\n"
"v_mfma_f32_16x16x32_bf16 v[144:147], a[224:227], v[56:59], v[144:147]\n"
"v_exp_f32 v26, v26\n"
"v_mfma_f32_16x16x32_bf16 v[144:147], a[228:231], v[60:63], v[144:147]\n"
"ds_read_b128 a[104:107], v10 offset:41472\n"
"ds_read_b32 v106, v109 offset:128\n"
"v_mfma_f32_16x16x32_bf16 v[148:151], a[232:235], v[56:59], v[148:151]\n"
"v_exp_f32 v27, v27\n"
"v_mfma_f32_16x16x32_bf16 v[148:151], a[236:239], v[60:63], v[148:151]\n"
"ds_read_b128 a[108:111], v10 offset:41728\n"
"v_perm_b32 v53, v27, v26, s27\n"
"v_mfma_f32_16x16x32_bf16 v[44:47], a[112:115], a[16:19], v[68:71]\n"
"v_exp_f32 v28, v28\n"
"v_mfma_f32_16x16x32_bf16 v[44:47], a[116:119], a[20:23], v[44:47]\n"
"ds_read_b128 a[112:115], v10 offset:45056\n"
"ds_read_b32 v107, v109 offset:192\n"
"v_mfma_f32_16x16x32_bf16 v[44:47], a[120:123], a[24:27], v[44:47]\n"
"v_exp_f32 v29, v29\n"
"v_mfma_f32_16x16x32_bf16 v[44:47], a[124:127], a[28:31], v[44:47]\n"
"ds_read_b128 a[116:119], v10 offset:45312\n"
"v_perm_b32 v54, v29, v28, s27\n"
"v_mfma_f32_16x16x32_bf16 v[152:155], a[240:243], v[56:59], v[152:155]\n"
"v_exp_f32 v30, v30\n"
"v_mfma_f32_16x16x32_bf16 v[152:155], a[244:247], v[60:63], v[152:155]\n"
"ds_read_b128 a[120:123], v10 offset:45568\n"
"v_add_u32 v117, 132352, v13\n"
"v_mfma_f32_16x16x32_bf16 v[156:159], a[248:251], v[56:59], v[156:159]\n"
"v_exp_f32 v31, v31\n"
"v_mfma_f32_16x16x32_bf16 v[156:159], a[252:255], v[60:63], v[156:159]\n"
"ds_read_b128 a[124:127], v10 offset:45824\n"
"v_perm_b32 v55, v31, v30, s27\n"
"v_mfma_f32_16x16x32_bf16 v[80:83], v[96:99], v[56:59], v[80:83]\n"
"v_add_u32 v118, 264704, v13\n"
"v_add_u32 v119, 397056, v13\n"
"v_mfma_f32_16x16x32_bf16 v[80:83], v[96:99], v[60:63], v[80:83]\n"
"v_mfma_f32_16x16x32_bf16 v[16:19], a[64:67], a[32:35], v[72:75]\n"
"v_exp_f32 v32, v32\n"
"v_mfma_f32_16x16x32_bf16 v[16:19], a[68:71], a[36:39], v[16:19]\n"
"ds_read_b128 a[64:67], v10 offset:49152\n"
"s_add_u32 m0, s24, 0\n"
"v_mfma_f32_16x16x32_bf16 v[16:19], a[72:75], a[40:43], v[16:19]\n"
"v_exp_f32 v33, v33\n"
"v_mfma_f32_16x16x32_bf16 v[16:19], a[76:79], a[44:47], v[16:19]\n"
"ds_read_b128 a[68:71], v10 offset:49408\n"
"buffer_load_dwordx4 v14, %[wo], s67 offen lds\n"
"v_mfma_f32_16x16x32_bf16 v[160:163], a[128:131], v[48:51], v[160:163]\n"
"v_exp_f32 v34, v34\n"
"v_mfma_f32_16x16x32_bf16 v[160:163], a[132:135], v[52:55], v[160:163]\n"
"ds_read_b128 a[72:75], v10 offset:49664\n"
"s_add_u32 s67, s67, 7168\n"
"v_rcp_f32 v110, v80\n"
"v_mfma_f32_16x16x32_bf16 v[164:167], a[136:139], v[48:51], v[164:167]\n"
"v_exp_f32 v35, v35\n"
"v_mfma_f32_16x16x32_bf16 v[164:167], a[140:143], v[52:55], v[164:167]\n"
"ds_read_b128 a[76:79], v10 offset:49920\n"
"v_perm_b32 v56, v33, v32, s27\n"
"v_mul_f32 v128, v110, v128\n"
"v_mul_f32 v129, v110, v129\n"
"v_mfma_f32_16x16x32_bf16 v[20:23], a[80:83], a[32:35], v[72:75]\n"
"v_exp_f32 v36, v36\n"
"s_waitcnt lgkmcnt(4)\n"
"v_mfma_f32_16x16x32_bf16 v[20:23], a[84:87], a[36:39], v[20:23]\n"
"ds_read_b128 a[80:83], v10 offset:53248\n"
"v_perm_b32 v57, v35, v34, s27\n"
"v_mul_f32 v130, v110, v130\n"
"v_mul_f32 v131, v110, v131\n"
"v_mfma_f32_16x16x32_bf16 v[20:23], a[88:91], a[40:43], v[20:23]\n"
"v_exp_f32 v37, v37\n"
"v_mfma_f32_16x16x32_bf16 v[20:23], a[92:95], a[44:47], v[20:23]\n"
"ds_read_b128 a[84:87], v10 offset:53504\n"
"buffer_load_dwordx4 v15, %[wo], s67 offen offset:1024 lds\n"
"v_mul_f32 v132, v110, v132\n"
"v_mfma_f32_16x16x32_bf16 v[168:171], a[144:147], v[48:51], v[168:171]\n"
"v_exp_f32 v38, v38\n"
"v_mfma_f32_16x16x32_bf16 v[168:171], a[148:151], v[52:55], v[168:171]\n"
"ds_read_b128 a[88:91], v10 offset:53760\n"
"s_add_u32 s67, s67, 7168\n"
"v_mul_f32 v133, v110, v133\n"
"v_mul_f32 v134, v110, v134\n"
"v_mfma_f32_16x16x32_bf16 v[172:175], a[152:155], v[48:51], v[172:175]\n"
"v_exp_f32 v39, v39\n"
"v_mfma_f32_16x16x32_bf16 v[172:175], a[156:159], v[52:55], v[172:175]\n"
"ds_read_b128 a[92:95], v10 offset:54016\n"
"v_perm_b32 v58, v37, v36, s27\n"
"v_mul_f32 v135, v110, v135\n"
"v_cvt_pk_bf16_f32 v128, v128, v129\n"
"v_mfma_f32_16x16x32_bf16 v[24:27], a[96:99], a[32:35], v[72:75]\n"
"v_exp_f32 v40, v40\n"
"v_mfma_f32_16x16x32_bf16 v[24:27], a[100:103], a[36:39], v[24:27]\n"
"ds_read_b128 a[96:99], v10 offset:57344\n"
"v_perm_b32 v59, v39, v38, s27\n"
"v_cvt_pk_bf16_f32 v129, v130, v131\n"
"v_cvt_pk_bf16_f32 v130, v132, v133\n"
"v_mfma_f32_16x16x32_bf16 v[24:27], a[104:107], a[40:43], v[24:27]\n"
"v_exp_f32 v41, v41\n"
"v_mfma_f32_16x16x32_bf16 v[24:27], a[108:111], a[44:47], v[24:27]\n"
"ds_read_b128 a[100:103], v10 offset:57600\n"
"buffer_load_dwordx4 v14, %[wo], s67 offen offset:2048 lds\n"
"v_cvt_pk_bf16_f32 v131, v134, v135\n"
"v_mfma_f32_16x16x32_bf16 v[176:179], a[160:163], v[48:51], v[176:179]\n"
"v_exp_f32 v42, v42\n"
"v_mfma_f32_16x16x32_bf16 v[176:179], a[164:167], v[52:55], v[176:179]\n"
"ds_read_b128 a[104:107], v10 offset:57856\n"
"buffer_store_dwordx4 v[128:131], v116, s[56:59], 0 offen sc1\n"
"s_add_u32 s67, s67, 7168\n"
"v_mfma_f32_16x16x32_bf16 v[180:183], a[168:171], v[48:51], v[180:183]\n"
"v_exp_f32 v43, v43\n"
"v_mfma_f32_16x16x32_bf16 v[180:183], a[172:175], v[52:55], v[180:183]\n"
"ds_read_b128 a[108:111], v10 offset:58112\n"
"v_perm_b32 v60, v41, v40, s27\n"
"v_mul_f32 v136, v110, v136\n"
"v_mul_f32 v137, v110, v137\n"
"v_mfma_f32_16x16x32_bf16 v[28:31], a[112:115], a[32:35], v[72:75]\n"
"v_exp_f32 v44, v44\n"
"v_mfma_f32_16x16x32_bf16 v[28:31], a[116:119], a[36:39], v[28:31]\n"
"ds_read_b128 a[112:115], v10 offset:61440\n"
"v_perm_b32 v61, v43, v42, s27\n"
"v_mul_f32 v138, v110, v138\n"
"v_mul_f32 v139, v110, v139\n"
"v_mfma_f32_16x16x32_bf16 v[28:31], a[120:123], a[40:43], v[28:31]\n"
"v_exp_f32 v45, v45\n"
"v_mfma_f32_16x16x32_bf16 v[28:31], a[124:127], a[44:47], v[28:31]\n"
"ds_read_b128 a[116:119], v10 offset:61696\n"
"buffer_load_dwordx4 v15, %[wo], s67 offen offset:3072 lds\n"
"v_mul_f32 v140, v110, v140\n"
"v_mfma_f32_16x16x32_bf16 v[184:187], a[176:179], v[48:51], v[184:187]\n"
"v_exp_f32 v46, v46\n"
"v_mfma_f32_16x16x32_bf16 v[184:187], a[180:183], v[52:55], v[184:187]\n"
"ds_read_b128 a[120:123], v10 offset:61952\n"
"s_add_u32 s67, s67, 11264\n"
"v_mul_f32 v141, v110, v141\n"
"v_mul_f32 v142, v110, v142\n"
"v_mfma_f32_16x16x32_bf16 v[188:191], a[184:187], v[48:51], v[188:191]\n"
"v_exp_f32 v47, v47\n"
"v_mfma_f32_16x16x32_bf16 v[188:191], a[188:191], v[52:55], v[188:191]\n"
"ds_read_b128 a[124:127], v10 offset:62208\n"
"v_perm_b32 v62, v45, v44, s27\n"
"v_mul_f32 v143, v110, v143\n"
"v_cvt_pk_bf16_f32 v136, v136, v137\n"
"v_mfma_f32_16x16x32_bf16 v[84:87], v[96:99], v[48:51], v[84:87]\n"
"v_perm_b32 v63, v47, v46, s27\n"
"v_cvt_pk_bf16_f32 v137, v138, v139\n"
"v_cvt_pk_bf16_f32 v138, v140, v141\n"
"v_cvt_pk_bf16_f32 v139, v142, v143\n"
"v_mfma_f32_16x16x32_bf16 v[84:87], v[96:99], v[52:55], v[84:87]\n"
"buffer_store_dwordx4 v[136:139], v116, s[56:59], 0 offen offset:1024 sc1\n"
"s_waitcnt lgkmcnt(3)\n"
"v_mfma_f32_16x16x32_bf16 v[32:35], a[64:67], a[32:35], v[72:75]\n"
"v_exp_f32 v16, v16\n"
"v_mfma_f32_16x16x32_bf16 v[32:35], a[68:71], a[36:39], v[32:35]\n"
"ds_read_b128 a[64:67], v10 offset:32768\n"
"s_add_u32 m0, s24, 4096\n"
"v_add_f32 v104, v104, v105\n"
"v_add_f32 v106, v106, v107\n"
"v_add_f32 v104, v104, v106\n"
"v_mfma_f32_16x16x32_bf16 v[32:35], a[72:75], a[40:43], v[32:35]\n"
"v_exp_f32 v17, v17\n"
"v_mfma_f32_16x16x32_bf16 v[32:35], a[76:79], a[44:47], v[32:35]\n"
"ds_read_b128 a[68:71], v10 offset:33024\n"
"buffer_load_dwordx4 v14, %[wo], s67 offen lds\n"
"v_cvt_pk_bf16_f32 v104, 0, v104\n"
"v_add_f32 v104, %[one], v104\n"
"v_mfma_f32_16x16x32_bf16 v[160:163], a[192:195], v[56:59], v[160:163]\n"
"v_exp_f32 v18, v18\n"
"v_mfma_f32_16x16x32_bf16 v[160:163], a[196:199], v[60:63], v[160:163]\n"
"ds_read_b128 a[72:75], v10 offset:33280\n"
"s_add_u32 s67, s67, 7168\n"
"v_cvt_pk_bf16_f32 v104, 0, v104\n"
"v_mfma_f32_16x16x32_bf16 v[164:167], a[200:203], v[56:59], v[164:167]\n"
"v_exp_f32 v19, v19\n"
"v_mfma_f32_16x16x32_bf16 v[164:167], a[204:207], v[60:63], v[164:167]\n"
"ds_read_b128 a[76:79], v10 offset:33536\n"
"buffer_store_dword v104, v108, s[28:31], 0 offen sc1\n"
"v_perm_b32 v48, v17, v16, s27\n"
"v_mul_f32 v144, v110, v144\n"
"v_mfma_f32_16x16x32_bf16 v[36:39], a[80:83], a[32:35], v[72:75]\n"
"v_exp_f32 v20, v20\n"
"v_mfma_f32_16x16x32_bf16 v[36:39], a[84:87], a[36:39], v[36:39]\n"
"ds_read_b128 a[80:83], v10 offset:36864\n"
"v_perm_b32 v49, v19, v18, s27\n"
"v_mul_f32 v145, v110, v145\n"
"v_mul_f32 v146, v110, v146\n"
"v_mul_f32 v147, v110, v147\n"
"v_mfma_f32_16x16x32_bf16 v[36:39], a[88:91], a[40:43], v[36:39]\n"
"v_exp_f32 v21, v21\n"
"v_mfma_f32_16x16x32_bf16 v[36:39], a[92:95], a[44:47], v[36:39]\n"
"ds_read_b128 a[84:87], v10 offset:37120\n"
"buffer_load_dwordx4 v15, %[wo], s67 offen offset:1024 lds\n"
"v_mul_f32 v148, v110, v148\n"
"v_mul_f32 v149, v110, v149\n"
"v_mfma_f32_16x16x32_bf16 v[168:171], a[208:211], v[56:59], v[168:171]\n"
"v_exp_f32 v22, v22\n"
"v_mfma_f32_16x16x32_bf16 v[168:171], a[212:215], v[60:63], v[168:171]\n"
"ds_read_b128 a[88:91], v10 offset:37376\n"
"s_add_u32 s67, s67, 7168\n"
"v_mul_f32 v150, v110, v150\n"
"v_mul_f32 v151, v110, v151\n"
"v_cvt_pk_bf16_f32 v144, v144, v145\n"
"v_mfma_f32_16x16x32_bf16 v[172:175], a[216:219], v[56:59], v[172:175]\n"
"v_exp_f32 v23, v23\n"
"v_mfma_f32_16x16x32_bf16 v[172:175], a[220:223], v[60:63], v[172:175]\n"
"ds_read_b128 a[92:95], v10 offset:37632\n"
"v_perm_b32 v50, v21, v20, s27\n"
"v_cvt_pk_bf16_f32 v145, v146, v147\n"
"v_cvt_pk_bf16_f32 v146, v148, v149\n"
"v_cvt_pk_bf16_f32 v147, v150, v151\n"
"v_mfma_f32_16x16x32_bf16 v[40:43], a[96:99], a[32:35], v[72:75]\n"
"v_exp_f32 v24, v24\n"
"v_mfma_f32_16x16x32_bf16 v[40:43], a[100:103], a[36:39], v[40:43]\n"
"ds_read_b128 a[96:99], v10 offset:40960\n"
"buffer_store_dwordx4 v[144:147], v116, s[56:59], 0 offen offset:2048 sc1\n"
"v_perm_b32 v51, v23, v22, s27\n"
"v_mul_f32 v152, v110, v152\n"
"v_mfma_f32_16x16x32_bf16 v[40:43], a[104:107], a[40:43], v[40:43]\n"
"v_exp_f32 v25, v25\n"
"v_mfma_f32_16x16x32_bf16 v[40:43], a[108:111], a[44:47], v[40:43]\n"
"ds_read_b128 a[100:103], v10 offset:41216\n"
"buffer_load_dwordx4 v14, %[wo], s67 offen offset:2048 lds\n"
"v_mul_f32 v153, v110, v153\n"
"v_mul_f32 v154, v110, v154\n"
"v_mfma_f32_16x16x32_bf16 v[176:179], a[224:227], v[56:59], v[176:179]\n"
"v_exp_f32 v26, v26\n"
"v_mfma_f32_16x16x32_bf16 v[176:179], a[228:231], v[60:63], v[176:179]\n"
"ds_read_b128 a[104:107], v10 offset:41472\n"
"s_add_u32 s67, s67, 7168\n"
"v_mul_f32 v155, v110, v155\n"
"v_mul_f32 v156, v110, v156\n"
"v_mul_f32 v157, v110, v157\n"
"v_mfma_f32_16x16x32_bf16 v[180:183], a[232:235], v[56:59], v[180:183]\n"
"v_exp_f32 v27, v27\n"
"v_mfma_f32_16x16x32_bf16 v[180:183], a[236:239], v[60:63], v[180:183]\n"
"ds_read_b128 a[108:111], v10 offset:41728\n"
"v_perm_b32 v52, v25, v24, s27\n"
"v_mul_f32 v158, v110, v158\n"
"v_mul_f32 v159, v110, v159\n"
"v_cvt_pk_bf16_f32 v152, v152, v153\n"
"v_mfma_f32_16x16x32_bf16 v[44:47], a[112:115], a[32:35], v[72:75]\n"
"v_exp_f32 v28, v28\n"
"s_waitcnt lgkmcnt(4)\n"
"v_mfma_f32_16x16x32_bf16 v[44:47], a[116:119], a[36:39], v[44:47]\n"
"ds_read_b128 a[112:115], v10 offset:45056\n"
"v_perm_b32 v53, v27, v26, s27\n"
"v_cvt_pk_bf16_f32 v153, v154, v155\n"
"v_cvt_pk_bf16_f32 v154, v156, v157\n"
"v_cvt_pk_bf16_f32 v155, v158, v159\n"
"v_mfma_f32_16x16x32_bf16 v[44:47], a[120:123], a[40:43], v[44:47]\n"
"v_exp_f32 v29, v29\n"
"v_mfma_f32_16x16x32_bf16 v[44:47], a[124:127], a[44:47], v[44:47]\n"
"ds_read_b128 a[116:119], v10 offset:45312\n"
"buffer_load_dwordx4 v15, %[wo], s67 offen offset:3072 lds\n"
"buffer_store_dwordx4 v[152:155], v116, s[56:59], 0 offen offset:3072 sc1\n"
"v_mfma_f32_16x16x32_bf16 v[184:187], a[240:243], v[56:59], v[184:187]\n"
"v_exp_f32 v30, v30\n"
"v_mfma_f32_16x16x32_bf16 v[184:187], a[244:247], v[60:63], v[184:187]\n"
"ds_read_b128 a[120:123], v10 offset:45568\n"
"s_add_u32 s67, s67, 207872\n"
"v_mfma_f32_16x16x32_bf16 v[188:191], a[248:251], v[56:59], v[188:191]\n"
"v_exp_f32 v31, v31\n"
"v_mfma_f32_16x16x32_bf16 v[188:191], a[252:255], v[60:63], v[188:191]\n"
"ds_read_b128 a[124:127], v10 offset:45824\n"
"v_perm_b32 v54, v29, v28, s27\n"
"v_mfma_f32_16x16x32_bf16 v[84:87], v[96:99], v[56:59], v[84:87]\n"
"buffer_load_dwordx4 v[100:103], v13, %[code], 0 offen\n"
"v_mfma_f32_16x16x32_bf16 v[84:87], v[96:99], v[60:63], v[84:87]\n"
"v_perm_b32 v55, v31, v30, s27\n"
"v_mfma_f32_16x16x32_bf16 v[16:19], a[64:67], a[48:51], v[76:79]\n"
"v_exp_f32 v32, v32\n"
"v_mfma_f32_16x16x32_bf16 v[16:19], a[68:71], a[52:55], v[16:19]\n"
"ds_read_b128 a[64:67], v10 offset:49152\n"
"s_add_u32 m0, s24, 98304\n"
"v_mfma_f32_16x16x32_bf16 v[16:19], a[72:75], a[56:59], v[16:19]\n"
"v_exp_f32 v33, v33\n"
"v_mfma_f32_16x16x32_bf16 v[16:19], a[76:79], a[60:63], v[16:19]\n"
"ds_read_b128 a[68:71], v10 offset:49408\n"
"buffer_load_dwordx4 v14, %[wo], s67 offen lds\n"
"v_mfma_f32_16x16x32_bf16 v[192:195], a[128:131], v[48:51], v[192:195]\n"
"v_exp_f32 v34, v34\n"
"v_mfma_f32_16x16x32_bf16 v[192:195], a[132:135], v[52:55], v[192:195]\n"
"ds_read_b128 a[72:75], v10 offset:49664\n"
"s_add_u32 s67, s67, 7168\n"
"v_rcp_f32 v110, v84\n"
"v_mfma_f32_16x16x32_bf16 v[196:199], a[136:139], v[48:51], v[196:199]\n"
"v_exp_f32 v35, v35\n"
"v_mfma_f32_16x16x32_bf16 v[196:199], a[140:143], v[52:55], v[196:199]\n"
"ds_read_b128 a[76:79], v10 offset:49920\n"
"v_perm_b32 v56, v33, v32, s27\n"
"v_mul_f32 v160, v110, v160\n"
"v_mul_f32 v161, v110, v161\n"
"v_mfma_f32_16x16x32_bf16 v[20:23], a[80:83], a[48:51], v[76:79]\n"
"v_exp_f32 v36, v36\n"
"v_mfma_f32_16x16x32_bf16 v[20:23], a[84:87], a[52:55], v[20:23]\n"
"ds_read_b128 a[80:83], v10 offset:53248\n"
"v_perm_b32 v57, v35, v34, s27\n"
"v_mul_f32 v162, v110, v162\n"
"v_mul_f32 v163, v110, v163\n"
"v_mfma_f32_16x16x32_bf16 v[20:23], a[88:91], a[56:59], v[20:23]\n"
"v_exp_f32 v37, v37\n"
"v_mfma_f32_16x16x32_bf16 v[20:23], a[92:95], a[60:63], v[20:23]\n"
"ds_read_b128 a[84:87], v10 offset:53504\n"
"buffer_load_dwordx4 v15, %[wo], s67 offen offset:1024 lds\n"
"v_mul_f32 v164, v110, v164\n"
"v_mfma_f32_16x16x32_bf16 v[200:203], a[144:147], v[48:51], v[200:203]\n"
"v_exp_f32 v38, v38\n"
"v_mfma_f32_16x16x32_bf16 v[200:203], a[148:151], v[52:55], v[200:203]\n"
"ds_read_b128 a[88:91], v10 offset:53760\n"
"s_add_u32 s67, s67, 7168\n"
"v_mul_f32 v165, v110, v165\n"
"v_mul_f32 v166, v110, v166\n"
"v_mfma_f32_16x16x32_bf16 v[204:207], a[152:155], v[48:51], v[204:207]\n"
"v_exp_f32 v39, v39\n"
"v_mfma_f32_16x16x32_bf16 v[204:207], a[156:159], v[52:55], v[204:207]\n"
"ds_read_b128 a[92:95], v10 offset:54016\n"
"v_perm_b32 v58, v37, v36, s27\n"
"v_mul_f32 v167, v110, v167\n"
"v_cvt_pk_bf16_f32 v160, v160, v161\n"
"s_waitcnt lgkmcnt(4)\n"
"v_mfma_f32_16x16x32_bf16 v[24:27], a[96:99], a[48:51], v[76:79]\n"
"v_exp_f32 v40, v40\n"
"v_mfma_f32_16x16x32_bf16 v[24:27], a[100:103], a[52:55], v[24:27]\n"
"ds_read_b128 a[96:99], v10 offset:57344\n"
"v_perm_b32 v59, v39, v38, s27\n"
"v_cvt_pk_bf16_f32 v161, v162, v163\n"
"v_cvt_pk_bf16_f32 v162, v164, v165\n"
"v_mfma_f32_16x16x32_bf16 v[24:27], a[104:107], a[56:59], v[24:27]\n"
"v_exp_f32 v41, v41\n"
"v_mfma_f32_16x16x32_bf16 v[24:27], a[108:111], a[60:63], v[24:27]\n"
"ds_read_b128 a[100:103], v10 offset:57600\n"
"buffer_load_dwordx4 v14, %[wo], s67 offen offset:2048 lds\n"
"v_cvt_pk_bf16_f32 v163, v166, v167\n"
"v_mfma_f32_16x16x32_bf16 v[208:211], a[160:163], v[48:51], v[208:211]\n"
"v_exp_f32 v42, v42\n"
"v_mfma_f32_16x16x32_bf16 v[208:211], a[164:167], v[52:55], v[208:211]\n"
"ds_read_b128 a[104:107], v10 offset:57856\n"
"buffer_store_dwordx4 v[160:163], v117, s[56:59], 0 offen sc1\n"
"s_add_u32 s67, s67, 7168\n"
"v_mfma_f32_16x16x32_bf16 v[212:215], a[168:171], v[48:51], v[212:215]\n"
"v_exp_f32 v43, v43\n"
"v_mfma_f32_16x16x32_bf16 v[212:215], a[172:175], v[52:55], v[212:215]\n"
"ds_read_b128 a[108:111], v10 offset:58112\n"
"v_perm_b32 v60, v41, v40, s27\n"
"v_mul_f32 v168, v110, v168\n"
"v_mul_f32 v169, v110, v169\n"
"v_mfma_f32_16x16x32_bf16 v[28:31], a[112:115], a[48:51], v[76:79]\n"
"v_exp_f32 v44, v44\n"
"v_mfma_f32_16x16x32_bf16 v[28:31], a[116:119], a[52:55], v[28:31]\n"
"ds_read_b128 a[112:115], v10 offset:61440\n"
"v_perm_b32 v61, v43, v42, s27\n"
"v_mul_f32 v170, v110, v170\n"
"v_mul_f32 v171, v110, v171\n"
"v_mfma_f32_16x16x32_bf16 v[28:31], a[120:123], a[56:59], v[28:31]\n"
"v_exp_f32 v45, v45\n"
"v_mfma_f32_16x16x32_bf16 v[28:31], a[124:127], a[60:63], v[28:31]\n"
"ds_read_b128 a[116:119], v10 offset:61696\n"
"buffer_load_dwordx4 v15, %[wo], s67 offen offset:3072 lds\n"
"v_mul_f32 v172, v110, v172\n"
"v_mfma_f32_16x16x32_bf16 v[216:219], a[176:179], v[48:51], v[216:219]\n"
"v_exp_f32 v46, v46\n"
"v_mfma_f32_16x16x32_bf16 v[216:219], a[180:183], v[52:55], v[216:219]\n"
"ds_read_b128 a[120:123], v10 offset:61952\n"
"s_add_u32 s67, s67, 11264\n"
"v_mul_f32 v173, v110, v173\n"
"v_mul_f32 v174, v110, v174\n"
"v_mfma_f32_16x16x32_bf16 v[220:223], a[184:187], v[48:51], v[220:223]\n"
"v_exp_f32 v47, v47\n"
"v_mfma_f32_16x16x32_bf16 v[220:223], a[188:191], v[52:55], v[220:223]\n"
"ds_read_b128 a[124:127], v10 offset:62208\n"
"v_perm_b32 v62, v45, v44, s27\n"
"v_mul_f32 v175, v110, v175\n"
"v_cvt_pk_bf16_f32 v168, v168, v169\n"
"v_mfma_f32_16x16x32_bf16 v[88:91], v[96:99], v[48:51], v[88:91]\n"
"v_perm_b32 v63, v47, v46, s27\n"
"v_cvt_pk_bf16_f32 v169, v170, v171\n"
"v_cvt_pk_bf16_f32 v170, v172, v173\n"
"v_cvt_pk_bf16_f32 v171, v174, v175\n"
"v_mfma_f32_16x16x32_bf16 v[88:91], v[96:99], v[52:55], v[88:91]\n"
"buffer_store_dwordx4 v[168:171], v117, s[56:59], 0 offen offset:1024 sc1\n"
"v_mfma_f32_16x16x32_bf16 v[32:35], a[64:67], a[48:51], v[76:79]\n"
"v_exp_f32 v16, v16\n"
"v_mfma_f32_16x16x32_bf16 v[32:35], a[68:71], a[52:55], v[32:35]\n"
"s_add_u32 m0, s24, 102400\n"
"v_mul_f32 v176, v110, v176\n"
"v_mul_f32 v177, v110, v177\n"
"v_mfma_f32_16x16x32_bf16 v[32:35], a[72:75], a[56:59], v[32:35]\n"
"v_exp_f32 v17, v17\n"
"v_mfma_f32_16x16x32_bf16 v[32:35], a[76:79], a[60:63], v[32:35]\n"
"buffer_load_dwordx4 v14, %[wo], s67 offen lds\n"
"v_mul_f32 v178, v110, v178\n"
"v_mfma_f32_16x16x32_bf16 v[192:195], a[192:195], v[56:59], v[192:195]\n"
"v_exp_f32 v18, v18\n"
"v_mfma_f32_16x16x32_bf16 v[192:195], a[196:199], v[60:63], v[192:195]\n"
"s_add_u32 s67, s67, 7168\n"
"v_perm_b32 v48, v17, v16, s27\n"
"v_mul_f32 v179, v110, v179\n"
"v_mfma_f32_16x16x32_bf16 v[196:199], a[200:203], v[56:59], v[196:199]\n"
"v_exp_f32 v19, v19\n"
"v_mfma_f32_16x16x32_bf16 v[196:199], a[204:207], v[60:63], v[196:199]\n"
"v_perm_b32 v49, v19, v18, s27\n"
"v_mul_f32 v180, v110, v180\n"
"v_mul_f32 v181, v110, v181\n"
"s_waitcnt lgkmcnt(0)\n"
"v_mfma_f32_16x16x32_bf16 v[36:39], a[80:83], a[48:51], v[76:79]\n"
"v_exp_f32 v20, v20\n"
"v_mfma_f32_16x16x32_bf16 v[36:39], a[84:87], a[52:55], v[36:39]\n"
"v_mul_f32 v182, v110, v182\n"
"v_mul_f32 v183, v110, v183\n"
"v_cvt_pk_bf16_f32 v176, v176, v177\n"
"v_mfma_f32_16x16x32_bf16 v[36:39], a[88:91], a[56:59], v[36:39]\n"
"v_exp_f32 v21, v21\n"
"v_mfma_f32_16x16x32_bf16 v[36:39], a[92:95], a[60:63], v[36:39]\n"
"buffer_load_dwordx4 v15, %[wo], s67 offen offset:1024 lds\n"
"v_cvt_pk_bf16_f32 v177, v178, v179\n"
"v_mfma_f32_16x16x32_bf16 v[200:203], a[208:211], v[56:59], v[200:203]\n"
"v_exp_f32 v22, v22\n"
"v_mfma_f32_16x16x32_bf16 v[200:203], a[212:215], v[60:63], v[200:203]\n"
"s_add_u32 s67, s67, 7168\n"
"v_perm_b32 v50, v21, v20, s27\n"
"v_cvt_pk_bf16_f32 v178, v180, v181\n"
"v_mfma_f32_16x16x32_bf16 v[204:207], a[216:219], v[56:59], v[204:207]\n"
"v_exp_f32 v23, v23\n"
"v_mfma_f32_16x16x32_bf16 v[204:207], a[220:223], v[60:63], v[204:207]\n"
"v_perm_b32 v51, v23, v22, s27\n"
"v_cvt_pk_bf16_f32 v179, v182, v183\n"
"v_mfma_f32_16x16x32_bf16 v[40:43], a[96:99], a[48:51], v[76:79]\n"
"v_exp_f32 v24, v24\n"
"v_mfma_f32_16x16x32_bf16 v[40:43], a[100:103], a[52:55], v[40:43]\n"
"buffer_store_dwordx4 v[176:179], v117, s[56:59], 0 offen offset:2048 sc1\n"
"v_mul_f32 v184, v110, v184\n"
"v_mfma_f32_16x16x32_bf16 v[40:43], a[104:107], a[56:59], v[40:43]\n"
"v_exp_f32 v25, v25\n"
"v_mfma_f32_16x16x32_bf16 v[40:43], a[108:111], a[60:63], v[40:43]\n"
"buffer_load_dwordx4 v14, %[wo], s67 offen offset:2048 lds\n"
"v_mul_f32 v185, v110, v185\n"
"v_mfma_f32_16x16x32_bf16 v[208:211], a[224:227], v[56:59], v[208:211]\n"
"v_exp_f32 v26, v26\n"
"v_mfma_f32_16x16x32_bf16 v[208:211], a[228:231], v[60:63], v[208:211]\n"
"s_add_u32 s67, s67, 7168\n"
"v_perm_b32 v52, v25, v24, s27\n"
"v_mul_f32 v186, v110, v186\n"
"v_mfma_f32_16x16x32_bf16 v[212:215], a[232:235], v[56:59], v[212:215]\n"
"v_exp_f32 v27, v27\n"
"v_mfma_f32_16x16x32_bf16 v[212:215], a[236:239], v[60:63], v[212:215]\n"
"v_perm_b32 v53, v27, v26, s27\n"
"v_mul_f32 v187, v110, v187\n"
"v_mul_f32 v188, v110, v188\n"
"v_mfma_f32_16x16x32_bf16 v[44:47], a[112:115], a[48:51], v[76:79]\n"
"v_exp_f32 v28, v28\n"
"v_mfma_f32_16x16x32_bf16 v[44:47], a[116:119], a[52:55], v[44:47]\n"
"v_mul_f32 v189, v110, v189\n"
"v_mul_f32 v190, v110, v190\n"
"v_mul_f32 v191, v110, v191\n"
"v_mfma_f32_16x16x32_bf16 v[44:47], a[120:123], a[56:59], v[44:47]\n"
"v_exp_f32 v29, v29\n"
"v_mfma_f32_16x16x32_bf16 v[44:47], a[124:127], a[60:63], v[44:47]\n"
"buffer_load_dwordx4 v15, %[wo], s67 offen offset:3072 lds\n"
"v_cvt_pk_bf16_f32 v184, v184, v185\n"
"v_mfma_f32_16x16x32_bf16 v[216:219], a[240:243], v[56:59], v[216:219]\n"
"v_exp_f32 v30, v30\n"
"v_mfma_f32_16x16x32_bf16 v[216:219], a[244:247], v[60:63], v[216:219]\n"
"s_add_u32 s67, s67, 207872\n"
"v_perm_b32 v54, v29, v28, s27\n"
"v_cvt_pk_bf16_f32 v185, v186, v187\n"
"v_mfma_f32_16x16x32_bf16 v[220:223], a[248:251], v[56:59], v[220:223]\n"
"v_exp_f32 v31, v31\n"
"v_mfma_f32_16x16x32_bf16 v[220:223], a[252:255], v[60:63], v[220:223]\n"
"v_perm_b32 v55, v31, v30, s27\n"
"v_cvt_pk_bf16_f32 v186, v188, v189\n"
"v_cvt_pk_bf16_f32 v187, v190, v191\n"
"v_mfma_f32_16x16x32_bf16 v[88:91], v[96:99], v[56:59], v[88:91]\n"
"buffer_store_dwordx4 v[184:187], v117, s[56:59], 0 offen offset:3072 sc1\n"
"v_mfma_f32_16x16x32_bf16 v[88:91], v[96:99], v[60:63], v[88:91]\n"
"v_mfma_f32_16x16x32_bf16 v[224:227], a[128:131], v[48:51], v[224:227]\n"
"v_exp_f32 v32, v32\n"
"v_exp_f32 v33, v33\n"
"v_mfma_f32_16x16x32_bf16 v[224:227], a[132:135], v[52:55], v[224:227]\n"
"v_exp_f32 v34, v34\n"
"v_exp_f32 v35, v35\n"
"v_mfma_f32_16x16x32_bf16 v[228:231], a[136:139], v[48:51], v[228:231]\n"
"v_perm_b32 v56, v33, v32, s27\n"
"v_exp_f32 v36, v36\n"
"v_mfma_f32_16x16x32_bf16 v[228:231], a[140:143], v[52:55], v[228:231]\n"
"v_exp_f32 v37, v37\n"
"v_perm_b32 v57, v35, v34, s27\n"
"v_mfma_f32_16x16x32_bf16 v[232:235], a[144:147], v[48:51], v[232:235]\n"
"v_exp_f32 v38, v38\n"
"v_exp_f32 v39, v39\n"
"v_mfma_f32_16x16x32_bf16 v[232:235], a[148:151], v[52:55], v[232:235]\n"
"v_perm_b32 v58, v37, v36, s27\n"
"v_exp_f32 v40, v40\n"
"v_mfma_f32_16x16x32_bf16 v[236:239], a[152:155], v[48:51], v[236:239]\n"
"v_exp_f32 v41, v41\n"
"v_perm_b32 v59, v39, v38, s27\n"
"v_mfma_f32_16x16x32_bf16 v[236:239], a[156:159], v[52:55], v[236:239]\n"
"v_exp_f32 v42, v42\n"
"v_exp_f32 v43, v43\n"
"v_mfma_f32_16x16x32_bf16 v[240:243], a[160:163], v[48:51], v[240:243]\n"
"v_perm_b32 v60, v41, v40, s27\n"
"v_exp_f32 v44, v44\n"
"v_mfma_f32_16x16x32_bf16 v[240:243], a[164:167], v[52:55], v[240:243]\n"
"v_exp_f32 v45, v45\n"
"v_perm_b32 v61, v43, v42, s27\n"
"v_mfma_f32_16x16x32_bf16 v[244:247], a[168:171], v[48:51], v[244:247]\n"
"v_exp_f32 v46, v46\n"
"v_exp_f32 v47, v47\n"
"v_mfma_f32_16x16x32_bf16 v[244:247], a[172:175], v[52:55], v[244:247]\n"
"v_perm_b32 v62, v45, v44, s27\n"
"v_perm_b32 v63, v47, v46, s27\n"
"v_rcp_f32 v110, v88\n"
"v_mfma_f32_16x16x32_bf16 v[248:251], a[176:179], v[48:51], v[248:251]\n"
"v_mul_f32 v192, v110, v192\n"
"v_mul_f32 v193, v110, v193\n"
"v_mul_f32 v194, v110, v194\n"
"v_mul_f32 v195, v110, v195\n"
"v_mfma_f32_16x16x32_bf16 v[248:251], a[180:183], v[52:55], v[248:251]\n"
"v_mul_f32 v196, v110, v196\n"
"v_mul_f32 v197, v110, v197\n"
"v_mul_f32 v198, v110, v198\n"
"v_mul_f32 v199, v110, v199\n"
"v_mfma_f32_16x16x32_bf16 v[252:255], a[184:187], v[48:51], v[252:255]\n"
"v_cvt_pk_bf16_f32 v192, v192, v193\n"
"v_cvt_pk_bf16_f32 v193, v194, v195\n"
"v_cvt_pk_bf16_f32 v194, v196, v197\n"
"v_cvt_pk_bf16_f32 v195, v198, v199\n"
"v_mfma_f32_16x16x32_bf16 v[252:255], a[188:191], v[52:55], v[252:255]\n"
"buffer_store_dwordx4 v[192:195], v118, s[56:59], 0 offen sc1\n"
"v_mfma_f32_16x16x32_bf16 v[92:95], v[96:99], v[48:51], v[92:95]\n"
"v_mfma_f32_16x16x32_bf16 v[92:95], v[96:99], v[52:55], v[92:95]\n"
"v_mfma_f32_16x16x32_bf16 v[224:227], a[192:195], v[56:59], v[224:227]\n"
"v_mul_f32 v200, v110, v200\n"
"v_mul_f32 v201, v110, v201\n"
"v_mul_f32 v202, v110, v202\n"
"v_mfma_f32_16x16x32_bf16 v[224:227], a[196:199], v[60:63], v[224:227]\n"
"v_mul_f32 v203, v110, v203\n"
"v_mul_f32 v204, v110, v204\n"
"v_mul_f32 v205, v110, v205\n"
"v_mfma_f32_16x16x32_bf16 v[228:231], a[200:203], v[56:59], v[228:231]\n"
"v_mul_f32 v206, v110, v206\n"
"v_mul_f32 v207, v110, v207\n"
"v_cvt_pk_bf16_f32 v200, v200, v201\n"
"v_mfma_f32_16x16x32_bf16 v[228:231], a[204:207], v[60:63], v[228:231]\n"
"v_cvt_pk_bf16_f32 v201, v202, v203\n"
"v_cvt_pk_bf16_f32 v202, v204, v205\n"
"v_cvt_pk_bf16_f32 v203, v206, v207\n"
"v_mfma_f32_16x16x32_bf16 v[232:235], a[208:211], v[56:59], v[232:235]\n"
"buffer_store_dwordx4 v[200:203], v118, s[56:59], 0 offen offset:1024 sc1\n"
"v_mul_f32 v208, v110, v208\n"
"v_mfma_f32_16x16x32_bf16 v[232:235], a[212:215], v[60:63], v[232:235]\n"
"v_mul_f32 v209, v110, v209\n"
"v_mul_f32 v210, v110, v210\n"
"v_mul_f32 v211, v110, v211\n"
"v_mfma_f32_16x16x32_bf16 v[236:239], a[216:219], v[56:59], v[236:239]\n"
"v_mul_f32 v212, v110, v212\n"
"v_mul_f32 v213, v110, v213\n"
"v_mul_f32 v214, v110, v214\n"
"v_mfma_f32_16x16x32_bf16 v[236:239], a[220:223], v[60:63], v[236:239]\n"
"v_mul_f32 v215, v110, v215\n"
"v_cvt_pk_bf16_f32 v208, v208, v209\n"
"v_cvt_pk_bf16_f32 v209, v210, v211\n"
"v_mfma_f32_16x16x32_bf16 v[240:243], a[224:227], v[56:59], v[240:243]\n"
"v_cvt_pk_bf16_f32 v210, v212, v213\n"
"v_cvt_pk_bf16_f32 v211, v214, v215\n"
"v_mfma_f32_16x16x32_bf16 v[240:243], a[228:231], v[60:63], v[240:243]\n"
"buffer_store_dwordx4 v[208:211], v118, s[56:59], 0 offen offset:2048 sc1\n"
"v_mul_f32 v216, v110, v216\n"
"v_mfma_f32_16x16x32_bf16 v[244:247], a[232:235], v[56:59], v[244:247]\n"
"v_mul_f32 v217, v110, v217\n"
"v_mul_f32 v218, v110, v218\n"
"v_mul_f32 v219, v110, v219\n"
"v_mfma_f32_16x16x32_bf16 v[244:247], a[236:239], v[60:63], v[244:247]\n"
"v_mul_f32 v220, v110, v220\n"
"v_mul_f32 v221, v110, v221\n"
"v_mul_f32 v222, v110, v222\n"
"v_mfma_f32_16x16x32_bf16 v[248:251], a[240:243], v[56:59], v[248:251]\n"
"v_mul_f32 v223, v110, v223\n"
"v_cvt_pk_bf16_f32 v216, v216, v217\n"
"v_cvt_pk_bf16_f32 v217, v218, v219\n"
"v_mfma_f32_16x16x32_bf16 v[248:251], a[244:247], v[60:63], v[248:251]\n"
"v_cvt_pk_bf16_f32 v218, v220, v221\n"
"v_cvt_pk_bf16_f32 v219, v222, v223\n"
"v_mfma_f32_16x16x32_bf16 v[252:255], a[248:251], v[56:59], v[252:255]\n"
"buffer_store_dwordx4 v[216:219], v118, s[56:59], 0 offen offset:3072 sc1\n"
"v_mfma_f32_16x16x32_bf16 v[252:255], a[252:255], v[60:63], v[252:255]\n"
"v_mfma_f32_16x16x32_bf16 v[92:95], v[96:99], v[56:59], v[92:95]\n"
"v_mfma_f32_16x16x32_bf16 v[92:95], v[96:99], v[60:63], v[92:95]\n"
"s_nop 7\n"
"v_rcp_f32 v110, v92\n"
"s_nop 0\n"
"v_mov_b32 v111, v110\n"
"v_pk_mul_f32 v[224:225], v[224:225], v[110:111]\n"
"v_pk_mul_f32 v[226:227], v[226:227], v[110:111]\n"
"v_pk_mul_f32 v[228:229], v[228:229], v[110:111]\n"
"v_pk_mul_f32 v[230:231], v[230:231], v[110:111]\n"
"v_cvt_pk_bf16_f32 v224, v224, v225\n"
"v_cvt_pk_bf16_f32 v225, v226, v227\n"
"v_cvt_pk_bf16_f32 v226, v228, v229\n"
"v_cvt_pk_bf16_f32 v227, v230, v231\n"
"buffer_store_dwordx4 v[224:227], v119, s[56:59], 0 offen sc1\n"
"v_pk_mul_f32 v[232:233], v[232:233], v[110:111]\n"
"v_pk_mul_f32 v[234:235], v[234:235], v[110:111]\n"
"v_pk_mul_f32 v[236:237], v[236:237], v[110:111]\n"
"v_pk_mul_f32 v[238:239], v[238:239], v[110:111]\n"
"v_cvt_pk_bf16_f32 v232, v232, v233\n"
"v_cvt_pk_bf16_f32 v233, v234, v235\n"
"v_cvt_pk_bf16_f32 v234, v236, v237\n"
"v_cvt_pk_bf16_f32 v235, v238, v239\n"
"buffer_store_dwordx4 v[232:235], v119, s[56:59], 0 offen offset:1024 sc1\n"
"v_pk_mul_f32 v[240:241], v[240:241], v[110:111]\n"
"v_pk_mul_f32 v[242:243], v[242:243], v[110:111]\n"
"v_pk_mul_f32 v[244:245], v[244:245], v[110:111]\n"
"v_pk_mul_f32 v[246:247], v[246:247], v[110:111]\n"
"v_cvt_pk_bf16_f32 v240, v240, v241\n"
"v_cvt_pk_bf16_f32 v241, v242, v243\n"
"v_cvt_pk_bf16_f32 v242, v244, v245\n"
"v_cvt_pk_bf16_f32 v243, v246, v247\n"
"buffer_store_dwordx4 v[240:243], v119, s[56:59], 0 offen offset:2048 sc1\n"
"v_pk_mul_f32 v[248:249], v[248:249], v[110:111]\n"
"v_pk_mul_f32 v[250:251], v[250:251], v[110:111]\n"
"v_pk_mul_f32 v[252:253], v[252:253], v[110:111]\n"
"v_pk_mul_f32 v[254:255], v[254:255], v[110:111]\n"
"v_cvt_pk_bf16_f32 v248, v248, v249\n"
"v_cvt_pk_bf16_f32 v249, v250, v251\n"
"v_cvt_pk_bf16_f32 v250, v252, v253\n"
"v_cvt_pk_bf16_f32 v251, v254, v255\n"
"buffer_store_dwordx4 v[248:251], v119, s[56:59], 0 offen offset:3072 sc1\n"
"v_mfma_f32_16x16x32_bf16 v[64:67], v[128:131], v[128:131], 0\n"
"v_lshrrev_b32 v16, 4, v13\n"
"v_mfma_f32_16x16x32_bf16 v[64:67], v[136:139], v[136:139], v[64:67]\n"
"v_bfe_u32 v17, v16, 2, 2\n"
"v_mfma_f32_16x16x32_bf16 v[64:67], v[144:147], v[144:147], v[64:67]\n"
"v_lshrrev_b32 v18, 4, v16\n"
"v_mfma_f32_16x16x32_bf16 v[64:67], v[152:155], v[152:155], v[64:67]\n"
"v_cmp_eq_u32 s[74:75], v18, v17\n"
"v_mfma_f32_16x16x32_bf16 v[68:71], v[160:163], v[160:163], 0\n"
"v_and_b32 v17, 3, v16\n"
"v_mfma_f32_16x16x32_bf16 v[68:71], v[168:171], v[168:171], v[68:71]\n"
"v_cmp_eq_u32 s[24:25], 1, v17\n"
"v_mfma_f32_16x16x32_bf16 v[68:71], v[176:179], v[176:179], v[68:71]\n"
"v_cmp_eq_u32 s[32:33], 2, v17\n"
"v_mfma_f32_16x16x32_bf16 v[68:71], v[184:187], v[184:187], v[68:71]\n"
"v_cmp_eq_u32 s[34:35], 3, v17\n"
"v_mfma_f32_16x16x32_bf16 v[72:75], v[192:195], v[192:195], 0\n"
"s_mov_b32 s68, 0x7b800000\n"
"v_mfma_f32_16x16x32_bf16 v[72:75], v[200:203], v[200:203], v[72:75]\n"
"s_mov_b32 s69, 0x31800000\n"
"v_mfma_f32_16x16x32_bf16 v[72:75], v[208:211], v[208:211], v[72:75]\n"
"v_add_f32 v20, v80, v84\n"
"v_mfma_f32_16x16x32_bf16 v[72:75], v[216:219], v[216:219], v[72:75]\n"
"v_add_f32 v21, v88, v92\n"
"v_mfma_f32_16x16x32_bf16 v[76:79], v[224:227], v[224:227], 0\n"
"v_add_f32 v20, v20, v21\n"
"v_mfma_f32_16x16x32_bf16 v[76:79], v[232:235], v[232:235], v[76:79]\n"
"v_mfma_f32_16x16x32_bf16 v[76:79], v[240:243], v[240:243], v[76:79]\n"
"v_mfma_f32_16x16x32_bf16 v[76:79], v[248:251], v[248:251], v[76:79]\n"
"v_cndmask_b32 v64, v64, v65, s[24:25]\n"
"v_cndmask_b32 v68, v68, v69, s[24:25]\n"
"v_cndmask_b32 v72, v72, v73, s[24:25]\n"
"s_nop 4\n"
"v_cndmask_b32 v76, v76, v77, s[24:25]\n"
"v_cndmask_b32 v64, v64, v66, s[32:33]\n"
"v_cndmask_b32 v68, v68, v70, s[32:33]\n"
"v_cndmask_b32 v72, v72, v74, s[32:33]\n"
"v_cndmask_b32 v76, v76, v78, s[32:33]\n"
"v_cndmask_b32 v64, v64, v67, s[34:35]\n"
"v_cndmask_b32 v68, v68, v71, s[34:35]\n"
"v_cndmask_b32 v72, v72, v75, s[34:35]\n"
"v_cndmask_b32 v76, v76, v79, s[34:35]\n"
"v_min3_f32 v22, v64, v68, v72\n"
"v_add_f32 v21, v64, v68\n"
"v_add_f32 v23, v72, v76\n"
"v_min_f32 v22, v22, v76\n"
"v_add_f32 v20, v20, v21\n"
"v_add_f32 v20, v20, v23\n"
"v_cmp_gt_f32 vcc, s69, v22\n"
"v_cmp_ngt_f32 s[72:73], s68, v20\n"
"s_nop 1\n"
"s_or_b64 s[72:73], s[72:73], vcc\n"
"s_and_b64 s[74:75], s[74:75], s[72:73]\n"
"s_or_b32 s68, s74, s75\n"
"s_mov_b32 %[bad], s68\n"
"s_waitcnt vmcnt(20) lgkmcnt(0)\n"
    : [bad] "=s"(bad), "+{v[100:115]}"(va), "+{v[116:119]}"(vb), "+{v[120:127]}"(vc8), "+{s[60:63]}"(so)
    : "{s[40:43]}"(sx), "{s[44:47]}"(sw), [k] "s"(kp), [v] "s"(vp), [o] "s"(op), [p] "s"(pp), [pn] "s"(pn), [r] "s"(rp),
      [gw] "s"(wmod + (size_t)wave * 256 * 6144 + col0), [gs] "s"(sil + qb * 1024 + wave * 256), [m] "s"(mod + col0), [code] "s"(code_srd(bid, wave)), [wo] "s"(mksrd(wo + (b & 1) * 2048 * 1024, 4 << 20)), [o1] "s"(__builtin_amdgcn_readfirstlane(wave * 65536 + (b >> 5) * 256)), [one] "s"((col0 >> 10) == 4 ? 1.f : 0.f), "{s70}"(wave), "s"((unsigned)(size_t)lds) : "v8", "v9", "v10", "v11", "v12", "v13", "v14", "v15", "v16", "v17", "v18", "v19", "v20", "v21", "v22", "v23", "v24", "v25", "v26", "v27", "v28", "v29", "v30", "v31", "v32", "v33", "v34", "v35", "v36", "v37", "v38", "v39", "v40", "v41", "v42", "v43", "v44", "v45", "v46", "v47", "v48", "v49", "v50", "v51", "v52", "v53", "v54", "v55", "v56", "v57", "v58", "v59", "v60", "v61", "v62", "v63", "v64", "v65", "v66", "v67", "v68", "v69", "v70", "v71", "v72", "v73", "v74", "v75", "v76", "v77", "v78", "v79", "v80", "v81", "v82", "v83", "v84", "v85", "v86", "v87", "v88", "v89", "v90", "v91", "v92", "v93", "v94", "v95", "v96", "v97", "v98", "v99", "v128", "v129", "v130", "v131", "v132", "v133", "v134", "v135", "v136", "v137", "v138", "v139", "v140", "v141", "v142", "v143", "v144", "v145", "v146", "v147", "v148", "v149", "v150", "v151", "v152", "v153", "v154", "v155", "v156", "v157", "v158", "v159", "v160", "v161", "v162", "v163", "v164", "v165", "v166", "v167", "v168", "v169", "v170", "v171", "v172", "v173", "v174", "v175", "v176", "v177", "v178", "v179", "v180", "v181", "v182", "v183", "v184", "v185", "v186", "v187", "v188", "v189", "v190", "v191", "v192", "v193", "v194", "v195", "v196", "v197", "v198", "v199", "v200", "v201", "v202", "v203", "v204", "v205", "v206", "v207", "v208", "v209", "v210", "v211", "v212", "v213", "v214", "v215", "v216", "v217", "v218", "v219", "v220", "v221", "v222", "v223", "v224", "v225", "v226", "v227", "v228", "v229", "v230", "v231", "v232", "v233", "v234", "v235", "v236", "v237", "v238", "v239", "v240", "v241", "v242", "v243", "v244", "v245", "v246", "v247", "v248", "v249", "v250", "v251", "v252", "v253", "v254", "v255", "a0", "a1", "a2", "a3", "a4", "a5", "a6", "a7", "a8", "a9", "a10", "a11", "a12", "a13", "a14", "a15", "a16", "a17", "a18", "a19", "a20", "a21", "a22", "a23", "a24", "a25", "a26", "a27", "a28", "a29", "a30", "a31", "a32", "a33", "a34", "a35", "a36", "a37", "a38", "a39", "a40", "a41", "a42", "a43", "a44", "a45", "a46", "a47", "a48", "a49", "a50", "a51", "a52", "a53", "a54", "a55", "a56", "a57", "a58", "a59", "a60", "a61", "a62", "a63", "a64", "a65", "a66", "a67", "a68", "a69", "a70", "a71", "a72", "a73", "a74", "a75", "a76", "a77", "a78", "a79", "a80", "a81", "a82", "a83", "a84", "a85", "a86", "a87", "a88", "a89", "a90", "a91", "a92", "a93", "a94", "a95", "a96", "a97", "a98", "a99", "a100", "a101", "a102", "a103", "a104", "a105", "a106", "a107", "a108", "a109", "a110", "a111", "a112", "a113", "a114", "a115", "a116", "a117", "a118", "a119", "a120", "a121", "a122", "a123", "a124", "a125", "a126", "a127", "a128", "a129", "a130", "a131", "a132", "a133", "a134", "a135", "a136", "a137", "a138", "a139", "a140", "a141", "a142", "a143", "a144", "a145", "a146", "a147", "a148", "a149", "a150", "a151", "a152", "a153", "a154", "a155", "a156", "a157", "a158", "a159", "a160", "a161", "a162", "a163", "a164", "a165", "a166", "a167", "a168", "a169", "a170", "a171", "a172", "a173", "a174", "a175", "a176", "a177", "a178", "a179", "a180", "a181", "a182", "a183", "a184", "a185", "a186", "a187", "a188", "a189", "a190", "a191", "a192", "a193", "a194", "a195", "a196", "a197", "a198", "a199", "a200", "a201", "a202", "a203", "a204", "a205", "a206", "a207", "a208", "a209", "a210", "a211", "a212", "a213", "a214", "a215", "a216", "a217", "a218", "a219", "a220", "a221", "a222", "a223", "a224", "a225", "a226", "a227", "a228", "a229", "a230", "a231", "a232", "a233", "a234", "a235", "a236", "a237", "a238", "a239", "a240", "a241", "a242", "a243", "a244", "a245", "a246", "a247", "a248", "a249", "a250", "a251", "a252", "a253", "a254", "a255", "s24", "s25", "s26", "s27", "s28", "s29", "s30", "s31", "s32", "s33", "s34", "s35", "s36", "s37", "s38", "s39", "s48", "s49", "s50", "s51", "s52", "s53", "s54", "s55", "s56", "s57", "s58", "s59", "s64", "s65", "s66", "s67", "s68", "s69", "s71", "s72", "s73", "s74", "s75", "s76", "s77", "s78", "s79", "vcc", "scc", "memory");

  if (bad) { asm volatile("s_waitcnt vmcnt(0)" ::: "memory");
    PAR; const unsigned hd = (bid & 7) * 4 + ((bid >> 3) & 3), r0 = (bid >> 5) * 16 + wave * 4;
    attn_slow((const u16*)(ws + O_H) + r0 * (HS / 2), P->wq + hd * 128, P->kc + (size_t)hd * 2048 * 128, P->vc + (size_t)hd * 2048 * 128, (u16*)(ws + O_O) + r0 * (OS / 2) + hd * 2048, tid & 63); }
}

DI void s2_map(unsigned b, unsigned& pm, unsigned& pn, unsigned& s) { pm = ((b & 7) >> 1) * 4 + ((b >> 3) & 3); pn = b >> 5; s = b & 1; }
DI u32x4 pk8(const f32x32& v, int c) { return (u32x4){pk2(v[8 * c], v[8 * c + 1]), pk2(v[8 * c + 2], v[8 * c + 3]), pk2(v[8 * c + 4], v[8 * c + 5]), pk2(v[8 * c + 6], v[8 * c + 7])}; }

DI void phase_oproj(char* lds, unsigned wv) {
  PAR; const u16 *wo = P->wo, *x = P->x; const u16* oft = (const u16*)(ws + O_O); const float* mod = (const float*)(ws + O_MOD); char* scr = ws + O_SCR; u16* zft = (u16*)(ws + O_Z);
  edge_signal<0>(tid, ws, 40 + (bid & 7), 32, 8);
  unsigned pm, pn, s; s2_map(bid, pm, pn, s);
  const unsigned kp = wave & 1, rb0 = pm * 8 + (wave >> 1) * 4, rbk = pm * 8 + wave * 2 + s;
  const u32x4 xr = {(2 * kp + s) * OS, (2 * kp + 1 - s) * OS, (2 - 2 * kp + s) * OS, (3 - 2 * kp - s) * OS};
  u32x16 va; u32x4 so;
  asm volatile(
"v_mbcnt_lo_u32_b32 v116, -1, 0\n"
"v_mbcnt_hi_u32_b32 v116, -1, v116\n"
"v_bfe_u32 v117, v116, 2, 2\n"
"v_lshrrev_b32 v119, 4, v116\n"
"v_and_b32 v118, 3, v116\n"
"s_mov_b32 s60, 0\n"
"s_mul_i32 s61, s70, 65536\n"
"s_lshl_b32 s62, s70, 13\n"
"v_lshlrev_b32 v122, 4, v118\n"
"v_lshl_add_u32 v122, v119, 6, v122\n"
"v_xor_b32 v123, 32, v122\n"
"v_mul_u32_u24 v120, 2048, v117\n"
"v_add_u32 v104, v120, v122\n"
"v_add_u32 v105, v120, v123\n"
"v_add_u32 v105, 7168, v105\n"
"v_add_u32 v106, v120, v122\n"
"v_add_u32 v106, 14336, v106\n"
"v_add_u32 v107, v120, v123\n"
"v_add_u32 v107, 21504, v107\n"
"v_lshl_add_u32 v100, v116, 4, s72\n"
"v_lshl_add_u32 v101, v116, 4, s73\n"
"v_lshl_add_u32 v102, v116, 4, s74\n"
"v_lshl_add_u32 v103, v116, 4, s75\n"
"v_lshlrev_b32 v120, 3, v118\n"
"v_lshl_add_u32 v120, v117, 6, v120\n"
"v_lshl_add_u32 v120, v119, 10, v120\n"
"v_add_u32 v120, s71, v120\n"
"v_and_b32 v121, 1, v119\n"
"v_lshl_add_u32 v108, v121, 5, v120\n"
"v_xor_b32 v109, 32, v108\n"
"v_lshlrev_b32 v112, 4, v116\n"
"v_add_u32 v110, 65536, v108\n"
"v_add_u32 v111, 65536, v109\n"
    : "={v[100:115]}"(va), "={s[60:63]}"(so) : "{s70}"(wave), "{s71}"(kp * 16384), "{s[72:75]}"(xr) : "scc", "v116", "v117", "v118", "v119", "v120", "v121", "v122", "v123");
  const u32x4 sx = mksrd(oft + rb0 * (OS / 2) + s * 32768 + kp * 1024, 3 * OS + 65536), sw = mksrd(wo + s * 2048 * 1024 + pn * 128, 2047 * 2048 + 256);
  asm volatile(
"s_add_u32 s61, s61, 524288\n"
"s_add_u32 m0, s62, 32768\n"
"s_nop 0\n"
"buffer_load_dwordx4 v104, s[44:47], s61 offen lds\n"
"buffer_load_dwordx4 v105, s[44:47], s61 offen offset:1024 lds\n"
"buffer_load_dwordx4 v106, s[44:47], s61 offen offset:2048 lds\n"
"buffer_load_dwordx4 v107, s[44:47], s61 offen offset:3072 lds\n"
"s_add_u32 s61, s61, 32768\n"
"s_add_u32 m0, s62, 36864\n"
"s_nop 0\n"
"buffer_load_dwordx4 v104, s[44:47], s61 offen lds\n"
"buffer_load_dwordx4 v105, s[44:47], s61 offen offset:1024 lds\n"
"buffer_load_dwordx4 v106, s[44:47], s61 offen offset:2048 lds\n"
"buffer_load_dwordx4 v107, s[44:47], s61 offen offset:3072 lds\n"
"s_add_u32 s61, s61, 229376\n"
"s_add_u32 m0, s62, 65536\n"
"s_nop 0\n"
"buffer_load_dwordx4 v104, s[44:47], s61 offen lds\n"
"buffer_load_dwordx4 v105, s[44:47], s61 offen offset:1024 lds\n"
"buffer_load_dwordx4 v106, s[44:47], s61 offen offset:2048 lds\n"
"buffer_load_dwordx4 v107, s[44:47], s61 offen offset:3072 lds\n"
"s_add_u32 s61, s61, 32768\n"
"s_add_u32 m0, s62, 69632\n"
"s_nop 0\n"
"buffer_load_dwordx4 v104, s[44:47], s61 offen lds\n"
"buffer_load_dwordx4 v105, s[44:47], s61 offen offset:1024 lds\n"
"buffer_load_dwordx4 v106, s[44:47], s61 offen offset:2048 lds\n"
"buffer_load_dwordx4 v107, s[44:47], s61 offen offset:3072 lds\n"
"s_add_u32 s61, s61, 229376\n"
    : "+{s[60:63]}"(so) : "{v[100:115]}"(va), "{s[44:47]}"(sw) : "scc", "memory");
  __builtin_amdgcn_sched_barrier(0);
  const int lane = tid & 63, i = lane & 15, g = lane >> 4;
  const u32x4 se = mksrd(x + rbk * 16 * 1024 + pn * 128, 15 * 2048 + 256);
  f32x32 keep; const unsigned tile = pm * 8 + pn;
  u32x4 xo;
#pragma unroll
  for (int p = 0; p < 4; ++p) xo[p] = (4 * p + g) * 2048 + ((i ^ (4 * p + g)) << 4) - 1024 * p;
  const u32x4 sp = mksrd(mod + 2048 + pn * 128, 2 * 4096 + 512);
  const u32x2 po = {(unsigned)((lane >> 5) * 4096 + (lane & 31) * 16), (unsigned)(8192 - 1024 + (lane & 31) * 16)};
  edge_wait(tid, bid, ws, 8, 8);
  asm volatile(
"buffer_load_dwordx4 a[64:67], v100, s[40:43], s60 offen sc1\n"
"buffer_load_dwordx4 a[68:71], v101, s[40:43], s60 offen sc1\n"
"buffer_load_dwordx4 a[72:75], v102, s[40:43], s60 offen sc1\n"
"buffer_load_dwordx4 a[76:79], v103, s[40:43], s60 offen sc1\n"
"s_waitcnt vmcnt(28)\n"
"s_barrier\n"
"ds_read_b64_tr_b16 a[0:1], v108 offset:0\n"
"ds_read_b64_tr_b16 a[2:3], v108 offset:4096\n"
"ds_read_b64_tr_b16 a[4:5], v109 offset:0\n"
"ds_read_b64_tr_b16 a[6:7], v109 offset:4096\n"
"ds_read_b64_tr_b16 a[8:9], v108 offset:256\n"
"ds_read_b64_tr_b16 a[10:11], v108 offset:4352\n"
"ds_read_b64_tr_b16 a[12:13], v109 offset:256\n"
"ds_read_b64_tr_b16 a[14:15], v109 offset:4352\n"
"s_waitcnt vmcnt(3) lgkmcnt(6)\n"
"v_mfma_f32_16x16x32_bf16 v[128:131], a[0:3], a[64:67], 0\n"
"ds_read_b64_tr_b16 a[16:17], v108 offset:512\n"
"s_waitcnt lgkmcnt(5)\n"
"v_mfma_f32_16x16x32_bf16 v[132:135], a[4:7], a[64:67], 0\n"
"buffer_load_dwordx4 a[80:83], v100, s[40:43], s60 offen offset:1024 sc1\n"
"s_waitcnt vmcnt(3)\n"
"v_mfma_f32_16x16x32_bf16 v[160:163], a[0:3], a[68:71], 0\n"
"ds_read_b64_tr_b16 a[18:19], v108 offset:4608\n"
"v_mfma_f32_16x16x32_bf16 v[164:167], a[4:7], a[68:71], 0\n"
"ds_read_b64_tr_b16 a[20:21], v109 offset:512\n"
"s_waitcnt vmcnt(2)\n"
"v_mfma_f32_16x16x32_bf16 v[192:195], a[0:3], a[72:75], 0\n"
"buffer_load_dwordx4 a[84:87], v101, s[40:43], s60 offen offset:1024 sc1\n"
"v_mfma_f32_16x16x32_bf16 v[196:199], a[4:7], a[72:75], 0\n"
"ds_read_b64_tr_b16 a[22:23], v109 offset:4608\n"
"s_waitcnt vmcnt(2)\n"
"v_mfma_f32_16x16x32_bf16 v[224:227], a[0:3], a[76:79], 0\n"
"v_mfma_f32_16x16x32_bf16 v[228:231], a[4:7], a[76:79], 0\n"
"buffer_load_dwordx4 a[88:91], v102, s[40:43], s60 offen offset:1024 sc1\n"
"s_waitcnt lgkmcnt(4)\n"
"v_mfma_f32_16x16x32_bf16 v[136:139], a[8:11], a[64:67], 0\n"
"ds_read_b64_tr_b16 a[24:25], v108 offset:768\n"
"v_mfma_f32_16x16x32_bf16 v[140:143], a[12:15], a[64:67], 0\n"
"ds_read_b64_tr_b16 a[26:27], v108 offset:4864\n"
"v_mfma_f32_16x16x32_bf16 v[168:171], a[8:11], a[68:71], 0\n"
"buffer_load_dwordx4 a[92:95], v103, s[40:43], s60 offen offset:1024 sc1\n"
"s_add_u32 s60, s60, 4096\n"
"v_mfma_f32_16x16x32_bf16 v[172:175], a[12:15], a[68:71], 0\n"
"ds_read_b64_tr_b16 a[28:29], v109 offset:768\n"
"v_mfma_f32_16x16x32_bf16 v[200:203], a[8:11], a[72:75], 0\n"
"ds_read_b64_tr_b16 a[30:31], v109 offset:4864\n"
"v_mfma_f32_16x16x32_bf16 v[204:207], a[12:15], a[72:75], 0\n"
"buffer_load_dwordx4 a[96:99], v100, s[40:43], s60 offen sc1\n"
"v_mfma_f32_16x16x32_bf16 v[232:235], a[8:11], a[76:79], 0\n"
"v_mfma_f32_16x16x32_bf16 v[236:239], a[12:15], a[76:79], 0\n"
"s_waitcnt lgkmcnt(4)\n"
"v_mfma_f32_16x16x32_bf16 v[144:147], a[16:19], a[64:67], 0\n"
"buffer_load_dwordx4 a[100:103], v101, s[40:43], s60 offen sc1\n"
"v_mfma_f32_16x16x32_bf16 v[148:151], a[20:23], a[64:67], 0\n"
"ds_read_b64_tr_b16 a[0:1], v108 offset:8192\n"
"v_mfma_f32_16x16x32_bf16 v[176:179], a[16:19], a[68:71], 0\n"
"ds_read_b64_tr_b16 a[2:3], v108 offset:12288\n"
"v_mfma_f32_16x16x32_bf16 v[180:183], a[20:23], a[68:71], 0\n"
"buffer_load_dwordx4 a[104:107], v102, s[40:43], s60 offen sc1\n"
"v_mfma_f32_16x16x32_bf16 v[208:211], a[16:19], a[72:75], 0\n"
"ds_read_b64_tr_b16 a[4:5], v109 offset:8192\n"
"v_mfma_f32_16x16x32_bf16 v[212:215], a[20:23], a[72:75], 0\n"
"ds_read_b64_tr_b16 a[6:7], v109 offset:12288\n"
"v_mfma_f32_16x16x32_bf16 v[240:243], a[16:19], a[76:79], 0\n"
"buffer_load_dwordx4 a[108:111], v103, s[40:43], s60 offen sc1\n"
"v_mfma_f32_16x16x32_bf16 v[244:247], a[20:23], a[76:79], 0\n"
"s_waitcnt lgkmcnt(4)\n"
"v_mfma_f32_16x16x32_bf16 v[152:155], a[24:27], a[64:67], 0\n"
"ds_read_b64_tr_b16 a[8:9], v108 offset:8448\n"
"v_mfma_f32_16x16x32_bf16 v[156:159], a[28:31], a[64:67], 0\n"
"buffer_load_dwordx4 a[112:115], v100, s[40:43], s60 offen offset:1024 sc1\n"
"v_mfma_f32_16x16x32_bf16 v[184:187], a[24:27], a[68:71], 0\n"
"ds_read_b64_tr_b16 a[10:11], v108 offset:12544\n"
"v_mfma_f32_16x16x32_bf16 v[188:191], a[28:31], a[68:71], 0\n"
"ds_read_b64_tr_b16 a[12:13], v109 offset:8448\n"
"v_mfma_f32_16x16x32_bf16 v[216:219], a[24:27], a[72:75], 0\n"
"buffer_load_dwordx4 a[116:119], v101, s[40:43], s60 offen offset:1024 sc1\n"
"v_mfma_f32_16x16x32_bf16 v[220:223], a[28:31], a[72:75], 0\n"
"ds_read_b64_tr_b16 a[14:15], v109 offset:12544\n"
"v_mfma_f32_16x16x32_bf16 v[248:251], a[24:27], a[76:79], 0\n"
"v_mfma_f32_16x16x32_bf16 v[252:255], a[28:31], a[76:79], 0\n"
"buffer_load_dwordx4 a[120:123], v102, s[40:43], s60 offen offset:1024 sc1\n"
"s_waitcnt vmcnt(10) lgkmcnt(4)\n"
"v_mfma_f32_16x16x32_bf16 v[128:131], a[0:3], a[80:83], v[128:131]\n"
"ds_read_b64_tr_b16 a[16:17], v108 offset:8704\n"
"v_mfma_f32_16x16x32_bf16 v[132:135], a[4:7], a[80:83], v[132:135]\n"
"ds_read_b64_tr_b16 a[18:19], v108 offset:12800\n"
"s_waitcnt vmcnt(9)\n"
"v_mfma_f32_16x16x32_bf16 v[160:163], a[0:3], a[84:87], v[160:163]\n"
"buffer_load_dwordx4 a[124:127], v103, s[40:43], s60 offen offset:1024 sc1\n"
"s_add_u32 s60, s60, 4096\n"
"v_mfma_f32_16x16x32_bf16 v[164:167], a[4:7], a[84:87], v[164:167]\n"
"ds_read_b64_tr_b16 a[20:21], v109 offset:8704\n"
"s_waitcnt vmcnt(9)\n"
"v_mfma_f32_16x16x32_bf16 v[192:195], a[0:3], a[88:91], v[192:195]\n"
"ds_read_b64_tr_b16 a[22:23], v109 offset:12800\n"
"v_mfma_f32_16x16x32_bf16 v[196:199], a[4:7], a[88:91], v[196:199]\n"
"buffer_load_dwordx4 a[128:131], v100, s[40:43], s60 offen sc1\n"
"s_waitcnt vmcnt(9)\n"
"v_mfma_f32_16x16x32_bf16 v[224:227], a[0:3], a[92:95], v[224:227]\n"
"v_mfma_f32_16x16x32_bf16 v[228:231], a[4:7], a[92:95], v[228:231]\n"
"s_waitcnt lgkmcnt(4)\n"
"v_mfma_f32_16x16x32_bf16 v[136:139], a[8:11], a[80:83], v[136:139]\n"
"buffer_load_dwordx4 a[132:135], v101, s[40:43], s60 offen sc1\n"
"v_mfma_f32_16x16x32_bf16 v[140:143], a[12:15], a[80:83], v[140:143]\n"
"ds_read_b64_tr_b16 a[24:25], v108 offset:8960\n"
"v_mfma_f32_16x16x32_bf16 v[168:171], a[8:11], a[84:87], v[168:171]\n"
"ds_read_b64_tr_b16 a[26:27], v108 offset:13056\n"
"v_mfma_f32_16x16x32_bf16 v[172:175], a[12:15], a[84:87], v[172:175]\n"
"buffer_load_dwordx4 a[136:139], v102, s[40:43], s60 offen sc1\n"
"v_mfma_f32_16x16x32_bf16 v[200:203], a[8:11], a[88:91], v[200:203]\n"
"ds_read_b64_tr_b16 a[28:29], v109 offset:8960\n"
"v_mfma_f32_16x16x32_bf16 v[204:207], a[12:15], a[88:91], v[204:207]\n"
"ds_read_b64_tr_b16 a[30:31], v109 offset:13056\n"
"v_mfma_f32_16x16x32_bf16 v[232:235], a[8:11], a[92:95], v[232:235]\n"
"buffer_load_dwordx4 a[140:143], v103, s[40:43], s60 offen sc1\n"
"v_mfma_f32_16x16x32_bf16 v[236:239], a[12:15], a[92:95], v[236:239]\n"
"s_barrier\n"
"s_waitcnt lgkmcnt(4)\n"
"v_mfma_f32_16x16x32_bf16 v[144:147], a[16:19], a[80:83], v[144:147]\n"
"ds_read_b64_tr_b16 a[0:1], v110 offset:32768\n"
"v_mfma_f32_16x16x32_bf16 v[148:151], a[20:23], a[80:83], v[148:151]\n"
"buffer_load_dwordx4 a[144:147], v100, s[40:43], s60 offen offset:1024 sc1\n"
"v_mfma_f32_16x16x32_bf16 v[176:179], a[16:19], a[84:87], v[176:179]\n"
"ds_read_b64_tr_b16 a[2:3], v110 offset:36864\n"
"v_mfma_f32_16x16x32_bf16 v[180:183], a[20:23], a[84:87], v[180:183]\n"
"ds_read_b64_tr_b16 a[4:5], v110 offset:40960\n"
"v_mfma_f32_16x16x32_bf16 v[208:211], a[16:19], a[88:91], v[208:211]\n"
"buffer_load_dwordx4 a[148:151], v101, s[40:43], s60 offen offset:1024 sc1\n"
"v_mfma_f32_16x16x32_bf16 v[212:215], a[20:23], a[88:91], v[212:215]\n"
"ds_read_b64_tr_b16 a[6:7], v110 offset:45056\n"
"v_mfma_f32_16x16x32_bf16 v[240:243], a[16:19], a[92:95], v[240:243]\n"
"v_mfma_f32_16x16x32_bf16 v[244:247], a[20:23], a[92:95], v[244:247]\n"
"buffer_load_dwordx4 a[152:155], v102, s[40:43], s60 offen offset:1024 sc1\n"
"s_waitcnt lgkmcnt(4)\n"
"v_mfma_f32_16x16x32_bf16 v[152:155], a[24:27], a[80:83], v[152:155]\n"
"ds_read_b64_tr_b16 a[8:9], v111 offset:32768\n"
"v_mfma_f32_16x16x32_bf16 v[156:159], a[28:31], a[80:83], v[156:159]\n"
"ds_read_b64_tr_b16 a[10:11], v111 offset:36864\n"
"v_mfma_f32_16x16x32_bf16 v[184:187], a[24:27], a[84:87], v[184:187]\n"
"buffer_load_dwordx4 a[156:159], v103, s[40:43], s60 offen offset:1024 sc1\n"
"s_add_u32 s60, s60, 4096\n"
"v_mfma_f32_16x16x32_bf16 v[188:191], a[28:31], a[84:87], v[188:191]\n"
"ds_read_b64_tr_b16 a[12:13], v111 offset:40960\n"
"v_mfma_f32_16x16x32_bf16 v[216:219], a[24:27], a[88:91], v[216:219]\n"
"ds_read_b64_tr_b16 a[14:15], v111 offset:45056\n"
"v_mfma_f32_16x16x32_bf16 v[220:223], a[28:31], a[88:91], v[220:223]\n"
"buffer_load_dwordx4 v[124:127], v112, s[64:67], 0 offen\n"
"v_mfma_f32_16x16x32_bf16 v[248:251], a[24:27], a[92:95], v[248:251]\n"
"v_mfma_f32_16x16x32_bf16 v[252:255], a[28:31], a[92:95], v[252:255]\n"
"s_waitcnt vmcnt(13) lgkmcnt(4)\n"
"v_mfma_f32_16x16x32_bf16 v[128:131], a[0:3], a[96:99], v[128:131]\n"
"buffer_load_dwordx4 a[160:163], v100, s[40:43], s60 offen sc1\n"
"s_waitcnt vmcnt(13)\n"
"v_mfma_f32_16x16x32_bf16 v[128:131], a[4:7], a[112:115], v[128:131]\n"
"ds_read_b64_tr_b16 a[16:17], v110 offset:33024\n"
"v_mfma_f32_16x16x32_bf16 v[160:163], a[0:3], a[100:103], v[160:163]\n"
"ds_read_b64_tr_b16 a[18:19], v110 offset:37120\n"
"s_waitcnt vmcnt(12)\n"
"v_mfma_f32_16x16x32_bf16 v[160:163], a[4:7], a[116:119], v[160:163]\n"
"buffer_load_dwordx4 a[164:167], v101, s[40:43], s60 offen sc1\n"
"v_mfma_f32_16x16x32_bf16 v[192:195], a[0:3], a[104:107], v[192:195]\n"
"ds_read_b64_tr_b16 a[20:21], v110 offset:41216\n"
"s_waitcnt vmcnt(12)\n"
"v_mfma_f32_16x16x32_bf16 v[192:195], a[4:7], a[120:123], v[192:195]\n"
"ds_read_b64_tr_b16 a[22:23], v110 offset:45312\n"
"v_mfma_f32_16x16x32_bf16 v[224:227], a[0:3], a[108:111], v[224:227]\n"
"buffer_load_dwordx4 a[168:171], v102, s[40:43], s60 offen sc1\n"
"s_waitcnt vmcnt(12)\n"
"v_mfma_f32_16x16x32_bf16 v[224:227], a[4:7], a[124:127], v[224:227]\n"
"s_waitcnt lgkmcnt(4)\n"
"v_mfma_f32_16x16x32_bf16 v[132:135], a[8:11], a[96:99], v[132:135]\n"
"ds_read_b64_tr_b16 a[24:25], v111 offset:33024\n"
"v_mfma_f32_16x16x32_bf16 v[132:135], a[12:15], a[112:115], v[132:135]\n"
"buffer_load_dwordx4 a[172:175], v103, s[40:43], s60 offen sc1\n"
"v_mfma_f32_16x16x32_bf16 v[164:167], a[8:11], a[100:103], v[164:167]\n"
"ds_read_b64_tr_b16 a[26:27], v111 offset:37120\n"
"v_mfma_f32_16x16x32_bf16 v[164:167], a[12:15], a[116:119], v[164:167]\n"
"ds_read_b64_tr_b16 a[28:29], v111 offset:41216\n"
"v_mfma_f32_16x16x32_bf16 v[196:199], a[8:11], a[104:107], v[196:199]\n"
"buffer_load_dwordx4 a[176:179], v100, s[40:43], s60 offen offset:1024 sc1\n"
"v_mfma_f32_16x16x32_bf16 v[196:199], a[12:15], a[120:123], v[196:199]\n"
"ds_read_b64_tr_b16 a[30:31], v111 offset:45312\n"
"v_mfma_f32_16x16x32_bf16 v[228:231], a[8:11], a[108:111], v[228:231]\n"
"v_mfma_f32_16x16x32_bf16 v[228:231], a[12:15], a[124:127], v[228:231]\n"
"buffer_load_dwordx4 a[180:183], v101, s[40:43], s60 offen offset:1024 sc1\n"
"s_waitcnt lgkmcnt(4)\n"
"v_mfma_f32_16x16x32_bf16 v[136:139], a[16:19], a[96:99], v[136:139]\n"
"ds_read_b64_tr_b16 a[0:1], v110 offset:33280\n"
"v_mfma_f32_16x16x32_bf16 v[136:139], a[20:23], a[112:115], v[136:139]\n"
"ds_read_b64_tr_b16 a[2:3], v110 offset:37376\n"
"v_mfma_f32_16x16x32_bf16 v[168:171], a[16:19], a[100:103], v[168:171]\n"
"buffer_load_dwordx4 a[184:187], v102, s[40:43], s60 offen offset:1024 sc1\n"
"v_mfma_f32_16x16x32_bf16 v[168:171], a[20:23], a[116:119], v[168:171]\n"
"ds_read_b64_tr_b16 a[4:5], v110 offset:41472\n"
"v_mfma_f32_16x16x32_bf16 v[200:203], a[16:19], a[104:107], v[200:203]\n"
"ds_read_b64_tr_b16 a[6:7], v110 offset:45568\n"
"v_mfma_f32_16x16x32_bf16 v[200:203], a[20:23], a[120:123], v[200:203]\n"
"buffer_load_dwordx4 a[188:191], v103, s[40:43], s60 offen offset:1024 sc1\n"
"s_add_u32 s60, s60, 4096\n"
"v_mfma_f32_16x16x32_bf16 v[232:235], a[16:19], a[108:111], v[232:235]\n"
"v_mfma_f32_16x16x32_bf16 v[232:235], a[20:23], a[124:127], v[232:235]\n"
"s_waitcnt lgkmcnt(4)\n"
"v_mfma_f32_16x16x32_bf16 v[140:143], a[24:27], a[96:99], v[140:143]\n"
"ds_read_b64_tr_b16 a[8:9], v111 offset:33280\n"
"v_mfma_f32_16x16x32_bf16 v[140:143], a[28:31], a[112:115], v[140:143]\n"
"ds_read_b64_tr_b16 a[10:11], v111 offset:37376\n"
"v_mfma_f32_16x16x32_bf16 v[172:175], a[24:27], a[100:103], v[172:175]\n"
"ds_read_b64_tr_b16 a[12:13], v111 offset:41472\n"
"v_mfma_f32_16x16x32_bf16 v[172:175], a[28:31], a[116:119], v[172:175]\n"
"ds_read_b64_tr_b16 a[14:15], v111 offset:45568\n"
"v_mfma_f32_16x16x32_bf16 v[204:207], a[24:27], a[104:107], v[204:207]\n"
"v_mfma_f32_16x16x32_bf16 v[204:207], a[28:31], a[120:123], v[204:207]\n"
"v_mfma_f32_16x16x32_bf16 v[236:239], a[24:27], a[108:111], v[236:239]\n"
"v_mfma_f32_16x16x32_bf16 v[236:239], a[28:31], a[124:127], v[236:239]\n"
"s_waitcnt lgkmcnt(4)\n"
"v_mfma_f32_16x16x32_bf16 v[144:147], a[0:3], a[96:99], v[144:147]\n"
"ds_read_b64_tr_b16 a[16:17], v110 offset:33536\n"
"v_mfma_f32_16x16x32_bf16 v[144:147], a[4:7], a[112:115], v[144:147]\n"
"ds_read_b64_tr_b16 a[18:19], v110 offset:37632\n"
"v_mfma_f32_16x16x32_bf16 v[176:179], a[0:3], a[100:103], v[176:179]\n"
"ds_read_b64_tr_b16 a[20:21], v110 offset:41728\n"
"v_mfma_f32_16x16x32_bf16 v[176:179], a[4:7], a[116:119], v[176:179]\n"
"ds_read_b64_tr_b16 a[22:23], v110 offset:45824\n"
"v_mfma_f32_16x16x32_bf16 v[208:211], a[0:3], a[104:107], v[208:211]\n"
"v_mfma_f32_16x16x32_bf16 v[208:211], a[4:7], a[120:123], v[208:211]\n"
"v_mfma_f32_16x16x32_bf16 v[240:243], a[0:3], a[108:111], v[240:243]\n"
"v_mfma_f32_16x16x32_bf16 v[240:243], a[4:7], a[124:127], v[240:243]\n"
"s_waitcnt lgkmcnt(4)\n"
"v_mfma_f32_16x16x32_bf16 v[148:151], a[8:11], a[96:99], v[148:151]\n"
"ds_read_b64_tr_b16 a[24:25], v111 offset:33536\n"
"v_mfma_f32_16x16x32_bf16 v[148:151], a[12:15], a[112:115], v[148:151]\n"
"ds_read_b64_tr_b16 a[26:27], v111 offset:37632\n"
"v_mfma_f32_16x16x32_bf16 v[180:183], a[8:11], a[100:103], v[180:183]\n"
"ds_read_b64_tr_b16 a[28:29], v111 offset:41728\n"
"v_mfma_f32_16x16x32_bf16 v[180:183], a[12:15], a[116:119], v[180:183]\n"
"ds_read_b64_tr_b16 a[30:31], v111 offset:45824\n"
"v_mfma_f32_16x16x32_bf16 v[212:215], a[8:11], a[104:107], v[212:215]\n"
"v_mfma_f32_16x16x32_bf16 v[212:215], a[12:15], a[120:123], v[212:215]\n"
"v_mfma_f32_16x16x32_bf16 v[244:247], a[8:11], a[108:111], v[244:247]\n"
"v_mfma_f32_16x16x32_bf16 v[244:247], a[12:15], a[124:127], v[244:247]\n"
"s_barrier\n"
"s_waitcnt lgkmcnt(4)\n"
"v_mfma_f32_16x16x32_bf16 v[152:155], a[16:19], a[96:99], v[152:155]\n"
"ds_read_b64_tr_b16 a[0:1], v108 offset:32768\n"
"v_mfma_f32_16x16x32_bf16 v[152:155], a[20:23], a[112:115], v[152:155]\n"
"buffer_load_dwordx4 a[64:67], v100, s[40:43], s60 offen sc1\n"
"v_mfma_f32_16x16x32_bf16 v[184:187], a[16:19], a[100:103], v[184:187]\n"
"ds_read_b64_tr_b16 a[2:3], v108 offset:36864\n"
"v_mfma_f32_16x16x32_bf16 v[184:187], a[20:23], a[116:119], v[184:187]\n"
"ds_read_b64_tr_b16 a[4:5], v108 offset:40960\n"
"v_mfma_f32_16x16x32_bf16 v[216:219], a[16:19], a[104:107], v[216:219]\n"
"ds_read_b64_tr_b16 a[6:7], v108 offset:45056\n"
"s_add_u32 m0, s62, 0\n"
"v_mfma_f32_16x16x32_bf16 v[216:219], a[20:23], a[120:123], v[216:219]\n"
"buffer_load_dwordx4 v104, s[44:47], s61 offen lds\n"
"v_mfma_f32_16x16x32_bf16 v[248:251], a[16:19], a[108:111], v[248:251]\n"
"v_mfma_f32_16x16x32_bf16 v[248:251], a[20:23], a[124:127], v[248:251]\n"
"s_waitcnt lgkmcnt(4)\n"
"v_mfma_f32_16x16x32_bf16 v[156:159], a[24:27], a[96:99], v[156:159]\n"
"ds_read_b64_tr_b16 a[8:9], v109 offset:32768\n"
"v_mfma_f32_16x16x32_bf16 v[156:159], a[28:31], a[112:115], v[156:159]\n"
"buffer_load_dwordx4 a[68:71], v101, s[40:43], s60 offen sc1\n"
"v_mfma_f32_16x16x32_bf16 v[188:191], a[24:27], a[100:103], v[188:191]\n"
"ds_read_b64_tr_b16 a[10:11], v109 offset:36864\n"
"v_mfma_f32_16x16x32_bf16 v[188:191], a[28:31], a[116:119], v[188:191]\n"
"ds_read_b64_tr_b16 a[12:13], v109 offset:40960\n"
"v_mfma_f32_16x16x32_bf16 v[220:223], a[24:27], a[104:107], v[220:223]\n"
"ds_read_b64_tr_b16 a[14:15], v109 offset:45056\n"
"v_mfma_f32_16x16x32_bf16 v[220:223], a[28:31], a[120:123], v[220:223]\n"
"buffer_load_dwordx4 v105, s[44:47], s61 offen offset:1024 lds\n"
"v_mfma_f32_16x16x32_bf16 v[252:255], a[24:27], a[108:111], v[252:255]\n"
"v_mfma_f32_16x16x32_bf16 v[252:255], a[28:31], a[124:127], v[252:255]\n"
"s_waitcnt vmcnt(4) lgkmcnt(4)\n"
"v_mfma_f32_16x16x32_bf16 v[128:131], a[0:3], a[128:131], v[128:131]\n"
"ds_read_b64_tr_b16 a[16:17], v108 offset:33024\n"
"v_mfma_f32_16x16x32_bf16 v[128:131], a[4:7], a[144:147], v[128:131]\n"
"buffer_load_dwordx4 a[72:75], v102, s[40:43], s60 offen sc1\n"
"v_mfma_f32_16x16x32_bf16 v[160:163], a[0:3], a[132:135], v[160:163]\n"
"ds_read_b64_tr_b16 a[18:19], v108 offset:37120\n"
"v_mfma_f32_16x16x32_bf16 v[160:163], a[4:7], a[148:151], v[160:163]\n"
"ds_read_b64_tr_b16 a[20:21], v108 offset:41216\n"
"v_mfma_f32_16x16x32_bf16 v[192:195], a[0:3], a[136:139], v[192:195]\n"
"ds_read_b64_tr_b16 a[22:23], v108 offset:45312\n"
"v_mfma_f32_16x16x32_bf16 v[192:195], a[4:7], a[152:155], v[192:195]\n"
"buffer_load_dwordx4 v106, s[44:47], s61 offen offset:2048 lds\n"
"v_mfma_f32_16x16x32_bf16 v[224:227], a[0:3], a[140:143], v[224:227]\n"
"v_mfma_f32_16x16x32_bf16 v[224:227], a[4:7], a[156:159], v[224:227]\n"
"s_waitcnt lgkmcnt(4)\n"
"v_mfma_f32_16x16x32_bf16 v[132:135], a[8:11], a[128:131], v[132:135]\n"
"ds_read_b64_tr_b16 a[24:25], v109 offset:33024\n"
"v_mfma_f32_16x16x32_bf16 v[132:135], a[12:15], a[144:147], v[132:135]\n"
"buffer_load_dwordx4 a[76:79], v103, s[40:43], s60 offen sc1\n"
"v_mfma_f32_16x16x32_bf16 v[164:167], a[8:11], a[132:135], v[164:167]\n"
"ds_read_b64_tr_b16 a[26:27], v109 offset:37120\n"
"v_mfma_f32_16x16x32_bf16 v[164:167], a[12:15], a[148:151], v[164:167]\n"
"ds_read_b64_tr_b16 a[28:29], v109 offset:41216\n"
"v_mfma_f32_16x16x32_bf16 v[196:199], a[8:11], a[136:139], v[196:199]\n"
"ds_read_b64_tr_b16 a[30:31], v109 offset:45312\n"
"v_mfma_f32_16x16x32_bf16 v[196:199], a[12:15], a[152:155], v[196:199]\n"
"buffer_load_dwordx4 v107, s[44:47], s61 offen offset:3072 lds\n"
"s_add_u32 s61, s61, 32768\n"
"v_mfma_f32_16x16x32_bf16 v[228:231], a[8:11], a[140:143], v[228:231]\n"
"v_mfma_f32_16x16x32_bf16 v[228:231], a[12:15], a[156:159], v[228:231]\n"
"s_waitcnt lgkmcnt(4)\n"
"v_mfma_f32_16x16x32_bf16 v[136:139], a[16:19], a[128:131], v[136:139]\n"
"buffer_load_dwordx4 a[80:83], v100, s[40:43], s60 offen offset:1024 sc1\n"
"v_mfma_f32_16x16x32_bf16 v[136:139], a[20:23], a[144:147], v[136:139]\n"
"ds_read_b64_tr_b16 a[0:1], v108 offset:33280\n"
"v_mfma_f32_16x16x32_bf16 v[168:171], a[16:19], a[132:135], v[168:171]\n"
"ds_read_b64_tr_b16 a[2:3], v108 offset:37376\n"
"v_mfma_f32_16x16x32_bf16 v[168:171], a[20:23], a[148:151], v[168:171]\n"
"ds_read_b64_tr_b16 a[4:5], v108 offset:41472\n"
"s_add_u32 m0, s62, 4096\n"
"v_mfma_f32_16x16x32_bf16 v[200:203], a[16:19], a[136:139], v[200:203]\n"
"buffer_load_dwordx4 v104, s[44:47], s61 offen lds\n"
"v_mfma_f32_16x16x32_bf16 v[200:203], a[20:23], a[152:155], v[200:203]\n"
"ds_read_b64_tr_b16 a[6:7], v108 offset:45568\n"
"v_mfma_f32_16x16x32_bf16 v[232:235], a[16:19], a[140:143], v[232:235]\n"
"v_mfma_f32_16x16x32_bf16 v[232:235], a[20:23], a[156:159], v[232:235]\n"
"s_waitcnt lgkmcnt(4)\n"
"v_mfma_f32_16x16x32_bf16 v[140:143], a[24:27], a[128:131], v[140:143]\n"
"buffer_load_dwordx4 a[84:87], v101, s[40:43], s60 offen offset:1024 sc1\n"
"v_mfma_f32_16x16x32_bf16 v[140:143], a[28:31], a[144:147], v[140:143]\n"
"ds_read_b64_tr_b16 a[8:9], v109 offset:33280\n"
"v_mfma_f32_16x16x32_bf16 v[172:175], a[24:27], a[132:135], v[172:175]\n"
"ds_read_b64_tr_b16 a[10:11], v109 offset:37376\n"
"v_mfma_f32_16x16x32_bf16 v[172:175], a[28:31], a[148:151], v[172:175]\n"
"ds_read_b64_tr_b16 a[12:13], v109 offset:41472\n"
"v_mfma_f32_16x16x32_bf16 v[204:207], a[24:27], a[136:139], v[204:207]\n"
"buffer_load_dwordx4 v105, s[44:47], s61 offen offset:1024 lds\n"
"v_mfma_f32_16x16x32_bf16 v[204:207], a[28:31], a[152:155], v[204:207]\n"
"ds_read_b64_tr_b16 a[14:15], v109 offset:45568\n"
"v_mfma_f32_16x16x32_bf16 v[236:239], a[24:27], a[140:143], v[236:239]\n"
"v_mfma_f32_16x16x32_bf16 v[236:239], a[28:31], a[156:159], v[236:239]\n"
"s_waitcnt lgkmcnt(4)\n"
"v_mfma_f32_16x16x32_bf16 v[144:147], a[0:3], a[128:131], v[144:147]\n"
"buffer_load_dwordx4 a[88:91], v102, s[40:43], s60 offen offset:1024 sc1\n"
"v_mfma_f32_16x16x32_bf16 v[144:147], a[4:7], a[144:147], v[144:147]\n"
"ds_read_b64_tr_b16 a[16:17], v108 offset:33536\n"
"v_mfma_f32_16x16x32_bf16 v[176:179], a[0:3], a[132:135], v[176:179]\n"
"ds_read_b64_tr_b16 a[18:19], v108 offset:37632\n"
"v_mfma_f32_16x16x32_bf16 v[176:179], a[4:7], a[148:151], v[176:179]\n"
"ds_read_b64_tr_b16 a[20:21], v108 offset:41728\n"
"v_mfma_f32_16x16x32_bf16 v[208:211], a[0:3], a[136:139], v[208:211]\n"
"buffer_load_dwordx4 v106, s[44:47], s61 offen offset:2048 lds\n"
"v_mfma_f32_16x16x32_bf16 v[208:211], a[4:7], a[152:155], v[208:211]\n"
"ds_read_b64_tr_b16 a[22:23], v108 offset:45824\n"
"v_mfma_f32_16x16x32_bf16 v[240:243], a[0:3], a[140:143], v[240:243]\n"
"v_mfma_f32_16x16x32_bf16 v[240:243], a[4:7], a[156:159], v[240:243]\n"
"s_waitcnt lgkmcnt(4)\n"
"v_mfma_f32_16x16x32_bf16 v[148:151], a[8:11], a[128:131], v[148:151]\n"
"buffer_load_dwordx4 a[92:95], v103, s[40:43], s60 offen offset:1024 sc1\n"
"s_add_u32 s60, s60, 4096\n"
"v_mfma_f32_16x16x32_bf16 v[148:151], a[12:15], a[144:147], v[148:151]\n"
"ds_read_b64_tr_b16 a[24:25], v109 offset:33536\n"
"v_mfma_f32_16x16x32_bf16 v[180:183], a[8:11], a[132:135], v[180:183]\n"
"ds_read_b64_tr_b16 a[26:27], v109 offset:37632\n"
"v_mfma_f32_16x16x32_bf16 v[180:183], a[12:15], a[148:151], v[180:183]\n"
"ds_read_b64_tr_b16 a[28:29], v109 offset:41728\n"
"v_mfma_f32_16x16x32_bf16 v[212:215], a[8:11], a[136:139], v[212:215]\n"
"buffer_load_dwordx4 v107, s[44:47], s61 offen offset:3072 lds\n"
"s_add_u32 s61, s61, 229376\n"
"v_mfma_f32_16x16x32_bf16 v[212:215], a[12:15], a[152:155], v[212:215]\n"
"ds_read_b64_tr_b16 a[30:31], v109 offset:45824\n"
"v_mfma_f32_16x16x32_bf16 v[244:247], a[8:11], a[140:143], v[244:247]\n"
"v_mfma_f32_16x16x32_bf16 v[244:247], a[12:15], a[156:159], v[244:247]\n"
"s_barrier\n"
"s_waitcnt lgkmcnt(4)\n"
"s_mov_b32 s63, 1\n"
"LD_%=:\n"
"v_mfma_f32_16x16x32_bf16 v[152:155], a[16:19], a[128:131], v[152:155]\n"
"ds_read_b64_tr_b16 a[0:1], v110 offset:0\n"
"v_mfma_f32_16x16x32_bf16 v[152:155], a[20:23], a[144:147], v[152:155]\n"
"buffer_load_dwordx4 a[96:99], v100, s[40:43], s60 offen sc1\n"
"v_mfma_f32_16x16x32_bf16 v[184:187], a[16:19], a[132:135], v[184:187]\n"
"ds_read_b64_tr_b16 a[2:3], v110 offset:4096\n"
"v_mfma_f32_16x16x32_bf16 v[184:187], a[20:23], a[148:151], v[184:187]\n"
"ds_read_b64_tr_b16 a[4:5], v110 offset:8192\n"
"v_mfma_f32_16x16x32_bf16 v[216:219], a[16:19], a[136:139], v[216:219]\n"
"ds_read_b64_tr_b16 a[6:7], v110 offset:12288\n"
"s_add_u32 m0, s62, 98304\n"
"v_mfma_f32_16x16x32_bf16 v[216:219], a[20:23], a[152:155], v[216:219]\n"
"buffer_load_dwordx4 v104, s[44:47], s61 offen lds\n"
"v_mfma_f32_16x16x32_bf16 v[248:251], a[16:19], a[140:143], v[248:251]\n"
"v_mfma_f32_16x16x32_bf16 v[248:251], a[20:23], a[156:159], v[248:251]\n"
"s_waitcnt lgkmcnt(4)\n"
"v_mfma_f32_16x16x32_bf16 v[156:159], a[24:27], a[128:131], v[156:159]\n"
"ds_read_b64_tr_b16 a[8:9], v111 offset:0\n"
"v_mfma_f32_16x16x32_bf16 v[156:159], a[28:31], a[144:147], v[156:159]\n"
"buffer_load_dwordx4 a[100:103], v101, s[40:43], s60 offen sc1\n"
"v_mfma_f32_16x16x32_bf16 v[188:191], a[24:27], a[132:135], v[188:191]\n"
"ds_read_b64_tr_b16 a[10:11], v111 offset:4096\n"
"v_mfma_f32_16x16x32_bf16 v[188:191], a[28:31], a[148:151], v[188:191]\n"
"ds_read_b64_tr_b16 a[12:13], v111 offset:8192\n"
"v_mfma_f32_16x16x32_bf16 v[220:223], a[24:27], a[136:139], v[220:223]\n"
"ds_read_b64_tr_b16 a[14:15], v111 offset:12288\n"
"v_mfma_f32_16x16x32_bf16 v[220:223], a[28:31], a[152:155], v[220:223]\n"
"buffer_load_dwordx4 v105, s[44:47], s61 offen offset:1024 lds\n"
"v_mfma_f32_16x16x32_bf16 v[252:255], a[24:27], a[140:143], v[252:255]\n"
"v_mfma_f32_16x16x32_bf16 v[252:255], a[28:31], a[156:159], v[252:255]\n"
"s_waitcnt lgkmcnt(4)\n"
"v_mfma_f32_16x16x32_bf16 v[128:131], a[0:3], a[160:163], v[128:131]\n"
"ds_read_b64_tr_b16 a[16:17], v110 offset:256\n"
"v_mfma_f32_16x16x32_bf16 v[128:131], a[4:7], a[176:179], v[128:131]\n"
"buffer_load_dwordx4 a[104:107], v102, s[40:43], s60 offen sc1\n"
"v_mfma_f32_16x16x32_bf16 v[160:163], a[0:3], a[164:167], v[160:163]\n"
"ds_read_b64_tr_b16 a[18:19], v110 offset:4352\n"
"v_mfma_f32_16x16x32_bf16 v[160:163], a[4:7], a[180:183], v[160:163]\n"
"ds_read_b64_tr_b16 a[20:21], v110 offset:8448\n"
"v_mfma_f32_16x16x32_bf16 v[192:195], a[0:3], a[168:171], v[192:195]\n"
"ds_read_b64_tr_b16 a[22:23], v110 offset:12544\n"
"v_mfma_f32_16x16x32_bf16 v[192:195], a[4:7], a[184:187], v[192:195]\n"
"buffer_load_dwordx4 v106, s[44:47], s61 offen offset:2048 lds\n"
"v_mfma_f32_16x16x32_bf16 v[224:227], a[0:3], a[172:175], v[224:227]\n"
"v_mfma_f32_16x16x32_bf16 v[224:227], a[4:7], a[188:191], v[224:227]\n"
"s_waitcnt lgkmcnt(4)\n"
"v_mfma_f32_16x16x32_bf16 v[132:135], a[8:11], a[160:163], v[132:135]\n"
"ds_read_b64_tr_b16 a[24:25], v111 offset:256\n"
"v_mfma_f32_16x16x32_bf16 v[132:135], a[12:15], a[176:179], v[132:135]\n"
"buffer_load_dwordx4 a[108:111], v103, s[40:43], s60 offen sc1\n"
"v_mfma_f32_16x16x32_bf16 v[164:167], a[8:11], a[164:167], v[164:167]\n"
"ds_read_b64_tr_b16 a[26:27], v111 offset:4352\n"
"v_mfma_f32_16x16x32_bf16 v[164:167], a[12:15], a[180:183], v[164:167]\n"
"ds_read_b64_tr_b16 a[28:29], v111 offset:8448\n"
"v_mfma_f32_16x16x32_bf16 v[196:199], a[8:11], a[168:171], v[196:199]\n"
"ds_read_b64_tr_b16 a[30:31], v111 offset:12544\n"
"v_mfma_f32_16x16x32_bf16 v[196:199], a[12:15], a[184:187], v[196:199]\n"
"buffer_load_dwordx4 v107, s[44:47], s61 offen offset:3072 lds\n"
"s_add_u32 s61, s61, 32768\n"
"v_mfma_f32_16x16x32_bf16 v[228:231], a[8:11], a[172:175], v[228:231]\n"
"v_mfma_f32_16x16x32_bf16 v[228:231], a[12:15], a[188:191], v[228:231]\n"
"s_waitcnt lgkmcnt(4)\n"
"v_mfma_f32_16x16x32_bf16 v[136:139], a[16:19], a[160:163], v[136:139]\n"
"buffer_load_dwordx4 a[112:115], v100, s[40:43], s60 offen offset:1024 sc1\n"
"v_mfma_f32_16x16x32_bf16 v[136:139], a[20:23], a[176:179], v[136:139]\n"
"ds_read_b64_tr_b16 a[0:1], v110 offset:512\n"
"v_mfma_f32_16x16x32_bf16 v[168:171], a[16:19], a[164:167], v[168:171]\n"
"ds_read_b64_tr_b16 a[2:3], v110 offset:4608\n"
"v_mfma_f32_16x16x32_bf16 v[168:171], a[20:23], a[180:183], v[168:171]\n"
"ds_read_b64_tr_b16 a[4:5], v110 offset:8704\n"
"s_add_u32 m0, s62, 102400\n"
"v_mfma_f32_16x16x32_bf16 v[200:203], a[16:19], a[168:171], v[200:203]\n"
"buffer_load_dwordx4 v104, s[44:47], s61 offen lds\n"
"v_mfma_f32_16x16x32_bf16 v[200:203], a[20:23], a[184:187], v[200:203]\n"
"ds_read_b64_tr_b16 a[6:7], v110 offset:12800\n"
"v_mfma_f32_16x16x32_bf16 v[232:235], a[16:19], a[172:175], v[232:235]\n"
"v_mfma_f32_16x16x32_bf16 v[232:235], a[20:23], a[188:191], v[232:235]\n"
"s_waitcnt lgkmcnt(4)\n"
"v_mfma_f32_16x16x32_bf16 v[140:143], a[24:27], a[160:163], v[140:143]\n"
"buffer_load_dwordx4 a[116:119], v101, s[40:43], s60 offen offset:1024 sc1\n"
"v_mfma_f32_16x16x32_bf16 v[140:143], a[28:31], a[176:179], v[140:143]\n"
"ds_read_b64_tr_b16 a[8:9], v111 offset:512\n"
"v_mfma_f32_16x16x32_bf16 v[172:175], a[24:27], a[164:167], v[172:175]\n"
"ds_read_b64_tr_b16 a[10:11], v111 offset:4608\n"
"v_mfma_f32_16x16x32_bf16 v[172:175], a[28:31], a[180:183], v[172:175]\n"
"ds_read_b64_tr_b16 a[12:13], v111 offset:8704\n"
"v_mfma_f32_16x16x32_bf16 v[204:207], a[24:27], a[168:171], v[204:207]\n"
"buffer_load_dwordx4 v105, s[44:47], s61 offen offset:1024 lds\n"
"v_mfma_f32_16x16x32_bf16 v[204:207], a[28:31], a[184:187], v[204:207]\n"
"ds_read_b64_tr_b16 a[14:15], v111 offset:12800\n"
"v_mfma_f32_16x16x32_bf16 v[236:239], a[24:27], a[172:175], v[236:239]\n"
"v_mfma_f32_16x16x32_bf16 v[236:239], a[28:31], a[188:191], v[236:239]\n"
"s_waitcnt lgkmcnt(4)\n"
"v_mfma_f32_16x16x32_bf16 v[144:147], a[0:3], a[160:163], v[144:147]\n"
"buffer_load_dwordx4 a[120:123], v102, s[40:43], s60 offen offset:1024 sc1\n"
"v_mfma_f32_16x16x32_bf16 v[144:147], a[4:7], a[176:179], v[144:147]\n"
"ds_read_b64_tr_b16 a[16:17], v110 offset:768\n"
"v_mfma_f32_16x16x32_bf16 v[176:179], a[0:3], a[164:167], v[176:179]\n"
"ds_read_b64_tr_b16 a[18:19], v110 offset:4864\n"
"v_mfma_f32_16x16x32_bf16 v[176:179], a[4:7], a[180:183], v[176:179]\n"
"ds_read_b64_tr_b16 a[20:21], v110 offset:8960\n"
"v_mfma_f32_16x16x32_bf16 v[208:211], a[0:3], a[168:171], v[208:211]\n"
"buffer_load_dwordx4 v106, s[44:47], s61 offen offset:2048 lds\n"
"v_mfma_f32_16x16x32_bf16 v[208:211], a[4:7], a[184:187], v[208:211]\n"
"ds_read_b64_tr_b16 a[22:23], v110 offset:13056\n"
"v_mfma_f32_16x16x32_bf16 v[240:243], a[0:3], a[172:175], v[240:243]\n"
"v_mfma_f32_16x16x32_bf16 v[240:243], a[4:7], a[188:191], v[240:243]\n"
"s_waitcnt lgkmcnt(4)\n"
"v_mfma_f32_16x16x32_bf16 v[148:151], a[8:11], a[160:163], v[148:151]\n"
"buffer_load_dwordx4 a[124:127], v103, s[40:43], s60 offen offset:1024 sc1\n"
"s_add_u32 s60, s60, 4096\n"
"v_mfma_f32_16x16x32_bf16 v[148:151], a[12:15], a[176:179], v[148:151]\n"
"ds_read_b64_tr_b16 a[24:25], v111 offset:768\n"
"v_mfma_f32_16x16x32_bf16 v[180:183], a[8:11], a[164:167], v[180:183]\n"
"ds_read_b64_tr_b16 a[26:27], v111 offset:4864\n"
"v_mfma_f32_16x16x32_bf16 v[180:183], a[12:15], a[180:183], v[180:183]\n"
"ds_read_b64_tr_b16 a[28:29], v111 offset:8960\n"
"v_mfma_f32_16x16x32_bf16 v[212:215], a[8:11], a[168:171], v[212:215]\n"
"buffer_load_dwordx4 v107, s[44:47], s61 offen offset:3072 lds\n"
"s_add_u32 s61, s61, 229376\n"
"v_mfma_f32_16x16x32_bf16 v[212:215], a[12:15], a[184:187], v[212:215]\n"
"ds_read_b64_tr_b16 a[30:31], v111 offset:13056\n"
"v_mfma_f32_16x16x32_bf16 v[244:247], a[8:11], a[172:175], v[244:247]\n"
"v_mfma_f32_16x16x32_bf16 v[244:247], a[12:15], a[188:191], v[244:247]\n"
"s_waitcnt vmcnt(16) lgkmcnt(4)\n"
"s_barrier\n"
"v_mfma_f32_16x16x32_bf16 v[152:155], a[16:19], a[160:163], v[152:155]\n"
"ds_read_b64_tr_b16 a[0:1], v108 offset:0\n"
"v_mfma_f32_16x16x32_bf16 v[152:155], a[20:23], a[176:179], v[152:155]\n"
"buffer_load_dwordx4 a[128:131], v100, s[40:43], s60 offen sc1\n"
"v_mfma_f32_16x16x32_bf16 v[184:187], a[16:19], a[164:167], v[184:187]\n"
"ds_read_b64_tr_b16 a[2:3], v108 offset:4096\n"
"v_mfma_f32_16x16x32_bf16 v[184:187], a[20:23], a[180:183], v[184:187]\n"
"ds_read_b64_tr_b16 a[4:5], v108 offset:8192\n"
"v_mfma_f32_16x16x32_bf16 v[216:219], a[16:19], a[168:171], v[216:219]\n"
"ds_read_b64_tr_b16 a[6:7], v108 offset:12288\n"
"s_add_u32 m0, s62, 32768\n"
"v_mfma_f32_16x16x32_bf16 v[216:219], a[20:23], a[184:187], v[216:219]\n"
"buffer_load_dwordx4 v104, s[44:47], s61 offen lds\n"
"v_mfma_f32_16x16x32_bf16 v[248:251], a[16:19], a[172:175], v[248:251]\n"
"v_mfma_f32_16x16x32_bf16 v[248:251], a[20:23], a[188:191], v[248:251]\n"
"s_waitcnt lgkmcnt(4)\n"
"v_mfma_f32_16x16x32_bf16 v[156:159], a[24:27], a[160:163], v[156:159]\n"
"ds_read_b64_tr_b16 a[8:9], v109 offset:0\n"
"v_mfma_f32_16x16x32_bf16 v[156:159], a[28:31], a[176:179], v[156:159]\n"
"buffer_load_dwordx4 a[132:135], v101, s[40:43], s60 offen sc1\n"
"v_mfma_f32_16x16x32_bf16 v[188:191], a[24:27], a[164:167], v[188:191]\n"
"ds_read_b64_tr_b16 a[10:11], v109 offset:4096\n"
"v_mfma_f32_16x16x32_bf16 v[188:191], a[28:31], a[180:183], v[188:191]\n"
"ds_read_b64_tr_b16 a[12:13], v109 offset:8192\n"
"v_mfma_f32_16x16x32_bf16 v[220:223], a[24:27], a[168:171], v[220:223]\n"
"ds_read_b64_tr_b16 a[14:15], v109 offset:12288\n"
"v_mfma_f32_16x16x32_bf16 v[220:223], a[28:31], a[184:187], v[220:223]\n"
"buffer_load_dwordx4 v105, s[44:47], s61 offen offset:1024 lds\n"
"v_mfma_f32_16x16x32_bf16 v[252:255], a[24:27], a[172:175], v[252:255]\n"
"v_mfma_f32_16x16x32_bf16 v[252:255], a[28:31], a[188:191], v[252:255]\n"
"s_waitcnt lgkmcnt(4)\n"
"v_mfma_f32_16x16x32_bf16 v[128:131], a[0:3], a[64:67], v[128:131]\n"
"ds_read_b64_tr_b16 a[16:17], v108 offset:256\n"
"v_mfma_f32_16x16x32_bf16 v[128:131], a[4:7], a[80:83], v[128:131]\n"
"buffer_load_dwordx4 a[136:139], v102, s[40:43], s60 offen sc1\n"
"v_mfma_f32_16x16x32_bf16 v[160:163], a[0:3], a[68:71], v[160:163]\n"
"ds_read_b64_tr_b16 a[18:19], v108 offset:4352\n"
"v_mfma_f32_16x16x32_bf16 v[160:163], a[4:7], a[84:87], v[160:163]\n"
"ds_read_b64_tr_b16 a[20:21], v108 offset:8448\n"
"v_mfma_f32_16x16x32_bf16 v[192:195], a[0:3], a[72:75], v[192:195]\n"
"ds_read_b64_tr_b16 a[22:23], v108 offset:12544\n"
"v_mfma_f32_16x16x32_bf16 v[192:195], a[4:7], a[88:91], v[192:195]\n"
"buffer_load_dwordx4 v106, s[44:47], s61 offen offset:2048 lds\n"
"v_mfma_f32_16x16x32_bf16 v[224:227], a[0:3], a[76:79], v[224:227]\n"
"v_mfma_f32_16x16x32_bf16 v[224:227], a[4:7], a[92:95], v[224:227]\n"
"s_waitcnt lgkmcnt(4)\n"
"v_mfma_f32_16x16x32_bf16 v[132:135], a[8:11], a[64:67], v[132:135]\n"
"ds_read_b64_tr_b16 a[24:25], v109 offset:256\n"
"v_mfma_f32_16x16x32_bf16 v[132:135], a[12:15], a[80:83], v[132:135]\n"
"buffer_load_dwordx4 a[140:143], v103, s[40:43], s60 offen sc1\n"
"v_mfma_f32_16x16x32_bf16 v[164:167], a[8:11], a[68:71], v[164:167]\n"
"ds_read_b64_tr_b16 a[26:27], v109 offset:4352\n"
"v_mfma_f32_16x16x32_bf16 v[164:167], a[12:15], a[84:87], v[164:167]\n"
"ds_read_b64_tr_b16 a[28:29], v109 offset:8448\n"
"v_mfma_f32_16x16x32_bf16 v[196:199], a[8:11], a[72:75], v[196:199]\n"
"ds_read_b64_tr_b16 a[30:31], v109 offset:12544\n"
"v_mfma_f32_16x16x32_bf16 v[196:199], a[12:15], a[88:91], v[196:199]\n"
"buffer_load_dwordx4 v107, s[44:47], s61 offen offset:3072 lds\n"
"s_add_u32 s61, s61, 32768\n"
"v_mfma_f32_16x16x32_bf16 v[228:231], a[8:11], a[76:79], v[228:231]\n"
"v_mfma_f32_16x16x32_bf16 v[228:231], a[12:15], a[92:95], v[228:231]\n"
"s_waitcnt lgkmcnt(4)\n"
"v_mfma_f32_16x16x32_bf16 v[136:139], a[16:19], a[64:67], v[136:139]\n"
"buffer_load_dwordx4 a[144:147], v100, s[40:43], s60 offen offset:1024 sc1\n"
"v_mfma_f32_16x16x32_bf16 v[136:139], a[20:23], a[80:83], v[136:139]\n"
"ds_read_b64_tr_b16 a[0:1], v108 offset:512\n"
"v_mfma_f32_16x16x32_bf16 v[168:171], a[16:19], a[68:71], v[168:171]\n"
"ds_read_b64_tr_b16 a[2:3], v108 offset:4608\n"
"v_mfma_f32_16x16x32_bf16 v[168:171], a[20:23], a[84:87], v[168:171]\n"
"ds_read_b64_tr_b16 a[4:5], v108 offset:8704\n"
"s_add_u32 m0, s62, 36864\n"
"v_mfma_f32_16x16x32_bf16 v[200:203], a[16:19], a[72:75], v[200:203]\n"
"buffer_load_dwordx4 v104, s[44:47], s61 offen lds\n"
"v_mfma_f32_16x16x32_bf16 v[200:203], a[20:23], a[88:91], v[200:203]\n"
"ds_read_b64_tr_b16 a[6:7], v108 offset:12800\n"
"v_mfma_f32_16x16x32_bf16 v[232:235], a[16:19], a[76:79], v[232:235]\n"
"v_mfma_f32_16x16x32_bf16 v[232:235], a[20:23], a[92:95], v[232:235]\n"
"s_waitcnt lgkmcnt(4)\n"
"v_mfma_f32_16x16x32_bf16 v[140:143], a[24:27], a[64:67], v[140:143]\n"
"buffer_load_dwordx4 a[148:151], v101, s[40:43], s60 offen offset:1024 sc1\n"
"v_mfma_f32_16x16x32_bf16 v[140:143], a[28:31], a[80:83], v[140:143]\n"
"ds_read_b64_tr_b16 a[8:9], v109 offset:512\n"
"v_mfma_f32_16x16x32_bf16 v[172:175], a[24:27], a[68:71], v[172:175]\n"
"ds_read_b64_tr_b16 a[10:11], v109 offset:4608\n"
"v_mfma_f32_16x16x32_bf16 v[172:175], a[28:31], a[84:87], v[172:175]\n"
"ds_read_b64_tr_b16 a[12:13], v109 offset:8704\n"
"v_mfma_f32_16x16x32_bf16 v[204:207], a[24:27], a[72:75], v[204:207]\n"
"buffer_load_dwordx4 v105, s[44:47], s61 offen offset:1024 lds\n"
"v_mfma_f32_16x16x32_bf16 v[204:207], a[28:31], a[88:91], v[204:207]\n"
"ds_read_b64_tr_b16 a[14:15], v109 offset:12800\n"
"v_mfma_f32_16x16x32_bf16 v[236:239], a[24:27], a[76:79], v[236:239]\n"
"v_mfma_f32_16x16x32_bf16 v[236:239], a[28:31], a[92:95], v[236:239]\n"
"s_waitcnt lgkmcnt(4)\n"
"v_mfma_f32_16x16x32_bf16 v[144:147], a[0:3], a[64:67], v[144:147]\n"
"buffer_load_dwordx4 a[152:155], v102, s[40:43], s60 offen offset:1024 sc1\n"
"v_mfma_f32_16x16x32_bf16 v[144:147], a[4:7], a[80:83], v[144:147]\n"
"ds_read_b64_tr_b16 a[16:17], v108 offset:768\n"
"v_mfma_f32_16x16x32_bf16 v[176:179], a[0:3], a[68:71], v[176:179]\n"
"ds_read_b64_tr_b16 a[18:19], v108 offset:4864\n"
"v_mfma_f32_16x16x32_bf16 v[176:179], a[4:7], a[84:87], v[176:179]\n"
"ds_read_b64_tr_b16 a[20:21], v108 offset:8960\n"
"v_mfma_f32_16x16x32_bf16 v[208:211], a[0:3], a[72:75], v[208:211]\n"
"buffer_load_dwordx4 v106, s[44:47], s61 offen offset:2048 lds\n"
"v_mfma_f32_16x16x32_bf16 v[208:211], a[4:7], a[88:91], v[208:211]\n"
"ds_read_b64_tr_b16 a[22:23], v108 offset:13056\n"
"v_mfma_f32_16x16x32_bf16 v[240:243], a[0:3], a[76:79], v[240:243]\n"
"v_mfma_f32_16x16x32_bf16 v[240:243], a[4:7], a[92:95], v[240:243]\n"
"s_waitcnt lgkmcnt(4)\n"
"v_mfma_f32_16x16x32_bf16 v[148:151], a[8:11], a[64:67], v[148:151]\n"
"buffer_load_dwordx4 a[156:159], v103, s[40:43], s60 offen offset:1024 sc1\n"
"s_add_u32 s60, s60, 4096\n"
"v_mfma_f32_16x16x32_bf16 v[148:151], a[12:15], a[80:83], v[148:151]\n"
"ds_read_b64_tr_b16 a[24:25], v109 offset:768\n"
"v_mfma_f32_16x16x32_bf16 v[180:183], a[8:11], a[68:71], v[180:183]\n"
"ds_read_b64_tr_b16 a[26:27], v109 offset:4864\n"
"v_mfma_f32_16x16x32_bf16 v[180:183], a[12:15], a[84:87], v[180:183]\n"
"ds_read_b64_tr_b16 a[28:29], v109 offset:8960\n"
"v_mfma_f32_16x16x32_bf16 v[212:215], a[8:11], a[72:75], v[212:215]\n"
"buffer_load_dwordx4 v107, s[44:47], s61 offen offset:3072 lds\n"
"s_add_u32 s61, s61, 229376\n"
"v_mfma_f32_16x16x32_bf16 v[212:215], a[12:15], a[88:91], v[212:215]\n"
"ds_read_b64_tr_b16 a[30:31], v109 offset:13056\n"
"v_mfma_f32_16x16x32_bf16 v[244:247], a[8:11], a[76:79], v[244:247]\n"
"v_mfma_f32_16x16x32_bf16 v[244:247], a[12:15], a[92:95], v[244:247]\n"
"s_waitcnt vmcnt(16) lgkmcnt(4)\n"
"s_barrier\n"
"v_mfma_f32_16x16x32_bf16 v[152:155], a[16:19], a[64:67], v[152:155]\n"
"ds_read_b64_tr_b16 a[0:1], v110 offset:32768\n"
"v_mfma_f32_16x16x32_bf16 v[152:155], a[20:23], a[80:83], v[152:155]\n"
"buffer_load_dwordx4 a[160:163], v100, s[40:43], s60 offen sc1\n"
"v_mfma_f32_16x16x32_bf16 v[184:187], a[16:19], a[68:71], v[184:187]\n"
"ds_read_b64_tr_b16 a[2:3], v110 offset:36864\n"
"v_mfma_f32_16x16x32_bf16 v[184:187], a[20:23], a[84:87], v[184:187]\n"
"ds_read_b64_tr_b16 a[4:5], v110 offset:40960\n"
"v_mfma_f32_16x16x32_bf16 v[216:219], a[16:19], a[72:75], v[216:219]\n"
"ds_read_b64_tr_b16 a[6:7], v110 offset:45056\n"
"s_add_u32 m0, s62, 65536\n"
"v_mfma_f32_16x16x32_bf16 v[216:219], a[20:23], a[88:91], v[216:219]\n"
"buffer_load_dwordx4 v104, s[44:47], s61 offen lds\n"
"v_mfma_f32_16x16x32_bf16 v[248:251], a[16:19], a[76:79], v[248:251]\n"
"v_mfma_f32_16x16x32_bf16 v[248:251], a[20:23], a[92:95], v[248:251]\n"
"s_waitcnt lgkmcnt(4)\n"
"v_mfma_f32_16x16x32_bf16 v[156:159], a[24:27], a[64:67], v[156:159]\n"
"ds_read_b64_tr_b16 a[8:9], v111 offset:32768\n"
"v_mfma_f32_16x16x32_bf16 v[156:159], a[28:31], a[80:83], v[156:159]\n"
"buffer_load_dwordx4 a[164:167], v101, s[40:43], s60 offen sc1\n"
"v_mfma_f32_16x16x32_bf16 v[188:191], a[24:27], a[68:71], v[188:191]\n"
"ds_read_b64_tr_b16 a[10:11], v111 offset:36864\n"
"v_mfma_f32_16x16x32_bf16 v[188:191], a[28:31], a[84:87], v[188:191]\n"
"ds_read_b64_tr_b16 a[12:13], v111 offset:40960\n"
"v_mfma_f32_16x16x32_bf16 v[220:223], a[24:27], a[72:75], v[220:223]\n"
"ds_read_b64_tr_b16 a[14:15], v111 offset:45056\n"
"v_mfma_f32_16x16x32_bf16 v[220:223], a[28:31], a[88:91], v[220:223]\n"
"buffer_load_dwordx4 v105, s[44:47], s61 offen offset:1024 lds\n"
"v_mfma_f32_16x16x32_bf16 v[252:255], a[24:27], a[76:79], v[252:255]\n"
"v_mfma_f32_16x16x32_bf16 v[252:255], a[28:31], a[92:95], v[252:255]\n"
"s_waitcnt lgkmcnt(4)\n"
"v_mfma_f32_16x16x32_bf16 v[128:131], a[0:3], a[96:99], v[128:131]\n"
"ds_read_b64_tr_b16 a[16:17], v110 offset:33024\n"
"v_mfma_f32_16x16x32_bf16 v[128:131], a[4:7], a[112:115], v[128:131]\n"
"buffer_load_dwordx4 a[168:171], v102, s[40:43], s60 offen sc1\n"
"v_mfma_f32_16x16x32_bf16 v[160:163], a[0:3], a[100:103], v[160:163]\n"
"ds_read_b64_tr_b16 a[18:19], v110 offset:37120\n"
"v_mfma_f32_16x16x32_bf16 v[160:163], a[4:7], a[116:119], v[160:163]\n"
"ds_read_b64_tr_b16 a[20:21], v110 offset:41216\n"
"v_mfma_f32_16x16x32_bf16 v[192:195], a[0:3], a[104:107], v[192:195]\n"
"ds_read_b64_tr_b16 a[22:23], v110 offset:45312\n"
"v_mfma_f32_16x16x32_bf16 v[192:195], a[4:7], a[120:123], v[192:195]\n"
"buffer_load_dwordx4 v106, s[44:47], s61 offen offset:2048 lds\n"
"v_mfma_f32_16x16x32_bf16 v[224:227], a[0:3], a[108:111], v[224:227]\n"
"v_mfma_f32_16x16x32_bf16 v[224:227], a[4:7], a[124:127], v[224:227]\n"
"s_waitcnt lgkmcnt(4)\n"
"v_mfma_f32_16x16x32_bf16 v[132:135], a[8:11], a[96:99], v[132:135]\n"
"ds_read_b64_tr_b16 a[24:25], v111 offset:33024\n"
"v_mfma_f32_16x16x32_bf16 v[132:135], a[12:15], a[112:115], v[132:135]\n"
"buffer_load_dwordx4 a[172:175], v103, s[40:43], s60 offen sc1\n"
"v_mfma_f32_16x16x32_bf16 v[164:167], a[8:11], a[100:103], v[164:167]\n"
"ds_read_b64_tr_b16 a[26:27], v111 offset:37120\n"
"v_mfma_f32_16x16x32_bf16 v[164:167], a[12:15], a[116:119], v[164:167]\n"
"ds_read_b64_tr_b16 a[28:29], v111 offset:41216\n"
"v_mfma_f32_16x16x32_bf16 v[196:199], a[8:11], a[104:107], v[196:199]\n"
"ds_read_b64_tr_b16 a[30:31], v111 offset:45312\n"
"v_mfma_f32_16x16x32_bf16 v[196:199], a[12:15], a[120:123], v[196:199]\n"
"buffer_load_dwordx4 v107, s[44:47], s61 offen offset:3072 lds\n"
"s_add_u32 s61, s61, 32768\n"
"v_mfma_f32_16x16x32_bf16 v[228:231], a[8:11], a[108:111], v[228:231]\n"
"v_mfma_f32_16x16x32_bf16 v[228:231], a[12:15], a[124:127], v[228:231]\n"
"s_waitcnt lgkmcnt(4)\n"
"v_mfma_f32_16x16x32_bf16 v[136:139], a[16:19], a[96:99], v[136:139]\n"
"buffer_load_dwordx4 a[176:179], v100, s[40:43], s60 offen offset:1024 sc1\n"
"v_mfma_f32_16x16x32_bf16 v[136:139], a[20:23], a[112:115], v[136:139]\n"
"ds_read_b64_tr_b16 a[0:1], v110 offset:33280\n"
"v_mfma_f32_16x16x32_bf16 v[168:171], a[16:19], a[100:103], v[168:171]\n"
"ds_read_b64_tr_b16 a[2:3], v110 offset:37376\n"
"v_mfma_f32_16x16x32_bf16 v[168:171], a[20:23], a[116:119], v[168:171]\n"
"ds_read_b64_tr_b16 a[4:5], v110 offset:41472\n"
"s_add_u32 m0, s62, 69632\n"
"v_mfma_f32_16x16x32_bf16 v[200:203], a[16:19], a[104:107], v[200:203]\n"
"buffer_load_dwordx4 v104, s[44:47], s61 offen lds\n"
"v_mfma_f32_16x16x32_bf16 v[200:203], a[20:23], a[120:123], v[200:203]\n"
"ds_read_b64_tr_b16 a[6:7], v110 offset:45568\n"
"v_mfma_f32_16x16x32_bf16 v[232:235], a[16:19], a[108:111], v[232:235]\n"
"v_mfma_f32_16x16x32_bf16 v[232:235], a[20:23], a[124:127], v[232:235]\n"
"s_waitcnt lgkmcnt(4)\n"
"v_mfma_f32_16x16x32_bf16 v[140:143], a[24:27], a[96:99], v[140:143]\n"
"buffer_load_dwordx4 a[180:183], v101, s[40:43], s60 offen offset:1024 sc1\n"
"v_mfma_f32_16x16x32_bf16 v[140:143], a[28:31], a[112:115], v[140:143]\n"
"ds_read_b64_tr_b16 a[8:9], v111 offset:33280\n"
"v_mfma_f32_16x16x32_bf16 v[172:175], a[24:27], a[100:103], v[172:175]\n"
"ds_read_b64_tr_b16 a[10:11], v111 offset:37376\n"
"v_mfma_f32_16x16x32_bf16 v[172:175], a[28:31], a[116:119], v[172:175]\n"
"ds_read_b64_tr_b16 a[12:13], v111 offset:41472\n"
"v_mfma_f32_16x16x32_bf16 v[204:207], a[24:27], a[104:107], v[204:207]\n"
"buffer_load_dwordx4 v105, s[44:47], s61 offen offset:1024 lds\n"
"v_mfma_f32_16x16x32_bf16 v[204:207], a[28:31], a[120:123], v[204:207]\n"
"ds_read_b64_tr_b16 a[14:15], v111 offset:45568\n"
"v_mfma_f32_16x16x32_bf16 v[236:239], a[24:27], a[108:111], v[236:239]\n"
"v_mfma_f32_16x16x32_bf16 v[236:239], a[28:31], a[124:127], v[236:239]\n"
"s_waitcnt lgkmcnt(4)\n"
"v_mfma_f32_16x16x32_bf16 v[144:147], a[0:3], a[96:99], v[144:147]\n"
"buffer_load_dwordx4 a[184:187], v102, s[40:43], s60 offen offset:1024 sc1\n"
"v_mfma_f32_16x16x32_bf16 v[144:147], a[4:7], a[112:115], v[144:147]\n"
"ds_read_b64_tr_b16 a[16:17], v110 offset:33536\n"
"v_mfma_f32_16x16x32_bf16 v[176:179], a[0:3], a[100:103], v[176:179]\n"
"ds_read_b64_tr_b16 a[18:19], v110 offset:37632\n"
"v_mfma_f32_16x16x32_bf16 v[176:179], a[4:7], a[116:119], v[176:179]\n"
"ds_read_b64_tr_b16 a[20:21], v110 offset:41728\n"
"v_mfma_f32_16x16x32_bf16 v[208:211], a[0:3], a[104:107], v[208:211]\n"
"buffer_load_dwordx4 v106, s[44:47], s61 offen offset:2048 lds\n"
"v_mfma_f32_16x16x32_bf16 v[208:211], a[4:7], a[120:123], v[208:211]\n"
"ds_read_b64_tr_b16 a[22:23], v110 offset:45824\n"
"v_mfma_f32_16x16x32_bf16 v[240:243], a[0:3], a[108:111], v[240:243]\n"
"v_mfma_f32_16x16x32_bf16 v[240:243], a[4:7], a[124:127], v[240:243]\n"
"s_waitcnt lgkmcnt(4)\n"
"v_mfma_f32_16x16x32_bf16 v[148:151], a[8:11], a[96:99], v[148:151]\n"
"buffer_load_dwordx4 a[188:191], v103, s[40:43], s60 offen offset:1024 sc1\n"
"s_add_u32 s60, s60, 4096\n"
"v_mfma_f32_16x16x32_bf16 v[148:151], a[12:15], a[112:115], v[148:151]\n"
"ds_read_b64_tr_b16 a[24:25], v111 offset:33536\n"
"v_mfma_f32_16x16x32_bf16 v[180:183], a[8:11], a[100:103], v[180:183]\n"
"ds_read_b64_tr_b16 a[26:27], v111 offset:37632\n"
"v_mfma_f32_16x16x32_bf16 v[180:183], a[12:15], a[116:119], v[180:183]\n"
"ds_read_b64_tr_b16 a[28:29], v111 offset:41728\n"
"v_mfma_f32_16x16x32_bf16 v[212:215], a[8:11], a[104:107], v[212:215]\n"
"buffer_load_dwordx4 v107, s[44:47], s61 offen offset:3072 lds\n"
"s_add_u32 s61, s61, 229376\n"
"v_mfma_f32_16x16x32_bf16 v[212:215], a[12:15], a[120:123], v[212:215]\n"
"ds_read_b64_tr_b16 a[30:31], v111 offset:45824\n"
"v_mfma_f32_16x16x32_bf16 v[244:247], a[8:11], a[108:111], v[244:247]\n"
"v_mfma_f32_16x16x32_bf16 v[244:247], a[12:15], a[124:127], v[244:247]\n"
"s_waitcnt vmcnt(16) lgkmcnt(4)\n"
"s_barrier\n"
"v_mfma_f32_16x16x32_bf16 v[152:155], a[16:19], a[96:99], v[152:155]\n"
"ds_read_b64_tr_b16 a[0:1], v108 offset:32768\n"
"v_mfma_f32_16x16x32_bf16 v[152:155], a[20:23], a[112:115], v[152:155]\n"
"buffer_load_dwordx4 a[64:67], v100, s[40:43], s60 offen sc1\n"
"v_mfma_f32_16x16x32_bf16 v[184:187], a[16:19], a[100:103], v[184:187]\n"
"ds_read_b64_tr_b16 a[2:3], v108 offset:36864\n"
"v_mfma_f32_16x16x32_bf16 v[184:187], a[20:23], a[116:119], v[184:187]\n"
"ds_read_b64_tr_b16 a[4:5], v108 offset:40960\n"
"v_mfma_f32_16x16x32_bf16 v[216:219], a[16:19], a[104:107], v[216:219]\n"
"ds_read_b64_tr_b16 a[6:7], v108 offset:45056\n"
"s_add_u32 m0, s62, 0\n"
"v_mfma_f32_16x16x32_bf16 v[216:219], a[20:23], a[120:123], v[216:219]\n"
"buffer_load_dwordx4 v104, s[44:47], s61 offen lds\n"
"v_mfma_f32_16x16x32_bf16 v[248:251], a[16:19], a[108:111], v[248:251]\n"
"v_mfma_f32_16x16x32_bf16 v[248:251], a[20:23], a[124:127], v[248:251]\n"
"s_waitcnt lgkmcnt(4)\n"
"v_mfma_f32_16x16x32_bf16 v[156:159], a[24:27], a[96:99], v[156:159]\n"
"ds_read_b64_tr_b16 a[8:9], v109 offset:32768\n"
"v_mfma_f32_16x16x32_bf16 v[156:159], a[28:31], a[112:115], v[156:159]\n"
"buffer_load_dwordx4 a[68:71], v101, s[40:43], s60 offen sc1\n"
"v_mfma_f32_16x16x32_bf16 v[188:191], a[24:27], a[100:103], v[188:191]\n"
"ds_read_b64_tr_b16 a[10:11], v109 offset:36864\n"
"v_mfma_f32_16x16x32_bf16 v[188:191], a[28:31], a[116:119], v[188:191]\n"
"ds_read_b64_tr_b16 a[12:13], v109 offset:40960\n"
"v_mfma_f32_16x16x32_bf16 v[220:223], a[24:27], a[104:107], v[220:223]\n"
"ds_read_b64_tr_b16 a[14:15], v109 offset:45056\n"
"v_mfma_f32_16x16x32_bf16 v[220:223], a[28:31], a[120:123], v[220:223]\n"
"buffer_load_dwordx4 v105, s[44:47], s61 offen offset:1024 lds\n"
"v_mfma_f32_16x16x32_bf16 v[252:255], a[24:27], a[108:111], v[252:255]\n"
"v_mfma_f32_16x16x32_bf16 v[252:255], a[28:31], a[124:127], v[252:255]\n"
"s_waitcnt lgkmcnt(4)\n"
"v_mfma_f32_16x16x32_bf16 v[128:131], a[0:3], a[128:131], v[128:131]\n"
"ds_read_b64_tr_b16 a[16:17], v108 offset:33024\n"
"v_mfma_f32_16x16x32_bf16 v[128:131], a[4:7], a[144:147], v[128:131]\n"
"buffer_load_dwordx4 a[72:75], v102, s[40:43], s60 offen sc1\n"
"v_mfma_f32_16x16x32_bf16 v[160:163], a[0:3], a[132:135], v[160:163]\n"
"ds_read_b64_tr_b16 a[18:19], v108 offset:37120\n"
"v_mfma_f32_16x16x32_bf16 v[160:163], a[4:7], a[148:151], v[160:163]\n"
"ds_read_b64_tr_b16 a[20:21], v108 offset:41216\n"
"v_mfma_f32_16x16x32_bf16 v[192:195], a[0:3], a[136:139], v[192:195]\n"
"ds_read_b64_tr_b16 a[22:23], v108 offset:45312\n"
"v_mfma_f32_16x16x32_bf16 v[192:195], a[4:7], a[152:155], v[192:195]\n"
"buffer_load_dwordx4 v106, s[44:47], s61 offen offset:2048 lds\n"
"v_mfma_f32_16x16x32_bf16 v[224:227], a[0:3], a[140:143], v[224:227]\n"
"v_mfma_f32_16x16x32_bf16 v[224:227], a[4:7], a[156:159], v[224:227]\n"
"s_waitcnt lgkmcnt(4)\n"
"v_mfma_f32_16x16x32_bf16 v[132:135], a[8:11], a[128:131], v[132:135]\n"
"ds_read_b64_tr_b16 a[24:25], v109 offset:33024\n"
"v_mfma_f32_16x16x32_bf16 v[132:135], a[12:15], a[144:147], v[132:135]\n"
"buffer_load_dwordx4 a[76:79], v103, s[40:43], s60 offen sc1\n"
"v_mfma_f32_16x16x32_bf16 v[164:167], a[8:11], a[132:135], v[164:167]\n"
"ds_read_b64_tr_b16 a[26:27], v109 offset:37120\n"
"v_mfma_f32_16x16x32_bf16 v[164:167], a[12:15], a[148:151], v[164:167]\n"
"ds_read_b64_tr_b16 a[28:29], v109 offset:41216\n"
"v_mfma_f32_16x16x32_bf16 v[196:199], a[8:11], a[136:139], v[196:199]\n"
"ds_read_b64_tr_b16 a[30:31], v109 offset:45312\n"
"v_mfma_f32_16x16x32_bf16 v[196:199], a[12:15], a[152:155], v[196:199]\n"
"buffer_load_dwordx4 v107, s[44:47], s61 offen offset:3072 lds\n"
"s_add_u32 s61, s61, 32768\n"
"v_mfma_f32_16x16x32_bf16 v[228:231], a[8:11], a[140:143], v[228:231]\n"
"v_mfma_f32_16x16x32_bf16 v[228:231], a[12:15], a[156:159], v[228:231]\n"
"s_waitcnt lgkmcnt(4)\n"
"v_mfma_f32_16x16x32_bf16 v[136:139], a[16:19], a[128:131], v[136:139]\n"
"buffer_load_dwordx4 a[80:83], v100, s[40:43], s60 offen offset:1024 sc1\n"
"v_mfma_f32_16x16x32_bf16 v[136:139], a[20:23], a[144:147], v[136:139]\n"
"ds_read_b64_tr_b16 a[0:1], v108 offset:33280\n"
"v_mfma_f32_16x16x32_bf16 v[168:171], a[16:19], a[132:135], v[168:171]\n"
"ds_read_b64_tr_b16 a[2:3], v108 offset:37376\n"
"v_mfma_f32_16x16x32_bf16 v[168:171], a[20:23], a[148:151], v[168:171]\n"
"ds_read_b64_tr_b16 a[4:5], v108 offset:41472\n"
"s_add_u32 m0, s62, 4096\n"
"v_mfma_f32_16x16x32_bf16 v[200:203], a[16:19], a[136:139], v[200:203]\n"
"buffer_load_dwordx4 v104, s[44:47], s61 offen lds\n"
"v_mfma_f32_16x16x32_bf16 v[200:203], a[20:23], a[152:155], v[200:203]\n"
"ds_read_b64_tr_b16 a[6:7], v108 offset:45568\n"
"v_mfma_f32_16x16x32_bf16 v[232:235], a[16:19], a[140:143], v[232:235]\n"
"v_mfma_f32_16x16x32_bf16 v[232:235], a[20:23], a[156:159], v[232:235]\n"
"s_waitcnt lgkmcnt(4)\n"
"v_mfma_f32_16x16x32_bf16 v[140:143], a[24:27], a[128:131], v[140:143]\n"
"buffer_load_dwordx4 a[84:87], v101, s[40:43], s60 offen offset:1024 sc1\n"
"v_mfma_f32_16x16x32_bf16 v[140:143], a[28:31], a[144:147], v[140:143]\n"
"ds_read_b64_tr_b16 a[8:9], v109 offset:33280\n"
"v_mfma_f32_16x16x32_bf16 v[172:175], a[24:27], a[132:135], v[172:175]\n"
"ds_read_b64_tr_b16 a[10:11], v109 offset:37376\n"
"v_mfma_f32_16x16x32_bf16 v[172:175], a[28:31], a[148:151], v[172:175]\n"
"ds_read_b64_tr_b16 a[12:13], v109 offset:41472\n"
"v_mfma_f32_16x16x32_bf16 v[204:207], a[24:27], a[136:139], v[204:207]\n"
"buffer_load_dwordx4 v105, s[44:47], s61 offen offset:1024 lds\n"
"v_mfma_f32_16x16x32_bf16 v[204:207], a[28:31], a[152:155], v[204:207]\n"
"ds_read_b64_tr_b16 a[14:15], v109 offset:45568\n"
"v_mfma_f32_16x16x32_bf16 v[236:239], a[24:27], a[140:143], v[236:239]\n"
"v_mfma_f32_16x16x32_bf16 v[236:239], a[28:31], a[156:159], v[236:239]\n"
"s_waitcnt lgkmcnt(4)\n"
"v_mfma_f32_16x16x32_bf16 v[144:147], a[0:3], a[128:131], v[144:147]\n"
"buffer_load_dwordx4 a[88:91], v102, s[40:43], s60 offen offset:1024 sc1\n"
"v_mfma_f32_16x16x32_bf16 v[144:147], a[4:7], a[144:147], v[144:147]\n"
"ds_read_b64_tr_b16 a[16:17], v108 offset:33536\n"
"v_mfma_f32_16x16x32_bf16 v[176:179], a[0:3], a[132:135], v[176:179]\n"
"ds_read_b64_tr_b16 a[18:19], v108 offset:37632\n"
"v_mfma_f32_16x16x32_bf16 v[176:179], a[4:7], a[148:151], v[176:179]\n"
"ds_read_b64_tr_b16 a[20:21], v108 offset:41728\n"
"v_mfma_f32_16x16x32_bf16 v[208:211], a[0:3], a[136:139], v[208:211]\n"
"buffer_load_dwordx4 v106, s[44:47], s61 offen offset:2048 lds\n"
"v_mfma_f32_16x16x32_bf16 v[208:211], a[4:7], a[152:155], v[208:211]\n"
"ds_read_b64_tr_b16 a[22:23], v108 offset:45824\n"
"v_mfma_f32_16x16x32_bf16 v[240:243], a[0:3], a[140:143], v[240:243]\n"
"v_mfma_f32_16x16x32_bf16 v[240:243], a[4:7], a[156:159], v[240:243]\n"
"s_waitcnt lgkmcnt(4)\n"
"v_mfma_f32_16x16x32_bf16 v[148:151], a[8:11], a[128:131], v[148:151]\n"
"buffer_load_dwordx4 a[92:95], v103, s[40:43], s60 offen offset:1024 sc1\n"
"s_add_u32 s60, s60, 4096\n"
"v_mfma_f32_16x16x32_bf16 v[148:151], a[12:15], a[144:147], v[148:151]\n"
"ds_read_b64_tr_b16 a[24:25], v109 offset:33536\n"
"v_mfma_f32_16x16x32_bf16 v[180:183], a[8:11], a[132:135], v[180:183]\n"
"ds_read_b64_tr_b16 a[26:27], v109 offset:37632\n"
"v_mfma_f32_16x16x32_bf16 v[180:183], a[12:15], a[148:151], v[180:183]\n"
"ds_read_b64_tr_b16 a[28:29], v109 offset:41728\n"
"v_mfma_f32_16x16x32_bf16 v[212:215], a[8:11], a[136:139], v[212:215]\n"
"buffer_load_dwordx4 v107, s[44:47], s61 offen offset:3072 lds\n"
"s_add_u32 s61, s61, 229376\n"
"v_mfma_f32_16x16x32_bf16 v[212:215], a[12:15], a[152:155], v[212:215]\n"
"ds_read_b64_tr_b16 a[30:31], v109 offset:45824\n"
"v_mfma_f32_16x16x32_bf16 v[244:247], a[8:11], a[140:143], v[244:247]\n"
"s_sub_u32 s63, s63, 1\n"
"v_mfma_f32_16x16x32_bf16 v[244:247], a[12:15], a[156:159], v[244:247]\n"
"s_waitcnt vmcnt(16) lgkmcnt(4)\n"
"s_barrier\n"
"s_cbranch_scc0 LD_%=\n"
"v_mfma_f32_16x16x32_bf16 v[152:155], a[16:19], a[128:131], v[152:155]\n"
"ds_read_b64_tr_b16 a[0:1], v110 offset:0\n"
"v_mfma_f32_16x16x32_bf16 v[152:155], a[20:23], a[144:147], v[152:155]\n"
"buffer_load_dwordx4 a[96:99], v100, s[40:43], s60 offen sc1\n"
"v_mfma_f32_16x16x32_bf16 v[184:187], a[16:19], a[132:135], v[184:187]\n"
"ds_read_b64_tr_b16 a[2:3], v110 offset:4096\n"
"v_mfma_f32_16x16x32_bf16 v[184:187], a[20:23], a[148:151], v[184:187]\n"
"ds_read_b64_tr_b16 a[4:5], v110 offset:8192\n"
"v_mfma_f32_16x16x32_bf16 v[216:219], a[16:19], a[136:139], v[216:219]\n"
"ds_read_b64_tr_b16 a[6:7], v110 offset:12288\n"
"s_add_u32 m0, s62, 98304\n"
"v_mfma_f32_16x16x32_bf16 v[216:219], a[20:23], a[152:155], v[216:219]\n"
"buffer_load_dwordx4 v104, s[44:47], s61 offen lds\n"
"v_mfma_f32_16x16x32_bf16 v[248:251], a[16:19], a[140:143], v[248:251]\n"
"v_mfma_f32_16x16x32_bf16 v[248:251], a[20:23], a[156:159], v[248:251]\n"
"s_waitcnt lgkmcnt(4)\n"
"v_mfma_f32_16x16x32_bf16 v[156:159], a[24:27], a[128:131], v[156:159]\n"
"ds_read_b64_tr_b16 a[8:9], v111 offset:0\n"
"v_mfma_f32_16x16x32_bf16 v[156:159], a[28:31], a[144:147], v[156:159]\n"
"buffer_load_dwordx4 a[100:103], v101, s[40:43], s60 offen sc1\n"
"v_mfma_f32_16x16x32_bf16 v[188:191], a[24:27], a[132:135], v[188:191]\n"
"ds_read_b64_tr_b16 a[10:11], v111 offset:4096\n"
"v_mfma_f32_16x16x32_bf16 v[188:191], a[28:31], a[148:151], v[188:191]\n"
"ds_read_b64_tr_b16 a[12:13], v111 offset:8192\n"
"v_mfma_f32_16x16x32_bf16 v[220:223], a[24:27], a[136:139], v[220:223]\n"
"ds_read_b64_tr_b16 a[14:15], v111 offset:12288\n"
"v_mfma_f32_16x16x32_bf16 v[220:223], a[28:31], a[152:155], v[220:223]\n"
"buffer_load_dwordx4 v105, s[44:47], s61 offen offset:1024 lds\n"
"v_mfma_f32_16x16x32_bf16 v[252:255], a[24:27], a[140:143], v[252:255]\n"
"v_mfma_f32_16x16x32_bf16 v[252:255], a[28:31], a[156:159], v[252:255]\n"
"s_waitcnt lgkmcnt(4)\n"
"v_mfma_f32_16x16x32_bf16 v[128:131], a[0:3], a[160:163], v[128:131]\n"
"ds_read_b64_tr_b16 a[16:17], v110 offset:256\n"
"v_mfma_f32_16x16x32_bf16 v[128:131], a[4:7], a[176:179], v[128:131]\n"
"buffer_load_dwordx4 a[104:107], v102, s[40:43], s60 offen sc1\n"
"v_mfma_f32_16x16x32_bf16 v[160:163], a[0:3], a[164:167], v[160:163]\n"
"ds_read_b64_tr_b16 a[18:19], v110 offset:4352\n"
"v_mfma_f32_16x16x32_bf16 v[160:163], a[4:7], a[180:183], v[160:163]\n"
"ds_read_b64_tr_b16 a[20:21], v110 offset:8448\n"
"v_mfma_f32_16x16x32_bf16 v[192:195], a[0:3], a[168:171], v[192:195]\n"
"ds_read_b64_tr_b16 a[22:23], v110 offset:12544\n"
"v_mfma_f32_16x16x32_bf16 v[192:195], a[4:7], a[184:187], v[192:195]\n"
"buffer_load_dwordx4 v106, s[44:47], s61 offen offset:2048 lds\n"
"v_mfma_f32_16x16x32_bf16 v[224:227], a[0:3], a[172:175], v[224:227]\n"
"v_mfma_f32_16x16x32_bf16 v[224:227], a[4:7], a[188:191], v[224:227]\n"
"s_waitcnt lgkmcnt(4)\n"
"v_mfma_f32_16x16x32_bf16 v[132:135], a[8:11], a[160:163], v[132:135]\n"
"ds_read_b64_tr_b16 a[24:25], v111 offset:256\n"
"v_mfma_f32_16x16x32_bf16 v[132:135], a[12:15], a[176:179], v[132:135]\n"
"buffer_load_dwordx4 a[108:111], v103, s[40:43], s60 offen sc1\n"
"v_mfma_f32_16x16x32_bf16 v[164:167], a[8:11], a[164:167], v[164:167]\n"
"ds_read_b64_tr_b16 a[26:27], v111 offset:4352\n"
"v_mfma_f32_16x16x32_bf16 v[164:167], a[12:15], a[180:183], v[164:167]\n"
"ds_read_b64_tr_b16 a[28:29], v111 offset:8448\n"
"v_mfma_f32_16x16x32_bf16 v[196:199], a[8:11], a[168:171], v[196:199]\n"
"ds_read_b64_tr_b16 a[30:31], v111 offset:12544\n"
"v_mfma_f32_16x16x32_bf16 v[196:199], a[12:15], a[184:187], v[196:199]\n"
"buffer_load_dwordx4 v107, s[44:47], s61 offen offset:3072 lds\n"
"s_add_u32 s61, s61, 32768\n"
"v_mfma_f32_16x16x32_bf16 v[228:231], a[8:11], a[172:175], v[228:231]\n"
"v_mfma_f32_16x16x32_bf16 v[228:231], a[12:15], a[188:191], v[228:231]\n"
"s_waitcnt lgkmcnt(4)\n"
"v_mfma_f32_16x16x32_bf16 v[136:139], a[16:19], a[160:163], v[136:139]\n"
"buffer_load_dwordx4 a[112:115], v100, s[40:43], s60 offen offset:1024 sc1\n"
"v_mfma_f32_16x16x32_bf16 v[136:139], a[20:23], a[176:179], v[136:139]\n"
"ds_read_b64_tr_b16 a[0:1], v110 offset:512\n"
"v_mfma_f32_16x16x32_bf16 v[168:171], a[16:19], a[164:167], v[168:171]\n"
"ds_read_b64_tr_b16 a[2:3], v110 offset:4608\n"
"v_mfma_f32_16x16x32_bf16 v[168:171], a[20:23], a[180:183], v[168:171]\n"
"ds_read_b64_tr_b16 a[4:5], v110 offset:8704\n"
"s_add_u32 m0, s62, 102400\n"
"v_mfma_f32_16x16x32_bf16 v[200:203], a[16:19], a[168:171], v[200:203]\n"
"buffer_load_dwordx4 v104, s[44:47], s61 offen lds\n"
"v_mfma_f32_16x16x32_bf16 v[200:203], a[20:23], a[184:187], v[200:203]\n"
"ds_read_b64_tr_b16 a[6:7], v110 offset:12800\n"
"v_mfma_f32_16x16x32_bf16 v[232:235], a[16:19], a[172:175], v[232:235]\n"
"v_mfma_f32_16x16x32_bf16 v[232:235], a[20:23], a[188:191], v[232:235]\n"
"s_waitcnt lgkmcnt(4)\n"
"v_mfma_f32_16x16x32_bf16 v[140:143], a[24:27], a[160:163], v[140:143]\n"
"buffer_load_dwordx4 a[116:119], v101, s[40:43], s60 offen offset:1024 sc1\n"
"v_mfma_f32_16x16x32_bf16 v[140:143], a[28:31], a[176:179], v[140:143]\n"
"ds_read_b64_tr_b16 a[8:9], v111 offset:512\n"
"v_mfma_f32_16x16x32_bf16 v[172:175], a[24:27], a[164:167], v[172:175]\n"
"ds_read_b64_tr_b16 a[10:11], v111 offset:4608\n"
"v_mfma_f32_16x16x32_bf16 v[172:175], a[28:31], a[180:183], v[172:175]\n"
"ds_read_b64_tr_b16 a[12:13], v111 offset:8704\n"
"v_mfma_f32_16x16x32_bf16 v[204:207], a[24:27], a[168:171], v[204:207]\n"
"buffer_load_dwordx4 v105, s[44:47], s61 offen offset:1024 lds\n"
"v_mfma_f32_16x16x32_bf16 v[204:207], a[28:31], a[184:187], v[204:207]\n"
"ds_read_b64_tr_b16 a[14:15], v111 offset:12800\n"
"v_mfma_f32_16x16x32_bf16 v[236:239], a[24:27], a[172:175], v[236:239]\n"
"v_mfma_f32_16x16x32_bf16 v[236:239], a[28:31], a[188:191], v[236:239]\n"
"s_waitcnt lgkmcnt(4)\n"
"v_mfma_f32_16x16x32_bf16 v[144:147], a[0:3], a[160:163], v[144:147]\n"
"buffer_load_dwordx4 a[120:123], v102, s[40:43], s60 offen offset:1024 sc1\n"
"v_mfma_f32_16x16x32_bf16 v[144:147], a[4:7], a[176:179], v[144:147]\n"
"ds_read_b64_tr_b16 a[16:17], v110 offset:768\n"
"v_mfma_f32_16x16x32_bf16 v[176:179], a[0:3], a[164:167], v[176:179]\n"
"ds_read_b64_tr_b16 a[18:19], v110 offset:4864\n"
"v_mfma_f32_16x16x32_bf16 v[176:179], a[4:7], a[180:183], v[176:179]\n"
"ds_read_b64_tr_b16 a[20:21], v110 offset:8960\n"
"v_mfma_f32_16x16x32_bf16 v[208:211], a[0:3], a[168:171], v[208:211]\n"
"buffer_load_dwordx4 v106, s[44:47], s61 offen offset:2048 lds\n"
"v_mfma_f32_16x16x32_bf16 v[208:211], a[4:7], a[184:187], v[208:211]\n"
"ds_read_b64_tr_b16 a[22:23], v110 offset:13056\n"
"v_mfma_f32_16x16x32_bf16 v[240:243], a[0:3], a[172:175], v[240:243]\n"
"v_mfma_f32_16x16x32_bf16 v[240:243], a[4:7], a[188:191], v[240:243]\n"
"s_waitcnt lgkmcnt(4)\n"
"v_mfma_f32_16x16x32_bf16 v[148:151], a[8:11], a[160:163], v[148:151]\n"
"buffer_load_dwordx4 a[124:127], v103, s[40:43], s60 offen offset:1024 sc1\n"
"s_add_u32 s60, s60, 4096\n"
"v_mfma_f32_16x16x32_bf16 v[148:151], a[12:15], a[176:179], v[148:151]\n"
"ds_read_b64_tr_b16 a[24:25], v111 offset:768\n"
"v_mfma_f32_16x16x32_bf16 v[180:183], a[8:11], a[164:167], v[180:183]\n"
"ds_read_b64_tr_b16 a[26:27], v111 offset:4864\n"
"v_mfma_f32_16x16x32_bf16 v[180:183], a[12:15], a[180:183], v[180:183]\n"
"ds_read_b64_tr_b16 a[28:29], v111 offset:8960\n"
"v_mfma_f32_16x16x32_bf16 v[212:215], a[8:11], a[168:171], v[212:215]\n"
"buffer_load_dwordx4 v107, s[44:47], s61 offen offset:3072 lds\n"
"s_add_u32 s61, s61, 229376\n"
"v_mfma_f32_16x16x32_bf16 v[212:215], a[12:15], a[184:187], v[212:215]\n"
"ds_read_b64_tr_b16 a[30:31], v111 offset:13056\n"
"v_mfma_f32_16x16x32_bf16 v[244:247], a[8:11], a[172:175], v[244:247]\n"
"v_mfma_f32_16x16x32_bf16 v[244:247], a[12:15], a[188:191], v[244:247]\n"
"s_waitcnt vmcnt(16) lgkmcnt(4)\n"
"s_barrier\n"
"v_mfma_f32_16x16x32_bf16 v[152:155], a[16:19], a[160:163], v[152:155]\n"
"ds_read_b64_tr_b16 a[0:1], v108 offset:0\n"
"v_mfma_f32_16x16x32_bf16 v[152:155], a[20:23], a[176:179], v[152:155]\n"
"buffer_load_dwordx4 a[128:131], v100, s[40:43], s60 offen sc1\n"
"v_mfma_f32_16x16x32_bf16 v[184:187], a[16:19], a[164:167], v[184:187]\n"
"ds_read_b64_tr_b16 a[2:3], v108 offset:4096\n"
"v_mfma_f32_16x16x32_bf16 v[184:187], a[20:23], a[180:183], v[184:187]\n"
"ds_read_b64_tr_b16 a[4:5], v108 offset:8192\n"
"v_mfma_f32_16x16x32_bf16 v[216:219], a[16:19], a[168:171], v[216:219]\n"
"ds_read_b64_tr_b16 a[6:7], v108 offset:12288\n"
"s_add_u32 m0, s62, 32768\n"
"v_mfma_f32_16x16x32_bf16 v[216:219], a[20:23], a[184:187], v[216:219]\n"
"buffer_load_dwordx4 v104, s[44:47], s61 offen lds\n"
"v_mfma_f32_16x16x32_bf16 v[248:251], a[16:19], a[172:175], v[248:251]\n"
"v_mfma_f32_16x16x32_bf16 v[248:251], a[20:23], a[188:191], v[248:251]\n"
"s_waitcnt lgkmcnt(4)\n"
"v_mfma_f32_16x16x32_bf16 v[156:159], a[24:27], a[160:163], v[156:159]\n"
"ds_read_b64_tr_b16 a[8:9], v109 offset:0\n"
"v_mfma_f32_16x16x32_bf16 v[156:159], a[28:31], a[176:179], v[156:159]\n"
"buffer_load_dwordx4 a[132:135], v101, s[40:43], s60 offen sc1\n"
"v_mfma_f32_16x16x32_bf16 v[188:191], a[24:27], a[164:167], v[188:191]\n"
"ds_read_b64_tr_b16 a[10:11], v109 offset:4096\n"
"v_mfma_f32_16x16x32_bf16 v[188:191], a[28:31], a[180:183], v[188:191]\n"
"ds_read_b64_tr_b16 a[12:13], v109 offset:8192\n"
"v_mfma_f32_16x16x32_bf16 v[220:223], a[24:27], a[168:171], v[220:223]\n"
"ds_read_b64_tr_b16 a[14:15], v109 offset:12288\n"
"v_mfma_f32_16x16x32_bf16 v[220:223], a[28:31], a[184:187], v[220:223]\n"
"buffer_load_dwordx4 v105, s[44:47], s61 offen offset:1024 lds\n"
"v_mfma_f32_16x16x32_bf16 v[252:255], a[24:27], a[172:175], v[252:255]\n"
"v_mfma_f32_16x16x32_bf16 v[252:255], a[28:31], a[188:191], v[252:255]\n"
"s_waitcnt lgkmcnt(4)\n"
"v_mfma_f32_16x16x32_bf16 v[128:131], a[0:3], a[64:67], v[128:131]\n"
"ds_read_b64_tr_b16 a[16:17], v108 offset:256\n"
"v_mfma_f32_16x16x32_bf16 v[128:131], a[4:7], a[80:83], v[128:131]\n"
"buffer_load_dwordx4 a[136:139], v102, s[40:43], s60 offen sc1\n"
"v_mfma_f32_16x16x32_bf16 v[160:163], a[0:3], a[68:71], v[160:163]\n"
"ds_read_b64_tr_b16 a[18:19], v108 offset:4352\n"
"v_mfma_f32_16x16x32_bf16 v[160:163], a[4:7], a[84:87], v[160:163]\n"
"ds_read_b64_tr_b16 a[20:21], v108 offset:8448\n"
"v_mfma_f32_16x16x32_bf16 v[192:195], a[0:3], a[72:75], v[192:195]\n"
"ds_read_b64_tr_b16 a[22:23], v108 offset:12544\n"
"v_mfma_f32_16x16x32_bf16 v[192:195], a[4:7], a[88:91], v[192:195]\n"
"buffer_load_dwordx4 v106, s[44:47], s61 offen offset:2048 lds\n"
"v_mfma_f32_16x16x32_bf16 v[224:227], a[0:3], a[76:79], v[224:227]\n"
"v_mfma_f32_16x16x32_bf16 v[224:227], a[4:7], a[92:95], v[224:227]\n"
"s_waitcnt lgkmcnt(4)\n"
"v_mfma_f32_16x16x32_bf16 v[132:135], a[8:11], a[64:67], v[132:135]\n"
"ds_read_b64_tr_b16 a[24:25], v109 offset:256\n"
"v_mfma_f32_16x16x32_bf16 v[132:135], a[12:15], a[80:83], v[132:135]\n"
"buffer_load_dwordx4 a[140:143], v103, s[40:43], s60 offen sc1\n"
"v_mfma_f32_16x16x32_bf16 v[164:167], a[8:11], a[68:71], v[164:167]\n"
"ds_read_b64_tr_b16 a[26:27], v109 offset:4352\n"
"v_mfma_f32_16x16x32_bf16 v[164:167], a[12:15], a[84:87], v[164:167]\n"
"ds_read_b64_tr_b16 a[28:29], v109 offset:8448\n"
"v_mfma_f32_16x16x32_bf16 v[196:199], a[8:11], a[72:75], v[196:199]\n"
"ds_read_b64_tr_b16 a[30:31], v109 offset:12544\n"
"v_mfma_f32_16x16x32_bf16 v[196:199], a[12:15], a[88:91], v[196:199]\n"
"buffer_load_dwordx4 v107, s[44:47], s61 offen offset:3072 lds\n"
"s_add_u32 s61, s61, 32768\n"
"v_mfma_f32_16x16x32_bf16 v[228:231], a[8:11], a[76:79], v[228:231]\n"
"v_mfma_f32_16x16x32_bf16 v[228:231], a[12:15], a[92:95], v[228:231]\n"
"s_waitcnt lgkmcnt(4)\n"
"v_mfma_f32_16x16x32_bf16 v[136:139], a[16:19], a[64:67], v[136:139]\n"
"buffer_load_dwordx4 a[144:147], v100, s[40:43], s60 offen offset:1024 sc1\n"
"v_mfma_f32_16x16x32_bf16 v[136:139], a[20:23], a[80:83], v[136:139]\n"
"ds_read_b64_tr_b16 a[0:1], v108 offset:512\n"
"v_mfma_f32_16x16x32_bf16 v[168:171], a[16:19], a[68:71], v[168:171]\n"
"ds_read_b64_tr_b16 a[2:3], v108 offset:4608\n"
"v_mfma_f32_16x16x32_bf16 v[168:171], a[20:23], a[84:87], v[168:171]\n"
"ds_read_b64_tr_b16 a[4:5], v108 offset:8704\n"
"s_add_u32 m0, s62, 36864\n"
"v_mfma_f32_16x16x32_bf16 v[200:203], a[16:19], a[72:75], v[200:203]\n"
"buffer_load_dwordx4 v104, s[44:47], s61 offen lds\n"
"v_mfma_f32_16x16x32_bf16 v[200:203], a[20:23], a[88:91], v[200:203]\n"
"ds_read_b64_tr_b16 a[6:7], v108 offset:12800\n"
"v_mfma_f32_16x16x32_bf16 v[232:235], a[16:19], a[76:79], v[232:235]\n"
"v_mfma_f32_16x16x32_bf16 v[232:235], a[20:23], a[92:95], v[232:235]\n"
"s_waitcnt lgkmcnt(4)\n"
"v_mfma_f32_16x16x32_bf16 v[140:143], a[24:27], a[64:67], v[140:143]\n"
"buffer_load_dwordx4 a[148:151], v101, s[40:43], s60 offen offset:1024 sc1\n"
"v_mfma_f32_16x16x32_bf16 v[140:143], a[28:31], a[80:83], v[140:143]\n"
"ds_read_b64_tr_b16 a[8:9], v109 offset:512\n"
"v_mfma_f32_16x16x32_bf16 v[172:175], a[24:27], a[68:71], v[172:175]\n"
"ds_read_b64_tr_b16 a[10:11], v109 offset:4608\n"
"v_mfma_f32_16x16x32_bf16 v[172:175], a[28:31], a[84:87], v[172:175]\n"
"ds_read_b64_tr_b16 a[12:13], v109 offset:8704\n"
"v_mfma_f32_16x16x32_bf16 v[204:207], a[24:27], a[72:75], v[204:207]\n"
"buffer_load_dwordx4 v105, s[44:47], s61 offen offset:1024 lds\n"
"v_mfma_f32_16x16x32_bf16 v[204:207], a[28:31], a[88:91], v[204:207]\n"
"ds_read_b64_tr_b16 a[14:15], v109 offset:12800\n"
"v_mfma_f32_16x16x32_bf16 v[236:239], a[24:27], a[76:79], v[236:239]\n"
"v_mfma_f32_16x16x32_bf16 v[236:239], a[28:31], a[92:95], v[236:239]\n"
"s_waitcnt lgkmcnt(4)\n"
"v_mfma_f32_16x16x32_bf16 v[144:147], a[0:3], a[64:67], v[144:147]\n"
"buffer_load_dwordx4 a[152:155], v102, s[40:43], s60 offen offset:1024 sc1\n"
"v_mfma_f32_16x16x32_bf16 v[144:147], a[4:7], a[80:83], v[144:147]\n"
"ds_read_b64_tr_b16 a[16:17], v108 offset:768\n"
"v_mfma_f32_16x16x32_bf16 v[176:179], a[0:3], a[68:71], v[176:179]\n"
"ds_read_b64_tr_b16 a[18:19], v108 offset:4864\n"
"v_mfma_f32_16x16x32_bf16 v[176:179], a[4:7], a[84:87], v[176:179]\n"
"ds_read_b64_tr_b16 a[20:21], v108 offset:8960\n"
"v_mfma_f32_16x16x32_bf16 v[208:211], a[0:3], a[72:75], v[208:211]\n"
"buffer_load_dwordx4 v106, s[44:47], s61 offen offset:2048 lds\n"
"v_mfma_f32_16x16x32_bf16 v[208:211], a[4:7], a[88:91], v[208:211]\n"
"ds_read_b64_tr_b16 a[22:23], v108 offset:13056\n"
"v_mfma_f32_16x16x32_bf16 v[240:243], a[0:3], a[76:79], v[240:243]\n"
"v_mfma_f32_16x16x32_bf16 v[240:243], a[4:7], a[92:95], v[240:243]\n"
"s_waitcnt lgkmcnt(4)\n"
"v_mfma_f32_16x16x32_bf16 v[148:151], a[8:11], a[64:67], v[148:151]\n"
"buffer_load_dwordx4 a[156:159], v103, s[40:43], s60 offen offset:1024 sc1\n"
"s_add_u32 s60, s60, 4096\n"
"v_mfma_f32_16x16x32_bf16 v[148:151], a[12:15], a[80:83], v[148:151]\n"
"ds_read_b64_tr_b16 a[24:25], v109 offset:768\n"
"v_mfma_f32_16x16x32_bf16 v[180:183], a[8:11], a[68:71], v[180:183]\n"
"ds_read_b64_tr_b16 a[26:27], v109 offset:4864\n"
"v_mfma_f32_16x16x32_bf16 v[180:183], a[12:15], a[84:87], v[180:183]\n"
"ds_read_b64_tr_b16 a[28:29], v109 offset:8960\n"
"v_mfma_f32_16x16x32_bf16 v[212:215], a[8:11], a[72:75], v[212:215]\n"
"buffer_load_dwordx4 v107, s[44:47], s61 offen offset:3072 lds\n"
"s_add_u32 s61, s61, 229376\n"
"v_mfma_f32_16x16x32_bf16 v[212:215], a[12:15], a[88:91], v[212:215]\n"
"ds_read_b64_tr_b16 a[30:31], v109 offset:13056\n"
"v_mfma_f32_16x16x32_bf16 v[244:247], a[8:11], a[76:79], v[244:247]\n"
"v_mfma_f32_16x16x32_bf16 v[244:247], a[12:15], a[92:95], v[244:247]\n"
"s_waitcnt vmcnt(16) lgkmcnt(4)\n"
"s_barrier\n"
"v_mfma_f32_16x16x32_bf16 v[152:155], a[16:19], a[64:67], v[152:155]\n"
"ds_read_b64_tr_b16 a[0:1], v110 offset:32768\n"
"v_mfma_f32_16x16x32_bf16 v[152:155], a[20:23], a[80:83], v[152:155]\n"
"buffer_load_dwordx4 a[160:163], v100, s[40:43], s60 offen sc1\n"
"v_mfma_f32_16x16x32_bf16 v[184:187], a[16:19], a[68:71], v[184:187]\n"
"ds_read_b64_tr_b16 a[2:3], v110 offset:36864\n"
"v_mfma_f32_16x16x32_bf16 v[184:187], a[20:23], a[84:87], v[184:187]\n"
"ds_read_b64_tr_b16 a[4:5], v110 offset:40960\n"
"v_mfma_f32_16x16x32_bf16 v[216:219], a[16:19], a[72:75], v[216:219]\n"
"ds_read_b64_tr_b16 a[6:7], v110 offset:45056\n"
"s_add_u32 m0, s62, 65536\n"
"v_mfma_f32_16x16x32_bf16 v[216:219], a[20:23], a[88:91], v[216:219]\n"
"buffer_load_dwordx4 v104, s[44:47], s61 offen lds\n"
"v_mfma_f32_16x16x32_bf16 v[248:251], a[16:19], a[76:79], v[248:251]\n"
"v_mfma_f32_16x16x32_bf16 v[248:251], a[20:23], a[92:95], v[248:251]\n"
"s_waitcnt lgkmcnt(4)\n"
"v_mfma_f32_16x16x32_bf16 v[156:159], a[24:27], a[64:67], v[156:159]\n"
"ds_read_b64_tr_b16 a[8:9], v111 offset:32768\n"
"v_mfma_f32_16x16x32_bf16 v[156:159], a[28:31], a[80:83], v[156:159]\n"
"buffer_load_dwordx4 a[164:167], v101, s[40:43], s60 offen sc1\n"
"v_mfma_f32_16x16x32_bf16 v[188:191], a[24:27], a[68:71], v[188:191]\n"
"ds_read_b64_tr_b16 a[10:11], v111 offset:36864\n"
"v_mfma_f32_16x16x32_bf16 v[188:191], a[28:31], a[84:87], v[188:191]\n"
"ds_read_b64_tr_b16 a[12:13], v111 offset:40960\n"
"v_mfma_f32_16x16x32_bf16 v[220:223], a[24:27], a[72:75], v[220:223]\n"
"ds_read_b64_tr_b16 a[14:15], v111 offset:45056\n"
"v_mfma_f32_16x16x32_bf16 v[220:223], a[28:31], a[88:91], v[220:223]\n"
"buffer_load_dwordx4 v105, s[44:47], s61 offen offset:1024 lds\n"
"v_mfma_f32_16x16x32_bf16 v[252:255], a[24:27], a[76:79], v[252:255]\n"
"v_mfma_f32_16x16x32_bf16 v[252:255], a[28:31], a[92:95], v[252:255]\n"
"s_waitcnt lgkmcnt(4)\n"
"v_mfma_f32_16x16x32_bf16 v[128:131], a[0:3], a[96:99], v[128:131]\n"
"ds_read_b64_tr_b16 a[16:17], v110 offset:33024\n"
"v_mfma_f32_16x16x32_bf16 v[128:131], a[4:7], a[112:115], v[128:131]\n"
"buffer_load_dwordx4 a[168:171], v102, s[40:43], s60 offen sc1\n"
"v_mfma_f32_16x16x32_bf16 v[160:163], a[0:3], a[100:103], v[160:163]\n"
"ds_read_b64_tr_b16 a[18:19], v110 offset:37120\n"
"v_mfma_f32_16x16x32_bf16 v[160:163], a[4:7], a[116:119], v[160:163]\n"
"ds_read_b64_tr_b16 a[20:21], v110 offset:41216\n"
"v_mfma_f32_16x16x32_bf16 v[192:195], a[0:3], a[104:107], v[192:195]\n"
"ds_read_b64_tr_b16 a[22:23], v110 offset:45312\n"
"v_mfma_f32_16x16x32_bf16 v[192:195], a[4:7], a[120:123], v[192:195]\n"
"buffer_load_dwordx4 v106, s[44:47], s61 offen offset:2048 lds\n"
"v_mfma_f32_16x16x32_bf16 v[224:227], a[0:3], a[108:111], v[224:227]\n"
"v_mfma_f32_16x16x32_bf16 v[224:227], a[4:7], a[124:127], v[224:227]\n"
"s_waitcnt lgkmcnt(4)\n"
"v_mfma_f32_16x16x32_bf16 v[132:135], a[8:11], a[96:99], v[132:135]\n"
"ds_read_b64_tr_b16 a[24:25], v111 offset:33024\n"
"v_mfma_f32_16x16x32_bf16 v[132:135], a[12:15], a[112:115], v[132:135]\n"
"buffer_load_dwordx4 a[172:175], v103, s[40:43], s60 offen sc1\n"
"v_mfma_f32_16x16x32_bf16 v[164:167], a[8:11], a[100:103], v[164:167]\n"
"ds_read_b64_tr_b16 a[26:27], v111 offset:37120\n"
"v_mfma_f32_16x16x32_bf16 v[164:167], a[12:15], a[116:119], v[164:167]\n"
"ds_read_b64_tr_b16 a[28:29], v111 offset:41216\n"
"v_mfma_f32_16x16x32_bf16 v[196:199], a[8:11], a[104:107], v[196:199]\n"
"ds_read_b64_tr_b16 a[30:31], v111 offset:45312\n"
"v_mfma_f32_16x16x32_bf16 v[196:199], a[12:15], a[120:123], v[196:199]\n"
"buffer_load_dwordx4 v107, s[44:47], s61 offen offset:3072 lds\n"
"s_add_u32 s61, s61, 32768\n"
"v_mfma_f32_16x16x32_bf16 v[228:231], a[8:11], a[108:111], v[228:231]\n"
"v_mfma_f32_16x16x32_bf16 v[228:231], a[12:15], a[124:127], v[228:231]\n"
"s_waitcnt lgkmcnt(4)\n"
"v_mfma_f32_16x16x32_bf16 v[136:139], a[16:19], a[96:99], v[136:139]\n"
"buffer_load_dwordx4 a[176:179], v100, s[40:43], s60 offen offset:1024 sc1\n"
"v_mfma_f32_16x16x32_bf16 v[136:139], a[20:23], a[112:115], v[136:139]\n"
"ds_read_b64_tr_b16 a[0:1], v110 offset:33280\n"
"v_mfma_f32_16x16x32_bf16 v[168:171], a[16:19], a[100:103], v[168:171]\n"
"ds_read_b64_tr_b16 a[2:3], v110 offset:37376\n"
"v_mfma_f32_16x16x32_bf16 v[168:171], a[20:23], a[116:119], v[168:171]\n"
"ds_read_b64_tr_b16 a[4:5], v110 offset:41472\n"
"s_add_u32 m0, s62, 69632\n"
"v_mfma_f32_16x16x32_bf16 v[200:203], a[16:19], a[104:107], v[200:203]\n"
"buffer_load_dwordx4 v104, s[44:47], s61 offen lds\n"
"v_mfma_f32_16x16x32_bf16 v[200:203], a[20:23], a[120:123], v[200:203]\n"
"ds_read_b64_tr_b16 a[6:7], v110 offset:45568\n"
"v_mfma_f32_16x16x32_bf16 v[232:235], a[16:19], a[108:111], v[232:235]\n"
"v_mfma_f32_16x16x32_bf16 v[232:235], a[20:23], a[124:127], v[232:235]\n"
"s_waitcnt lgkmcnt(4)\n"
"v_mfma_f32_16x16x32_bf16 v[140:143], a[24:27], a[96:99], v[140:143]\n"
"buffer_load_dwordx4 a[180:183], v101, s[40:43], s60 offen offset:1024 sc1\n"
"v_mfma_f32_16x16x32_bf16 v[140:143], a[28:31], a[112:115], v[140:143]\n"
"ds_read_b64_tr_b16 a[8:9], v111 offset:33280\n"
"v_mfma_f32_16x16x32_bf16 v[172:175], a[24:27], a[100:103], v[172:175]\n"
"ds_read_b64_tr_b16 a[10:11], v111 offset:37376\n"
"v_mfma_f32_16x16x32_bf16 v[172:175], a[28:31], a[116:119], v[172:175]\n"
"ds_read_b64_tr_b16 a[12:13], v111 offset:41472\n"
"v_mfma_f32_16x16x32_bf16 v[204:207], a[24:27], a[104:107], v[204:207]\n"
"buffer_load_dwordx4 v105, s[44:47], s61 offen offset:1024 lds\n"
"v_mfma_f32_16x16x32_bf16 v[204:207], a[28:31], a[120:123], v[204:207]\n"
"ds_read_b64_tr_b16 a[14:15], v111 offset:45568\n"
"v_mfma_f32_16x16x32_bf16 v[236:239], a[24:27], a[108:111], v[236:239]\n"
"v_mfma_f32_16x16x32_bf16 v[236:239], a[28:31], a[124:127], v[236:239]\n"
"s_waitcnt lgkmcnt(4)\n"
"v_mfma_f32_16x16x32_bf16 v[144:147], a[0:3], a[96:99], v[144:147]\n"
"buffer_load_dwordx4 a[184:187], v102, s[40:43], s60 offen offset:1024 sc1\n"
"v_mfma_f32_16x16x32_bf16 v[144:147], a[4:7], a[112:115], v[144:147]\n"
"ds_read_b64_tr_b16 a[16:17], v110 offset:33536\n"
"v_mfma_f32_16x16x32_bf16 v[176:179], a[0:3], a[100:103], v[176:179]\n"
"ds_read_b64_tr_b16 a[18:19], v110 offset:37632\n"
"v_mfma_f32_16x16x32_bf16 v[176:179], a[4:7], a[116:119], v[176:179]\n"
"ds_read_b64_tr_b16 a[20:21], v110 offset:41728\n"
"v_mfma_f32_16x16x32_bf16 v[208:211], a[0:3], a[104:107], v[208:211]\n"
"buffer_load_dwordx4 v106, s[44:47], s61 offen offset:2048 lds\n"
"v_mfma_f32_16x16x32_bf16 v[208:211], a[4:7], a[120:123], v[208:211]\n"
"ds_read_b64_tr_b16 a[22:23], v110 offset:45824\n"
"v_mfma_f32_16x16x32_bf16 v[240:243], a[0:3], a[108:111], v[240:243]\n"
"v_mfma_f32_16x16x32_bf16 v[240:243], a[4:7], a[124:127], v[240:243]\n"
"s_waitcnt lgkmcnt(4)\n"
"v_mfma_f32_16x16x32_bf16 v[148:151], a[8:11], a[96:99], v[148:151]\n"
"buffer_load_dwordx4 a[188:191], v103, s[40:43], s60 offen offset:1024 sc1\n"
"s_add_u32 s60, s60, 4096\n"
"v_mfma_f32_16x16x32_bf16 v[148:151], a[12:15], a[112:115], v[148:151]\n"
"ds_read_b64_tr_b16 a[24:25], v111 offset:33536\n"
"v_mfma_f32_16x16x32_bf16 v[180:183], a[8:11], a[100:103], v[180:183]\n"
"ds_read_b64_tr_b16 a[26:27], v111 offset:37632\n"
"v_mfma_f32_16x16x32_bf16 v[180:183], a[12:15], a[116:119], v[180:183]\n"
"ds_read_b64_tr_b16 a[28:29], v111 offset:41728\n"
"v_mfma_f32_16x16x32_bf16 v[212:215], a[8:11], a[104:107], v[212:215]\n"
"buffer_load_dwordx4 v107, s[44:47], s61 offen offset:3072 lds\n"
"s_add_u32 s61, s61, 229376\n"
"v_mfma_f32_16x16x32_bf16 v[212:215], a[12:15], a[120:123], v[212:215]\n"
"ds_read_b64_tr_b16 a[30:31], v111 offset:45824\n"
"v_mfma_f32_16x16x32_bf16 v[244:247], a[8:11], a[108:111], v[244:247]\n"
"v_mfma_f32_16x16x32_bf16 v[244:247], a[12:15], a[124:127], v[244:247]\n"
"s_waitcnt vmcnt(16) lgkmcnt(4)\n"
"s_barrier\n"
"v_mfma_f32_16x16x32_bf16 v[152:155], a[16:19], a[96:99], v[152:155]\n"
"ds_read_b64_tr_b16 a[0:1], v108 offset:32768\n"
"s_add_u32 m0, s62, 131072\n"
"v_mfma_f32_16x16x32_bf16 v[152:155], a[20:23], a[112:115], v[152:155]\n"
"buffer_load_dwordx4 v96, s[48:51], 0 offen sc1 lds\n"
"v_mfma_f32_16x16x32_bf16 v[184:187], a[16:19], a[100:103], v[184:187]\n"
"ds_read_b64_tr_b16 a[2:3], v108 offset:36864\n"
"v_mfma_f32_16x16x32_bf16 v[184:187], a[20:23], a[116:119], v[184:187]\n"
"ds_read_b64_tr_b16 a[4:5], v108 offset:40960\n"
"v_mfma_f32_16x16x32_bf16 v[216:219], a[16:19], a[104:107], v[216:219]\n"
"buffer_load_dwordx4 v97, s[48:51], 0 offen offset:1024 sc1 lds\n"
"v_mfma_f32_16x16x32_bf16 v[216:219], a[20:23], a[120:123], v[216:219]\n"
"ds_read_b64_tr_b16 a[6:7], v108 offset:45056\n"
"v_mfma_f32_16x16x32_bf16 v[248:251], a[16:19], a[108:111], v[248:251]\n"
"v_mfma_f32_16x16x32_bf16 v[248:251], a[20:23], a[124:127], v[248:251]\n"
"buffer_load_dwordx4 v98, s[48:51], 0 offen offset:2048 sc1 lds\n"
"s_waitcnt lgkmcnt(4)\n"
"v_mfma_f32_16x16x32_bf16 v[156:159], a[24:27], a[96:99], v[156:159]\n"
"ds_read_b64_tr_b16 a[8:9], v109 offset:32768\n"
"v_mfma_f32_16x16x32_bf16 v[156:159], a[28:31], a[112:115], v[156:159]\n"
"ds_read_b64_tr_b16 a[10:11], v109 offset:36864\n"
"v_mfma_f32_16x16x32_bf16 v[188:191], a[24:27], a[100:103], v[188:191]\n"
"buffer_load_dwordx4 v99, s[48:51], 0 offen offset:3072 sc1 lds\n"
"v_mfma_f32_16x16x32_bf16 v[188:191], a[28:31], a[116:119], v[188:191]\n"
"ds_read_b64_tr_b16 a[12:13], v109 offset:40960\n"
"v_mfma_f32_16x16x32_bf16 v[220:223], a[24:27], a[104:107], v[220:223]\n"
"ds_read_b64_tr_b16 a[14:15], v109 offset:45056\n"
"s_add_u32 m0, s62, 135168\n"
"v_mfma_f32_16x16x32_bf16 v[220:223], a[28:31], a[120:123], v[220:223]\n"
"buffer_load_dwordx4 v94, s[52:55], 0 offen sc1 lds\n"
"v_mfma_f32_16x16x32_bf16 v[252:255], a[24:27], a[108:111], v[252:255]\n"
"v_mfma_f32_16x16x32_bf16 v[252:255], a[28:31], a[124:127], v[252:255]\n"
"s_waitcnt lgkmcnt(4)\n"
"v_mfma_f32_16x16x32_bf16 v[128:131], a[0:3], a[128:131], v[128:131]\n"
"buffer_load_dwordx4 v95, s[52:55], 0 offen offset:1024 sc1 lds\n"
"v_mfma_f32_16x16x32_bf16 v[128:131], a[4:7], a[144:147], v[128:131]\n"
"ds_read_b64_tr_b16 a[16:17], v108 offset:33024\n"
"v_mfma_f32_16x16x32_bf16 v[160:163], a[0:3], a[132:135], v[160:163]\n"
"ds_read_b64_tr_b16 a[18:19], v108 offset:37120\n"
"v_mfma_f32_16x16x32_bf16 v[160:163], a[4:7], a[148:151], v[160:163]\n"
"ds_read_b64_tr_b16 a[20:21], v108 offset:41216\n"
"v_mfma_f32_16x16x32_bf16 v[192:195], a[0:3], a[136:139], v[192:195]\n"
"ds_read_b64_tr_b16 a[22:23], v108 offset:45312\n"
"v_mfma_f32_16x16x32_bf16 v[192:195], a[4:7], a[152:155], v[192:195]\n"
"v_mfma_f32_16x16x32_bf16 v[224:227], a[0:3], a[140:143], v[224:227]\n"
"v_mfma_f32_16x16x32_bf16 v[224:227], a[4:7], a[156:159], v[224:227]\n"
"s_waitcnt lgkmcnt(4)\n"
"v_mfma_f32_16x16x32_bf16 v[132:135], a[8:11], a[128:131], v[132:135]\n"
"ds_read_b64_tr_b16 a[24:25], v109 offset:33024\n"
"v_mfma_f32_16x16x32_bf16 v[132:135], a[12:15], a[144:147], v[132:135]\n"
"ds_read_b64_tr_b16 a[26:27], v109 offset:37120\n"
"v_mfma_f32_16x16x32_bf16 v[164:167], a[8:11], a[132:135], v[164:167]\n"
"ds_read_b64_tr_b16 a[28:29], v109 offset:41216\n"
"v_mfma_f32_16x16x32_bf16 v[164:167], a[12:15], a[148:151], v[164:167]\n"
"ds_read_b64_tr_b16 a[30:31], v109 offset:45312\n"
"v_mfma_f32_16x16x32_bf16 v[196:199], a[8:11], a[136:139], v[196:199]\n"
"v_mfma_f32_16x16x32_bf16 v[196:199], a[12:15], a[152:155], v[196:199]\n"
"v_mfma_f32_16x16x32_bf16 v[228:231], a[8:11], a[140:143], v[228:231]\n"
"v_mfma_f32_16x16x32_bf16 v[228:231], a[12:15], a[156:159], v[228:231]\n"
"s_waitcnt lgkmcnt(4)\n"
"v_mfma_f32_16x16x32_bf16 v[136:139], a[16:19], a[128:131], v[136:139]\n"
"ds_read_b64_tr_b16 a[0:1], v108 offset:33280\n"
"v_mfma_f32_16x16x32_bf16 v[136:139], a[20:23], a[144:147], v[136:139]\n"
"ds_read_b64_tr_b16 a[2:3], v108 offset:37376\n"
"v_mfma_f32_16x16x32_bf16 v[168:171], a[16:19], a[132:135], v[168:171]\n"
"ds_read_b64_tr_b16 a[4:5], v108 offset:41472\n"
"v_mfma_f32_16x16x32_bf16 v[168:171], a[20:23], a[148:151], v[168:171]\n"
"ds_read_b64_tr_b16 a[6:7], v108 offset:45568\n"
"v_mfma_f32_16x16x32_bf16 v[200:203], a[16:19], a[136:139], v[200:203]\n"
"v_mfma_f32_16x16x32_bf16 v[200:203], a[20:23], a[152:155], v[200:203]\n"
"v_mfma_f32_16x16x32_bf16 v[232:235], a[16:19], a[140:143], v[232:235]\n"
"v_mfma_f32_16x16x32_bf16 v[232:235], a[20:23], a[156:159], v[232:235]\n"
"s_waitcnt lgkmcnt(4)\n"
"v_mfma_f32_16x16x32_bf16 v[140:143], a[24:27], a[128:131], v[140:143]\n"
"ds_read_b64_tr_b16 a[8:9], v109 offset:33280\n"
"v_mfma_f32_16x16x32_bf16 v[140:143], a[28:31], a[144:147], v[140:143]\n"
"ds_read_b64_tr_b16 a[10:11], v109 offset:37376\n"
"v_mfma_f32_16x16x32_bf16 v[172:175], a[24:27], a[132:135], v[172:175]\n"
"ds_read_b64_tr_b16 a[12:13], v109 offset:41472\n"
"v_mfma_f32_16x16x32_bf16 v[172:175], a[28:31], a[148:151], v[172:175]\n"
"ds_read_b64_tr_b16 a[14:15], v109 offset:45568\n"
"v_mfma_f32_16x16x32_bf16 v[204:207], a[24:27], a[136:139], v[204:207]\n"
"v_mfma_f32_16x16x32_bf16 v[204:207], a[28:31], a[152:155], v[204:207]\n"
"v_mfma_f32_16x16x32_bf16 v[236:239], a[24:27], a[140:143], v[236:239]\n"
"v_mfma_f32_16x16x32_bf16 v[236:239], a[28:31], a[156:159], v[236:239]\n"
"s_waitcnt vmcnt(6) lgkmcnt(4)\n"
"s_barrier\n"
"v_mfma_f32_16x16x32_bf16 v[144:147], a[0:3], a[128:131], v[144:147]\n"
"ds_read_b64_tr_b16 a[16:17], v108 offset:33536\n"
"ds_read_b64_tr_b16 a[252:253], v111 offset:8960\n"
"v_mfma_f32_16x16x32_bf16 v[144:147], a[4:7], a[144:147], v[144:147]\n"
"ds_read_b64_tr_b16 a[18:19], v108 offset:37632\n"
"ds_read_b64_tr_b16 a[254:255], v111 offset:13056\n"
"v_mfma_f32_16x16x32_bf16 v[176:179], a[0:3], a[132:135], v[176:179]\n"
"buffer_load_dwordx4 v[124:127], v112, s[64:67], 0 offen\n"
"v_add_u32 v116, s62, v112\n"
"v_mfma_f32_16x16x32_bf16 v[176:179], a[4:7], a[148:151], v[176:179]\n"
"ds_read_b64_tr_b16 a[20:21], v108 offset:41728\n"
"v_add_u32 v113, 0, v116\n"
"v_mfma_f32_16x16x32_bf16 v[208:211], a[0:3], a[136:139], v[208:211]\n"
"ds_read_b64_tr_b16 a[22:23], v108 offset:45824\n"
"v_xor_b32 v114, 8192, v113\n"
"v_mfma_f32_16x16x32_bf16 v[208:211], a[4:7], a[152:155], v[208:211]\n"
"ds_read_b64_tr_b16 a[192:193], v110 offset:0\n"
"v_add_u32 v116, 98304, v116\n"
"v_mfma_f32_16x16x32_bf16 v[240:243], a[0:3], a[140:143], v[240:243]\n"
"ds_read_b64_tr_b16 a[194:195], v110 offset:4096\n"
"v_xor_b32 v117, 8192, v116\n"
"v_mfma_f32_16x16x32_bf16 v[240:243], a[4:7], a[156:159], v[240:243]\n"
"ds_read_b64_tr_b16 a[196:197], v110 offset:8192\n"
"s_waitcnt lgkmcnt(9)\n"
"v_mfma_f32_16x16x32_bf16 v[148:151], a[8:11], a[128:131], v[148:151]\n"
"ds_read_b64_tr_b16 a[24:25], v109 offset:33536\n"
"v_mfma_f32_16x16x32_bf16 v[148:151], a[12:15], a[144:147], v[148:151]\n"
"ds_read_b64_tr_b16 a[26:27], v109 offset:37632\n"
"v_mfma_f32_16x16x32_bf16 v[180:183], a[8:11], a[132:135], v[180:183]\n"
"ds_read_b64_tr_b16 a[28:29], v109 offset:41728\n"
"v_mfma_f32_16x16x32_bf16 v[180:183], a[12:15], a[148:151], v[180:183]\n"
"ds_read_b64_tr_b16 a[30:31], v109 offset:45824\n"
"v_mfma_f32_16x16x32_bf16 v[212:215], a[8:11], a[136:139], v[212:215]\n"
"ds_read_b64_tr_b16 a[198:199], v110 offset:12288\n"
"v_mfma_f32_16x16x32_bf16 v[212:215], a[12:15], a[152:155], v[212:215]\n"
"ds_read_b64_tr_b16 a[200:201], v111 offset:0\n"
"v_mfma_f32_16x16x32_bf16 v[244:247], a[8:11], a[140:143], v[244:247]\n"
"ds_read_b64_tr_b16 a[202:203], v111 offset:4096\n"
"v_mfma_f32_16x16x32_bf16 v[244:247], a[12:15], a[156:159], v[244:247]\n"
"ds_read_b64_tr_b16 a[204:205], v111 offset:8192\n"
"s_waitcnt lgkmcnt(8)\n"
"v_mfma_f32_16x16x32_bf16 v[152:155], a[16:19], a[128:131], v[152:155]\n"
"ds_read_b64_tr_b16 a[206:207], v111 offset:12288\n"
"v_mfma_f32_16x16x32_bf16 v[152:155], a[20:23], a[144:147], v[152:155]\n"
"ds_read_b64_tr_b16 a[208:209], v110 offset:256\n"
"v_mfma_f32_16x16x32_bf16 v[184:187], a[16:19], a[132:135], v[184:187]\n"
"ds_read_b64_tr_b16 a[210:211], v110 offset:4352\n"
"v_mfma_f32_16x16x32_bf16 v[184:187], a[20:23], a[148:151], v[184:187]\n"
"ds_read_b64_tr_b16 a[212:213], v110 offset:8448\n"
"v_mfma_f32_16x16x32_bf16 v[216:219], a[16:19], a[136:139], v[216:219]\n"
"ds_read_b64_tr_b16 a[214:215], v110 offset:12544\n"
"v_mfma_f32_16x16x32_bf16 v[216:219], a[20:23], a[152:155], v[216:219]\n"
"ds_read_b64_tr_b16 a[216:217], v111 offset:256\n"
"v_mfma_f32_16x16x32_bf16 v[248:251], a[16:19], a[140:143], v[248:251]\n"
"ds_read_b64_tr_b16 a[218:219], v111 offset:4352\n"
"v_mfma_f32_16x16x32_bf16 v[248:251], a[20:23], a[156:159], v[248:251]\n"
"ds_read_b64_tr_b16 a[220:221], v111 offset:8448\n"
"s_waitcnt lgkmcnt(8)\n"
"v_mfma_f32_16x16x32_bf16 v[156:159], a[24:27], a[128:131], v[156:159]\n"
"ds_read_b64_tr_b16 a[222:223], v111 offset:12544\n"
"v_mfma_f32_16x16x32_bf16 v[156:159], a[28:31], a[144:147], v[156:159]\n"
"ds_read_b64_tr_b16 a[224:225], v110 offset:512\n"
"v_mfma_f32_16x16x32_bf16 v[188:191], a[24:27], a[132:135], v[188:191]\n"
"ds_read_b64_tr_b16 a[226:227], v110 offset:4608\n"
"v_mfma_f32_16x16x32_bf16 v[188:191], a[28:31], a[148:151], v[188:191]\n"
"ds_read_b64_tr_b16 a[228:229], v110 offset:8704\n"
"v_mfma_f32_16x16x32_bf16 v[220:223], a[24:27], a[136:139], v[220:223]\n"
"ds_read_b64_tr_b16 a[230:231], v110 offset:12800\n"
"v_mfma_f32_16x16x32_bf16 v[220:223], a[28:31], a[152:155], v[220:223]\n"
"ds_read_b64_tr_b16 a[232:233], v111 offset:512\n"
"v_mfma_f32_16x16x32_bf16 v[252:255], a[24:27], a[140:143], v[252:255]\n"
"ds_read_b64_tr_b16 a[234:235], v111 offset:4608\n"
"v_mfma_f32_16x16x32_bf16 v[252:255], a[28:31], a[156:159], v[252:255]\n"
"ds_read_b64_tr_b16 a[236:237], v111 offset:8704\n"
"v_mfma_f32_16x16x32_bf16 v[224:227], a[192:195], a[172:175], v[224:227]\n"
"ds_read_b64_tr_b16 a[238:239], v111 offset:12800\n"
"v_mfma_f32_16x16x32_bf16 v[224:227], a[196:199], a[188:191], v[224:227]\n"
"ds_read_b64_tr_b16 a[240:241], v110 offset:768\n"
"v_mfma_f32_16x16x32_bf16 v[228:231], a[200:203], a[172:175], v[228:231]\n"
"ds_read_b64_tr_b16 a[242:243], v110 offset:4864\n"
"s_waitcnt lgkmcnt(8)\n"
"v_mfma_f32_16x16x32_bf16 v[228:231], a[204:207], a[188:191], v[228:231]\n"
"ds_read_b64_tr_b16 a[244:245], v110 offset:8960\n"
"v_mfma_f32_16x16x32_bf16 v[232:235], a[208:211], a[172:175], v[232:235]\n"
"ds_read_b64_tr_b16 a[246:247], v110 offset:13056\n"
"v_mfma_f32_16x16x32_bf16 v[232:235], a[212:215], a[188:191], v[232:235]\n"
"ds_read_b64_tr_b16 a[248:249], v111 offset:768\n"
"v_mfma_f32_16x16x32_bf16 v[236:239], a[216:219], a[172:175], v[236:239]\n"
"ds_read_b64_tr_b16 a[250:251], v111 offset:4864\n"
"v_mfma_f32_16x16x32_bf16 v[236:239], a[220:223], a[188:191], v[236:239]\n"
"ds_write_b128 v113, v[224:227] offset:0\n"
"v_mfma_f32_16x16x32_bf16 v[240:243], a[224:227], a[172:175], v[240:243]\n"
"ds_write_b128 v113, v[228:231] offset:1024\n"
"s_waitcnt lgkmcnt(8)\n"
"v_mfma_f32_16x16x32_bf16 v[240:243], a[228:231], a[188:191], v[240:243]\n"
"v_mfma_f32_16x16x32_bf16 v[244:247], a[232:235], a[172:175], v[244:247]\n"
"ds_write_b128 v113, v[232:235] offset:2048\n"
"v_mfma_f32_16x16x32_bf16 v[244:247], a[236:239], a[188:191], v[244:247]\n"
"s_waitcnt lgkmcnt(6)\n"
"v_mfma_f32_16x16x32_bf16 v[248:251], a[240:243], a[172:175], v[248:251]\n"
"ds_write_b128 v113, v[236:239] offset:3072\n"
"s_waitcnt lgkmcnt(6)\n"
"v_mfma_f32_16x16x32_bf16 v[248:251], a[244:247], a[188:191], v[248:251]\n"
"s_waitcnt lgkmcnt(4)\n"
"v_mfma_f32_16x16x32_bf16 v[252:255], a[248:251], a[172:175], v[252:255]\n"
"ds_write_b128 v113, v[240:243] offset:4096\n"
"v_mfma_f32_16x16x32_bf16 v[252:255], a[252:255], a[188:191], v[252:255]\n"
"v_mfma_f32_16x16x32_bf16 v[160:163], a[192:195], a[164:167], v[160:163]\n"
"ds_write_b128 v113, v[244:247] offset:5120\n"
"v_mfma_f32_16x16x32_bf16 v[160:163], a[196:199], a[180:183], v[160:163]\n"
"v_mfma_f32_16x16x32_bf16 v[164:167], a[200:203], a[164:167], v[164:167]\n"
"ds_write_b128 v113, v[248:251] offset:6144\n"
"v_mfma_f32_16x16x32_bf16 v[164:167], a[204:207], a[180:183], v[164:167]\n"
"v_mfma_f32_16x16x32_bf16 v[168:171], a[208:211], a[164:167], v[168:171]\n"
"s_nop 0\n"
"ds_write_b128 v113, v[252:255] offset:7168\n"
"v_mfma_f32_16x16x32_bf16 v[168:171], a[212:215], a[180:183], v[168:171]\n"
"v_mfma_f32_16x16x32_bf16 v[172:175], a[216:219], a[164:167], v[172:175]\n"
"v_mfma_f32_16x16x32_bf16 v[172:175], a[220:223], a[180:183], v[172:175]\n"
"v_mfma_f32_16x16x32_bf16 v[176:179], a[224:227], a[164:167], v[176:179]\n"
"v_mfma_f32_16x16x32_bf16 v[176:179], a[228:231], a[180:183], v[176:179]\n"
"v_mfma_f32_16x16x32_bf16 v[180:183], a[232:235], a[164:167], v[180:183]\n"
"s_waitcnt lgkmcnt(0)\n"
"s_barrier\n"
"v_mfma_f32_16x16x32_bf16 v[180:183], a[236:239], a[180:183], v[180:183]\n"
"ds_read_b128 v[16:19], v114 offset:0\n"
"v_mfma_f32_16x16x32_bf16 v[184:187], a[240:243], a[164:167], v[184:187]\n"
"ds_read_b128 v[20:23], v114 offset:1024\n"
"v_mfma_f32_16x16x32_bf16 v[184:187], a[244:247], a[180:183], v[184:187]\n"
"ds_read_b128 v[24:27], v114 offset:2048\n"
"v_mfma_f32_16x16x32_bf16 v[188:191], a[248:251], a[164:167], v[188:191]\n"
"ds_read_b128 v[28:31], v114 offset:3072\n"
"v_mfma_f32_16x16x32_bf16 v[188:191], a[252:255], a[180:183], v[188:191]\n"
"ds_read_b128 v[32:35], v114 offset:4096\n"
"v_mfma_f32_16x16x32_bf16 v[192:195], a[192:195], a[168:171], v[192:195]\n"
"ds_read_b128 v[36:39], v114 offset:5120\n"
"v_mfma_f32_16x16x32_bf16 v[192:195], a[196:199], a[184:187], v[192:195]\n"
"ds_read_b128 v[40:43], v114 offset:6144\n"
"v_mfma_f32_16x16x32_bf16 v[196:199], a[200:203], a[168:171], v[196:199]\n"
"ds_read_b128 v[44:47], v114 offset:7168\n"
"v_mfma_f32_16x16x32_bf16 v[196:199], a[204:207], a[184:187], v[196:199]\n"
"s_waitcnt lgkmcnt(7)\n"
"v_add_f32 v160, v160, v16\n"
"v_add_f32 v161, v161, v17\n"
"v_add_f32 v162, v162, v18\n"
"v_mfma_f32_16x16x32_bf16 v[200:203], a[208:211], a[168:171], v[200:203]\n"
"v_add_f32 v163, v163, v19\n"
"s_waitcnt lgkmcnt(6)\n"
"v_add_f32 v164, v164, v20\n"
"v_add_f32 v165, v165, v21\n"
"v_mfma_f32_16x16x32_bf16 v[200:203], a[212:215], a[184:187], v[200:203]\n"
"v_add_f32 v166, v166, v22\n"
"v_add_f32 v167, v167, v23\n"
"v_cvt_pk_bf16_f32 v48, v160, v161\n"
"v_mfma_f32_16x16x32_bf16 v[204:207], a[216:219], a[168:171], v[204:207]\n"
"ds_write_b128 v116, v[192:195] offset:0\n"
"v_cvt_pk_bf16_f32 v49, v162, v163\n"
"v_cvt_pk_bf16_f32 v50, v164, v165\n"
"v_mfma_f32_16x16x32_bf16 v[204:207], a[220:223], a[184:187], v[204:207]\n"
"v_cvt_pk_bf16_f32 v51, v166, v167\n"
"v_min_u32 v48, -2, v48\n"
"v_min_u32 v49, -2, v49\n"
"v_mfma_f32_16x16x32_bf16 v[208:211], a[224:227], a[168:171], v[208:211]\n"
"ds_write_b128 v116, v[196:199] offset:1024\n"
"v_min_u32 v50, -2, v50\n"
"v_min_u32 v51, -2, v51\n"
"v_mfma_f32_16x16x32_bf16 v[208:211], a[228:231], a[184:187], v[208:211]\n"
"buffer_store_dwordx4 v[48:51], v93, s[76:79], 0 offen sc1\n"
"s_waitcnt lgkmcnt(4)\n"
"v_add_f32 v168, v168, v24\n"
"v_add_f32 v169, v169, v25\n"
"v_mfma_f32_16x16x32_bf16 v[212:215], a[232:235], a[168:171], v[212:215]\n"
"ds_write_b128 v116, v[200:203] offset:2048\n"
"v_add_f32 v170, v170, v26\n"
"v_add_f32 v171, v171, v27\n"
"v_mfma_f32_16x16x32_bf16 v[212:215], a[236:239], a[184:187], v[212:215]\n"
"v_add_f32 v172, v172, v28\n"
"v_add_f32 v173, v173, v29\n"
"v_add_f32 v174, v174, v30\n"
"v_mfma_f32_16x16x32_bf16 v[216:219], a[240:243], a[168:171], v[216:219]\n"
"ds_write_b128 v116, v[204:207] offset:3072\n"
"v_add_f32 v175, v175, v31\n"
"v_cvt_pk_bf16_f32 v52, v168, v169\n"
"v_mfma_f32_16x16x32_bf16 v[216:219], a[244:247], a[184:187], v[216:219]\n"
"v_cvt_pk_bf16_f32 v53, v170, v171\n"
"v_cvt_pk_bf16_f32 v54, v172, v173\n"
"v_cvt_pk_bf16_f32 v55, v174, v175\n"
"v_mfma_f32_16x16x32_bf16 v[220:223], a[248:251], a[168:171], v[220:223]\n"
"ds_write_b128 v116, v[208:211] offset:4096\n"
"v_min_u32 v52, -2, v52\n"
"v_min_u32 v53, -2, v53\n"
"v_mfma_f32_16x16x32_bf16 v[220:223], a[252:255], a[184:187], v[220:223]\n"
"v_min_u32 v54, -2, v54\n"
"v_min_u32 v55, -2, v55\n"
"v_mfma_f32_16x16x32_bf16 v[128:131], a[192:195], a[160:163], v[128:131]\n"
"ds_write_b128 v116, v[212:215] offset:5120\n"
"buffer_store_dwordx4 v[52:55], v93, s[76:79], 0 offen offset:1024 sc1\n"
"v_add_f32 v176, v176, v32\n"
"v_mfma_f32_16x16x32_bf16 v[128:131], a[196:199], a[176:179], v[128:131]\n"
"v_add_f32 v177, v177, v33\n"
"v_add_f32 v178, v178, v34\n"
"v_add_f32 v179, v179, v35\n"
"v_mfma_f32_16x16x32_bf16 v[132:135], a[200:203], a[160:163], v[132:135]\n"
"ds_write_b128 v116, v[216:219] offset:6144\n"
"v_add_f32 v180, v180, v36\n"
"v_add_f32 v181, v181, v37\n"
"v_mfma_f32_16x16x32_bf16 v[132:135], a[204:207], a[176:179], v[132:135]\n"
"v_add_f32 v182, v182, v38\n"
"v_add_f32 v183, v183, v39\n"
"v_cvt_pk_bf16_f32 v56, v176, v177\n"
"v_mfma_f32_16x16x32_bf16 v[136:139], a[208:211], a[160:163], v[136:139]\n"
"ds_write_b128 v116, v[220:223] offset:7168\n"
"v_cvt_pk_bf16_f32 v57, v178, v179\n"
"v_cvt_pk_bf16_f32 v58, v180, v181\n"
"v_mfma_f32_16x16x32_bf16 v[136:139], a[212:215], a[176:179], v[136:139]\n"
"v_cvt_pk_bf16_f32 v59, v182, v183\n"
"v_min_u32 v56, -2, v56\n"
"v_min_u32 v57, -2, v57\n"
"v_mfma_f32_16x16x32_bf16 v[140:143], a[216:219], a[160:163], v[140:143]\n"
"v_min_u32 v58, -2, v58\n"
"v_min_u32 v59, -2, v59\n"
"v_mfma_f32_16x16x32_bf16 v[140:143], a[220:223], a[176:179], v[140:143]\n"
"buffer_store_dwordx4 v[56:59], v93, s[76:79], 0 offen offset:2048 sc1\n"
"s_waitcnt lgkmcnt(3)\n"
"v_add_f32 v184, v184, v40\n"
"v_add_f32 v185, v185, v41\n"
"v_mfma_f32_16x16x32_bf16 v[144:147], a[224:227], a[160:163], v[144:147]\n"
"v_add_f32 v186, v186, v42\n"
"v_add_f32 v187, v187, v43\n"
"v_add_f32 v188, v188, v44\n"
"v_mfma_f32_16x16x32_bf16 v[144:147], a[228:231], a[176:179], v[144:147]\n"
"v_add_f32 v189, v189, v45\n"
"v_add_f32 v190, v190, v46\n"
"v_add_f32 v191, v191, v47\n"
"v_mfma_f32_16x16x32_bf16 v[148:151], a[232:235], a[160:163], v[148:151]\n"
"v_cvt_pk_bf16_f32 v60, v184, v185\n"
"v_cvt_pk_bf16_f32 v61, v186, v187\n"
"v_cvt_pk_bf16_f32 v62, v188, v189\n"
"v_mfma_f32_16x16x32_bf16 v[148:151], a[236:239], a[176:179], v[148:151]\n"
"v_cvt_pk_bf16_f32 v63, v190, v191\n"
"v_min_u32 v60, -2, v60\n"
"v_min_u32 v61, -2, v61\n"
"v_mfma_f32_16x16x32_bf16 v[152:155], a[240:243], a[160:163], v[152:155]\n"
"v_min_u32 v62, -2, v62\n"
"v_min_u32 v63, -2, v63\n"
"v_mfma_f32_16x16x32_bf16 v[152:155], a[244:247], a[176:179], v[152:155]\n"
"buffer_store_dwordx4 v[60:63], v93, s[76:79], 0 offen offset:3072 sc1\n"
"s_waitcnt lgkmcnt(0)\n"
"s_barrier\n"
"v_mfma_f32_16x16x32_bf16 v[156:159], a[248:251], a[160:163], v[156:159]\n"
"ds_read_b128 v[16:19], v117 offset:0\n"
"ds_read_b128 v[20:23], v117 offset:1024\n"
"ds_read_b128 v[24:27], v117 offset:2048\n"
"ds_read_b128 v[28:31], v117 offset:3072\n"
"v_mfma_f32_16x16x32_bf16 v[156:159], a[252:255], a[176:179], v[156:159]\n"
"ds_read_b128 v[32:35], v117 offset:4096\n"
"ds_read_b128 v[36:39], v117 offset:5120\n"
"ds_read_b128 v[40:43], v117 offset:6144\n"
"ds_read_b128 v[44:47], v117 offset:7168\n"
"s_waitcnt vmcnt(4)\n"
"s_nop 7\n"
"s_waitcnt lgkmcnt(0)\n"
"v_pk_add_f32 v[128:129], v[128:129], v[16:17]\n"
"v_pk_add_f32 v[130:131], v[130:131], v[18:19]\n"
"v_pk_add_f32 v[132:133], v[132:133], v[20:21]\n"
"v_pk_add_f32 v[134:135], v[134:135], v[22:23]\n"
"v_pk_add_f32 v[136:137], v[136:137], v[24:25]\n"
"v_pk_add_f32 v[138:139], v[138:139], v[26:27]\n"
"v_pk_add_f32 v[140:141], v[140:141], v[28:29]\n"
"v_pk_add_f32 v[142:143], v[142:143], v[30:31]\n"
"v_pk_add_f32 v[144:145], v[144:145], v[32:33]\n"
"v_pk_add_f32 v[146:147], v[146:147], v[34:35]\n"
"v_pk_add_f32 v[148:149], v[148:149], v[36:37]\n"
"v_pk_add_f32 v[150:151], v[150:151], v[38:39]\n"
"v_pk_add_f32 v[152:153], v[152:153], v[40:41]\n"
"v_pk_add_f32 v[154:155], v[154:155], v[42:43]\n"
"v_pk_add_f32 v[156:157], v[156:157], v[44:45]\n"
"v_pk_add_f32 v[158:159], v[158:159], v[46:47]\n"
"s_nop 1\n"
    : "={v[128:159]}"(keep), "+{s[60:63]}"(so), "+{v[100:115]}"(va)
    : "{s[40:43]}"(sx), "{s[44:47]}"(sw), "{s[48:51]}"(se), "{v[96:99]}"(xo), "{s[52:55]}"(sp), "{v[94:95]}"(po), "{s[64:67]}"(code_srd(bid, wave)),
      "{s[76:79]}"(mksrd(scr + (8 << 20) + ((tile * 2 + (1 - s)) * 4 + wave) * 4096, 4096)), "{v93}"(lane * 16), "s"((unsigned)(size_t)lds) : "v16", "v17", "v18", "v19", "v20", "v21", "v22", "v23", "v24", "v25", "v26", "v27", "v28", "v29", "v30", "v31", "v32", "v33", "v34", "v35", "v36", "v37", "v38", "v39", "v40", "v41", "v42", "v43", "v44", "v45", "v46", "v47", "v48", "v49", "v50", "v51", "v52", "v53", "v54", "v55", "v56", "v57", "v58", "v59", "v60", "v61", "v62", "v63", "v116", "v117", "v118", "v119", "v120", "v121", "v122", "v123", "v124", "v125", "v126", "v127", "v160", "v161", "v162", "v163", "v164", "v165", "v166", "v167", "v168", "v169", "v170", "v171", "v172", "v173", "v174", "v175", "v176", "v177", "v178", "v179", "v180", "v181", "v182", "v183", "v184", "v185", "v186", "v187", "v188", "v189", "v190", "v191", "v192", "v193", "v194", "v195", "v196", "v197", "v198", "v199", "v200", "v201", "v202", "v203", "v204", "v205", "v206", "v207", "v208", "v209", "v210", "v211", "v212", "v213", "v214", "v215", "v216", "v217", "v218", "v219", "v220", "v221", "v222", "v223", "v224", "v225", "v226", "v227", "v228", "v229", "v230", "v231", "v232", "v233", "v234", "v235", "v236", "v237", "v238", "v239", "v240", "v241", "v242", "v243", "v244", "v245", "v246", "v247", "v248", "v249", "v250", "v251", "v252", "v253", "v254", "v255", "a0", "a1", "a2", "a3", "a4", "a5", "a6", "a7", "a8", "a9", "a10", "a11", "a12", "a13", "a14", "a15", "a16", "a17", "a18", "a19", "a20", "a21", "a22", "a23", "a24", "a25", "a26", "a27", "a28", "a29", "a30", "a31", "a32", "a33", "a34", "a35", "a36", "a37", "a38", "a39", "a40", "a41", "a42", "a43", "a44", "a45", "a46", "a47", "a48", "a49", "a50", "a51", "a52", "a53", "a54", "a55", "a56", "a57", "a58", "a59", "a60", "a61", "a62", "a63", "a64", "a65", "a66", "a67", "a68", "a69", "a70", "a71", "a72", "a73", "a74", "a75", "a76", "a77", "a78", "a79", "a80", "a81", "a82", "a83", "a84", "a85", "a86", "a87", "a88", "a89", "a90", "a91", "a92", "a93", "a94", "a95", "a96", "a97", "a98", "a99", "a100", "a101", "a102", "a103", "a104", "a105", "a106", "a107", "a108", "a109", "a110", "a111", "a112", "a113", "a114", "a115", "a116", "a117", "a118", "a119", "a120", "a121", "a122", "a123", "a124", "a125", "a126", "a127", "a128", "a129", "a130", "a131", "a132", "a133", "a134", "a135", "a136", "a137", "a138", "a139", "a140", "a141", "a142", "a143", "a144", "a145", "a146", "a147", "a148", "a149", "a150", "a151", "a152", "a153", "a154", "a155", "a156", "a157", "a158", "a159", "a160", "a161", "a162", "a163", "a164", "a165", "a166", "a167", "a168", "a169", "a170", "a171", "a172", "a173", "a174", "a175", "a176", "a177", "a178", "a179", "a180", "a181", "a182", "a183", "a184", "a185", "a186", "a187", "a188", "a189", "a190", "a191", "a192", "a193", "a194", "a195", "a196", "a197", "a198", "a199", "a200", "a201", "a202", "a203", "a204", "a205", "a206", "a207", "a208", "a209", "a210", "a211", "a212", "a213", "a214", "a215", "a216", "a217", "a218", "a219", "a220", "a221", "a222", "a223", "a224", "a225", "a226", "a227", "a228", "a229", "a230", "a231", "a232", "a233", "a234", "a235", "a236", "a237", "a238", "a239", "a240", "a241", "a242", "a243", "a244", "a245", "a246", "a247", "a248", "a249", "a250", "a251", "a252", "a253", "a254", "a255", "s68", "s69", "vcc", "scc", "memory");
  if (bid == 0 && tid == 0) __hip_atomic_fetch_add((unsigned*)(ws + O_EP), 1u, __ATOMIC_RELAXED, __HIP_MEMORY_SCOPE_AGENT);
  const auto rx = __builtin_amdgcn_make_buffer_rsrc(scr, 0, 13 << 20, 0x20000);
  const auto rz = __builtin_amdgcn_make_buffer_rsrc(zft, 0, 128 * HS, 0x20000);
  u32x4 pu[4];
  f32x4 ga[8], gg[8], sb[8];
  const char* stg = lds + 131072 + wave * 8192;
#pragma unroll
  for (int nb = 0; nb < 8; ++nb) { ga[nb] = *(const f32x4*)(stg + 4096 + nb * 64 + g * 16); sb[nb] = *(const f32x4*)(stg + 4608 + nb * 64 + g * 16); gg[nb] = *(const f32x4*)(stg + 5120 + nb * 64 + g * 16); }
#pragma unroll
  for (int nb = 0; nb < 8; ++nb) { const u32x2 xv = *(const u32x2*)(stg + i * 256 + (((2 * nb + (g >> 1)) ^ i) << 4) + (g & 1) * 8);
    const f32x4 v = (f32x4){lo16(xv[0]), hi16(xv[0]), lo16(xv[1]), hi16(xv[1])} + ga[nb] * (f32x4){keep[4 * nb], keep[4 * nb + 1], keep[4 * nb + 2], keep[4 * nb + 3]};
    keep[4 * nb] = v[0]; keep[4 * nb + 1] = v[1]; keep[4 * nb + 2] = v[2]; keep[4 * nb + 3] = v[3]; asm volatile("" : "+v"(gg[nb]), "+v"(sb[nb])); }
  asm volatile("" : "+v"(keep));
  poll_load4(pu, scr, (8 << 20) + ((tile * 2 + s) * 4 + wave) * 4096 + lane * 16);
  f32x32 z;
#pragma unroll
  for (int c = 0; c < 4; ++c) {
#pragma unroll
    for (int h = 0; h < 2; ++h) { const int nb = 2 * c + h; const unsigned p0 = pu[c][2 * h], p1 = pu[c][2 * h + 1];
      const f32x4 v = (f32x4){keep[4 * nb], keep[4 * nb + 1], keep[4 * nb + 2], keep[4 * nb + 3]} + ga[nb] * (f32x4){lo16(p0), hi16(p0), lo16(p1), hi16(p1)}, zz = v * gg[nb];
#pragma unroll
      for (int r = 0; r < 4; ++r) { keep[4 * nb + r] = v[r]; z[4 * nb + r] = zz[r]; } }
    __builtin_amdgcn_raw_buffer_store_b128(pk8(z, c), rz, rbk * HS + ((pn * 4 + c) * 64 + lane) * 16, 0, 16);
  }
  __builtin_amdgcn_sched_barrier(0);
#pragma unroll
  for (int c = 0; c < 4; ++c) *(u32x4*)(lds + 131072 + wave * 8192 + c * 1024 + lane * 16) = pk8(keep, c);
  { f32x4 c[6] = {{0, 0, 0, 0}, {0, 0, 0, 0}, {0, 0, 0, 0}, {0, 0, 0, 0}, {0, 0, 0, 0}, {0, 0, 0, 0}};
#pragma unroll
    for (int nb = 0; nb < 8; ++nb) { const f32x4 v = {keep[4 * nb], keep[4 * nb + 1], keep[4 * nb + 2], keep[4 * nb + 3]}, zz = {z[4 * nb], z[4 * nb + 1], z[4 * nb + 2], z[4 * nb + 3]};
      c[0] += v; c[1] += v * v; c[2] += zz; c[3] += zz * gg[nb]; c[4] += zz * zz; c[5] += zz * sb[nb]; }
    float t[6];
#pragma unroll
    for (int e = 0; e < 6; ++e) t[e] = (c[e][0] + c[e][1]) + (c[e][2] + c[e][3]);
    f32x4 o = {0, 0, 0, 0};
#pragma unroll
    for (int e = 0; e < 3; ++e) { const float u = swapadd16(t[e], t[3 + e]); o[e] = swapadd32(u, u); }
    __builtin_amdgcn_raw_buffer_store_b128(nosent(__builtin_bit_cast(u32x4, o)), rx, (12 << 20) + 262144 + (rbk * 8 + pn) * 512 + (fresh_lane() & 31) * 16, 0, 16); }
}

DI u32x2 split_a(float v) { const float h = rbf(v); return (u32x2){pk2(h, v - h), pk2(h, 0.f)}; }
DI u32x2 split_b(float v) { const float h = rbf(v); return (u32x2){pk2(h, h), pk2(v - h, 0.f)}; }
DI void phase_mlp1(char* lds, unsigned wv) {
  PAR; const u16 *w1 = P->w1, *b1 = P->b1, *w2 = P->w2; const u16* zft = (const u16*)(ws + O_Z); char* scr = ws + O_SCR; u16* hm = (u16*)(scr + (17 << 20));
  const unsigned dpanel = 9 + ((bid & 7) >> 1) * 2 + ((bid >> 4) & 1);
  edge_signal<1>(tid, ws, dpanel, 32, dpanel);
  char* ut = scr + (12 << 20);
  const int lane = tid & 63, i = lane & 15, g = lane >> 4;
  if (bid >= 224) {
    const int t = tid, w = t >> 6, pn = bid - 224;
    float* gs = (float*)lds; float* red = gs + 2048; char* xo = lds + 16384;
    const auto ru = __builtin_amdgcn_make_buffer_rsrc(ut, 0, 60 * 4096, 0x20000);
    const u32x4 sw = mksrd(w1 + pn * 128, pn < 28 ? 1023 * 7168 + 256 : 0);
    const unsigned wu = wave, q4 = (lane >> 2) & 3, vs0 = q4 * 7168 + g * 64 + (lane & 3) * 16, vs1 = (vs0 ^ 32) + 4 * 7168 - 1024;
    auto fill = [&](int it) {
#pragma unroll
      for (int p = 0; p < 4; ++p)
        asm volatile("s_mov_b32 m0, %0\n\ts_nop 0\n\tbuffer_load_dwordx4 %1, %3, %4 offen lds\n\tbuffer_load_dwordx4 %2, %3, %4 offen offset:1024 lds"
                     :: "s"(24576 + wu * 16384 + (it & 1) * 8192 + p * 2048), "v"(vs0), "v"(vs1), "s"(sw), "s"(((it * 4 + wu) * 32 + p * 8) * 7168) : "memory"); };
    fill(0); fill(1);
    const auto rm = __builtin_amdgcn_make_buffer_rsrc(ws, 0, O_SIL, 0x20000);
    *(u32x4*)(gs + t * 4) = __builtin_amdgcn_raw_buffer_load_b128(rm, O_MOD + 3072 * 4 + t * 16, 0, 16); *(u32x4*)(gs + 1024 + t * 4) = __builtin_amdgcn_raw_buffer_load_b128(rm, O_MOD + 4096 * 4 + t * 16, 0, 16);
    wg_barrier();
    {
      f32x4 a[8];
#pragma unroll
      for (int nb = 0; nb < 8; ++nb) a[nb] = (f32x4){0, 0, 0, 0};
      const unsigned rde = 24576 + w * 16384 + g * 1024 + q4 * 64 + (lane & 3) * 8 + (g & 1) * 32;
#pragma unroll 2
      for (int it = 0; it < 8; ++it) {
        const float* gp = gs + (i == 0 ? 1024 : 0) + (it * 4 + w) * 32 + 4 * g; const f32x4 x0 = *(const f32x4*)gp, x1 = *(const f32x4*)(gp + 16);
        u32x4 xf = {pk2(x0[0], x0[1]), pk2(x0[2], x0[3]), pk2(x1[0], x1[1]), pk2(x1[2], x1[3])};
        if (i >= 2) { const unsigned o = i == 2 ? 0x3f803f80u : 0u; xf = (u32x4){o, o, o, o}; }
        if (it < 7) asm volatile("s_waitcnt vmcnt(8)" ::: "memory"); else asm volatile("s_waitcnt vmcnt(0)" ::: "memory");
        u32x2 lo[8], hi[8]; const unsigned re = rde + (it & 1) * 8192, ro = re ^ 32;
        asm volatile("ds_read_b64_tr_b16 %0, %16\n\tds_read_b64_tr_b16 %8, %16 offset:4096\n\tds_read_b64_tr_b16 %1, %17\n\tds_read_b64_tr_b16 %9, %17 offset:4096\n\t"
                     "ds_read_b64_tr_b16 %2, %16 offset:256\n\tds_read_b64_tr_b16 %10, %16 offset:4352\n\tds_read_b64_tr_b16 %3, %17 offset:256\n\tds_read_b64_tr_b16 %11, %17 offset:4352\n\t"
                     "ds_read_b64_tr_b16 %4, %16 offset:512\n\tds_read_b64_tr_b16 %12, %16 offset:4608\n\tds_read_b64_tr_b16 %5, %17 offset:512\n\tds_read_b64_tr_b16 %13, %17 offset:4608\n\t"
                     "ds_read_b64_tr_b16 %6, %16 offset:768\n\tds_read_b64_tr_b16 %14, %16 offset:4864\n\tds_read_b64_tr_b16 %7, %17 offset:768\n\tds_read_b64_tr_b16 %15, %17 offset:4864\n\t"
                     "s_waitcnt lgkmcnt(0)"
                     : "=&v"(lo[0]), "=&v"(lo[1]), "=&v"(lo[2]), "=&v"(lo[3]), "=&v"(lo[4]), "=&v"(lo[5]), "=&v"(lo[6]), "=&v"(lo[7]),
                       "=&v"(hi[0]), "=&v"(hi[1]), "=&v"(hi[2]), "=&v"(hi[3]), "=&v"(hi[4]), "=&v"(hi[5]), "=&v"(hi[6]), "=&v"(hi[7]) : "v"(re), "v"(ro) : "memory");
        if (it < 6) fill(it + 2);
#pragma unroll
        for (int nb = 0; nb < 8; ++nb) a[nb] = __builtin_amdgcn_mfma_f32_16x16x32_bf16(__builtin_bit_cast(bf16x8, (u32x4){lo[nb][0], lo[nb][1], hi[nb][0], hi[nb][1]}), __builtin_bit_cast(bf16x8, xf), a[nb], 0, 0, 0);
      }
      if (i < 3) {
#pragma unroll
        for (int nb = 0; nb < 8; ++nb) *(f32x4*)(red + w * 384 + i * 128 + nb * 16 + g * 4) = a[nb]; }
      __syncthreads();
      u32x4 o;
#pragma unroll
      for (int h = 0; h < 2; ++h) { const int n = (2 * w + h) * 16 + i, q = (g < 3 ? g : 0) * 128 + n;
        const u32x2 p = split_a(g < 3 ? (red[q] + red[384 + q]) + (red[768 + q] + red[1152 + q]) : bf2f(b1[(pn < 28 ? pn : 0) * 128 + n])); o[2 * h] = p[0]; o[2 * h + 1] = p[1]; }
      if (pn < 28) __builtin_amdgcn_raw_buffer_store_b128(nosent(o), ru, pn * 4096 + t * 16, 0, 16);
    }
    u32x4 su_[4]; f32x4 sv[4];
    poll_load4(su_, ut, 262144 + (pn * 4 + w) * 4096 + lane * 16);
#pragma unroll
    for (int e = 0; e < 4; ++e) sv[e] = __builtin_bit_cast(f32x4, su_[e]);
    { float c[5] = {0, 0, 0, 0, 0}, st[6];
#pragma unroll
      for (int k = 0; k < 16; ++k) { const float s = gs[k * 64 + lane], gv = gs[1024 + k * 64 + lane]; c[0] += gv; c[1] += gv * gv; c[2] += gv * s; c[3] += s; c[4] += s * s; }
#pragma unroll
      for (int e = 0; e < 5; ++e) c[e] = wsum(c[e]);
#pragma unroll
      for (int e = 0; e < 3; ++e) { const float q = (sv[0][e] + sv[1][e]) + (sv[2][e] + sv[3][e]), u = swapadd32(q, q);
        const auto p = __builtin_amdgcn_permlane16_swap(__float_as_uint(u), __float_as_uint(u), false, false); st[e] = __uint_as_float(p[0]); st[3 + e] = __uint_as_float(p[1]); }
      const float N1 = 1.f / 1024.f, m1 = st[0] * N1, d1 = sqrtf(fmaxf(st[1] * N1 - m1 * m1, 0.f) + 1e-5f), r1 = 1.f / d1;
      const float m2 = (r1 * (st[2] - m1 * c[0]) + c[3]) * N1;
      const float q2 = (r1 * r1 * (st[4] - 2.f * m1 * st[3] + m1 * m1 * c[1]) + 2.f * r1 * (st[5] - m1 * c[2]) + c[4]) * N1;
      const float d2 = sqrtf(fmaxf(q2 - m2 * m2, 0.f) + 1e-5f);
      *(u32x2*)(xo + (w >> 1) * 1024 + lane * 16 + (w & 1) * 8) = split_b(g == 0 ? -m1 : g == 1 ? d1 : g == 2 ? -m2 * d1 : d1 * d2);
      *(float*)(xo + 2048 + lane * 16 + w * 4) = 1.f / (d1 * d2); *(float*)(xo + 3072 + lane * 16 + w * 4) = d1 * d2; }
    wg_barrier();
    __builtin_amdgcn_raw_buffer_store_b128(nosent(*(const u32x4*)(xo + t * 16)), ru, (28 + pn) * 4096 + t * 16, 0, 16);
    {
      const unsigned cls = (1u - (unsigned)((size_t)w2 >> 8)) & 3u;
      const u32x4 sz = mksrd(w2 + (pn & 1) * 1792 * 1024 + cls * 128, 1791 * 2048 + 1024 + 256);
      const unsigned vo = (lane >> 5) * 2048 + ((lane >> 4) & 1) * 1024 + i * 16;
#pragma unroll 1
      for (int c = 55; c >= 0; --c)
        asm volatile("s_mov_b32 m0, %0\n\ts_nop 0\n\tbuffer_load_dwordx4 %1, %2, %3 offen lds\n\ts_waitcnt vmcnt(12)"
                     :: "s"(24576 + wu * 16384), "v"(vo), "s"(sz), "s"((c * 32 + (pn >> 3) * 8 + wu * 2) * 2048) : "memory"); }
    asm volatile("s_waitcnt vmcnt(0) lgkmcnt(0)\n\ts_barrier" ::: "memory");
    asm volatile(
"v_mbcnt_lo_u32_b32 v116, -1, 0\n"
"v_mbcnt_hi_u32_b32 v116, -1, v116\n"
"v_bfe_u32 v117, v116, 2, 2\n"
"v_lshrrev_b32 v119, 4, v116\n"
"v_and_b32 v118, 3, v116\n"
"s_mov_b32 s60, 0\n"
"s_mul_i32 s61, s70, 65536\n"
"s_lshl_b32 s62, s70, 13\n"
"v_lshlrev_b32 v122, 4, v118\n"
"v_lshl_add_u32 v122, v119, 6, v122\n"
"v_xor_b32 v123, 32, v122\n"
"v_mul_u32_u24 v120, 2048, v117\n"
"v_add_u32 v104, v120, v122\n"
"v_add_u32 v105, v120, v123\n"
"v_add_u32 v105, 7168, v105\n"
"v_add_u32 v106, v120, v122\n"
"v_add_u32 v106, 14336, v106\n"
"v_add_u32 v107, v120, v123\n"
"v_add_u32 v107, 21504, v107\n"
"v_lshl_add_u32 v100, v116, 4, s72\n"
"v_lshl_add_u32 v101, v116, 4, s73\n"
"v_lshl_add_u32 v102, v116, 4, s74\n"
"v_lshl_add_u32 v103, v116, 4, s75\n"
"v_lshlrev_b32 v120, 3, v118\n"
"v_lshl_add_u32 v120, v117, 6, v120\n"
"v_lshl_add_u32 v120, v119, 10, v120\n"
"v_add_u32 v120, s71, v120\n"
"v_and_b32 v121, 1, v119\n"
"v_lshl_add_u32 v108, v121, 5, v120\n"
"v_xor_b32 v109, 32, v108\n"
"v_lshlrev_b32 v112, 4, v116\n"
"v_add_u32 v110, 65536, v108\n"
"v_add_u32 v111, 65536, v109\n"
"s_add_u32 m0, s62, 65536\n"
"s_nop 0\n"
"buffer_load_dwordx4 v104, s[44:47], s61 offen lds\n"
"buffer_load_dwordx4 v105, s[44:47], s61 offen offset:1024 lds\n"
"buffer_load_dwordx4 v106, s[44:47], s61 offen offset:2048 lds\n"
"buffer_load_dwordx4 v107, s[44:47], s61 offen offset:3072 lds\n"
"s_add_u32 s61, s61, 32768\n"
"s_add_u32 m0, s62, 69632\n"
"s_nop 0\n"
"buffer_load_dwordx4 v104, s[44:47], s61 offen lds\n"
"buffer_load_dwordx4 v105, s[44:47], s61 offen offset:1024 lds\n"
"buffer_load_dwordx4 v106, s[44:47], s61 offen offset:2048 lds\n"
"buffer_load_dwordx4 v107, s[44:47], s61 offen offset:3072 lds\n"
"s_add_u32 s61, s61, 229376\n"
"s_add_u32 m0, s62, 98304\n"
"s_nop 0\n"
"buffer_load_dwordx4 v104, s[44:47], s61 offen lds\n"
"buffer_load_dwordx4 v105, s[44:47], s61 offen offset:1024 lds\n"
"buffer_load_dwordx4 v106, s[44:47], s61 offen offset:2048 lds\n"
"buffer_load_dwordx4 v107, s[44:47], s61 offen offset:3072 lds\n"
"s_add_u32 s61, s61, 32768\n"
"s_add_u32 m0, s62, 102400\n"
"s_nop 0\n"
"buffer_load_dwordx4 v104, s[44:47], s61 offen lds\n"
"buffer_load_dwordx4 v105, s[44:47], s61 offen offset:1024 lds\n"
"buffer_load_dwordx4 v106, s[44:47], s61 offen offset:2048 lds\n"
"buffer_load_dwordx4 v107, s[44:47], s61 offen offset:3072 lds\n"
"s_add_u32 s61, s61, 229376\n"
      :: "{s70}"(wave), "{s71}"(0), "{s[72:75]}"((u32x4){0, 0, 0, 0}), "{s[44:47]}"(mksrd(w2 + (bid & 1) * 1792 * 1024 + (bid >> 5) * 128, 1791 * 2048 + 256)) : "v100", "v101", "v102", "v103", "v104", "v105", "v106", "v107", "v108", "v109", "v110", "v111", "v112", "v113", "v114", "v115", "v116", "v117", "v118", "v119", "v120", "v121", "v122", "v123", "s60", "s61", "s62", "s63", "vcc", "scc", "memory");
    return;
  }
  const unsigned b = bid, xcd = b & 7, r = b >> 3, pm = (xcd >> 1) * 2 + (r & 1), pn = (xcd & 1) * 14 + (r >> 1);
  const unsigned rb0 = pm * 16 + wave * 4;
  u32x16 va; u32x4 so;
  asm volatile(
"v_mbcnt_lo_u32_b32 v116, -1, 0\n"
"v_mbcnt_hi_u32_b32 v116, -1, v116\n"
"v_bfe_u32 v117, v116, 2, 2\n"
"v_lshrrev_b32 v119, 4, v116\n"
"v_and_b32 v118, 3, v116\n"
"s_mov_b32 s60, 0\n"
"s_mul_i32 s61, s70, 114688\n"
"s_lshl_b32 s62, s70, 12\n"
"v_lshlrev_b32 v122, 4, v118\n"
"v_lshl_add_u32 v122, v119, 6, v122\n"
"v_xor_b32 v123, 32, v122\n"
"v_mul_u32_u24 v120, 7168, v117\n"
"v_add_u32 v104, v120, v122\n"
"v_add_u32 v105, v120, v123\n"
"v_add_u32 v105, 27648, v105\n"
"v_add_u32 v106, v120, v122\n"
"v_add_u32 v106, 55296, v106\n"
"v_add_u32 v107, v120, v123\n"
"v_add_u32 v107, 82944, v107\n"
"v_lshlrev_b32 v100, 4, v116\n"
"v_add_u32 v101, 34048, v100\n"
"v_add_u32 v102, 68096, v100\n"
"v_add_u32 v103, 102144, v100\n"
"v_lshlrev_b32 v120, 3, v118\n"
"v_lshl_add_u32 v120, v117, 6, v120\n"
"v_lshl_add_u32 v120, v119, 10, v120\n"
"v_and_b32 v121, 1, v119\n"
"v_lshl_add_u32 v108, v121, 5, v120\n"
"v_xor_b32 v109, 32, v108\n"
"v_lshlrev_b32 v112, 4, v116\n"
    : "={v[100:115]}"(va), "={s[60:63]}"(so) : "{s70}"(wave) : "scc", "v116", "v117", "v118", "v119", "v120", "v121", "v122", "v123");
  const u32x4 sx = mksrd(zft + rb0 * (HS / 2), 4 * HS), sw = mksrd(w1 + pn * 128, 1023 * 7168 + 256);
  asm volatile(
"s_add_u32 m0, s62, 0\n"
"s_nop 0\n"
"buffer_load_dwordx4 v104, s[44:47], s61 offen lds\n"
"buffer_load_dwordx4 v105, s[44:47], s61 offen offset:1024 lds\n"
"buffer_load_dwordx4 v106, s[44:47], s61 offen offset:2048 lds\n"
"buffer_load_dwordx4 v107, s[44:47], s61 offen offset:3072 lds\n"
"s_add_u32 s61, s61, 458752\n"
"s_add_u32 m0, s62, 16384\n"
"s_nop 0\n"
"buffer_load_dwordx4 v104, s[44:47], s61 offen lds\n"
"buffer_load_dwordx4 v105, s[44:47], s61 offen offset:1024 lds\n"
"buffer_load_dwordx4 v106, s[44:47], s61 offen offset:2048 lds\n"
"buffer_load_dwordx4 v107, s[44:47], s61 offen offset:3072 lds\n"
"s_add_u32 s61, s61, 458752\n"
    : "+{s[60:63]}"(so) : "{v[100:115]}"(va), "{s[44:47]}"(sw) : "scc", "memory");
  __builtin_amdgcn_sched_barrier(0);
  const u32x4 se = mksrd(ut + (28 + pm * 4 + wave) * 4096, 4096), su = mksrd(ut + pn * 4096, 4096);
  f32x32 acc[4]; u32x16 av, bv;
  edge_wait(tid, bid, ws, 9 + pm, 1);
  asm volatile(
"buffer_load_dwordx4 a[64:67], v100, s[40:43], s60 offen sc1\n"
"buffer_load_dwordx4 a[68:71], v101, s[40:43], s60 offen sc1\n"
"buffer_load_dwordx4 a[72:75], v102, s[40:43], s60 offen sc1\n"
"buffer_load_dwordx4 a[76:79], v103, s[40:43], s60 offen sc1\n"
"buffer_load_dwordx4 a[80:83], v100, s[40:43], s60 offen offset:1024 sc1\n"
"buffer_load_dwordx4 a[84:87], v101, s[40:43], s60 offen offset:1024 sc1\n"
"buffer_load_dwordx4 a[88:91], v102, s[40:43], s60 offen offset:1024 sc1\n"
"buffer_load_dwordx4 a[92:95], v103, s[40:43], s60 offen offset:1024 sc1\n"
"s_waitcnt vmcnt(12)\n"
"s_barrier\n"
"ds_read_b64_tr_b16 a[0:1], v108 offset:0\n"
"ds_read_b64_tr_b16 a[2:3], v108 offset:4096\n"
"ds_read_b64_tr_b16 a[4:5], v109 offset:0\n"
"ds_read_b64_tr_b16 a[6:7], v109 offset:4096\n"
"ds_read_b64_tr_b16 a[8:9], v108 offset:256\n"
"ds_read_b64_tr_b16 a[10:11], v108 offset:4352\n"
"ds_read_b64_tr_b16 a[12:13], v109 offset:256\n"
"ds_read_b64_tr_b16 a[14:15], v109 offset:4352\n"
"s_waitcnt vmcnt(7) lgkmcnt(6)\n"
"v_mfma_f32_16x16x32_bf16 v[128:131], a[0:3], a[64:67], 0\n"
"ds_read_b64_tr_b16 a[16:17], v108 offset:512\n"
"s_waitcnt lgkmcnt(5)\n"
"v_mfma_f32_16x16x32_bf16 v[132:135], a[4:7], a[64:67], 0\n"
"buffer_load_dwordx4 a[96:99], v100, s[40:43], s60 offen offset:2048 sc1\n"
"s_waitcnt vmcnt(7)\n"
"v_mfma_f32_16x16x32_bf16 v[160:163], a[0:3], a[68:71], 0\n"
"ds_read_b64_tr_b16 a[18:19], v108 offset:4608\n"
"v_mfma_f32_16x16x32_bf16 v[164:167], a[4:7], a[68:71], 0\n"
"ds_read_b64_tr_b16 a[20:21], v109 offset:512\n"
"s_waitcnt vmcnt(6)\n"
"v_mfma_f32_16x16x32_bf16 v[192:195], a[0:3], a[72:75], 0\n"
"ds_read_b64_tr_b16 a[22:23], v109 offset:4608\n"
"v_mfma_f32_16x16x32_bf16 v[196:199], a[4:7], a[72:75], 0\n"
"buffer_load_dwordx4 a[100:103], v101, s[40:43], s60 offen offset:2048 sc1\n"
"s_waitcnt vmcnt(6)\n"
"v_mfma_f32_16x16x32_bf16 v[224:227], a[0:3], a[76:79], 0\n"
"v_mfma_f32_16x16x32_bf16 v[228:231], a[4:7], a[76:79], 0\n"
"s_waitcnt lgkmcnt(4)\n"
"v_mfma_f32_16x16x32_bf16 v[136:139], a[8:11], a[64:67], 0\n"
"ds_read_b64_tr_b16 a[24:25], v108 offset:768\n"
"v_mfma_f32_16x16x32_bf16 v[140:143], a[12:15], a[64:67], 0\n"
"buffer_load_dwordx4 a[104:107], v102, s[40:43], s60 offen offset:2048 sc1\n"
"v_mfma_f32_16x16x32_bf16 v[168:171], a[8:11], a[68:71], 0\n"
"ds_read_b64_tr_b16 a[26:27], v108 offset:4864\n"
"v_mfma_f32_16x16x32_bf16 v[172:175], a[12:15], a[68:71], 0\n"
"ds_read_b64_tr_b16 a[28:29], v109 offset:768\n"
"v_mfma_f32_16x16x32_bf16 v[200:203], a[8:11], a[72:75], 0\n"
"ds_read_b64_tr_b16 a[30:31], v109 offset:4864\n"
"v_mfma_f32_16x16x32_bf16 v[204:207], a[12:15], a[72:75], 0\n"
"buffer_load_dwordx4 a[108:111], v103, s[40:43], s60 offen offset:2048 sc1\n"
"v_mfma_f32_16x16x32_bf16 v[232:235], a[8:11], a[76:79], 0\n"
"v_mfma_f32_16x16x32_bf16 v[236:239], a[12:15], a[76:79], 0\n"
"s_waitcnt lgkmcnt(4)\n"
"v_mfma_f32_16x16x32_bf16 v[144:147], a[16:19], a[64:67], 0\n"
"ds_read_b64_tr_b16 a[0:1], v108 offset:8192\n"
"v_mfma_f32_16x16x32_bf16 v[148:151], a[20:23], a[64:67], 0\n"
"buffer_load_dwordx4 a[112:115], v100, s[40:43], s60 offen offset:3072 sc1\n"
"v_mfma_f32_16x16x32_bf16 v[176:179], a[16:19], a[68:71], 0\n"
"ds_read_b64_tr_b16 a[2:3], v108 offset:12288\n"
"v_mfma_f32_16x16x32_bf16 v[180:183], a[20:23], a[68:71], 0\n"
"ds_read_b64_tr_b16 a[4:5], v109 offset:8192\n"
"v_mfma_f32_16x16x32_bf16 v[208:211], a[16:19], a[72:75], 0\n"
"ds_read_b64_tr_b16 a[6:7], v109 offset:12288\n"
"v_mfma_f32_16x16x32_bf16 v[212:215], a[20:23], a[72:75], 0\n"
"buffer_load_dwordx4 a[116:119], v101, s[40:43], s60 offen offset:3072 sc1\n"
"v_mfma_f32_16x16x32_bf16 v[240:243], a[16:19], a[76:79], 0\n"
"v_mfma_f32_16x16x32_bf16 v[244:247], a[20:23], a[76:79], 0\n"
"s_waitcnt lgkmcnt(4)\n"
"v_mfma_f32_16x16x32_bf16 v[152:155], a[24:27], a[64:67], 0\n"
"ds_read_b64_tr_b16 a[8:9], v108 offset:8448\n"
"v_mfma_f32_16x16x32_bf16 v[156:159], a[28:31], a[64:67], 0\n"
"buffer_load_dwordx4 a[120:123], v102, s[40:43], s60 offen offset:3072 sc1\n"
"v_mfma_f32_16x16x32_bf16 v[184:187], a[24:27], a[68:71], 0\n"
"ds_read_b64_tr_b16 a[10:11], v108 offset:12544\n"
"v_mfma_f32_16x16x32_bf16 v[188:191], a[28:31], a[68:71], 0\n"
"ds_read_b64_tr_b16 a[12:13], v109 offset:8448\n"
"v_mfma_f32_16x16x32_bf16 v[216:219], a[24:27], a[72:75], 0\n"
"ds_read_b64_tr_b16 a[14:15], v109 offset:12544\n"
"v_mfma_f32_16x16x32_bf16 v[220:223], a[28:31], a[72:75], 0\n"
"buffer_load_dwordx4 a[124:127], v103, s[40:43], s60 offen offset:3072 sc1\n"
"s_add_u32 s60, s60, 4096\n"
"v_mfma_f32_16x16x32_bf16 v[248:251], a[24:27], a[76:79], 0\n"
"v_mfma_f32_16x16x32_bf16 v[252:255], a[28:31], a[76:79], 0\n"
"s_waitcnt vmcnt(11) lgkmcnt(4)\n"
"v_mfma_f32_16x16x32_bf16 v[128:131], a[0:3], a[80:83], v[128:131]\n"
"ds_read_b64_tr_b16 a[16:17], v108 offset:8704\n"
"v_mfma_f32_16x16x32_bf16 v[132:135], a[4:7], a[80:83], v[132:135]\n"
"buffer_load_dwordx4 a[128:131], v100, s[40:43], s60 offen sc1\n"
"s_waitcnt vmcnt(11)\n"
"v_mfma_f32_16x16x32_bf16 v[160:163], a[0:3], a[84:87], v[160:163]\n"
"ds_read_b64_tr_b16 a[18:19], v108 offset:12800\n"
"v_mfma_f32_16x16x32_bf16 v[164:167], a[4:7], a[84:87], v[164:167]\n"
"ds_read_b64_tr_b16 a[20:21], v109 offset:8704\n"
"s_waitcnt vmcnt(10)\n"
"v_mfma_f32_16x16x32_bf16 v[192:195], a[0:3], a[88:91], v[192:195]\n"
"ds_read_b64_tr_b16 a[22:23], v109 offset:12800\n"
"v_mfma_f32_16x16x32_bf16 v[196:199], a[4:7], a[88:91], v[196:199]\n"
"buffer_load_dwordx4 a[132:135], v101, s[40:43], s60 offen sc1\n"
"s_waitcnt vmcnt(10)\n"
"v_mfma_f32_16x16x32_bf16 v[224:227], a[0:3], a[92:95], v[224:227]\n"
"v_mfma_f32_16x16x32_bf16 v[228:231], a[4:7], a[92:95], v[228:231]\n"
"s_waitcnt lgkmcnt(4)\n"
"v_mfma_f32_16x16x32_bf16 v[136:139], a[8:11], a[80:83], v[136:139]\n"
"ds_read_b64_tr_b16 a[24:25], v108 offset:8960\n"
"s_add_u32 m0, s62, 32768\n"
"v_mfma_f32_16x16x32_bf16 v[140:143], a[12:15], a[80:83], v[140:143]\n"
"buffer_load_dwordx4 v104, s[44:47], s61 offen lds\n"
"v_mfma_f32_16x16x32_bf16 v[168:171], a[8:11], a[84:87], v[168:171]\n"
"ds_read_b64_tr_b16 a[26:27], v108 offset:13056\n"
"v_mfma_f32_16x16x32_bf16 v[172:175], a[12:15], a[84:87], v[172:175]\n"
"ds_read_b64_tr_b16 a[28:29], v109 offset:8960\n"
"v_mfma_f32_16x16x32_bf16 v[200:203], a[8:11], a[88:91], v[200:203]\n"
"ds_read_b64_tr_b16 a[30:31], v109 offset:13056\n"
"v_mfma_f32_16x16x32_bf16 v[204:207], a[12:15], a[88:91], v[204:207]\n"
"buffer_load_dwordx4 a[136:139], v102, s[40:43], s60 offen sc1\n"
"v_mfma_f32_16x16x32_bf16 v[232:235], a[8:11], a[92:95], v[232:235]\n"
"v_mfma_f32_16x16x32_bf16 v[236:239], a[12:15], a[92:95], v[236:239]\n"
"s_barrier\n"
"s_waitcnt lgkmcnt(4)\n"
"v_mfma_f32_16x16x32_bf16 v[144:147], a[16:19], a[80:83], v[144:147]\n"
"ds_read_b64_tr_b16 a[0:1], v108 offset:16384\n"
"v_mfma_f32_16x16x32_bf16 v[148:151], a[20:23], a[80:83], v[148:151]\n"
"buffer_load_dwordx4 a[140:143], v103, s[40:43], s60 offen sc1\n"
"v_mfma_f32_16x16x32_bf16 v[176:179], a[16:19], a[84:87], v[176:179]\n"
"ds_read_b64_tr_b16 a[2:3], v108 offset:20480\n"
"v_mfma_f32_16x16x32_bf16 v[180:183], a[20:23], a[84:87], v[180:183]\n"
"ds_read_b64_tr_b16 a[4:5], v108 offset:24576\n"
"v_mfma_f32_16x16x32_bf16 v[208:211], a[16:19], a[88:91], v[208:211]\n"
"ds_read_b64_tr_b16 a[6:7], v108 offset:28672\n"
"v_mfma_f32_16x16x32_bf16 v[212:215], a[20:23], a[88:91], v[212:215]\n"
"buffer_load_dwordx4 v105, s[44:47], s61 offen offset:1024 lds\n"
"v_mfma_f32_16x16x32_bf16 v[240:243], a[16:19], a[92:95], v[240:243]\n"
"v_mfma_f32_16x16x32_bf16 v[244:247], a[20:23], a[92:95], v[244:247]\n"
"s_waitcnt lgkmcnt(4)\n"
"v_mfma_f32_16x16x32_bf16 v[152:155], a[24:27], a[80:83], v[152:155]\n"
"ds_read_b64_tr_b16 a[8:9], v109 offset:16384\n"
"v_mfma_f32_16x16x32_bf16 v[156:159], a[28:31], a[80:83], v[156:159]\n"
"buffer_load_dwordx4 a[144:147], v100, s[40:43], s60 offen offset:1024 sc1\n"
"v_mfma_f32_16x16x32_bf16 v[184:187], a[24:27], a[84:87], v[184:187]\n"
"ds_read_b64_tr_b16 a[10:11], v109 offset:20480\n"
"v_mfma_f32_16x16x32_bf16 v[188:191], a[28:31], a[84:87], v[188:191]\n"
"ds_read_b64_tr_b16 a[12:13], v109 offset:24576\n"
"v_mfma_f32_16x16x32_bf16 v[216:219], a[24:27], a[88:91], v[216:219]\n"
"ds_read_b64_tr_b16 a[14:15], v109 offset:28672\n"
"v_mfma_f32_16x16x32_bf16 v[220:223], a[28:31], a[88:91], v[220:223]\n"
"buffer_load_dwordx4 a[148:151], v101, s[40:43], s60 offen offset:1024 sc1\n"
"v_mfma_f32_16x16x32_bf16 v[248:251], a[24:27], a[92:95], v[248:251]\n"
"v_mfma_f32_16x16x32_bf16 v[252:255], a[28:31], a[92:95], v[252:255]\n"
"s_waitcnt vmcnt(10) lgkmcnt(4)\n"
"v_mfma_f32_16x16x32_bf16 v[128:131], a[0:3], a[96:99], v[128:131]\n"
"ds_read_b64_tr_b16 a[16:17], v108 offset:16640\n"
"v_mfma_f32_16x16x32_bf16 v[128:131], a[4:7], a[112:115], v[128:131]\n"
"buffer_load_dwordx4 v106, s[44:47], s61 offen offset:2048 lds\n"
"v_mfma_f32_16x16x32_bf16 v[160:163], a[0:3], a[100:103], v[160:163]\n"
"ds_read_b64_tr_b16 a[18:19], v108 offset:20736\n"
"v_mfma_f32_16x16x32_bf16 v[160:163], a[4:7], a[116:119], v[160:163]\n"
"ds_read_b64_tr_b16 a[20:21], v108 offset:24832\n"
"v_mfma_f32_16x16x32_bf16 v[192:195], a[0:3], a[104:107], v[192:195]\n"
"ds_read_b64_tr_b16 a[22:23], v108 offset:28928\n"
"s_waitcnt vmcnt(9)\n"
"v_mfma_f32_16x16x32_bf16 v[192:195], a[4:7], a[120:123], v[192:195]\n"
"buffer_load_dwordx4 a[152:155], v102, s[40:43], s60 offen offset:1024 sc1\n"
"v_mfma_f32_16x16x32_bf16 v[224:227], a[0:3], a[108:111], v[224:227]\n"
"v_mfma_f32_16x16x32_bf16 v[224:227], a[4:7], a[124:127], v[224:227]\n"
"s_waitcnt lgkmcnt(4)\n"
"v_mfma_f32_16x16x32_bf16 v[132:135], a[8:11], a[96:99], v[132:135]\n"
"ds_read_b64_tr_b16 a[24:25], v109 offset:16640\n"
"v_mfma_f32_16x16x32_bf16 v[132:135], a[12:15], a[112:115], v[132:135]\n"
"buffer_load_dwordx4 a[156:159], v103, s[40:43], s60 offen offset:1024 sc1\n"
"v_mfma_f32_16x16x32_bf16 v[164:167], a[8:11], a[100:103], v[164:167]\n"
"ds_read_b64_tr_b16 a[26:27], v109 offset:20736\n"
"v_mfma_f32_16x16x32_bf16 v[164:167], a[12:15], a[116:119], v[164:167]\n"
"ds_read_b64_tr_b16 a[28:29], v109 offset:24832\n"
"v_mfma_f32_16x16x32_bf16 v[196:199], a[8:11], a[104:107], v[196:199]\n"
"ds_read_b64_tr_b16 a[30:31], v109 offset:28928\n"
"v_mfma_f32_16x16x32_bf16 v[196:199], a[12:15], a[120:123], v[196:199]\n"
"buffer_load_dwordx4 v107, s[44:47], s61 offen offset:3072 lds\n"
"s_add_u32 s61, s61, 458752\n"
"v_mfma_f32_16x16x32_bf16 v[228:231], a[8:11], a[108:111], v[228:231]\n"
"v_mfma_f32_16x16x32_bf16 v[228:231], a[12:15], a[124:127], v[228:231]\n"
"s_waitcnt lgkmcnt(4)\n"
"v_mfma_f32_16x16x32_bf16 v[136:139], a[16:19], a[96:99], v[136:139]\n"
"ds_read_b64_tr_b16 a[0:1], v108 offset:16896\n"
"v_mfma_f32_16x16x32_bf16 v[136:139], a[20:23], a[112:115], v[136:139]\n"
"buffer_load_dwordx4 v[124:127], v112, s[64:67], 0 offen\n"
"v_mfma_f32_16x16x32_bf16 v[168:171], a[16:19], a[100:103], v[168:171]\n"
"ds_read_b64_tr_b16 a[2:3], v108 offset:20992\n"
"v_mfma_f32_16x16x32_bf16 v[168:171], a[20:23], a[116:119], v[168:171]\n"
"ds_read_b64_tr_b16 a[4:5], v108 offset:25088\n"
"v_mfma_f32_16x16x32_bf16 v[200:203], a[16:19], a[104:107], v[200:203]\n"
"ds_read_b64_tr_b16 a[6:7], v108 offset:29184\n"
"v_mfma_f32_16x16x32_bf16 v[200:203], a[20:23], a[120:123], v[200:203]\n"
"buffer_load_dwordx4 a[160:163], v100, s[40:43], s60 offen offset:2048 sc1\n"
"v_mfma_f32_16x16x32_bf16 v[232:235], a[16:19], a[108:111], v[232:235]\n"
"v_mfma_f32_16x16x32_bf16 v[232:235], a[20:23], a[124:127], v[232:235]\n"
"s_waitcnt lgkmcnt(4)\n"
"v_mfma_f32_16x16x32_bf16 v[140:143], a[24:27], a[96:99], v[140:143]\n"
"ds_read_b64_tr_b16 a[8:9], v109 offset:16896\n"
"v_mfma_f32_16x16x32_bf16 v[140:143], a[28:31], a[112:115], v[140:143]\n"
"buffer_load_dwordx4 a[164:167], v101, s[40:43], s60 offen offset:2048 sc1\n"
"v_mfma_f32_16x16x32_bf16 v[172:175], a[24:27], a[100:103], v[172:175]\n"
"ds_read_b64_tr_b16 a[10:11], v109 offset:20992\n"
"v_mfma_f32_16x16x32_bf16 v[172:175], a[28:31], a[116:119], v[172:175]\n"
"ds_read_b64_tr_b16 a[12:13], v109 offset:25088\n"
"v_mfma_f32_16x16x32_bf16 v[204:207], a[24:27], a[104:107], v[204:207]\n"
"ds_read_b64_tr_b16 a[14:15], v109 offset:29184\n"
"s_add_u32 m0, s62, 49152\n"
"v_mfma_f32_16x16x32_bf16 v[204:207], a[28:31], a[120:123], v[204:207]\n"
"buffer_load_dwordx4 v104, s[44:47], s61 offen lds\n"
"v_mfma_f32_16x16x32_bf16 v[236:239], a[24:27], a[108:111], v[236:239]\n"
"v_mfma_f32_16x16x32_bf16 v[236:239], a[28:31], a[124:127], v[236:239]\n"
"s_waitcnt lgkmcnt(4)\n"
"v_mfma_f32_16x16x32_bf16 v[144:147], a[0:3], a[96:99], v[144:147]\n"
"ds_read_b64_tr_b16 a[16:17], v108 offset:17152\n"
"v_mfma_f32_16x16x32_bf16 v[144:147], a[4:7], a[112:115], v[144:147]\n"
"buffer_load_dwordx4 a[168:171], v102, s[40:43], s60 offen offset:2048 sc1\n"
"v_mfma_f32_16x16x32_bf16 v[176:179], a[0:3], a[100:103], v[176:179]\n"
"ds_read_b64_tr_b16 a[18:19], v108 offset:21248\n"
"v_mfma_f32_16x16x32_bf16 v[176:179], a[4:7], a[116:119], v[176:179]\n"
"ds_read_b64_tr_b16 a[20:21], v108 offset:25344\n"
"v_mfma_f32_16x16x32_bf16 v[208:211], a[0:3], a[104:107], v[208:211]\n"
"ds_read_b64_tr_b16 a[22:23], v108 offset:29440\n"
"v_mfma_f32_16x16x32_bf16 v[208:211], a[4:7], a[120:123], v[208:211]\n"
"buffer_load_dwordx4 a[172:175], v103, s[40:43], s60 offen offset:2048 sc1\n"
"v_mfma_f32_16x16x32_bf16 v[240:243], a[0:3], a[108:111], v[240:243]\n"
"v_mfma_f32_16x16x32_bf16 v[240:243], a[4:7], a[124:127], v[240:243]\n"
"s_waitcnt lgkmcnt(4)\n"
"v_mfma_f32_16x16x32_bf16 v[148:151], a[8:11], a[96:99], v[148:151]\n"
"ds_read_b64_tr_b16 a[24:25], v109 offset:17152\n"
"v_mfma_f32_16x16x32_bf16 v[148:151], a[12:15], a[112:115], v[148:151]\n"
"buffer_load_dwordx4 v105, s[44:47], s61 offen offset:1024 lds\n"
"v_mfma_f32_16x16x32_bf16 v[180:183], a[8:11], a[100:103], v[180:183]\n"
"ds_read_b64_tr_b16 a[26:27], v109 offset:21248\n"
"v_mfma_f32_16x16x32_bf16 v[180:183], a[12:15], a[116:119], v[180:183]\n"
"ds_read_b64_tr_b16 a[28:29], v109 offset:25344\n"
"v_mfma_f32_16x16x32_bf16 v[212:215], a[8:11], a[104:107], v[212:215]\n"
"ds_read_b64_tr_b16 a[30:31], v109 offset:29440\n"
"v_mfma_f32_16x16x32_bf16 v[212:215], a[12:15], a[120:123], v[212:215]\n"
"buffer_load_dwordx4 a[176:179], v100, s[40:43], s60 offen offset:3072 sc1\n"
"v_mfma_f32_16x16x32_bf16 v[244:247], a[8:11], a[108:111], v[244:247]\n"
"v_mfma_f32_16x16x32_bf16 v[244:247], a[12:15], a[124:127], v[244:247]\n"
"s_waitcnt vmcnt(8) lgkmcnt(4)\n"
"s_barrier\n"
"v_mfma_f32_16x16x32_bf16 v[152:155], a[16:19], a[96:99], v[152:155]\n"
"ds_read_b64_tr_b16 a[0:1], v108 offset:32768\n"
"v_mfma_f32_16x16x32_bf16 v[152:155], a[20:23], a[112:115], v[152:155]\n"
"buffer_load_dwordx4 a[180:183], v101, s[40:43], s60 offen offset:3072 sc1\n"
"v_mfma_f32_16x16x32_bf16 v[184:187], a[16:19], a[100:103], v[184:187]\n"
"ds_read_b64_tr_b16 a[2:3], v108 offset:36864\n"
"v_mfma_f32_16x16x32_bf16 v[184:187], a[20:23], a[116:119], v[184:187]\n"
"ds_read_b64_tr_b16 a[4:5], v108 offset:40960\n"
"v_mfma_f32_16x16x32_bf16 v[216:219], a[16:19], a[104:107], v[216:219]\n"
"ds_read_b64_tr_b16 a[6:7], v108 offset:45056\n"
"v_mfma_f32_16x16x32_bf16 v[216:219], a[20:23], a[120:123], v[216:219]\n"
"buffer_load_dwordx4 v106, s[44:47], s61 offen offset:2048 lds\n"
"v_mfma_f32_16x16x32_bf16 v[248:251], a[16:19], a[108:111], v[248:251]\n"
"v_mfma_f32_16x16x32_bf16 v[248:251], a[20:23], a[124:127], v[248:251]\n"
"s_waitcnt lgkmcnt(4)\n"
"v_mfma_f32_16x16x32_bf16 v[156:159], a[24:27], a[96:99], v[156:159]\n"
"ds_read_b64_tr_b16 a[8:9], v109 offset:32768\n"
"v_mfma_f32_16x16x32_bf16 v[156:159], a[28:31], a[112:115], v[156:159]\n"
"buffer_load_dwordx4 a[184:187], v102, s[40:43], s60 offen offset:3072 sc1\n"
"v_mfma_f32_16x16x32_bf16 v[188:191], a[24:27], a[100:103], v[188:191]\n"
"ds_read_b64_tr_b16 a[10:11], v109 offset:36864\n"
"v_mfma_f32_16x16x32_bf16 v[188:191], a[28:31], a[116:119], v[188:191]\n"
"ds_read_b64_tr_b16 a[12:13], v109 offset:40960\n"
"v_mfma_f32_16x16x32_bf16 v[220:223], a[24:27], a[104:107], v[220:223]\n"
"ds_read_b64_tr_b16 a[14:15], v109 offset:45056\n"
"v_mfma_f32_16x16x32_bf16 v[220:223], a[28:31], a[120:123], v[220:223]\n"
"buffer_load_dwordx4 a[188:191], v103, s[40:43], s60 offen offset:3072 sc1\n"
"s_add_u32 s60, s60, 4096\n"
"v_mfma_f32_16x16x32_bf16 v[252:255], a[24:27], a[108:111], v[252:255]\n"
"v_mfma_f32_16x16x32_bf16 v[252:255], a[28:31], a[124:127], v[252:255]\n"
"s_waitcnt lgkmcnt(4)\n"
"v_mfma_f32_16x16x32_bf16 v[128:131], a[0:3], a[128:131], v[128:131]\n"
"ds_read_b64_tr_b16 a[16:17], v108 offset:33024\n"
"v_mfma_f32_16x16x32_bf16 v[128:131], a[4:7], a[144:147], v[128:131]\n"
"buffer_load_dwordx4 v107, s[44:47], s61 offen offset:3072 lds\n"
"s_add_u32 s61, s61, 458752\n"
"v_mfma_f32_16x16x32_bf16 v[160:163], a[0:3], a[132:135], v[160:163]\n"
"ds_read_b64_tr_b16 a[18:19], v108 offset:37120\n"
"v_mfma_f32_16x16x32_bf16 v[160:163], a[4:7], a[148:151], v[160:163]\n"
"ds_read_b64_tr_b16 a[20:21], v108 offset:41216\n"
"v_mfma_f32_16x16x32_bf16 v[192:195], a[0:3], a[136:139], v[192:195]\n"
"ds_read_b64_tr_b16 a[22:23], v108 offset:45312\n"
"v_mfma_f32_16x16x32_bf16 v[192:195], a[4:7], a[152:155], v[192:195]\n"
"buffer_load_dwordx4 a[64:67], v100, s[40:43], s60 offen sc1\n"
"v_mfma_f32_16x16x32_bf16 v[224:227], a[0:3], a[140:143], v[224:227]\n"
"v_mfma_f32_16x16x32_bf16 v[224:227], a[4:7], a[156:159], v[224:227]\n"
"s_waitcnt lgkmcnt(4)\n"
"v_mfma_f32_16x16x32_bf16 v[132:135], a[8:11], a[128:131], v[132:135]\n"
"ds_read_b64_tr_b16 a[24:25], v109 offset:33024\n"
"v_mfma_f32_16x16x32_bf16 v[132:135], a[12:15], a[144:147], v[132:135]\n"
"buffer_load_dwordx4 a[68:71], v101, s[40:43], s60 offen sc1\n"
"v_mfma_f32_16x16x32_bf16 v[164:167], a[8:11], a[132:135], v[164:167]\n"
"ds_read_b64_tr_b16 a[26:27], v109 offset:37120\n"
"v_mfma_f32_16x16x32_bf16 v[164:167], a[12:15], a[148:151], v[164:167]\n"
"ds_read_b64_tr_b16 a[28:29], v109 offset:41216\n"
"v_mfma_f32_16x16x32_bf16 v[196:199], a[8:11], a[136:139], v[196:199]\n"
"ds_read_b64_tr_b16 a[30:31], v109 offset:45312\n"
"s_add_u32 m0, s62, 0\n"
"v_mfma_f32_16x16x32_bf16 v[196:199], a[12:15], a[152:155], v[196:199]\n"
"buffer_load_dwordx4 v104, s[44:47], s61 offen lds\n"
"v_mfma_f32_16x16x32_bf16 v[228:231], a[8:11], a[140:143], v[228:231]\n"
"v_mfma_f32_16x16x32_bf16 v[228:231], a[12:15], a[156:159], v[228:231]\n"
"s_waitcnt lgkmcnt(4)\n"
"v_mfma_f32_16x16x32_bf16 v[136:139], a[16:19], a[128:131], v[136:139]\n"
"ds_read_b64_tr_b16 a[0:1], v108 offset:33280\n"
"v_mfma_f32_16x16x32_bf16 v[136:139], a[20:23], a[144:147], v[136:139]\n"
"buffer_load_dwordx4 a[72:75], v102, s[40:43], s60 offen sc1\n"
"v_mfma_f32_16x16x32_bf16 v[168:171], a[16:19], a[132:135], v[168:171]\n"
"ds_read_b64_tr_b16 a[2:3], v108 offset:37376\n"
"v_mfma_f32_16x16x32_bf16 v[168:171], a[20:23], a[148:151], v[168:171]\n"
"ds_read_b64_tr_b16 a[4:5], v108 offset:41472\n"
"v_mfma_f32_16x16x32_bf16 v[200:203], a[16:19], a[136:139], v[200:203]\n"
"ds_read_b64_tr_b16 a[6:7], v108 offset:45568\n"
"v_mfma_f32_16x16x32_bf16 v[200:203], a[20:23], a[152:155], v[200:203]\n"
"buffer_load_dwordx4 a[76:79], v103, s[40:43], s60 offen sc1\n"
"v_mfma_f32_16x16x32_bf16 v[232:235], a[16:19], a[140:143], v[232:235]\n"
"v_mfma_f32_16x16x32_bf16 v[232:235], a[20:23], a[156:159], v[232:235]\n"
"s_waitcnt lgkmcnt(4)\n"
"v_mfma_f32_16x16x32_bf16 v[140:143], a[24:27], a[128:131], v[140:143]\n"
"ds_read_b64_tr_b16 a[8:9], v109 offset:33280\n"
"v_mfma_f32_16x16x32_bf16 v[140:143], a[28:31], a[144:147], v[140:143]\n"
"buffer_load_dwordx4 v105, s[44:47], s61 offen offset:1024 lds\n"
"v_mfma_f32_16x16x32_bf16 v[172:175], a[24:27], a[132:135], v[172:175]\n"
"ds_read_b64_tr_b16 a[10:11], v109 offset:37376\n"
"v_mfma_f32_16x16x32_bf16 v[172:175], a[28:31], a[148:151], v[172:175]\n"
"ds_read_b64_tr_b16 a[12:13], v109 offset:41472\n"
"v_mfma_f32_16x16x32_bf16 v[204:207], a[24:27], a[136:139], v[204:207]\n"
"ds_read_b64_tr_b16 a[14:15], v109 offset:45568\n"
"v_mfma_f32_16x16x32_bf16 v[204:207], a[28:31], a[152:155], v[204:207]\n"
"buffer_load_dwordx4 a[80:83], v100, s[40:43], s60 offen offset:1024 sc1\n"
"v_mfma_f32_16x16x32_bf16 v[236:239], a[24:27], a[140:143], v[236:239]\n"
"v_mfma_f32_16x16x32_bf16 v[236:239], a[28:31], a[156:159], v[236:239]\n"
"s_waitcnt lgkmcnt(4)\n"
"v_mfma_f32_16x16x32_bf16 v[144:147], a[0:3], a[128:131], v[144:147]\n"
"ds_read_b64_tr_b16 a[16:17], v108 offset:33536\n"
"v_mfma_f32_16x16x32_bf16 v[144:147], a[4:7], a[144:147], v[144:147]\n"
"buffer_load_dwordx4 a[84:87], v101, s[40:43], s60 offen offset:1024 sc1\n"
"v_mfma_f32_16x16x32_bf16 v[176:179], a[0:3], a[132:135], v[176:179]\n"
"ds_read_b64_tr_b16 a[18:19], v108 offset:37632\n"
"v_mfma_f32_16x16x32_bf16 v[176:179], a[4:7], a[148:151], v[176:179]\n"
"ds_read_b64_tr_b16 a[20:21], v108 offset:41728\n"
"v_mfma_f32_16x16x32_bf16 v[208:211], a[0:3], a[136:139], v[208:211]\n"
"ds_read_b64_tr_b16 a[22:23], v108 offset:45824\n"
"v_mfma_f32_16x16x32_bf16 v[208:211], a[4:7], a[152:155], v[208:211]\n"
"buffer_load_dwordx4 v106, s[44:47], s61 offen offset:2048 lds\n"
"v_mfma_f32_16x16x32_bf16 v[240:243], a[0:3], a[140:143], v[240:243]\n"
"v_mfma_f32_16x16x32_bf16 v[240:243], a[4:7], a[156:159], v[240:243]\n"
"s_waitcnt lgkmcnt(4)\n"
"v_mfma_f32_16x16x32_bf16 v[148:151], a[8:11], a[128:131], v[148:151]\n"
"ds_read_b64_tr_b16 a[24:25], v109 offset:33536\n"
"v_mfma_f32_16x16x32_bf16 v[148:151], a[12:15], a[144:147], v[148:151]\n"
"buffer_load_dwordx4 a[88:91], v102, s[40:43], s60 offen offset:1024 sc1\n"
"v_mfma_f32_16x16x32_bf16 v[180:183], a[8:11], a[132:135], v[180:183]\n"
"ds_read_b64_tr_b16 a[26:27], v109 offset:37632\n"
"v_mfma_f32_16x16x32_bf16 v[180:183], a[12:15], a[148:151], v[180:183]\n"
"ds_read_b64_tr_b16 a[28:29], v109 offset:41728\n"
"v_mfma_f32_16x16x32_bf16 v[212:215], a[8:11], a[136:139], v[212:215]\n"
"ds_read_b64_tr_b16 a[30:31], v109 offset:45824\n"
"v_mfma_f32_16x16x32_bf16 v[212:215], a[12:15], a[152:155], v[212:215]\n"
"buffer_load_dwordx4 a[92:95], v103, s[40:43], s60 offen offset:1024 sc1\n"
"v_mfma_f32_16x16x32_bf16 v[244:247], a[8:11], a[140:143], v[244:247]\n"
"v_mfma_f32_16x16x32_bf16 v[244:247], a[12:15], a[156:159], v[244:247]\n"
"s_waitcnt vmcnt(11) lgkmcnt(4)\n"
"s_barrier\n"
"v_mfma_f32_16x16x32_bf16 v[152:155], a[16:19], a[128:131], v[152:155]\n"
"ds_read_b64_tr_b16 a[0:1], v108 offset:49152\n"
"v_mfma_f32_16x16x32_bf16 v[152:155], a[20:23], a[144:147], v[152:155]\n"
"buffer_load_dwordx4 v107, s[44:47], s61 offen offset:3072 lds\n"
"s_add_u32 s61, s61, 458752\n"
"v_mfma_f32_16x16x32_bf16 v[184:187], a[16:19], a[132:135], v[184:187]\n"
"ds_read_b64_tr_b16 a[2:3], v108 offset:53248\n"
"v_mfma_f32_16x16x32_bf16 v[184:187], a[20:23], a[148:151], v[184:187]\n"
"ds_read_b64_tr_b16 a[4:5], v108 offset:57344\n"
"v_mfma_f32_16x16x32_bf16 v[216:219], a[16:19], a[136:139], v[216:219]\n"
"ds_read_b64_tr_b16 a[6:7], v108 offset:61440\n"
"v_mfma_f32_16x16x32_bf16 v[216:219], a[20:23], a[152:155], v[216:219]\n"
"buffer_load_dwordx4 a[96:99], v100, s[40:43], s60 offen offset:2048 sc1\n"
"v_mfma_f32_16x16x32_bf16 v[248:251], a[16:19], a[140:143], v[248:251]\n"
"v_mfma_f32_16x16x32_bf16 v[248:251], a[20:23], a[156:159], v[248:251]\n"
"s_waitcnt lgkmcnt(4)\n"
"v_mfma_f32_16x16x32_bf16 v[156:159], a[24:27], a[128:131], v[156:159]\n"
"ds_read_b64_tr_b16 a[8:9], v109 offset:49152\n"
"v_mfma_f32_16x16x32_bf16 v[156:159], a[28:31], a[144:147], v[156:159]\n"
"buffer_load_dwordx4 a[100:103], v101, s[40:43], s60 offen offset:2048 sc1\n"
"v_mfma_f32_16x16x32_bf16 v[188:191], a[24:27], a[132:135], v[188:191]\n"
"ds_read_b64_tr_b16 a[10:11], v109 offset:53248\n"
"v_mfma_f32_16x16x32_bf16 v[188:191], a[28:31], a[148:151], v[188:191]\n"
"ds_read_b64_tr_b16 a[12:13], v109 offset:57344\n"
"v_mfma_f32_16x16x32_bf16 v[220:223], a[24:27], a[136:139], v[220:223]\n"
"ds_read_b64_tr_b16 a[14:15], v109 offset:61440\n"
"s_add_u32 m0, s62, 16384\n"
"v_mfma_f32_16x16x32_bf16 v[220:223], a[28:31], a[152:155], v[220:223]\n"
"buffer_load_dwordx4 v104, s[44:47], s61 offen lds\n"
"v_mfma_f32_16x16x32_bf16 v[252:255], a[24:27], a[140:143], v[252:255]\n"
"v_mfma_f32_16x16x32_bf16 v[252:255], a[28:31], a[156:159], v[252:255]\n"
"s_waitcnt lgkmcnt(4)\n"
"v_mfma_f32_16x16x32_bf16 v[128:131], a[0:3], a[160:163], v[128:131]\n"
"ds_read_b64_tr_b16 a[16:17], v108 offset:49408\n"
"v_mfma_f32_16x16x32_bf16 v[128:131], a[4:7], a[176:179], v[128:131]\n"
"buffer_load_dwordx4 a[104:107], v102, s[40:43], s60 offen offset:2048 sc1\n"
"v_mfma_f32_16x16x32_bf16 v[160:163], a[0:3], a[164:167], v[160:163]\n"
"ds_read_b64_tr_b16 a[18:19], v108 offset:53504\n"
"v_mfma_f32_16x16x32_bf16 v[160:163], a[4:7], a[180:183], v[160:163]\n"
"ds_read_b64_tr_b16 a[20:21], v108 offset:57600\n"
"v_mfma_f32_16x16x32_bf16 v[192:195], a[0:3], a[168:171], v[192:195]\n"
"ds_read_b64_tr_b16 a[22:23], v108 offset:61696\n"
"v_mfma_f32_16x16x32_bf16 v[192:195], a[4:7], a[184:187], v[192:195]\n"
"v_mfma_f32_16x16x32_bf16 v[224:227], a[0:3], a[172:175], v[224:227]\n"
"buffer_load_dwordx4 a[108:111], v103, s[40:43], s60 offen offset:2048 sc1\n"
"v_mfma_f32_16x16x32_bf16 v[224:227], a[4:7], a[188:191], v[224:227]\n"
"s_waitcnt lgkmcnt(4)\n"
"v_mfma_f32_16x16x32_bf16 v[132:135], a[8:11], a[160:163], v[132:135]\n"
"ds_read_b64_tr_b16 a[24:25], v109 offset:49408\n"
"v_mfma_f32_16x16x32_bf16 v[132:135], a[12:15], a[176:179], v[132:135]\n"
"ds_read_b64_tr_b16 a[26:27], v109 offset:53504\n"
"v_mfma_f32_16x16x32_bf16 v[164:167], a[8:11], a[164:167], v[164:167]\n"
"ds_read_b64_tr_b16 a[28:29], v109 offset:57600\n"
"v_mfma_f32_16x16x32_bf16 v[164:167], a[12:15], a[180:183], v[164:167]\n"
"ds_read_b64_tr_b16 a[30:31], v109 offset:61696\n"
"v_mfma_f32_16x16x32_bf16 v[196:199], a[8:11], a[168:171], v[196:199]\n"
"buffer_load_dwordx4 v105, s[44:47], s61 offen offset:1024 lds\n"
"v_mfma_f32_16x16x32_bf16 v[196:199], a[12:15], a[184:187], v[196:199]\n"
"v_mfma_f32_16x16x32_bf16 v[228:231], a[8:11], a[172:175], v[228:231]\n"
"v_mfma_f32_16x16x32_bf16 v[228:231], a[12:15], a[188:191], v[228:231]\n"
"s_waitcnt lgkmcnt(4)\n"
"v_mfma_f32_16x16x32_bf16 v[136:139], a[16:19], a[160:163], v[136:139]\n"
"ds_read_b64_tr_b16 a[0:1], v108 offset:49664\n"
"v_mfma_f32_16x16x32_bf16 v[136:139], a[20:23], a[176:179], v[136:139]\n"
"buffer_load_dwordx4 a[112:115], v100, s[40:43], s60 offen offset:3072 sc1\n"
"v_mfma_f32_16x16x32_bf16 v[168:171], a[16:19], a[164:167], v[168:171]\n"
"ds_read_b64_tr_b16 a[2:3], v108 offset:53760\n"
"v_mfma_f32_16x16x32_bf16 v[168:171], a[20:23], a[180:183], v[168:171]\n"
"ds_read_b64_tr_b16 a[4:5], v108 offset:57856\n"
"v_mfma_f32_16x16x32_bf16 v[200:203], a[16:19], a[168:171], v[200:203]\n"
"ds_read_b64_tr_b16 a[6:7], v108 offset:61952\n"
"v_mfma_f32_16x16x32_bf16 v[200:203], a[20:23], a[184:187], v[200:203]\n"
"v_mfma_f32_16x16x32_bf16 v[232:235], a[16:19], a[172:175], v[232:235]\n"
"buffer_load_dwordx4 a[116:119], v101, s[40:43], s60 offen offset:3072 sc1\n"
"v_mfma_f32_16x16x32_bf16 v[232:235], a[20:23], a[188:191], v[232:235]\n"
"s_waitcnt lgkmcnt(4)\n"
"v_mfma_f32_16x16x32_bf16 v[140:143], a[24:27], a[160:163], v[140:143]\n"
"ds_read_b64_tr_b16 a[8:9], v109 offset:49664\n"
"v_mfma_f32_16x16x32_bf16 v[140:143], a[28:31], a[176:179], v[140:143]\n"
"ds_read_b64_tr_b16 a[10:11], v109 offset:53760\n"
"v_mfma_f32_16x16x32_bf16 v[172:175], a[24:27], a[164:167], v[172:175]\n"
"ds_read_b64_tr_b16 a[12:13], v109 offset:57856\n"
"v_mfma_f32_16x16x32_bf16 v[172:175], a[28:31], a[180:183], v[172:175]\n"
"buffer_load_dwordx4 v106, s[44:47], s61 offen offset:2048 lds\n"
"v_mfma_f32_16x16x32_bf16 v[204:207], a[24:27], a[168:171], v[204:207]\n"
"ds_read_b64_tr_b16 a[14:15], v109 offset:61952\n"
"v_mfma_f32_16x16x32_bf16 v[204:207], a[28:31], a[184:187], v[204:207]\n"
"v_mfma_f32_16x16x32_bf16 v[236:239], a[24:27], a[172:175], v[236:239]\n"
"v_mfma_f32_16x16x32_bf16 v[236:239], a[28:31], a[188:191], v[236:239]\n"
"s_waitcnt lgkmcnt(4)\n"
"v_mfma_f32_16x16x32_bf16 v[144:147], a[0:3], a[160:163], v[144:147]\n"
"buffer_load_dwordx4 a[120:123], v102, s[40:43], s60 offen offset:3072 sc1\n"
"v_mfma_f32_16x16x32_bf16 v[144:147], a[4:7], a[176:179], v[144:147]\n"
"ds_read_b64_tr_b16 a[16:17], v108 offset:49920\n"
"v_mfma_f32_16x16x32_bf16 v[176:179], a[0:3], a[164:167], v[176:179]\n"
"ds_read_b64_tr_b16 a[18:19], v108 offset:54016\n"
"v_mfma_f32_16x16x32_bf16 v[176:179], a[4:7], a[180:183], v[176:179]\n"
"ds_read_b64_tr_b16 a[20:21], v108 offset:58112\n"
"v_mfma_f32_16x16x32_bf16 v[208:211], a[0:3], a[168:171], v[208:211]\n"
"ds_read_b64_tr_b16 a[22:23], v108 offset:62208\n"
"v_mfma_f32_16x16x32_bf16 v[208:211], a[4:7], a[184:187], v[208:211]\n"
"buffer_load_dwordx4 a[124:127], v103, s[40:43], s60 offen offset:3072 sc1\n"
"s_add_u32 s60, s60, 4096\n"
"v_mfma_f32_16x16x32_bf16 v[240:243], a[0:3], a[172:175], v[240:243]\n"
"v_mfma_f32_16x16x32_bf16 v[240:243], a[4:7], a[188:191], v[240:243]\n"
"s_waitcnt lgkmcnt(4)\n"
"v_mfma_f32_16x16x32_bf16 v[148:151], a[8:11], a[160:163], v[148:151]\n"
"ds_read_b64_tr_b16 a[24:25], v109 offset:49920\n"
"v_mfma_f32_16x16x32_bf16 v[148:151], a[12:15], a[176:179], v[148:151]\n"
"ds_read_b64_tr_b16 a[26:27], v109 offset:54016\n"
"v_mfma_f32_16x16x32_bf16 v[180:183], a[8:11], a[164:167], v[180:183]\n"
"ds_read_b64_tr_b16 a[28:29], v109 offset:58112\n"
"v_mfma_f32_16x16x32_bf16 v[180:183], a[12:15], a[180:183], v[180:183]\n"
"buffer_load_dwordx4 v107, s[44:47], s61 offen offset:3072 lds\n"
"s_add_u32 s61, s61, 458752\n"
"v_mfma_f32_16x16x32_bf16 v[212:215], a[8:11], a[168:171], v[212:215]\n"
"ds_read_b64_tr_b16 a[30:31], v109 offset:62208\n"
"v_mfma_f32_16x16x32_bf16 v[212:215], a[12:15], a[184:187], v[212:215]\n"
"v_mfma_f32_16x16x32_bf16 v[244:247], a[8:11], a[172:175], v[244:247]\n"
"v_mfma_f32_16x16x32_bf16 v[244:247], a[12:15], a[188:191], v[244:247]\n"
"s_waitcnt vmcnt(12) lgkmcnt(4)\n"
"s_barrier\n"
"s_mov_b32 s63, 1\n"
"LF_%=:\n"
"v_mfma_f32_16x16x32_bf16 v[152:155], a[16:19], a[160:163], v[152:155]\n"
"ds_read_b64_tr_b16 a[0:1], v108 offset:0\n"
"v_mfma_f32_16x16x32_bf16 v[152:155], a[20:23], a[176:179], v[152:155]\n"
"ds_read_b64_tr_b16 a[2:3], v108 offset:4096\n"
"v_mfma_f32_16x16x32_bf16 v[184:187], a[16:19], a[164:167], v[184:187]\n"
"buffer_load_dwordx4 a[128:131], v100, s[40:43], s60 offen sc1\n"
"v_mfma_f32_16x16x32_bf16 v[184:187], a[20:23], a[180:183], v[184:187]\n"
"ds_read_b64_tr_b16 a[4:5], v108 offset:8192\n"
"v_mfma_f32_16x16x32_bf16 v[216:219], a[16:19], a[168:171], v[216:219]\n"
"ds_read_b64_tr_b16 a[6:7], v108 offset:12288\n"
"v_mfma_f32_16x16x32_bf16 v[216:219], a[20:23], a[184:187], v[216:219]\n"
"v_mfma_f32_16x16x32_bf16 v[248:251], a[16:19], a[172:175], v[248:251]\n"
"v_mfma_f32_16x16x32_bf16 v[248:251], a[20:23], a[188:191], v[248:251]\n"
"buffer_load_dwordx4 a[132:135], v101, s[40:43], s60 offen sc1\n"
"s_waitcnt lgkmcnt(4)\n"
"v_mfma_f32_16x16x32_bf16 v[156:159], a[24:27], a[160:163], v[156:159]\n"
"ds_read_b64_tr_b16 a[8:9], v109 offset:0\n"
"v_mfma_f32_16x16x32_bf16 v[156:159], a[28:31], a[176:179], v[156:159]\n"
"ds_read_b64_tr_b16 a[10:11], v109 offset:4096\n"
"v_mfma_f32_16x16x32_bf16 v[188:191], a[24:27], a[164:167], v[188:191]\n"
"ds_read_b64_tr_b16 a[12:13], v109 offset:8192\n"
"v_mfma_f32_16x16x32_bf16 v[188:191], a[28:31], a[180:183], v[188:191]\n"
"ds_read_b64_tr_b16 a[14:15], v109 offset:12288\n"
"s_add_u32 m0, s62, 32768\n"
"v_mfma_f32_16x16x32_bf16 v[220:223], a[24:27], a[168:171], v[220:223]\n"
"buffer_load_dwordx4 v104, s[44:47], s61 offen lds\n"
"v_mfma_f32_16x16x32_bf16 v[220:223], a[28:31], a[184:187], v[220:223]\n"
"v_mfma_f32_16x16x32_bf16 v[252:255], a[24:27], a[172:175], v[252:255]\n"
"v_mfma_f32_16x16x32_bf16 v[252:255], a[28:31], a[188:191], v[252:255]\n"
"s_waitcnt lgkmcnt(4)\n"
"v_mfma_f32_16x16x32_bf16 v[128:131], a[0:3], a[64:67], v[128:131]\n"
"ds_read_b64_tr_b16 a[16:17], v108 offset:256\n"
"v_mfma_f32_16x16x32_bf16 v[128:131], a[4:7], a[80:83], v[128:131]\n"
"buffer_load_dwordx4 a[136:139], v102, s[40:43], s60 offen sc1\n"
"v_mfma_f32_16x16x32_bf16 v[160:163], a[0:3], a[68:71], v[160:163]\n"
"ds_read_b64_tr_b16 a[18:19], v108 offset:4352\n"
"v_mfma_f32_16x16x32_bf16 v[160:163], a[4:7], a[84:87], v[160:163]\n"
"ds_read_b64_tr_b16 a[20:21], v108 offset:8448\n"
"v_mfma_f32_16x16x32_bf16 v[192:195], a[0:3], a[72:75], v[192:195]\n"
"ds_read_b64_tr_b16 a[22:23], v108 offset:12544\n"
"v_mfma_f32_16x16x32_bf16 v[192:195], a[4:7], a[88:91], v[192:195]\n"
"v_mfma_f32_16x16x32_bf16 v[224:227], a[0:3], a[76:79], v[224:227]\n"
"buffer_load_dwordx4 a[140:143], v103, s[40:43], s60 offen sc1\n"
"v_mfma_f32_16x16x32_bf16 v[224:227], a[4:7], a[92:95], v[224:227]\n"
"s_waitcnt lgkmcnt(4)\n"
"v_mfma_f32_16x16x32_bf16 v[132:135], a[8:11], a[64:67], v[132:135]\n"
"ds_read_b64_tr_b16 a[24:25], v109 offset:256\n"
"v_mfma_f32_16x16x32_bf16 v[132:135], a[12:15], a[80:83], v[132:135]\n"
"ds_read_b64_tr_b16 a[26:27], v109 offset:4352\n"
"v_mfma_f32_16x16x32_bf16 v[164:167], a[8:11], a[68:71], v[164:167]\n"
"ds_read_b64_tr_b16 a[28:29], v109 offset:8448\n"
"v_mfma_f32_16x16x32_bf16 v[164:167], a[12:15], a[84:87], v[164:167]\n"
"ds_read_b64_tr_b16 a[30:31], v109 offset:12544\n"
"v_mfma_f32_16x16x32_bf16 v[196:199], a[8:11], a[72:75], v[196:199]\n"
"buffer_load_dwordx4 v105, s[44:47], s61 offen offset:1024 lds\n"
"v_mfma_f32_16x16x32_bf16 v[196:199], a[12:15], a[88:91], v[196:199]\n"
"v_mfma_f32_16x16x32_bf16 v[228:231], a[8:11], a[76:79], v[228:231]\n"
"v_mfma_f32_16x16x32_bf16 v[228:231], a[12:15], a[92:95], v[228:231]\n"
"s_waitcnt lgkmcnt(4)\n"
"v_mfma_f32_16x16x32_bf16 v[136:139], a[16:19], a[64:67], v[136:139]\n"
"ds_read_b64_tr_b16 a[0:1], v108 offset:512\n"
"v_mfma_f32_16x16x32_bf16 v[136:139], a[20:23], a[80:83], v[136:139]\n"
"buffer_load_dwordx4 a[144:147], v100, s[40:43], s60 offen offset:1024 sc1\n"
"v_mfma_f32_16x16x32_bf16 v[168:171], a[16:19], a[68:71], v[168:171]\n"
"ds_read_b64_tr_b16 a[2:3], v108 offset:4608\n"
"v_mfma_f32_16x16x32_bf16 v[168:171], a[20:23], a[84:87], v[168:171]\n"
"ds_read_b64_tr_b16 a[4:5], v108 offset:8704\n"
"v_mfma_f32_16x16x32_bf16 v[200:203], a[16:19], a[72:75], v[200:203]\n"
"ds_read_b64_tr_b16 a[6:7], v108 offset:12800\n"
"v_mfma_f32_16x16x32_bf16 v[200:203], a[20:23], a[88:91], v[200:203]\n"
"v_mfma_f32_16x16x32_bf16 v[232:235], a[16:19], a[76:79], v[232:235]\n"
"buffer_load_dwordx4 a[148:151], v101, s[40:43], s60 offen offset:1024 sc1\n"
"v_mfma_f32_16x16x32_bf16 v[232:235], a[20:23], a[92:95], v[232:235]\n"
"s_waitcnt lgkmcnt(4)\n"
"v_mfma_f32_16x16x32_bf16 v[140:143], a[24:27], a[64:67], v[140:143]\n"
"ds_read_b64_tr_b16 a[8:9], v109 offset:512\n"
"v_mfma_f32_16x16x32_bf16 v[140:143], a[28:31], a[80:83], v[140:143]\n"
"ds_read_b64_tr_b16 a[10:11], v109 offset:4608\n"
"v_mfma_f32_16x16x32_bf16 v[172:175], a[24:27], a[68:71], v[172:175]\n"
"ds_read_b64_tr_b16 a[12:13], v109 offset:8704\n"
"v_mfma_f32_16x16x32_bf16 v[172:175], a[28:31], a[84:87], v[172:175]\n"
"buffer_load_dwordx4 v106, s[44:47], s61 offen offset:2048 lds\n"
"v_mfma_f32_16x16x32_bf16 v[204:207], a[24:27], a[72:75], v[204:207]\n"
"ds_read_b64_tr_b16 a[14:15], v109 offset:12800\n"
"v_mfma_f32_16x16x32_bf16 v[204:207], a[28:31], a[88:91], v[204:207]\n"
"v_mfma_f32_16x16x32_bf16 v[236:239], a[24:27], a[76:79], v[236:239]\n"
"v_mfma_f32_16x16x32_bf16 v[236:239], a[28:31], a[92:95], v[236:239]\n"
"s_waitcnt lgkmcnt(4)\n"
"v_mfma_f32_16x16x32_bf16 v[144:147], a[0:3], a[64:67], v[144:147]\n"
"buffer_load_dwordx4 a[152:155], v102, s[40:43], s60 offen offset:1024 sc1\n"
"v_mfma_f32_16x16x32_bf16 v[144:147], a[4:7], a[80:83], v[144:147]\n"
"ds_read_b64_tr_b16 a[16:17], v108 offset:768\n"
"v_mfma_f32_16x16x32_bf16 v[176:179], a[0:3], a[68:71], v[176:179]\n"
"ds_read_b64_tr_b16 a[18:19], v108 offset:4864\n"
"v_mfma_f32_16x16x32_bf16 v[176:179], a[4:7], a[84:87], v[176:179]\n"
"ds_read_b64_tr_b16 a[20:21], v108 offset:8960\n"
"v_mfma_f32_16x16x32_bf16 v[208:211], a[0:3], a[72:75], v[208:211]\n"
"ds_read_b64_tr_b16 a[22:23], v108 offset:13056\n"
"v_mfma_f32_16x16x32_bf16 v[208:211], a[4:7], a[88:91], v[208:211]\n"
"buffer_load_dwordx4 a[156:159], v103, s[40:43], s60 offen offset:1024 sc1\n"
"v_mfma_f32_16x16x32_bf16 v[240:243], a[0:3], a[76:79], v[240:243]\n"
"v_mfma_f32_16x16x32_bf16 v[240:243], a[4:7], a[92:95], v[240:243]\n"
"s_waitcnt lgkmcnt(4)\n"
"v_mfma_f32_16x16x32_bf16 v[148:151], a[8:11], a[64:67], v[148:151]\n"
"ds_read_b64_tr_b16 a[24:25], v109 offset:768\n"
"v_mfma_f32_16x16x32_bf16 v[148:151], a[12:15], a[80:83], v[148:151]\n"
"ds_read_b64_tr_b16 a[26:27], v109 offset:4864\n"
"v_mfma_f32_16x16x32_bf16 v[180:183], a[8:11], a[68:71], v[180:183]\n"
"ds_read_b64_tr_b16 a[28:29], v109 offset:8960\n"
"v_mfma_f32_16x16x32_bf16 v[180:183], a[12:15], a[84:87], v[180:183]\n"
"buffer_load_dwordx4 v107, s[44:47], s61 offen offset:3072 lds\n"
"s_add_u32 s61, s61, 458752\n"
"v_mfma_f32_16x16x32_bf16 v[212:215], a[8:11], a[72:75], v[212:215]\n"
"ds_read_b64_tr_b16 a[30:31], v109 offset:13056\n"
"v_mfma_f32_16x16x32_bf16 v[212:215], a[12:15], a[88:91], v[212:215]\n"
"v_mfma_f32_16x16x32_bf16 v[244:247], a[8:11], a[76:79], v[244:247]\n"
"v_mfma_f32_16x16x32_bf16 v[244:247], a[12:15], a[92:95], v[244:247]\n"
"s_waitcnt vmcnt(12) lgkmcnt(4)\n"
"s_barrier\n"
"v_mfma_f32_16x16x32_bf16 v[152:155], a[16:19], a[64:67], v[152:155]\n"
"ds_read_b64_tr_b16 a[0:1], v108 offset:16384\n"
"v_mfma_f32_16x16x32_bf16 v[152:155], a[20:23], a[80:83], v[152:155]\n"
"ds_read_b64_tr_b16 a[2:3], v108 offset:20480\n"
"v_mfma_f32_16x16x32_bf16 v[184:187], a[16:19], a[68:71], v[184:187]\n"
"buffer_load_dwordx4 a[160:163], v100, s[40:43], s60 offen offset:2048 sc1\n"
"v_mfma_f32_16x16x32_bf16 v[184:187], a[20:23], a[84:87], v[184:187]\n"
"ds_read_b64_tr_b16 a[4:5], v108 offset:24576\n"
"v_mfma_f32_16x16x32_bf16 v[216:219], a[16:19], a[72:75], v[216:219]\n"
"ds_read_b64_tr_b16 a[6:7], v108 offset:28672\n"
"v_mfma_f32_16x16x32_bf16 v[216:219], a[20:23], a[88:91], v[216:219]\n"
"v_mfma_f32_16x16x32_bf16 v[248:251], a[16:19], a[76:79], v[248:251]\n"
"v_mfma_f32_16x16x32_bf16 v[248:251], a[20:23], a[92:95], v[248:251]\n"
"buffer_load_dwordx4 a[164:167], v101, s[40:43], s60 offen offset:2048 sc1\n"
"s_waitcnt lgkmcnt(4)\n"
"v_mfma_f32_16x16x32_bf16 v[156:159], a[24:27], a[64:67], v[156:159]\n"
"ds_read_b64_tr_b16 a[8:9], v109 offset:16384\n"
"v_mfma_f32_16x16x32_bf16 v[156:159], a[28:31], a[80:83], v[156:159]\n"
"ds_read_b64_tr_b16 a[10:11], v109 offset:20480\n"
"v_mfma_f32_16x16x32_bf16 v[188:191], a[24:27], a[68:71], v[188:191]\n"
"ds_read_b64_tr_b16 a[12:13], v109 offset:24576\n"
"v_mfma_f32_16x16x32_bf16 v[188:191], a[28:31], a[84:87], v[188:191]\n"
"ds_read_b64_tr_b16 a[14:15], v109 offset:28672\n"
"s_add_u32 m0, s62, 49152\n"
"v_mfma_f32_16x16x32_bf16 v[220:223], a[24:27], a[72:75], v[220:223]\n"
"buffer_load_dwordx4 v104, s[44:47], s61 offen lds\n"
"v_mfma_f32_16x16x32_bf16 v[220:223], a[28:31], a[88:91], v[220:223]\n"
"v_mfma_f32_16x16x32_bf16 v[252:255], a[24:27], a[76:79], v[252:255]\n"
"v_mfma_f32_16x16x32_bf16 v[252:255], a[28:31], a[92:95], v[252:255]\n"
"s_waitcnt lgkmcnt(4)\n"
"v_mfma_f32_16x16x32_bf16 v[128:131], a[0:3], a[96:99], v[128:131]\n"
"ds_read_b64_tr_b16 a[16:17], v108 offset:16640\n"
"v_mfma_f32_16x16x32_bf16 v[128:131], a[4:7], a[112:115], v[128:131]\n"
"buffer_load_dwordx4 a[168:171], v102, s[40:43], s60 offen offset:2048 sc1\n"
"v_mfma_f32_16x16x32_bf16 v[160:163], a[0:3], a[100:103], v[160:163]\n"
"ds_read_b64_tr_b16 a[18:19], v108 offset:20736\n"
"v_mfma_f32_16x16x32_bf16 v[160:163], a[4:7], a[116:119], v[160:163]\n"
"ds_read_b64_tr_b16 a[20:21], v108 offset:24832\n"
"v_mfma_f32_16x16x32_bf16 v[192:195], a[0:3], a[104:107], v[192:195]\n"
"ds_read_b64_tr_b16 a[22:23], v108 offset:28928\n"
"v_mfma_f32_16x16x32_bf16 v[192:195], a[4:7], a[120:123], v[192:195]\n"
"v_mfma_f32_16x16x32_bf16 v[224:227], a[0:3], a[108:111], v[224:227]\n"
"buffer_load_dwordx4 a[172:175], v103, s[40:43], s60 offen offset:2048 sc1\n"
"v_mfma_f32_16x16x32_bf16 v[224:227], a[4:7], a[124:127], v[224:227]\n"
"s_waitcnt lgkmcnt(4)\n"
"v_mfma_f32_16x16x32_bf16 v[132:135], a[8:11], a[96:99], v[132:135]\n"
"ds_read_b64_tr_b16 a[24:25], v109 offset:16640\n"
"v_mfma_f32_16x16x32_bf16 v[132:135], a[12:15], a[112:115], v[132:135]\n"
"ds_read_b64_tr_b16 a[26:27], v109 offset:20736\n"
"v_mfma_f32_16x16x32_bf16 v[164:167], a[8:11], a[100:103], v[164:167]\n"
"ds_read_b64_tr_b16 a[28:29], v109 offset:24832\n"
"v_mfma_f32_16x16x32_bf16 v[164:167], a[12:15], a[116:119], v[164:167]\n"
"ds_read_b64_tr_b16 a[30:31], v109 offset:28928\n"
"v_mfma_f32_16x16x32_bf16 v[196:199], a[8:11], a[104:107], v[196:199]\n"
"buffer_load_dwordx4 v105, s[44:47], s61 offen offset:1024 lds\n"
"v_mfma_f32_16x16x32_bf16 v[196:199], a[12:15], a[120:123], v[196:199]\n"
"v_mfma_f32_16x16x32_bf16 v[228:231], a[8:11], a[108:111], v[228:231]\n"
"v_mfma_f32_16x16x32_bf16 v[228:231], a[12:15], a[124:127], v[228:231]\n"
"s_waitcnt lgkmcnt(4)\n"
"v_mfma_f32_16x16x32_bf16 v[136:139], a[16:19], a[96:99], v[136:139]\n"
"ds_read_b64_tr_b16 a[0:1], v108 offset:16896\n"
"v_mfma_f32_16x16x32_bf16 v[136:139], a[20:23], a[112:115], v[136:139]\n"
"buffer_load_dwordx4 a[176:179], v100, s[40:43], s60 offen offset:3072 sc1\n"
"v_mfma_f32_16x16x32_bf16 v[168:171], a[16:19], a[100:103], v[168:171]\n"
"ds_read_b64_tr_b16 a[2:3], v108 offset:20992\n"
"v_mfma_f32_16x16x32_bf16 v[168:171], a[20:23], a[116:119], v[168:171]\n"
"ds_read_b64_tr_b16 a[4:5], v108 offset:25088\n"
"v_mfma_f32_16x16x32_bf16 v[200:203], a[16:19], a[104:107], v[200:203]\n"
"ds_read_b64_tr_b16 a[6:7], v108 offset:29184\n"
"v_mfma_f32_16x16x32_bf16 v[200:203], a[20:23], a[120:123], v[200:203]\n"
"v_mfma_f32_16x16x32_bf16 v[232:235], a[16:19], a[108:111], v[232:235]\n"
"buffer_load_dwordx4 a[180:183], v101, s[40:43], s60 offen offset:3072 sc1\n"
"v_mfma_f32_16x16x32_bf16 v[232:235], a[20:23], a[124:127], v[232:235]\n"
"s_waitcnt lgkmcnt(4)\n"
"v_mfma_f32_16x16x32_bf16 v[140:143], a[24:27], a[96:99], v[140:143]\n"
"ds_read_b64_tr_b16 a[8:9], v109 offset:16896\n"
"v_mfma_f32_16x16x32_bf16 v[140:143], a[28:31], a[112:115], v[140:143]\n"
"ds_read_b64_tr_b16 a[10:11], v109 offset:20992\n"
"v_mfma_f32_16x16x32_bf16 v[172:175], a[24:27], a[100:103], v[172:175]\n"
"ds_read_b64_tr_b16 a[12:13], v109 offset:25088\n"
"v_mfma_f32_16x16x32_bf16 v[172:175], a[28:31], a[116:119], v[172:175]\n"
"buffer_load_dwordx4 v106, s[44:47], s61 offen offset:2048 lds\n"
"v_mfma_f32_16x16x32_bf16 v[204:207], a[24:27], a[104:107], v[204:207]\n"
"ds_read_b64_tr_b16 a[14:15], v109 offset:29184\n"
"v_mfma_f32_16x16x32_bf16 v[204:207], a[28:31], a[120:123], v[204:207]\n"
"v_mfma_f32_16x16x32_bf16 v[236:239], a[24:27], a[108:111], v[236:239]\n"
"v_mfma_f32_16x16x32_bf16 v[236:239], a[28:31], a[124:127], v[236:239]\n"
"s_waitcnt lgkmcnt(4)\n"
"v_mfma_f32_16x16x32_bf16 v[144:147], a[0:3], a[96:99], v[144:147]\n"
"buffer_load_dwordx4 a[184:187], v102, s[40:43], s60 offen offset:3072 sc1\n"
"v_mfma_f32_16x16x32_bf16 v[144:147], a[4:7], a[112:115], v[144:147]\n"
"ds_read_b64_tr_b16 a[16:17], v108 offset:17152\n"
"v_mfma_f32_16x16x32_bf16 v[176:179], a[0:3], a[100:103], v[176:179]\n"
"ds_read_b64_tr_b16 a[18:19], v108 offset:21248\n"
"v_mfma_f32_16x16x32_bf16 v[176:179], a[4:7], a[116:119], v[176:179]\n"
"ds_read_b64_tr_b16 a[20:21], v108 offset:25344\n"
"v_mfma_f32_16x16x32_bf16 v[208:211], a[0:3], a[104:107], v[208:211]\n"
"ds_read_b64_tr_b16 a[22:23], v108 offset:29440\n"
"v_mfma_f32_16x16x32_bf16 v[208:211], a[4:7], a[120:123], v[208:211]\n"
"buffer_load_dwordx4 a[188:191], v103, s[40:43], s60 offen offset:3072 sc1\n"
"s_add_u32 s60, s60, 4096\n"
"v_mfma_f32_16x16x32_bf16 v[240:243], a[0:3], a[108:111], v[240:243]\n"
"v_mfma_f32_16x16x32_bf16 v[240:243], a[4:7], a[124:127], v[240:243]\n"
"s_waitcnt lgkmcnt(4)\n"
"v_mfma_f32_16x16x32_bf16 v[148:151], a[8:11], a[96:99], v[148:151]\n"
"ds_read_b64_tr_b16 a[24:25], v109 offset:17152\n"
"v_mfma_f32_16x16x32_bf16 v[148:151], a[12:15], a[112:115], v[148:151]\n"
"ds_read_b64_tr_b16 a[26:27], v109 offset:21248\n"
"v_mfma_f32_16x16x32_bf16 v[180:183], a[8:11], a[100:103], v[180:183]\n"
"ds_read_b64_tr_b16 a[28:29], v109 offset:25344\n"
"v_mfma_f32_16x16x32_bf16 v[180:183], a[12:15], a[116:119], v[180:183]\n"
"buffer_load_dwordx4 v107, s[44:47], s61 offen offset:3072 lds\n"
"s_add_u32 s61, s61, 458752\n"
"v_mfma_f32_16x16x32_bf16 v[212:215], a[8:11], a[104:107], v[212:215]\n"
"ds_read_b64_tr_b16 a[30:31], v109 offset:29440\n"
"v_mfma_f32_16x16x32_bf16 v[212:215], a[12:15], a[120:123], v[212:215]\n"
"v_mfma_f32_16x16x32_bf16 v[244:247], a[8:11], a[108:111], v[244:247]\n"
"v_mfma_f32_16x16x32_bf16 v[244:247], a[12:15], a[124:127], v[244:247]\n"
"s_waitcnt vmcnt(12) lgkmcnt(4)\n"
"s_barrier\n"
"v_mfma_f32_16x16x32_bf16 v[152:155], a[16:19], a[96:99], v[152:155]\n"
"ds_read_b64_tr_b16 a[0:1], v108 offset:32768\n"
"v_mfma_f32_16x16x32_bf16 v[152:155], a[20:23], a[112:115], v[152:155]\n"
"ds_read_b64_tr_b16 a[2:3], v108 offset:36864\n"
"v_mfma_f32_16x16x32_bf16 v[184:187], a[16:19], a[100:103], v[184:187]\n"
"buffer_load_dwordx4 a[64:67], v100, s[40:43], s60 offen sc1\n"
"v_mfma_f32_16x16x32_bf16 v[184:187], a[20:23], a[116:119], v[184:187]\n"
"ds_read_b64_tr_b16 a[4:5], v108 offset:40960\n"
"v_mfma_f32_16x16x32_bf16 v[216:219], a[16:19], a[104:107], v[216:219]\n"
"ds_read_b64_tr_b16 a[6:7], v108 offset:45056\n"
"v_mfma_f32_16x16x32_bf16 v[216:219], a[20:23], a[120:123], v[216:219]\n"
"v_mfma_f32_16x16x32_bf16 v[248:251], a[16:19], a[108:111], v[248:251]\n"
"v_mfma_f32_16x16x32_bf16 v[248:251], a[20:23], a[124:127], v[248:251]\n"
"buffer_load_dwordx4 a[68:71], v101, s[40:43], s60 offen sc1\n"
"s_waitcnt lgkmcnt(4)\n"
"v_mfma_f32_16x16x32_bf16 v[156:159], a[24:27], a[96:99], v[156:159]\n"
"ds_read_b64_tr_b16 a[8:9], v109 offset:32768\n"
"v_mfma_f32_16x16x32_bf16 v[156:159], a[28:31], a[112:115], v[156:159]\n"
"ds_read_b64_tr_b16 a[10:11], v109 offset:36864\n"
"v_mfma_f32_16x16x32_bf16 v[188:191], a[24:27], a[100:103], v[188:191]\n"
"ds_read_b64_tr_b16 a[12:13], v109 offset:40960\n"
"v_mfma_f32_16x16x32_bf16 v[188:191], a[28:31], a[116:119], v[188:191]\n"
"ds_read_b64_tr_b16 a[14:15], v109 offset:45056\n"
"s_add_u32 m0, s62, 0\n"
"v_mfma_f32_16x16x32_bf16 v[220:223], a[24:27], a[104:107], v[220:223]\n"
"buffer_load_dwordx4 v104, s[44:47], s61 offen lds\n"
"v_mfma_f32_16x16x32_bf16 v[220:223], a[28:31], a[120:123], v[220:223]\n"
"v_mfma_f32_16x16x32_bf16 v[252:255], a[24:27], a[108:111], v[252:255]\n"
"v_mfma_f32_16x16x32_bf16 v[252:255], a[28:31], a[124:127], v[252:255]\n"
"s_waitcnt lgkmcnt(4)\n"
"v_mfma_f32_16x16x32_bf16 v[128:131], a[0:3], a[128:131], v[128:131]\n"
"ds_read_b64_tr_b16 a[16:17], v108 offset:33024\n"
"v_mfma_f32_16x16x32_bf16 v[128:131], a[4:7], a[144:147], v[128:131]\n"
"buffer_load_dwordx4 a[72:75], v102, s[40:43], s60 offen sc1\n"
"v_mfma_f32_16x16x32_bf16 v[160:163], a[0:3], a[132:135], v[160:163]\n"
"ds_read_b64_tr_b16 a[18:19], v108 offset:37120\n"
"v_mfma_f32_16x16x32_bf16 v[160:163], a[4:7], a[148:151], v[160:163]\n"
"ds_read_b64_tr_b16 a[20:21], v108 offset:41216\n"
"v_mfma_f32_16x16x32_bf16 v[192:195], a[0:3], a[136:139], v[192:195]\n"
"ds_read_b64_tr_b16 a[22:23], v108 offset:45312\n"
"v_mfma_f32_16x16x32_bf16 v[192:195], a[4:7], a[152:155], v[192:195]\n"
"v_mfma_f32_16x16x32_bf16 v[224:227], a[0:3], a[140:143], v[224:227]\n"
"buffer_load_dwordx4 a[76:79], v103, s[40:43], s60 offen sc1\n"
"v_mfma_f32_16x16x32_bf16 v[224:227], a[4:7], a[156:159], v[224:227]\n"
"s_waitcnt lgkmcnt(4)\n"
"v_mfma_f32_16x16x32_bf16 v[132:135], a[8:11], a[128:131], v[132:135]\n"
"ds_read_b64_tr_b16 a[24:25], v109 offset:33024\n"
"v_mfma_f32_16x16x32_bf16 v[132:135], a[12:15], a[144:147], v[132:135]\n"
"ds_read_b64_tr_b16 a[26:27], v109 offset:37120\n"
"v_mfma_f32_16x16x32_bf16 v[164:167], a[8:11], a[132:135], v[164:167]\n"
"ds_read_b64_tr_b16 a[28:29], v109 offset:41216\n"
"v_mfma_f32_16x16x32_bf16 v[164:167], a[12:15], a[148:151], v[164:167]\n"
"ds_read_b64_tr_b16 a[30:31], v109 offset:45312\n"
"v_mfma_f32_16x16x32_bf16 v[196:199], a[8:11], a[136:139], v[196:199]\n"
"buffer_load_dwordx4 v105, s[44:47], s61 offen offset:1024 lds\n"
"v_mfma_f32_16x16x32_bf16 v[196:199], a[12:15], a[152:155], v[196:199]\n"
"v_mfma_f32_16x16x32_bf16 v[228:231], a[8:11], a[140:143], v[228:231]\n"
"v_mfma_f32_16x16x32_bf16 v[228:231], a[12:15], a[156:159], v[228:231]\n"
"s_waitcnt lgkmcnt(4)\n"
"v_mfma_f32_16x16x32_bf16 v[136:139], a[16:19], a[128:131], v[136:139]\n"
"ds_read_b64_tr_b16 a[0:1], v108 offset:33280\n"
"v_mfma_f32_16x16x32_bf16 v[136:139], a[20:23], a[144:147], v[136:139]\n"
"buffer_load_dwordx4 a[80:83], v100, s[40:43], s60 offen offset:1024 sc1\n"
"v_mfma_f32_16x16x32_bf16 v[168:171], a[16:19], a[132:135], v[168:171]\n"
"ds_read_b64_tr_b16 a[2:3], v108 offset:37376\n"
"v_mfma_f32_16x16x32_bf16 v[168:171], a[20:23], a[148:151], v[168:171]\n"
"ds_read_b64_tr_b16 a[4:5], v108 offset:41472\n"
"v_mfma_f32_16x16x32_bf16 v[200:203], a[16:19], a[136:139], v[200:203]\n"
"ds_read_b64_tr_b16 a[6:7], v108 offset:45568\n"
"v_mfma_f32_16x16x32_bf16 v[200:203], a[20:23], a[152:155], v[200:203]\n"
"v_mfma_f32_16x16x32_bf16 v[232:235], a[16:19], a[140:143], v[232:235]\n"
"buffer_load_dwordx4 a[84:87], v101, s[40:43], s60 offen offset:1024 sc1\n"
"v_mfma_f32_16x16x32_bf16 v[232:235], a[20:23], a[156:159], v[232:235]\n"
"s_waitcnt lgkmcnt(4)\n"
"v_mfma_f32_16x16x32_bf16 v[140:143], a[24:27], a[128:131], v[140:143]\n"
"ds_read_b64_tr_b16 a[8:9], v109 offset:33280\n"
"v_mfma_f32_16x16x32_bf16 v[140:143], a[28:31], a[144:147], v[140:143]\n"
"ds_read_b64_tr_b16 a[10:11], v109 offset:37376\n"
"v_mfma_f32_16x16x32_bf16 v[172:175], a[24:27], a[132:135], v[172:175]\n"
"ds_read_b64_tr_b16 a[12:13], v109 offset:41472\n"
"v_mfma_f32_16x16x32_bf16 v[172:175], a[28:31], a[148:151], v[172:175]\n"
"buffer_load_dwordx4 v106, s[44:47], s61 offen offset:2048 lds\n"
"v_mfma_f32_16x16x32_bf16 v[204:207], a[24:27], a[136:139], v[204:207]\n"
"ds_read_b64_tr_b16 a[14:15], v109 offset:45568\n"
"v_mfma_f32_16x16x32_bf16 v[204:207], a[28:31], a[152:155], v[204:207]\n"
"v_mfma_f32_16x16x32_bf16 v[236:239], a[24:27], a[140:143], v[236:239]\n"
"v_mfma_f32_16x16x32_bf16 v[236:239], a[28:31], a[156:159], v[236:239]\n"
"s_waitcnt lgkmcnt(4)\n"
"v_mfma_f32_16x16x32_bf16 v[144:147], a[0:3], a[128:131], v[144:147]\n"
"buffer_load_dwordx4 a[88:91], v102, s[40:43], s60 offen offset:1024 sc1\n"
"v_mfma_f32_16x16x32_bf16 v[144:147], a[4:7], a[144:147], v[144:147]\n"
"ds_read_b64_tr_b16 a[16:17], v108 offset:33536\n"
"v_mfma_f32_16x16x32_bf16 v[176:179], a[0:3], a[132:135], v[176:179]\n"
"ds_read_b64_tr_b16 a[18:19], v108 offset:37632\n"
"v_mfma_f32_16x16x32_bf16 v[176:179], a[4:7], a[148:151], v[176:179]\n"
"ds_read_b64_tr_b16 a[20:21], v108 offset:41728\n"
"v_mfma_f32_16x16x32_bf16 v[208:211], a[0:3], a[136:139], v[208:211]\n"
"ds_read_b64_tr_b16 a[22:23], v108 offset:45824\n"
"v_mfma_f32_16x16x32_bf16 v[208:211], a[4:7], a[152:155], v[208:211]\n"
"buffer_load_dwordx4 a[92:95], v103, s[40:43], s60 offen offset:1024 sc1\n"
"v_mfma_f32_16x16x32_bf16 v[240:243], a[0:3], a[140:143], v[240:243]\n"
"v_mfma_f32_16x16x32_bf16 v[240:243], a[4:7], a[156:159], v[240:243]\n"
"s_waitcnt lgkmcnt(4)\n"
"v_mfma_f32_16x16x32_bf16 v[148:151], a[8:11], a[128:131], v[148:151]\n"
"ds_read_b64_tr_b16 a[24:25], v109 offset:33536\n"
"v_mfma_f32_16x16x32_bf16 v[148:151], a[12:15], a[144:147], v[148:151]\n"
"ds_read_b64_tr_b16 a[26:27], v109 offset:37632\n"
"v_mfma_f32_16x16x32_bf16 v[180:183], a[8:11], a[132:135], v[180:183]\n"
"ds_read_b64_tr_b16 a[28:29], v109 offset:41728\n"
"v_mfma_f32_16x16x32_bf16 v[180:183], a[12:15], a[148:151], v[180:183]\n"
"buffer_load_dwordx4 v107, s[44:47], s61 offen offset:3072 lds\n"
"s_add_u32 s61, s61, 458752\n"
"v_mfma_f32_16x16x32_bf16 v[212:215], a[8:11], a[136:139], v[212:215]\n"
"ds_read_b64_tr_b16 a[30:31], v109 offset:45824\n"
"v_mfma_f32_16x16x32_bf16 v[212:215], a[12:15], a[152:155], v[212:215]\n"
"v_mfma_f32_16x16x32_bf16 v[244:247], a[8:11], a[140:143], v[244:247]\n"
"v_mfma_f32_16x16x32_bf16 v[244:247], a[12:15], a[156:159], v[244:247]\n"
"s_waitcnt vmcnt(12) lgkmcnt(4)\n"
"s_barrier\n"
"v_mfma_f32_16x16x32_bf16 v[152:155], a[16:19], a[128:131], v[152:155]\n"
"ds_read_b64_tr_b16 a[0:1], v108 offset:49152\n"
"v_mfma_f32_16x16x32_bf16 v[152:155], a[20:23], a[144:147], v[152:155]\n"
"ds_read_b64_tr_b16 a[2:3], v108 offset:53248\n"
"v_mfma_f32_16x16x32_bf16 v[184:187], a[16:19], a[132:135], v[184:187]\n"
"buffer_load_dwordx4 a[96:99], v100, s[40:43], s60 offen offset:2048 sc1\n"
"v_mfma_f32_16x16x32_bf16 v[184:187], a[20:23], a[148:151], v[184:187]\n"
"ds_read_b64_tr_b16 a[4:5], v108 offset:57344\n"
"v_mfma_f32_16x16x32_bf16 v[216:219], a[16:19], a[136:139], v[216:219]\n"
"ds_read_b64_tr_b16 a[6:7], v108 offset:61440\n"
"v_mfma_f32_16x16x32_bf16 v[216:219], a[20:23], a[152:155], v[216:219]\n"
"v_mfma_f32_16x16x32_bf16 v[248:251], a[16:19], a[140:143], v[248:251]\n"
"v_mfma_f32_16x16x32_bf16 v[248:251], a[20:23], a[156:159], v[248:251]\n"
"buffer_load_dwordx4 a[100:103], v101, s[40:43], s60 offen offset:2048 sc1\n"
"s_waitcnt lgkmcnt(4)\n"
"v_mfma_f32_16x16x32_bf16 v[156:159], a[24:27], a[128:131], v[156:159]\n"
"ds_read_b64_tr_b16 a[8:9], v109 offset:49152\n"
"v_mfma_f32_16x16x32_bf16 v[156:159], a[28:31], a[144:147], v[156:159]\n"
"ds_read_b64_tr_b16 a[10:11], v109 offset:53248\n"
"v_mfma_f32_16x16x32_bf16 v[188:191], a[24:27], a[132:135], v[188:191]\n"
"ds_read_b64_tr_b16 a[12:13], v109 offset:57344\n"
"v_mfma_f32_16x16x32_bf16 v[188:191], a[28:31], a[148:151], v[188:191]\n"
"ds_read_b64_tr_b16 a[14:15], v109 offset:61440\n"
"s_add_u32 m0, s62, 16384\n"
"v_mfma_f32_16x16x32_bf16 v[220:223], a[24:27], a[136:139], v[220:223]\n"
"buffer_load_dwordx4 v104, s[44:47], s61 offen lds\n"
"v_mfma_f32_16x16x32_bf16 v[220:223], a[28:31], a[152:155], v[220:223]\n"
"v_mfma_f32_16x16x32_bf16 v[252:255], a[24:27], a[140:143], v[252:255]\n"
"v_mfma_f32_16x16x32_bf16 v[252:255], a[28:31], a[156:159], v[252:255]\n"
"s_waitcnt lgkmcnt(4)\n"
"v_mfma_f32_16x16x32_bf16 v[128:131], a[0:3], a[160:163], v[128:131]\n"
"ds_read_b64_tr_b16 a[16:17], v108 offset:49408\n"
"v_mfma_f32_16x16x32_bf16 v[128:131], a[4:7], a[176:179], v[128:131]\n"
"buffer_load_dwordx4 a[104:107], v102, s[40:43], s60 offen offset:2048 sc1\n"
"v_mfma_f32_16x16x32_bf16 v[160:163], a[0:3], a[164:167], v[160:163]\n"
"ds_read_b64_tr_b16 a[18:19], v108 offset:53504\n"
"v_mfma_f32_16x16x32_bf16 v[160:163], a[4:7], a[180:183], v[160:163]\n"
"ds_read_b64_tr_b16 a[20:21], v108 offset:57600\n"
"v_mfma_f32_16x16x32_bf16 v[192:195], a[0:3], a[168:171], v[192:195]\n"
"ds_read_b64_tr_b16 a[22:23], v108 offset:61696\n"
"v_mfma_f32_16x16x32_bf16 v[192:195], a[4:7], a[184:187], v[192:195]\n"
"v_mfma_f32_16x16x32_bf16 v[224:227], a[0:3], a[172:175], v[224:227]\n"
"buffer_load_dwordx4 a[108:111], v103, s[40:43], s60 offen offset:2048 sc1\n"
"v_mfma_f32_16x16x32_bf16 v[224:227], a[4:7], a[188:191], v[224:227]\n"
"s_waitcnt lgkmcnt(4)\n"
"v_mfma_f32_16x16x32_bf16 v[132:135], a[8:11], a[160:163], v[132:135]\n"
"ds_read_b64_tr_b16 a[24:25], v109 offset:49408\n"
"v_mfma_f32_16x16x32_bf16 v[132:135], a[12:15], a[176:179], v[132:135]\n"
"ds_read_b64_tr_b16 a[26:27], v109 offset:53504\n"
"v_mfma_f32_16x16x32_bf16 v[164:167], a[8:11], a[164:167], v[164:167]\n"
"ds_read_b64_tr_b16 a[28:29], v109 offset:57600\n"
"v_mfma_f32_16x16x32_bf16 v[164:167], a[12:15], a[180:183], v[164:167]\n"
"ds_read_b64_tr_b16 a[30:31], v109 offset:61696\n"
"v_mfma_f32_16x16x32_bf16 v[196:199], a[8:11], a[168:171], v[196:199]\n"
"buffer_load_dwordx4 v105, s[44:47], s61 offen offset:1024 lds\n"
"v_mfma_f32_16x16x32_bf16 v[196:199], a[12:15], a[184:187], v[196:199]\n"
"v_mfma_f32_16x16x32_bf16 v[228:231], a[8:11], a[172:175], v[228:231]\n"
"v_mfma_f32_16x16x32_bf16 v[228:231], a[12:15], a[188:191], v[228:231]\n"
"s_waitcnt lgkmcnt(4)\n"
"v_mfma_f32_16x16x32_bf16 v[136:139], a[16:19], a[160:163], v[136:139]\n"
"ds_read_b64_tr_b16 a[0:1], v108 offset:49664\n"
"v_mfma_f32_16x16x32_bf16 v[136:139], a[20:23], a[176:179], v[136:139]\n"
"buffer_load_dwordx4 a[112:115], v100, s[40:43], s60 offen offset:3072 sc1\n"
"v_mfma_f32_16x16x32_bf16 v[168:171], a[16:19], a[164:167], v[168:171]\n"
"ds_read_b64_tr_b16 a[2:3], v108 offset:53760\n"
"v_mfma_f32_16x16x32_bf16 v[168:171], a[20:23], a[180:183], v[168:171]\n"
"ds_read_b64_tr_b16 a[4:5], v108 offset:57856\n"
"v_mfma_f32_16x16x32_bf16 v[200:203], a[16:19], a[168:171], v[200:203]\n"
"ds_read_b64_tr_b16 a[6:7], v108 offset:61952\n"
"v_mfma_f32_16x16x32_bf16 v[200:203], a[20:23], a[184:187], v[200:203]\n"
"v_mfma_f32_16x16x32_bf16 v[232:235], a[16:19], a[172:175], v[232:235]\n"
"buffer_load_dwordx4 a[116:119], v101, s[40:43], s60 offen offset:3072 sc1\n"
"v_mfma_f32_16x16x32_bf16 v[232:235], a[20:23], a[188:191], v[232:235]\n"
"s_waitcnt lgkmcnt(4)\n"
"v_mfma_f32_16x16x32_bf16 v[140:143], a[24:27], a[160:163], v[140:143]\n"
"ds_read_b64_tr_b16 a[8:9], v109 offset:49664\n"
"v_mfma_f32_16x16x32_bf16 v[140:143], a[28:31], a[176:179], v[140:143]\n"
"ds_read_b64_tr_b16 a[10:11], v109 offset:53760\n"
"v_mfma_f32_16x16x32_bf16 v[172:175], a[24:27], a[164:167], v[172:175]\n"
"ds_read_b64_tr_b16 a[12:13], v109 offset:57856\n"
"v_mfma_f32_16x16x32_bf16 v[172:175], a[28:31], a[180:183], v[172:175]\n"
"buffer_load_dwordx4 v106, s[44:47], s61 offen offset:2048 lds\n"
"v_mfma_f32_16x16x32_bf16 v[204:207], a[24:27], a[168:171], v[204:207]\n"
"ds_read_b64_tr_b16 a[14:15], v109 offset:61952\n"
"v_mfma_f32_16x16x32_bf16 v[204:207], a[28:31], a[184:187], v[204:207]\n"
"v_mfma_f32_16x16x32_bf16 v[236:239], a[24:27], a[172:175], v[236:239]\n"
"v_mfma_f32_16x16x32_bf16 v[236:239], a[28:31], a[188:191], v[236:239]\n"
"s_waitcnt lgkmcnt(4)\n"
"v_mfma_f32_16x16x32_bf16 v[144:147], a[0:3], a[160:163], v[144:147]\n"
"buffer_load_dwordx4 a[120:123], v102, s[40:43], s60 offen offset:3072 sc1\n"
"v_mfma_f32_16x16x32_bf16 v[144:147], a[4:7], a[176:179], v[144:147]\n"
"ds_read_b64_tr_b16 a[16:17], v108 offset:49920\n"
"v_mfma_f32_16x16x32_bf16 v[176:179], a[0:3], a[164:167], v[176:179]\n"
"ds_read_b64_tr_b16 a[18:19], v108 offset:54016\n"
"v_mfma_f32_16x16x32_bf16 v[176:179], a[4:7], a[180:183], v[176:179]\n"
"ds_read_b64_tr_b16 a[20:21], v108 offset:58112\n"
"v_mfma_f32_16x16x32_bf16 v[208:211], a[0:3], a[168:171], v[208:211]\n"
"ds_read_b64_tr_b16 a[22:23], v108 offset:62208\n"
"v_mfma_f32_16x16x32_bf16 v[208:211], a[4:7], a[184:187], v[208:211]\n"
"buffer_load_dwordx4 a[124:127], v103, s[40:43], s60 offen offset:3072 sc1\n"
"s_add_u32 s60, s60, 4096\n"
"v_mfma_f32_16x16x32_bf16 v[240:243], a[0:3], a[172:175], v[240:243]\n"
"v_mfma_f32_16x16x32_bf16 v[240:243], a[4:7], a[188:191], v[240:243]\n"
"s_waitcnt lgkmcnt(4)\n"
"v_mfma_f32_16x16x32_bf16 v[148:151], a[8:11], a[160:163], v[148:151]\n"
"ds_read_b64_tr_b16 a[24:25], v109 offset:49920\n"
"v_mfma_f32_16x16x32_bf16 v[148:151], a[12:15], a[176:179], v[148:151]\n"
"ds_read_b64_tr_b16 a[26:27], v109 offset:54016\n"
"v_mfma_f32_16x16x32_bf16 v[180:183], a[8:11], a[164:167], v[180:183]\n"
"ds_read_b64_tr_b16 a[28:29], v109 offset:58112\n"
"v_mfma_f32_16x16x32_bf16 v[180:183], a[12:15], a[180:183], v[180:183]\n"
"buffer_load_dwordx4 v107, s[44:47], s61 offen offset:3072 lds\n"
"s_add_u32 s61, s61, 458752\n"
"v_mfma_f32_16x16x32_bf16 v[212:215], a[8:11], a[168:171], v[212:215]\n"
"ds_read_b64_tr_b16 a[30:31], v109 offset:62208\n"
"v_mfma_f32_16x16x32_bf16 v[212:215], a[12:15], a[184:187], v[212:215]\n"
"v_mfma_f32_16x16x32_bf16 v[244:247], a[8:11], a[172:175], v[244:247]\n"
"s_sub_u32 s63, s63, 1\n"
"v_mfma_f32_16x16x32_bf16 v[244:247], a[12:15], a[188:191], v[244:247]\n"
"s_waitcnt vmcnt(12) lgkmcnt(4)\n"
"s_barrier\n"
"s_cbranch_scc0 LF_%=\n"
"v_mfma_f32_16x16x32_bf16 v[152:155], a[16:19], a[160:163], v[152:155]\n"
"ds_read_b64_tr_b16 a[0:1], v108 offset:0\n"
"v_mfma_f32_16x16x32_bf16 v[152:155], a[20:23], a[176:179], v[152:155]\n"
"ds_read_b64_tr_b16 a[2:3], v108 offset:4096\n"
"v_mfma_f32_16x16x32_bf16 v[184:187], a[16:19], a[164:167], v[184:187]\n"
"buffer_load_dwordx4 a[128:131], v100, s[40:43], s60 offen sc1\n"
"v_mfma_f32_16x16x32_bf16 v[184:187], a[20:23], a[180:183], v[184:187]\n"
"ds_read_b64_tr_b16 a[4:5], v108 offset:8192\n"
"v_mfma_f32_16x16x32_bf16 v[216:219], a[16:19], a[168:171], v[216:219]\n"
"ds_read_b64_tr_b16 a[6:7], v108 offset:12288\n"
"v_mfma_f32_16x16x32_bf16 v[216:219], a[20:23], a[184:187], v[216:219]\n"
"v_mfma_f32_16x16x32_bf16 v[248:251], a[16:19], a[172:175], v[248:251]\n"
"v_mfma_f32_16x16x32_bf16 v[248:251], a[20:23], a[188:191], v[248:251]\n"
"buffer_load_dwordx4 a[132:135], v101, s[40:43], s60 offen sc1\n"
"s_waitcnt lgkmcnt(4)\n"
"v_mfma_f32_16x16x32_bf16 v[156:159], a[24:27], a[160:163], v[156:159]\n"
"ds_read_b64_tr_b16 a[8:9], v109 offset:0\n"
"v_mfma_f32_16x16x32_bf16 v[156:159], a[28:31], a[176:179], v[156:159]\n"
"ds_read_b64_tr_b16 a[10:11], v109 offset:4096\n"
"v_mfma_f32_16x16x32_bf16 v[188:191], a[24:27], a[164:167], v[188:191]\n"
"ds_read_b64_tr_b16 a[12:13], v109 offset:8192\n"
"v_mfma_f32_16x16x32_bf16 v[188:191], a[28:31], a[180:183], v[188:191]\n"
"ds_read_b64_tr_b16 a[14:15], v109 offset:12288\n"
"s_add_u32 m0, s62, 32768\n"
"v_mfma_f32_16x16x32_bf16 v[220:223], a[24:27], a[168:171], v[220:223]\n"
"buffer_load_dwordx4 v104, s[44:47], s61 offen lds\n"
"v_mfma_f32_16x16x32_bf16 v[220:223], a[28:31], a[184:187], v[220:223]\n"
"v_mfma_f32_16x16x32_bf16 v[252:255], a[24:27], a[172:175], v[252:255]\n"
"v_mfma_f32_16x16x32_bf16 v[252:255], a[28:31], a[188:191], v[252:255]\n"
"s_waitcnt lgkmcnt(4)\n"
"v_mfma_f32_16x16x32_bf16 v[128:131], a[0:3], a[64:67], v[128:131]\n"
"ds_read_b64_tr_b16 a[16:17], v108 offset:256\n"
"v_mfma_f32_16x16x32_bf16 v[128:131], a[4:7], a[80:83], v[128:131]\n"
"buffer_load_dwordx4 a[136:139], v102, s[40:43], s60 offen sc1\n"
"v_mfma_f32_16x16x32_bf16 v[160:163], a[0:3], a[68:71], v[160:163]\n"
"ds_read_b64_tr_b16 a[18:19], v108 offset:4352\n"
"v_mfma_f32_16x16x32_bf16 v[160:163], a[4:7], a[84:87], v[160:163]\n"
"ds_read_b64_tr_b16 a[20:21], v108 offset:8448\n"
"v_mfma_f32_16x16x32_bf16 v[192:195], a[0:3], a[72:75], v[192:195]\n"
"ds_read_b64_tr_b16 a[22:23], v108 offset:12544\n"
"v_mfma_f32_16x16x32_bf16 v[192:195], a[4:7], a[88:91], v[192:195]\n"
"v_mfma_f32_16x16x32_bf16 v[224:227], a[0:3], a[76:79], v[224:227]\n"
"buffer_load_dwordx4 a[140:143], v103, s[40:43], s60 offen sc1\n"
"v_mfma_f32_16x16x32_bf16 v[224:227], a[4:7], a[92:95], v[224:227]\n"
"s_waitcnt lgkmcnt(4)\n"
"v_mfma_f32_16x16x32_bf16 v[132:135], a[8:11], a[64:67], v[132:135]\n"
"ds_read_b64_tr_b16 a[24:25], v109 offset:256\n"
"v_mfma_f32_16x16x32_bf16 v[132:135], a[12:15], a[80:83], v[132:135]\n"
"ds_read_b64_tr_b16 a[26:27], v109 offset:4352\n"
"v_mfma_f32_16x16x32_bf16 v[164:167], a[8:11], a[68:71], v[164:167]\n"
"ds_read_b64_tr_b16 a[28:29], v109 offset:8448\n"
"v_mfma_f32_16x16x32_bf16 v[164:167], a[12:15], a[84:87], v[164:167]\n"
"ds_read_b64_tr_b16 a[30:31], v109 offset:12544\n"
"v_mfma_f32_16x16x32_bf16 v[196:199], a[8:11], a[72:75], v[196:199]\n"
"buffer_load_dwordx4 v105, s[44:47], s61 offen offset:1024 lds\n"
"v_mfma_f32_16x16x32_bf16 v[196:199], a[12:15], a[88:91], v[196:199]\n"
"v_mfma_f32_16x16x32_bf16 v[228:231], a[8:11], a[76:79], v[228:231]\n"
"v_mfma_f32_16x16x32_bf16 v[228:231], a[12:15], a[92:95], v[228:231]\n"
"s_waitcnt lgkmcnt(4)\n"
"v_mfma_f32_16x16x32_bf16 v[136:139], a[16:19], a[64:67], v[136:139]\n"
"ds_read_b64_tr_b16 a[0:1], v108 offset:512\n"
"v_mfma_f32_16x16x32_bf16 v[136:139], a[20:23], a[80:83], v[136:139]\n"
"buffer_load_dwordx4 a[144:147], v100, s[40:43], s60 offen offset:1024 sc1\n"
"v_mfma_f32_16x16x32_bf16 v[168:171], a[16:19], a[68:71], v[168:171]\n"
"ds_read_b64_tr_b16 a[2:3], v108 offset:4608\n"
"v_mfma_f32_16x16x32_bf16 v[168:171], a[20:23], a[84:87], v[168:171]\n"
"ds_read_b64_tr_b16 a[4:5], v108 offset:8704\n"
"v_mfma_f32_16x16x32_bf16 v[200:203], a[16:19], a[72:75], v[200:203]\n"
"ds_read_b64_tr_b16 a[6:7], v108 offset:12800\n"
"v_mfma_f32_16x16x32_bf16 v[200:203], a[20:23], a[88:91], v[200:203]\n"
"v_mfma_f32_16x16x32_bf16 v[232:235], a[16:19], a[76:79], v[232:235]\n"
"buffer_load_dwordx4 a[148:151], v101, s[40:43], s60 offen offset:1024 sc1\n"
"v_mfma_f32_16x16x32_bf16 v[232:235], a[20:23], a[92:95], v[232:235]\n"
"s_waitcnt lgkmcnt(4)\n"
"v_mfma_f32_16x16x32_bf16 v[140:143], a[24:27], a[64:67], v[140:143]\n"
"ds_read_b64_tr_b16 a[8:9], v109 offset:512\n"
"v_mfma_f32_16x16x32_bf16 v[140:143], a[28:31], a[80:83], v[140:143]\n"
"ds_read_b64_tr_b16 a[10:11], v109 offset:4608\n"
"v_mfma_f32_16x16x32_bf16 v[172:175], a[24:27], a[68:71], v[172:175]\n"
"ds_read_b64_tr_b16 a[12:13], v109 offset:8704\n"
"v_mfma_f32_16x16x32_bf16 v[172:175], a[28:31], a[84:87], v[172:175]\n"
"buffer_load_dwordx4 v106, s[44:47], s61 offen offset:2048 lds\n"
"v_mfma_f32_16x16x32_bf16 v[204:207], a[24:27], a[72:75], v[204:207]\n"
"ds_read_b64_tr_b16 a[14:15], v109 offset:12800\n"
"v_mfma_f32_16x16x32_bf16 v[204:207], a[28:31], a[88:91], v[204:207]\n"
"v_mfma_f32_16x16x32_bf16 v[236:239], a[24:27], a[76:79], v[236:239]\n"
"v_mfma_f32_16x16x32_bf16 v[236:239], a[28:31], a[92:95], v[236:239]\n"
"s_waitcnt lgkmcnt(4)\n"
"v_mfma_f32_16x16x32_bf16 v[144:147], a[0:3], a[64:67], v[144:147]\n"
"buffer_load_dwordx4 a[152:155], v102, s[40:43], s60 offen offset:1024 sc1\n"
"v_mfma_f32_16x16x32_bf16 v[144:147], a[4:7], a[80:83], v[144:147]\n"
"ds_read_b64_tr_b16 a[16:17], v108 offset:768\n"
"v_mfma_f32_16x16x32_bf16 v[176:179], a[0:3], a[68:71], v[176:179]\n"
"ds_read_b64_tr_b16 a[18:19], v108 offset:4864\n"
"v_mfma_f32_16x16x32_bf16 v[176:179], a[4:7], a[84:87], v[176:179]\n"
"ds_read_b64_tr_b16 a[20:21], v108 offset:8960\n"
"v_mfma_f32_16x16x32_bf16 v[208:211], a[0:3], a[72:75], v[208:211]\n"
"ds_read_b64_tr_b16 a[22:23], v108 offset:13056\n"
"v_mfma_f32_16x16x32_bf16 v[208:211], a[4:7], a[88:91], v[208:211]\n"
"buffer_load_dwordx4 a[156:159], v103, s[40:43], s60 offen offset:1024 sc1\n"
"v_mfma_f32_16x16x32_bf16 v[240:243], a[0:3], a[76:79], v[240:243]\n"
"v_mfma_f32_16x16x32_bf16 v[240:243], a[4:7], a[92:95], v[240:243]\n"
"s_waitcnt lgkmcnt(4)\n"
"v_mfma_f32_16x16x32_bf16 v[148:151], a[8:11], a[64:67], v[148:151]\n"
"ds_read_b64_tr_b16 a[24:25], v109 offset:768\n"
"v_mfma_f32_16x16x32_bf16 v[148:151], a[12:15], a[80:83], v[148:151]\n"
"ds_read_b64_tr_b16 a[26:27], v109 offset:4864\n"
"v_mfma_f32_16x16x32_bf16 v[180:183], a[8:11], a[68:71], v[180:183]\n"
"ds_read_b64_tr_b16 a[28:29], v109 offset:8960\n"
"v_mfma_f32_16x16x32_bf16 v[180:183], a[12:15], a[84:87], v[180:183]\n"
"buffer_load_dwordx4 v107, s[44:47], s61 offen offset:3072 lds\n"
"s_add_u32 s61, s61, 458752\n"
"v_mfma_f32_16x16x32_bf16 v[212:215], a[8:11], a[72:75], v[212:215]\n"
"ds_read_b64_tr_b16 a[30:31], v109 offset:13056\n"
"v_mfma_f32_16x16x32_bf16 v[212:215], a[12:15], a[88:91], v[212:215]\n"
"v_mfma_f32_16x16x32_bf16 v[244:247], a[8:11], a[76:79], v[244:247]\n"
"v_mfma_f32_16x16x32_bf16 v[244:247], a[12:15], a[92:95], v[244:247]\n"
"s_waitcnt vmcnt(12) lgkmcnt(4)\n"
"s_barrier\n"
"v_mfma_f32_16x16x32_bf16 v[152:155], a[16:19], a[64:67], v[152:155]\n"
"ds_read_b64_tr_b16 a[0:1], v108 offset:16384\n"
"v_lshrrev_b32 v115, 6, v112\n"
"v_mfma_f32_16x16x32_bf16 v[152:155], a[20:23], a[80:83], v[152:155]\n"
"ds_read_b64_tr_b16 a[2:3], v108 offset:20480\n"
"v_and_b32 v115, 3, v115\n"
"v_mfma_f32_16x16x32_bf16 v[184:187], a[16:19], a[68:71], v[184:187]\n"
"buffer_load_dwordx4 a[160:163], v100, s[40:43], s60 offen offset:2048 sc1\n"
"v_mul_u32_u24 v115, 5120, v115\n"
"v_mfma_f32_16x16x32_bf16 v[184:187], a[20:23], a[84:87], v[184:187]\n"
"ds_read_b64_tr_b16 a[4:5], v108 offset:24576\n"
"v_sub_u32 v113, v104, v115\n"
"v_mfma_f32_16x16x32_bf16 v[216:219], a[16:19], a[72:75], v[216:219]\n"
"ds_read_b64_tr_b16 a[6:7], v108 offset:28672\n"
"v_xor_b32 v114, 32, v113\n"
"v_mfma_f32_16x16x32_bf16 v[216:219], a[20:23], a[88:91], v[216:219]\n"
"s_lshl_b32 s69, s70, 16\n"
"s_lshl_b32 s68, s70, 13\n"
"v_mfma_f32_16x16x32_bf16 v[248:251], a[16:19], a[76:79], v[248:251]\n"
"v_mfma_f32_16x16x32_bf16 v[248:251], a[20:23], a[92:95], v[248:251]\n"
"buffer_load_dwordx4 a[164:167], v101, s[40:43], s60 offen offset:2048 sc1\n"
"s_waitcnt lgkmcnt(4)\n"
"v_mfma_f32_16x16x32_bf16 v[156:159], a[24:27], a[64:67], v[156:159]\n"
"ds_read_b64_tr_b16 a[8:9], v109 offset:16384\n"
"v_mfma_f32_16x16x32_bf16 v[156:159], a[28:31], a[80:83], v[156:159]\n"
"ds_read_b64_tr_b16 a[10:11], v109 offset:20480\n"
"v_mfma_f32_16x16x32_bf16 v[188:191], a[24:27], a[68:71], v[188:191]\n"
"ds_read_b64_tr_b16 a[12:13], v109 offset:24576\n"
"v_mfma_f32_16x16x32_bf16 v[188:191], a[28:31], a[84:87], v[188:191]\n"
"ds_read_b64_tr_b16 a[14:15], v109 offset:28672\n"
"s_add_u32 m0, s62, 49152\n"
"v_mfma_f32_16x16x32_bf16 v[220:223], a[24:27], a[72:75], v[220:223]\n"
"buffer_load_dwordx4 v104, s[44:47], s61 offen lds\n"
"v_mfma_f32_16x16x32_bf16 v[220:223], a[28:31], a[88:91], v[220:223]\n"
"v_mfma_f32_16x16x32_bf16 v[252:255], a[24:27], a[76:79], v[252:255]\n"
"v_mfma_f32_16x16x32_bf16 v[252:255], a[28:31], a[92:95], v[252:255]\n"
"s_waitcnt lgkmcnt(4)\n"
"v_mfma_f32_16x16x32_bf16 v[128:131], a[0:3], a[96:99], v[128:131]\n"
"ds_read_b64_tr_b16 a[16:17], v108 offset:16640\n"
"v_mfma_f32_16x16x32_bf16 v[128:131], a[4:7], a[112:115], v[128:131]\n"
"buffer_load_dwordx4 a[168:171], v102, s[40:43], s60 offen offset:2048 sc1\n"
"v_mfma_f32_16x16x32_bf16 v[160:163], a[0:3], a[100:103], v[160:163]\n"
"ds_read_b64_tr_b16 a[18:19], v108 offset:20736\n"
"v_mfma_f32_16x16x32_bf16 v[160:163], a[4:7], a[116:119], v[160:163]\n"
"ds_read_b64_tr_b16 a[20:21], v108 offset:24832\n"
"v_mfma_f32_16x16x32_bf16 v[192:195], a[0:3], a[104:107], v[192:195]\n"
"ds_read_b64_tr_b16 a[22:23], v108 offset:28928\n"
"v_mfma_f32_16x16x32_bf16 v[192:195], a[4:7], a[120:123], v[192:195]\n"
"v_mfma_f32_16x16x32_bf16 v[224:227], a[0:3], a[108:111], v[224:227]\n"
"buffer_load_dwordx4 a[172:175], v103, s[40:43], s60 offen offset:2048 sc1\n"
"v_mfma_f32_16x16x32_bf16 v[224:227], a[4:7], a[124:127], v[224:227]\n"
"s_waitcnt lgkmcnt(4)\n"
"v_mfma_f32_16x16x32_bf16 v[132:135], a[8:11], a[96:99], v[132:135]\n"
"ds_read_b64_tr_b16 a[24:25], v109 offset:16640\n"
"v_mfma_f32_16x16x32_bf16 v[132:135], a[12:15], a[112:115], v[132:135]\n"
"ds_read_b64_tr_b16 a[26:27], v109 offset:20736\n"
"v_mfma_f32_16x16x32_bf16 v[164:167], a[8:11], a[100:103], v[164:167]\n"
"ds_read_b64_tr_b16 a[28:29], v109 offset:24832\n"
"v_mfma_f32_16x16x32_bf16 v[164:167], a[12:15], a[116:119], v[164:167]\n"
"ds_read_b64_tr_b16 a[30:31], v109 offset:28928\n"
"v_mfma_f32_16x16x32_bf16 v[196:199], a[8:11], a[104:107], v[196:199]\n"
"buffer_load_dwordx4 v105, s[44:47], s61 offen offset:1024 lds\n"
"v_mfma_f32_16x16x32_bf16 v[196:199], a[12:15], a[120:123], v[196:199]\n"
"v_mfma_f32_16x16x32_bf16 v[228:231], a[8:11], a[108:111], v[228:231]\n"
"v_mfma_f32_16x16x32_bf16 v[228:231], a[12:15], a[124:127], v[228:231]\n"
"s_waitcnt lgkmcnt(4)\n"
"v_mfma_f32_16x16x32_bf16 v[136:139], a[16:19], a[96:99], v[136:139]\n"
"ds_read_b64_tr_b16 a[0:1], v108 offset:16896\n"
"v_mfma_f32_16x16x32_bf16 v[136:139], a[20:23], a[112:115], v[136:139]\n"
"buffer_load_dwordx4 a[176:179], v100, s[40:43], s60 offen offset:3072 sc1\n"
"v_mfma_f32_16x16x32_bf16 v[168:171], a[16:19], a[100:103], v[168:171]\n"
"ds_read_b64_tr_b16 a[2:3], v108 offset:20992\n"
"v_mfma_f32_16x16x32_bf16 v[168:171], a[20:23], a[116:119], v[168:171]\n"
"ds_read_b64_tr_b16 a[4:5], v108 offset:25088\n"
"v_mfma_f32_16x16x32_bf16 v[200:203], a[16:19], a[104:107], v[200:203]\n"
"ds_read_b64_tr_b16 a[6:7], v108 offset:29184\n"
"v_mfma_f32_16x16x32_bf16 v[200:203], a[20:23], a[120:123], v[200:203]\n"
"v_mfma_f32_16x16x32_bf16 v[232:235], a[16:19], a[108:111], v[232:235]\n"
"buffer_load_dwordx4 a[180:183], v101, s[40:43], s60 offen offset:3072 sc1\n"
"v_mfma_f32_16x16x32_bf16 v[232:235], a[20:23], a[124:127], v[232:235]\n"
"s_waitcnt lgkmcnt(4)\n"
"v_mfma_f32_16x16x32_bf16 v[140:143], a[24:27], a[96:99], v[140:143]\n"
"ds_read_b64_tr_b16 a[8:9], v109 offset:16896\n"
"v_mfma_f32_16x16x32_bf16 v[140:143], a[28:31], a[112:115], v[140:143]\n"
"ds_read_b64_tr_b16 a[10:11], v109 offset:20992\n"
"v_mfma_f32_16x16x32_bf16 v[172:175], a[24:27], a[100:103], v[172:175]\n"
"ds_read_b64_tr_b16 a[12:13], v109 offset:25088\n"
"v_mfma_f32_16x16x32_bf16 v[172:175], a[28:31], a[116:119], v[172:175]\n"
"buffer_load_dwordx4 v106, s[44:47], s61 offen offset:2048 lds\n"
"v_mfma_f32_16x16x32_bf16 v[204:207], a[24:27], a[104:107], v[204:207]\n"
"ds_read_b64_tr_b16 a[14:15], v109 offset:29184\n"
"v_mfma_f32_16x16x32_bf16 v[204:207], a[28:31], a[120:123], v[204:207]\n"
"v_mfma_f32_16x16x32_bf16 v[236:239], a[24:27], a[108:111], v[236:239]\n"
"v_mfma_f32_16x16x32_bf16 v[236:239], a[28:31], a[124:127], v[236:239]\n"
"s_waitcnt lgkmcnt(4)\n"
"v_mfma_f32_16x16x32_bf16 v[144:147], a[0:3], a[96:99], v[144:147]\n"
"buffer_load_dwordx4 a[184:187], v102, s[40:43], s60 offen offset:3072 sc1\n"
"v_mfma_f32_16x16x32_bf16 v[144:147], a[4:7], a[112:115], v[144:147]\n"
"ds_read_b64_tr_b16 a[16:17], v108 offset:17152\n"
"v_mfma_f32_16x16x32_bf16 v[176:179], a[0:3], a[100:103], v[176:179]\n"
"ds_read_b64_tr_b16 a[18:19], v108 offset:21248\n"
"v_mfma_f32_16x16x32_bf16 v[176:179], a[4:7], a[116:119], v[176:179]\n"
"ds_read_b64_tr_b16 a[20:21], v108 offset:25344\n"
"v_mfma_f32_16x16x32_bf16 v[208:211], a[0:3], a[104:107], v[208:211]\n"
"ds_read_b64_tr_b16 a[22:23], v108 offset:29440\n"
"v_mfma_f32_16x16x32_bf16 v[208:211], a[4:7], a[120:123], v[208:211]\n"
"buffer_load_dwordx4 a[188:191], v103, s[40:43], s60 offen offset:3072 sc1\n"
"s_add_u32 s60, s60, 4096\n"
"v_mfma_f32_16x16x32_bf16 v[240:243], a[0:3], a[108:111], v[240:243]\n"
"v_mfma_f32_16x16x32_bf16 v[240:243], a[4:7], a[124:127], v[240:243]\n"
"s_waitcnt lgkmcnt(4)\n"
"v_mfma_f32_16x16x32_bf16 v[148:151], a[8:11], a[96:99], v[148:151]\n"
"ds_read_b64_tr_b16 a[24:25], v109 offset:17152\n"
"v_mfma_f32_16x16x32_bf16 v[148:151], a[12:15], a[112:115], v[148:151]\n"
"ds_read_b64_tr_b16 a[26:27], v109 offset:21248\n"
"v_mfma_f32_16x16x32_bf16 v[180:183], a[8:11], a[100:103], v[180:183]\n"
"ds_read_b64_tr_b16 a[28:29], v109 offset:25344\n"
"v_mfma_f32_16x16x32_bf16 v[180:183], a[12:15], a[116:119], v[180:183]\n"
"buffer_load_dwordx4 v107, s[44:47], s61 offen offset:3072 lds\n"
"s_add_u32 s61, s61, 458752\n"
"v_mfma_f32_16x16x32_bf16 v[212:215], a[8:11], a[104:107], v[212:215]\n"
"ds_read_b64_tr_b16 a[30:31], v109 offset:29440\n"
"v_mfma_f32_16x16x32_bf16 v[212:215], a[12:15], a[120:123], v[212:215]\n"
"v_mfma_f32_16x16x32_bf16 v[244:247], a[8:11], a[108:111], v[244:247]\n"
"v_mfma_f32_16x16x32_bf16 v[244:247], a[12:15], a[124:127], v[244:247]\n"
"s_waitcnt vmcnt(12) lgkmcnt(4)\n"
"s_barrier\n"
"v_mfma_f32_16x16x32_bf16 v[152:155], a[16:19], a[96:99], v[152:155]\n"
"ds_read_b64_tr_b16 a[0:1], v108 offset:32768\n"
"v_mfma_f32_16x16x32_bf16 v[152:155], a[20:23], a[112:115], v[152:155]\n"
"ds_read_b64_tr_b16 a[2:3], v108 offset:36864\n"
"v_mfma_f32_16x16x32_bf16 v[184:187], a[16:19], a[100:103], v[184:187]\n"
"buffer_load_dwordx4 v[80:83], v100, s[52:55], 0 offen sc1\n"
"v_mfma_f32_16x16x32_bf16 v[184:187], a[20:23], a[116:119], v[184:187]\n"
"ds_read_b64_tr_b16 a[4:5], v108 offset:40960\n"
"v_mfma_f32_16x16x32_bf16 v[216:219], a[16:19], a[104:107], v[216:219]\n"
"ds_read_b64_tr_b16 a[6:7], v108 offset:45056\n"
"v_mfma_f32_16x16x32_bf16 v[216:219], a[20:23], a[120:123], v[216:219]\n"
"v_mfma_f32_16x16x32_bf16 v[248:251], a[16:19], a[108:111], v[248:251]\n"
"v_mfma_f32_16x16x32_bf16 v[248:251], a[20:23], a[124:127], v[248:251]\n"
"buffer_load_dwordx4 v[84:87], v100, s[52:55], 0 offen offset:1024 sc1\n"
"s_waitcnt lgkmcnt(4)\n"
"v_mfma_f32_16x16x32_bf16 v[156:159], a[24:27], a[96:99], v[156:159]\n"
"ds_read_b64_tr_b16 a[8:9], v109 offset:32768\n"
"v_mfma_f32_16x16x32_bf16 v[156:159], a[28:31], a[112:115], v[156:159]\n"
"ds_read_b64_tr_b16 a[10:11], v109 offset:36864\n"
"v_mfma_f32_16x16x32_bf16 v[188:191], a[24:27], a[100:103], v[188:191]\n"
"ds_read_b64_tr_b16 a[12:13], v109 offset:40960\n"
"v_mfma_f32_16x16x32_bf16 v[188:191], a[28:31], a[116:119], v[188:191]\n"
"ds_read_b64_tr_b16 a[14:15], v109 offset:45056\n"
"v_mfma_f32_16x16x32_bf16 v[220:223], a[24:27], a[104:107], v[220:223]\n"
"buffer_load_dwordx4 v[88:91], v100, s[52:55], 0 offen offset:2048 sc1\n"
"v_mfma_f32_16x16x32_bf16 v[220:223], a[28:31], a[120:123], v[220:223]\n"
"v_mfma_f32_16x16x32_bf16 v[252:255], a[24:27], a[108:111], v[252:255]\n"
"v_mfma_f32_16x16x32_bf16 v[252:255], a[28:31], a[124:127], v[252:255]\n"
"s_waitcnt lgkmcnt(4)\n"
"v_mfma_f32_16x16x32_bf16 v[128:131], a[0:3], a[128:131], v[128:131]\n"
"ds_read_b64_tr_b16 a[16:17], v108 offset:33024\n"
"v_mfma_f32_16x16x32_bf16 v[128:131], a[4:7], a[144:147], v[128:131]\n"
"buffer_load_dwordx4 v[92:95], v100, s[52:55], 0 offen offset:3072 sc1\n"
"v_mfma_f32_16x16x32_bf16 v[160:163], a[0:3], a[132:135], v[160:163]\n"
"ds_read_b64_tr_b16 a[18:19], v108 offset:37120\n"
"v_mfma_f32_16x16x32_bf16 v[160:163], a[4:7], a[148:151], v[160:163]\n"
"ds_read_b64_tr_b16 a[20:21], v108 offset:41216\n"
"v_mfma_f32_16x16x32_bf16 v[192:195], a[0:3], a[136:139], v[192:195]\n"
"ds_read_b64_tr_b16 a[22:23], v108 offset:45312\n"
"v_mfma_f32_16x16x32_bf16 v[192:195], a[4:7], a[152:155], v[192:195]\n"
"s_add_u32 m0, s68, 65536\n"
"v_mfma_f32_16x16x32_bf16 v[224:227], a[0:3], a[140:143], v[224:227]\n"
"buffer_load_dwordx4 v113, s[76:79], s69 offen lds\n"
"s_add_u32 s69, s69, 7168\n"
"v_mfma_f32_16x16x32_bf16 v[224:227], a[4:7], a[156:159], v[224:227]\n"
"s_waitcnt lgkmcnt(4)\n"
"v_mfma_f32_16x16x32_bf16 v[132:135], a[8:11], a[128:131], v[132:135]\n"
"ds_read_b64_tr_b16 a[24:25], v109 offset:33024\n"
"v_mfma_f32_16x16x32_bf16 v[132:135], a[12:15], a[144:147], v[132:135]\n"
"ds_read_b64_tr_b16 a[26:27], v109 offset:37120\n"
"v_mfma_f32_16x16x32_bf16 v[164:167], a[8:11], a[132:135], v[164:167]\n"
"ds_read_b64_tr_b16 a[28:29], v109 offset:41216\n"
"v_mfma_f32_16x16x32_bf16 v[164:167], a[12:15], a[148:151], v[164:167]\n"
"ds_read_b64_tr_b16 a[30:31], v109 offset:45312\n"
"v_mfma_f32_16x16x32_bf16 v[196:199], a[8:11], a[136:139], v[196:199]\n"
"buffer_load_dwordx4 v114, s[76:79], s69 offen offset:1024 lds\n"
"s_add_u32 s69, s69, 7168\n"
"v_mfma_f32_16x16x32_bf16 v[196:199], a[12:15], a[152:155], v[196:199]\n"
"v_mfma_f32_16x16x32_bf16 v[228:231], a[8:11], a[140:143], v[228:231]\n"
"v_mfma_f32_16x16x32_bf16 v[228:231], a[12:15], a[156:159], v[228:231]\n"
"s_waitcnt lgkmcnt(4)\n"
"v_mfma_f32_16x16x32_bf16 v[136:139], a[16:19], a[128:131], v[136:139]\n"
"ds_read_b64_tr_b16 a[0:1], v108 offset:33280\n"
"v_mfma_f32_16x16x32_bf16 v[136:139], a[20:23], a[144:147], v[136:139]\n"
"buffer_load_dwordx4 v113, s[76:79], s69 offen offset:2048 lds\n"
"s_add_u32 s69, s69, 7168\n"
"v_mfma_f32_16x16x32_bf16 v[168:171], a[16:19], a[132:135], v[168:171]\n"
"ds_read_b64_tr_b16 a[2:3], v108 offset:37376\n"
"v_mfma_f32_16x16x32_bf16 v[168:171], a[20:23], a[148:151], v[168:171]\n"
"ds_read_b64_tr_b16 a[4:5], v108 offset:41472\n"
"v_mfma_f32_16x16x32_bf16 v[200:203], a[16:19], a[136:139], v[200:203]\n"
"ds_read_b64_tr_b16 a[6:7], v108 offset:45568\n"
"v_mfma_f32_16x16x32_bf16 v[200:203], a[20:23], a[152:155], v[200:203]\n"
"v_mfma_f32_16x16x32_bf16 v[232:235], a[16:19], a[140:143], v[232:235]\n"
"buffer_load_dwordx4 v114, s[76:79], s69 offen offset:3072 lds\n"
"s_add_u32 s69, s69, 11264\n"
"v_mfma_f32_16x16x32_bf16 v[232:235], a[20:23], a[156:159], v[232:235]\n"
"s_waitcnt lgkmcnt(4)\n"
"v_mfma_f32_16x16x32_bf16 v[140:143], a[24:27], a[128:131], v[140:143]\n"
"ds_read_b64_tr_b16 a[8:9], v109 offset:33280\n"
"v_mfma_f32_16x16x32_bf16 v[140:143], a[28:31], a[144:147], v[140:143]\n"
"ds_read_b64_tr_b16 a[10:11], v109 offset:37376\n"
"v_mfma_f32_16x16x32_bf16 v[172:175], a[24:27], a[132:135], v[172:175]\n"
"ds_read_b64_tr_b16 a[12:13], v109 offset:41472\n"
"s_add_u32 m0, s68, 69632\n"
"v_mfma_f32_16x16x32_bf16 v[172:175], a[28:31], a[148:151], v[172:175]\n"
"buffer_load_dwordx4 v113, s[76:79], s69 offen lds\n"
"s_add_u32 s69, s69, 7168\n"
"v_mfma_f32_16x16x32_bf16 v[204:207], a[24:27], a[136:139], v[204:207]\n"
"ds_read_b64_tr_b16 a[14:15], v109 offset:45568\n"
"v_mfma_f32_16x16x32_bf16 v[204:207], a[28:31], a[152:155], v[204:207]\n"
"v_mfma_f32_16x16x32_bf16 v[236:239], a[24:27], a[140:143], v[236:239]\n"
"v_mfma_f32_16x16x32_bf16 v[236:239], a[28:31], a[156:159], v[236:239]\n"
"s_waitcnt lgkmcnt(4)\n"
"v_mfma_f32_16x16x32_bf16 v[144:147], a[0:3], a[128:131], v[144:147]\n"
"buffer_load_dwordx4 v114, s[76:79], s69 offen offset:1024 lds\n"
"s_add_u32 s69, s69, 7168\n"
"v_mfma_f32_16x16x32_bf16 v[144:147], a[4:7], a[144:147], v[144:147]\n"
"ds_read_b64_tr_b16 a[16:17], v108 offset:33536\n"
"v_mfma_f32_16x16x32_bf16 v[176:179], a[0:3], a[132:135], v[176:179]\n"
"ds_read_b64_tr_b16 a[18:19], v108 offset:37632\n"
"v_mfma_f32_16x16x32_bf16 v[176:179], a[4:7], a[148:151], v[176:179]\n"
"ds_read_b64_tr_b16 a[20:21], v108 offset:41728\n"
"v_mfma_f32_16x16x32_bf16 v[208:211], a[0:3], a[136:139], v[208:211]\n"
"ds_read_b64_tr_b16 a[22:23], v108 offset:45824\n"
"v_mfma_f32_16x16x32_bf16 v[208:211], a[4:7], a[152:155], v[208:211]\n"
"buffer_load_dwordx4 v113, s[76:79], s69 offen offset:2048 lds\n"
"s_add_u32 s69, s69, 7168\n"
"v_mfma_f32_16x16x32_bf16 v[240:243], a[0:3], a[140:143], v[240:243]\n"
"v_mfma_f32_16x16x32_bf16 v[240:243], a[4:7], a[156:159], v[240:243]\n"
"s_waitcnt lgkmcnt(4)\n"
"v_mfma_f32_16x16x32_bf16 v[148:151], a[8:11], a[128:131], v[148:151]\n"
"ds_read_b64_tr_b16 a[24:25], v109 offset:33536\n"
"v_mfma_f32_16x16x32_bf16 v[148:151], a[12:15], a[144:147], v[148:151]\n"
"ds_read_b64_tr_b16 a[26:27], v109 offset:37632\n"
"v_mfma_f32_16x16x32_bf16 v[180:183], a[8:11], a[132:135], v[180:183]\n"
"ds_read_b64_tr_b16 a[28:29], v109 offset:41728\n"
"v_mfma_f32_16x16x32_bf16 v[180:183], a[12:15], a[148:151], v[180:183]\n"
"ds_read_b64_tr_b16 a[30:31], v109 offset:45824\n"
"v_mfma_f32_16x16x32_bf16 v[212:215], a[8:11], a[136:139], v[212:215]\n"
"v_mfma_f32_16x16x32_bf16 v[212:215], a[12:15], a[152:155], v[212:215]\n"
"v_mfma_f32_16x16x32_bf16 v[244:247], a[8:11], a[140:143], v[244:247]\n"
"v_mfma_f32_16x16x32_bf16 v[244:247], a[12:15], a[156:159], v[244:247]\n"
"s_waitcnt vmcnt(11) lgkmcnt(4)\n"
"s_barrier\n"
"v_mfma_f32_16x16x32_bf16 v[152:155], a[16:19], a[128:131], v[152:155]\n"
"ds_read_b64_tr_b16 a[0:1], v108 offset:49152\n"
"v_mfma_f32_16x16x32_bf16 v[152:155], a[20:23], a[144:147], v[152:155]\n"
"ds_read_b64_tr_b16 a[2:3], v108 offset:53248\n"
"v_mfma_f32_16x16x32_bf16 v[184:187], a[16:19], a[132:135], v[184:187]\n"
"buffer_load_dwordx4 v[64:67], v100, s[48:51], 0 offen sc1\n"
"v_mfma_f32_16x16x32_bf16 v[184:187], a[20:23], a[148:151], v[184:187]\n"
"ds_read_b64_tr_b16 a[4:5], v108 offset:57344\n"
"v_mfma_f32_16x16x32_bf16 v[216:219], a[16:19], a[136:139], v[216:219]\n"
"ds_read_b64_tr_b16 a[6:7], v108 offset:61440\n"
"v_mfma_f32_16x16x32_bf16 v[216:219], a[20:23], a[152:155], v[216:219]\n"
"v_mfma_f32_16x16x32_bf16 v[248:251], a[16:19], a[140:143], v[248:251]\n"
"v_mfma_f32_16x16x32_bf16 v[248:251], a[20:23], a[156:159], v[248:251]\n"
"s_waitcnt lgkmcnt(4)\n"
"v_mfma_f32_16x16x32_bf16 v[156:159], a[24:27], a[128:131], v[156:159]\n"
"buffer_load_dwordx4 v[68:71], v100, s[48:51], 0 offen offset:1024 sc1\n"
"v_mfma_f32_16x16x32_bf16 v[156:159], a[28:31], a[144:147], v[156:159]\n"
"ds_read_b64_tr_b16 a[8:9], v109 offset:49152\n"
"v_mfma_f32_16x16x32_bf16 v[188:191], a[24:27], a[132:135], v[188:191]\n"
"ds_read_b64_tr_b16 a[10:11], v109 offset:53248\n"
"v_mfma_f32_16x16x32_bf16 v[188:191], a[28:31], a[148:151], v[188:191]\n"
"ds_read_b64_tr_b16 a[12:13], v109 offset:57344\n"
"v_mfma_f32_16x16x32_bf16 v[220:223], a[24:27], a[136:139], v[220:223]\n"
"ds_read_b64_tr_b16 a[14:15], v109 offset:61440\n"
"v_mfma_f32_16x16x32_bf16 v[220:223], a[28:31], a[152:155], v[220:223]\n"
"buffer_load_dwordx4 v[72:75], v100, s[48:51], 0 offen offset:2048 sc1\n"
"v_mfma_f32_16x16x32_bf16 v[252:255], a[24:27], a[140:143], v[252:255]\n"
"v_mfma_f32_16x16x32_bf16 v[252:255], a[28:31], a[156:159], v[252:255]\n"
"s_waitcnt lgkmcnt(4)\n"
"v_mfma_f32_16x16x32_bf16 v[128:131], a[0:3], a[160:163], v[128:131]\n"
"ds_read_b64_tr_b16 a[16:17], v108 offset:49408\n"
"v_mfma_f32_16x16x32_bf16 v[128:131], a[4:7], a[176:179], v[128:131]\n"
"ds_read_b64_tr_b16 a[18:19], v108 offset:53504\n"
"v_mfma_f32_16x16x32_bf16 v[160:163], a[0:3], a[164:167], v[160:163]\n"
"ds_read_b64_tr_b16 a[20:21], v108 offset:57600\n"
"v_mfma_f32_16x16x32_bf16 v[160:163], a[4:7], a[180:183], v[160:163]\n"
"buffer_load_dwordx4 v[76:79], v100, s[48:51], 0 offen offset:3072 sc1\n"
"v_mfma_f32_16x16x32_bf16 v[192:195], a[0:3], a[168:171], v[192:195]\n"
"ds_read_b64_tr_b16 a[22:23], v108 offset:61696\n"
"v_mfma_f32_16x16x32_bf16 v[192:195], a[4:7], a[184:187], v[192:195]\n"
"v_mfma_f32_16x16x32_bf16 v[224:227], a[0:3], a[172:175], v[224:227]\n"
"v_mfma_f32_16x16x32_bf16 v[224:227], a[4:7], a[188:191], v[224:227]\n"
"s_waitcnt lgkmcnt(4)\n"
"v_mfma_f32_16x16x32_bf16 v[132:135], a[8:11], a[160:163], v[132:135]\n"
"buffer_load_dwordx4 v[124:127], v112, s[64:67], 0 offen\n"
"v_mfma_f32_16x16x32_bf16 v[132:135], a[12:15], a[176:179], v[132:135]\n"
"ds_read_b64_tr_b16 a[24:25], v109 offset:49408\n"
"v_mfma_f32_16x16x32_bf16 v[164:167], a[8:11], a[164:167], v[164:167]\n"
"ds_read_b64_tr_b16 a[26:27], v109 offset:53504\n"
"v_mfma_f32_16x16x32_bf16 v[164:167], a[12:15], a[180:183], v[164:167]\n"
"ds_read_b64_tr_b16 a[28:29], v109 offset:57600\n"
"v_mfma_f32_16x16x32_bf16 v[196:199], a[8:11], a[168:171], v[196:199]\n"
"ds_read_b64_tr_b16 a[30:31], v109 offset:61696\n"
"v_mfma_f32_16x16x32_bf16 v[196:199], a[12:15], a[184:187], v[196:199]\n"
"v_mfma_f32_16x16x32_bf16 v[228:231], a[8:11], a[172:175], v[228:231]\n"
"buffer_load_dwordx4 v114, s[76:79], s69 offen offset:3072 lds\n"
"s_add_u32 s69, s69, 207872\n"
"v_mfma_f32_16x16x32_bf16 v[228:231], a[12:15], a[188:191], v[228:231]\n"
"s_waitcnt lgkmcnt(4)\n"
"v_mfma_f32_16x16x32_bf16 v[136:139], a[16:19], a[160:163], v[136:139]\n"
"ds_read_b64_tr_b16 a[0:1], v108 offset:49664\n"
"v_mfma_f32_16x16x32_bf16 v[136:139], a[20:23], a[176:179], v[136:139]\n"
"ds_read_b64_tr_b16 a[2:3], v108 offset:53760\n"
"v_mfma_f32_16x16x32_bf16 v[168:171], a[16:19], a[164:167], v[168:171]\n"
"ds_read_b64_tr_b16 a[4:5], v108 offset:57856\n"
"v_mfma_f32_16x16x32_bf16 v[168:171], a[20:23], a[180:183], v[168:171]\n"
"ds_read_b64_tr_b16 a[6:7], v108 offset:61952\n"
"s_add_u32 m0, s68, 98304\n"
"v_mfma_f32_16x16x32_bf16 v[200:203], a[16:19], a[168:171], v[200:203]\n"
"buffer_load_dwordx4 v113, s[76:79], s69 offen lds\n"
"s_add_u32 s69, s69, 7168\n"
"v_mfma_f32_16x16x32_bf16 v[200:203], a[20:23], a[184:187], v[200:203]\n"
"v_mfma_f32_16x16x32_bf16 v[232:235], a[16:19], a[172:175], v[232:235]\n"
"v_mfma_f32_16x16x32_bf16 v[232:235], a[20:23], a[188:191], v[232:235]\n"
"s_waitcnt lgkmcnt(4)\n"
"v_mfma_f32_16x16x32_bf16 v[140:143], a[24:27], a[160:163], v[140:143]\n"
"ds_read_b64_tr_b16 a[8:9], v109 offset:49664\n"
"v_mfma_f32_16x16x32_bf16 v[140:143], a[28:31], a[176:179], v[140:143]\n"
"buffer_load_dwordx4 v114, s[76:79], s69 offen offset:1024 lds\n"
"s_add_u32 s69, s69, 7168\n"
"v_mfma_f32_16x16x32_bf16 v[172:175], a[24:27], a[164:167], v[172:175]\n"
"ds_read_b64_tr_b16 a[10:11], v109 offset:53760\n"
"v_mfma_f32_16x16x32_bf16 v[172:175], a[28:31], a[180:183], v[172:175]\n"
"ds_read_b64_tr_b16 a[12:13], v109 offset:57856\n"
"v_mfma_f32_16x16x32_bf16 v[204:207], a[24:27], a[168:171], v[204:207]\n"
"ds_read_b64_tr_b16 a[14:15], v109 offset:61952\n"
"v_mfma_f32_16x16x32_bf16 v[204:207], a[28:31], a[184:187], v[204:207]\n"
"v_mfma_f32_16x16x32_bf16 v[236:239], a[24:27], a[172:175], v[236:239]\n"
"v_mfma_f32_16x16x32_bf16 v[236:239], a[28:31], a[188:191], v[236:239]\n"
"buffer_load_dwordx4 v113, s[76:79], s69 offen offset:2048 lds\n"
"s_add_u32 s69, s69, 7168\n"
"s_waitcnt lgkmcnt(4)\n"
"v_mfma_f32_16x16x32_bf16 v[144:147], a[0:3], a[160:163], v[144:147]\n"
"ds_read_b64_tr_b16 a[16:17], v108 offset:49920\n"
"v_mfma_f32_16x16x32_bf16 v[144:147], a[4:7], a[176:179], v[144:147]\n"
"ds_read_b64_tr_b16 a[18:19], v108 offset:54016\n"
"v_mfma_f32_16x16x32_bf16 v[176:179], a[0:3], a[164:167], v[176:179]\n"
"ds_read_b64_tr_b16 a[20:21], v108 offset:58112\n"
"v_mfma_f32_16x16x32_bf16 v[176:179], a[4:7], a[180:183], v[176:179]\n"
"ds_read_b64_tr_b16 a[22:23], v108 offset:62208\n"
"v_mfma_f32_16x16x32_bf16 v[208:211], a[0:3], a[168:171], v[208:211]\n"
"v_mfma_f32_16x16x32_bf16 v[208:211], a[4:7], a[184:187], v[208:211]\n"
"buffer_load_dwordx4 v114, s[76:79], s69 offen offset:3072 lds\n"
"s_add_u32 s69, s69, 11264\n"
"v_mfma_f32_16x16x32_bf16 v[240:243], a[0:3], a[172:175], v[240:243]\n"
"v_mfma_f32_16x16x32_bf16 v[240:243], a[4:7], a[188:191], v[240:243]\n"
"s_waitcnt lgkmcnt(4)\n"
"v_mfma_f32_16x16x32_bf16 v[148:151], a[8:11], a[160:163], v[148:151]\n"
"ds_read_b64_tr_b16 a[24:25], v109 offset:49920\n"
"v_mfma_f32_16x16x32_bf16 v[148:151], a[12:15], a[176:179], v[148:151]\n"
"ds_read_b64_tr_b16 a[26:27], v109 offset:54016\n"
"s_add_u32 m0, s68, 102400\n"
"v_mfma_f32_16x16x32_bf16 v[180:183], a[8:11], a[164:167], v[180:183]\n"
"buffer_load_dwordx4 v113, s[76:79], s69 offen lds\n"
"s_add_u32 s69, s69, 7168\n"
"v_mfma_f32_16x16x32_bf16 v[180:183], a[12:15], a[180:183], v[180:183]\n"
"ds_read_b64_tr_b16 a[28:29], v109 offset:58112\n"
"v_mfma_f32_16x16x32_bf16 v[212:215], a[8:11], a[168:171], v[212:215]\n"
"ds_read_b64_tr_b16 a[30:31], v109 offset:62208\n"
"v_mfma_f32_16x16x32_bf16 v[212:215], a[12:15], a[184:187], v[212:215]\n"
"v_mfma_f32_16x16x32_bf16 v[244:247], a[8:11], a[172:175], v[244:247]\n"
"v_mfma_f32_16x16x32_bf16 v[244:247], a[12:15], a[188:191], v[244:247]\n"
"s_waitcnt lgkmcnt(4)\n"
"v_mfma_f32_16x16x32_bf16 v[152:155], a[16:19], a[160:163], v[152:155]\n"
"buffer_load_dwordx4 v114, s[76:79], s69 offen offset:1024 lds\n"
"s_add_u32 s69, s69, 7168\n"
"v_mfma_f32_16x16x32_bf16 v[152:155], a[20:23], a[176:179], v[152:155]\n"
"v_mfma_f32_16x16x32_bf16 v[184:187], a[16:19], a[164:167], v[184:187]\n"
"v_mfma_f32_16x16x32_bf16 v[184:187], a[20:23], a[180:183], v[184:187]\n"
"v_mfma_f32_16x16x32_bf16 v[216:219], a[16:19], a[168:171], v[216:219]\n"
"v_mfma_f32_16x16x32_bf16 v[216:219], a[20:23], a[184:187], v[216:219]\n"
"v_mfma_f32_16x16x32_bf16 v[248:251], a[16:19], a[172:175], v[248:251]\n"
"buffer_load_dwordx4 v113, s[76:79], s69 offen offset:2048 lds\n"
"s_add_u32 s69, s69, 7168\n"
"v_mfma_f32_16x16x32_bf16 v[248:251], a[20:23], a[188:191], v[248:251]\n"
"s_waitcnt lgkmcnt(0)\n"
"v_mfma_f32_16x16x32_bf16 v[156:159], a[24:27], a[160:163], v[156:159]\n"
"v_mfma_f32_16x16x32_bf16 v[156:159], a[28:31], a[176:179], v[156:159]\n"
"v_mfma_f32_16x16x32_bf16 v[188:191], a[24:27], a[164:167], v[188:191]\n"
"v_mfma_f32_16x16x32_bf16 v[188:191], a[28:31], a[180:183], v[188:191]\n"
"buffer_load_dwordx4 v114, s[76:79], s69 offen offset:3072 lds\n"
"s_add_u32 s69, s69, 207872\n"
"v_mfma_f32_16x16x32_bf16 v[220:223], a[24:27], a[168:171], v[220:223]\n"
"v_mfma_f32_16x16x32_bf16 v[220:223], a[28:31], a[184:187], v[220:223]\n"
"v_mfma_f32_16x16x32_bf16 v[252:255], a[24:27], a[172:175], v[252:255]\n"
"v_mfma_f32_16x16x32_bf16 v[252:255], a[28:31], a[188:191], v[252:255]\n"
"s_waitcnt vmcnt(9)\n"
"s_nop 7\n"
"s_nop 1\n"
    : "={v[128:159]}"(acc[0]), "={v[160:191]}"(acc[1]), "={v[192:223]}"(acc[2]), "={v[224:255]}"(acc[3]), "={v[64:79]}"(bv), "={v[80:95]}"(av), "+{s[60:63]}"(so), "+{v[100:115]}"(va)
    : "{s[40:43]}"(sx), "{s[44:47]}"(sw), "{s[48:51]}"(se), "{s[52:55]}"(su), "{s[64:67]}"(code_srd(bid, wave)),
      "{s[76:79]}"(mksrd(w2 + (bid & 1) * 1792 * 1024 + (bid >> 5) * 128, 1791 * 2048 + 256)), "{s70}"(wave), "s"((unsigned)(size_t)lds) : "v116", "v117", "v118", "v119", "v120", "v121", "v122", "v123", "v124", "v125", "v126", "v127", "a0", "a1", "a2", "a3", "a4", "a5", "a6", "a7", "a8", "a9", "a10", "a11", "a12", "a13", "a14", "a15", "a16", "a17", "a18", "a19", "a20", "a21", "a22", "a23", "a24", "a25", "a26", "a27", "a28", "a29", "a30", "a31", "a32", "a33", "a34", "a35", "a36", "a37", "a38", "a39", "a40", "a41", "a42", "a43", "a44", "a45", "a46", "a47", "a48", "a49", "a50", "a51", "a52", "a53", "a54", "a55", "a56", "a57", "a58", "a59", "a60", "a61", "a62", "a63", "a64", "a65", "a66", "a67", "a68", "a69", "a70", "a71", "a72", "a73", "a74", "a75", "a76", "a77", "a78", "a79", "a80", "a81", "a82", "a83", "a84", "a85", "a86", "a87", "a88", "a89", "a90", "a91", "a92", "a93", "a94", "a95", "a96", "a97", "a98", "a99", "a100", "a101", "a102", "a103", "a104", "a105", "a106", "a107", "a108", "a109", "a110", "a111", "a112", "a113", "a114", "a115", "a116", "a117", "a118", "a119", "a120", "a121", "a122", "a123", "a124", "a125", "a126", "a127", "a128", "a129", "a130", "a131", "a132", "a133", "a134", "a135", "a136", "a137", "a138", "a139", "a140", "a141", "a142", "a143", "a144", "a145", "a146", "a147", "a148", "a149", "a150", "a151", "a152", "a153", "a154", "a155", "a156", "a157", "a158", "a159", "a160", "a161", "a162", "a163", "a164", "a165", "a166", "a167", "a168", "a169", "a170", "a171", "a172", "a173", "a174", "a175", "a176", "a177", "a178", "a179", "a180", "a181", "a182", "a183", "a184", "a185", "a186", "a187", "a188", "a189", "a190", "a191", "a192", "a193", "a194", "a195", "a196", "a197", "a198", "a199", "a200", "a201", "a202", "a203", "a204", "a205", "a206", "a207", "a208", "a209", "a210", "a211", "a212", "a213", "a214", "a215", "a216", "a217", "a218", "a219", "a220", "a221", "a222", "a223", "a224", "a225", "a226", "a227", "a228", "a229", "a230", "a231", "a232", "a233", "a234", "a235", "a236", "a237", "a238", "a239", "a240", "a241", "a242", "a243", "a244", "a245", "a246", "a247", "a248", "a249", "a250", "a251", "a252", "a253", "a254", "a255", "s68", "s69", "vcc", "scc", "memory");
  { u32x4 ua[4], ub[4];
#pragma unroll
    for (int e = 0; e < 4; ++e) { ua[e] = (u32x4){av[4 * e], av[4 * e + 1], av[4 * e + 2], av[4 * e + 3]}; ub[e] = (u32x4){bv[4 * e], bv[4 * e + 1], bv[4 * e + 2], bv[4 * e + 3]}; }
    if (has_sent(__builtin_elementwise_max(ua[0], ub[0]), __builtin_elementwise_max(ua[1], ub[1]), __builtin_elementwise_max(ua[2], ub[2]), __builtin_elementwise_max(ua[3], ub[3]))) {
      poll_load4(ua, ut, pn * 4096 + lane * 16); poll_load4(ub, ut, (28 + pm * 4 + wave) * 4096 + lane * 16);
#pragma unroll
      for (int e = 0; e < 16; ++e) { av[e] = ua[e >> 2][e & 3]; bv[e] = ub[e >> 2][e & 3]; } } }
  asm volatile(
"s_nop 1\n"
"v_mfma_f32_16x16x16_bf16 v[128:131], v[80:81], v[64:65], v[128:131]\n"
"v_mul_f32 v100, 0xc0135761, v72\n"
"v_mfma_f32_16x16x16_bf16 v[160:163], v[80:81], v[66:67], v[160:163]\n"
"v_mul_f32 v104, v72, v72\n"
"v_mfma_f32_16x16x16_bf16 v[192:195], v[80:81], v[68:69], v[192:195]\n"
"v_add_u32 v108, 0, v96\n"
"v_mfma_f32_16x16x16_bf16 v[224:227], v[80:81], v[70:71], v[224:227]\n"
"v_mul_f32 v101, 0xc0135761, v73\n"
"v_mfma_f32_16x16x16_bf16 v[132:135], v[82:83], v[64:65], v[132:135]\n"
"v_mul_f32 v105, v73, v73\n"
"v_mfma_f32_16x16x16_bf16 v[164:167], v[82:83], v[66:67], v[164:167]\n"
"v_add_u32 v109, 115968, v96\n"
"v_mfma_f32_16x16x16_bf16 v[196:199], v[82:83], v[68:69], v[196:199]\n"
"v_mul_f32 v102, 0xc0135761, v74\n"
"v_mfma_f32_16x16x16_bf16 v[228:231], v[82:83], v[70:71], v[228:231]\n"
"v_mul_f32 v106, v74, v74\n"
"v_mfma_f32_16x16x16_bf16 v[136:139], v[84:85], v[64:65], v[136:139]\n"
"v_add_u32 v110, 231936, v96\n"
"v_mfma_f32_16x16x16_bf16 v[168:171], v[84:85], v[66:67], v[168:171]\n"
"v_mul_f32 v103, 0xc0135761, v75\n"
"v_mfma_f32_16x16x16_bf16 v[200:203], v[84:85], v[68:69], v[200:203]\n"
"v_mul_f32 v107, v75, v75\n"
"v_mfma_f32_16x16x16_bf16 v[232:235], v[84:85], v[70:71], v[232:235]\n"
"v_add_u32 v111, 347904, v96\n"
"v_mfma_f32_16x16x16_bf16 v[140:143], v[86:87], v[64:65], v[140:143]\n"
"v_mul_f32 v104, v104, v72\n"
"v_mfma_f32_16x16x16_bf16 v[172:175], v[86:87], v[66:67], v[172:175]\n"
"v_mul_f32 v105, v105, v73\n"
"v_mfma_f32_16x16x16_bf16 v[204:207], v[86:87], v[68:69], v[204:207]\n"
"v_mul_f32 v106, v106, v74\n"
"v_mfma_f32_16x16x16_bf16 v[236:239], v[86:87], v[70:71], v[236:239]\n"
"v_mul_f32 v107, v107, v75\n"
"v_mfma_f32_16x16x16_bf16 v[144:147], v[88:89], v[64:65], v[144:147]\n"
"v_mul_f32 v104, 0xbdd2d3e8, v104\n"
"v_mfma_f32_16x16x16_bf16 v[176:179], v[88:89], v[66:67], v[176:179]\n"
"v_mul_f32 v105, 0xbdd2d3e8, v105\n"
"v_mfma_f32_16x16x16_bf16 v[208:211], v[88:89], v[68:69], v[208:211]\n"
"v_mul_f32 v106, 0xbdd2d3e8, v106\n"
"v_mfma_f32_16x16x16_bf16 v[240:243], v[88:89], v[70:71], v[240:243]\n"
"v_mul_f32 v107, 0xbdd2d3e8, v107\n"
"v_mfma_f32_16x16x16_bf16 v[148:151], v[90:91], v[64:65], v[148:151]\n"
"v_mfma_f32_16x16x16_bf16 v[180:183], v[90:91], v[66:67], v[180:183]\n"
"v_mfma_f32_16x16x16_bf16 v[212:215], v[90:91], v[68:69], v[212:215]\n"
"v_mfma_f32_16x16x16_bf16 v[244:247], v[90:91], v[70:71], v[244:247]\n"
"v_mfma_f32_16x16x16_bf16 v[152:155], v[92:93], v[64:65], v[152:155]\n"
"v_mfma_f32_16x16x16_bf16 v[184:187], v[92:93], v[66:67], v[184:187]\n"
"v_mfma_f32_16x16x16_bf16 v[216:219], v[92:93], v[68:69], v[216:219]\n"
"v_mfma_f32_16x16x16_bf16 v[248:251], v[92:93], v[70:71], v[248:251]\n"
"v_mfma_f32_16x16x16_bf16 v[156:159], v[94:95], v[64:65], v[156:159]\n"
"v_mfma_f32_16x16x16_bf16 v[188:191], v[94:95], v[66:67], v[188:191]\n"
"v_mfma_f32_16x16x16_bf16 v[220:223], v[94:95], v[68:69], v[220:223]\n"
"v_mfma_f32_16x16x16_bf16 v[252:255], v[94:95], v[70:71], v[252:255]\n"
"v_pk_mul_f32 v[16:17], v[128:129], v[128:129]\n"
"v_pk_mul_f32 v[18:19], v[130:131], v[130:131]\n"
"v_pk_mul_f32 v[20:21], v[132:133], v[132:133]\n"
"v_pk_mul_f32 v[22:23], v[134:135], v[134:135]\n"
"v_pk_fma_f32 v[16:17], v[16:17], v[104:105], v[100:101] op_sel_hi:[1,0,0]\n"
"v_pk_fma_f32 v[18:19], v[18:19], v[104:105], v[100:101] op_sel_hi:[1,0,0]\n"
"v_pk_fma_f32 v[20:21], v[20:21], v[104:105], v[100:101] op_sel_hi:[1,0,0]\n"
"v_pk_fma_f32 v[22:23], v[22:23], v[104:105], v[100:101] op_sel_hi:[1,0,0]\n"
"v_pk_mul_f32 v[16:17], v[16:17], v[128:129]\n"
"v_pk_mul_f32 v[18:19], v[18:19], v[130:131]\n"
"v_pk_mul_f32 v[20:21], v[20:21], v[132:133]\n"
"v_pk_mul_f32 v[22:23], v[22:23], v[134:135]\n"
"v_exp_f32 v16, v16\n"
"v_exp_f32 v17, v17\n"
"v_exp_f32 v18, v18\n"
"v_exp_f32 v19, v19\n"
"v_exp_f32 v20, v20\n"
"v_exp_f32 v21, v21\n"
"v_exp_f32 v22, v22\n"
"v_exp_f32 v23, v23\n"
"v_pk_fma_f32 v[16:17], v[16:17], v[76:77], v[76:77] op_sel_hi:[1,0,0]\n"
"v_pk_fma_f32 v[18:19], v[18:19], v[76:77], v[76:77] op_sel_hi:[1,0,0]\n"
"v_pk_fma_f32 v[20:21], v[20:21], v[76:77], v[76:77] op_sel_hi:[1,0,0]\n"
"v_pk_fma_f32 v[22:23], v[22:23], v[76:77], v[76:77] op_sel_hi:[1,0,0]\n"
"v_rcp_f32 v16, v16\n"
"v_rcp_f32 v17, v17\n"
"v_rcp_f32 v18, v18\n"
"v_rcp_f32 v19, v19\n"
"v_rcp_f32 v20, v20\n"
"v_rcp_f32 v21, v21\n"
"v_rcp_f32 v22, v22\n"
"v_rcp_f32 v23, v23\n"
"v_pk_mul_f32 v[16:17], v[16:17], v[128:129]\n"
"v_pk_mul_f32 v[18:19], v[18:19], v[130:131]\n"
"v_pk_mul_f32 v[20:21], v[20:21], v[132:133]\n"
"v_pk_mul_f32 v[22:23], v[22:23], v[134:135]\n"
"v_cvt_pk_bf16_f32 v32, v16, v17\n"
"v_cvt_pk_bf16_f32 v33, v18, v19\n"
"v_cvt_pk_bf16_f32 v34, v20, v21\n"
"v_cvt_pk_bf16_f32 v35, v22, v23\n"
"buffer_store_dwordx4 v[32:35], v108, s[40:43], 0 offen sc1\n"
"v_pk_mul_f32 v[24:25], v[160:161], v[160:161]\n"
"v_pk_mul_f32 v[26:27], v[162:163], v[162:163]\n"
"v_pk_mul_f32 v[28:29], v[164:165], v[164:165]\n"
"v_pk_mul_f32 v[30:31], v[166:167], v[166:167]\n"
"v_pk_fma_f32 v[24:25], v[24:25], v[104:105], v[100:101] op_sel:[0,1,1]\n"
"v_pk_fma_f32 v[26:27], v[26:27], v[104:105], v[100:101] op_sel:[0,1,1]\n"
"v_pk_fma_f32 v[28:29], v[28:29], v[104:105], v[100:101] op_sel:[0,1,1]\n"
"v_pk_fma_f32 v[30:31], v[30:31], v[104:105], v[100:101] op_sel:[0,1,1]\n"
"v_pk_mul_f32 v[24:25], v[24:25], v[160:161]\n"
"v_pk_mul_f32 v[26:27], v[26:27], v[162:163]\n"
"v_pk_mul_f32 v[28:29], v[28:29], v[164:165]\n"
"v_pk_mul_f32 v[30:31], v[30:31], v[166:167]\n"
"v_exp_f32 v24, v24\n"
"v_exp_f32 v25, v25\n"
"v_exp_f32 v26, v26\n"
"v_exp_f32 v27, v27\n"
"v_exp_f32 v28, v28\n"
"v_exp_f32 v29, v29\n"
"v_exp_f32 v30, v30\n"
"v_exp_f32 v31, v31\n"
"v_pk_fma_f32 v[24:25], v[24:25], v[76:77], v[76:77] op_sel:[0,1,1]\n"
"v_pk_fma_f32 v[26:27], v[26:27], v[76:77], v[76:77] op_sel:[0,1,1]\n"
"v_pk_fma_f32 v[28:29], v[28:29], v[76:77], v[76:77] op_sel:[0,1,1]\n"
"v_pk_fma_f32 v[30:31], v[30:31], v[76:77], v[76:77] op_sel:[0,1,1]\n"
"v_rcp_f32 v24, v24\n"
"v_rcp_f32 v25, v25\n"
"v_rcp_f32 v26, v26\n"
"v_rcp_f32 v27, v27\n"
"v_rcp_f32 v28, v28\n"
"v_rcp_f32 v29, v29\n"
"v_rcp_f32 v30, v30\n"
"v_rcp_f32 v31, v31\n"
"v_pk_mul_f32 v[24:25], v[24:25], v[160:161]\n"
"v_pk_mul_f32 v[26:27], v[26:27], v[162:163]\n"
"v_pk_mul_f32 v[28:29], v[28:29], v[164:165]\n"
"v_pk_mul_f32 v[30:31], v[30:31], v[166:167]\n"
"v_cvt_pk_bf16_f32 v36, v24, v25\n"
"v_cvt_pk_bf16_f32 v37, v26, v27\n"
"v_cvt_pk_bf16_f32 v38, v28, v29\n"
"v_cvt_pk_bf16_f32 v39, v30, v31\n"
"buffer_store_dwordx4 v[36:39], v109, s[40:43], 0 offen sc1\n"
"v_pk_mul_f32 v[16:17], v[192:193], v[192:193]\n"
"v_pk_mul_f32 v[18:19], v[194:195], v[194:195]\n"
"v_pk_mul_f32 v[20:21], v[196:197], v[196:197]\n"
"v_pk_mul_f32 v[22:23], v[198:199], v[198:199]\n"
"v_pk_fma_f32 v[16:17], v[16:17], v[106:107], v[102:103] op_sel_hi:[1,0,0]\n"
"v_pk_fma_f32 v[18:19], v[18:19], v[106:107], v[102:103] op_sel_hi:[1,0,0]\n"
"v_pk_fma_f32 v[20:21], v[20:21], v[106:107], v[102:103] op_sel_hi:[1,0,0]\n"
"v_pk_fma_f32 v[22:23], v[22:23], v[106:107], v[102:103] op_sel_hi:[1,0,0]\n"
"v_pk_mul_f32 v[16:17], v[16:17], v[192:193]\n"
"v_pk_mul_f32 v[18:19], v[18:19], v[194:195]\n"
"v_pk_mul_f32 v[20:21], v[20:21], v[196:197]\n"
"v_pk_mul_f32 v[22:23], v[22:23], v[198:199]\n"
"v_exp_f32 v16, v16\n"
"v_exp_f32 v17, v17\n"
"v_exp_f32 v18, v18\n"
"v_exp_f32 v19, v19\n"
"v_exp_f32 v20, v20\n"
"v_exp_f32 v21, v21\n"
"v_exp_f32 v22, v22\n"
"v_exp_f32 v23, v23\n"
"v_pk_fma_f32 v[16:17], v[16:17], v[78:79], v[78:79] op_sel_hi:[1,0,0]\n"
"v_pk_fma_f32 v[18:19], v[18:19], v[78:79], v[78:79] op_sel_hi:[1,0,0]\n"
"v_pk_fma_f32 v[20:21], v[20:21], v[78:79], v[78:79] op_sel_hi:[1,0,0]\n"
"v_pk_fma_f32 v[22:23], v[22:23], v[78:79], v[78:79] op_sel_hi:[1,0,0]\n"
"v_rcp_f32 v16, v16\n"
"v_rcp_f32 v17, v17\n"
"v_rcp_f32 v18, v18\n"
"v_rcp_f32 v19, v19\n"
"v_rcp_f32 v20, v20\n"
"v_rcp_f32 v21, v21\n"
"v_rcp_f32 v22, v22\n"
"v_rcp_f32 v23, v23\n"
"v_pk_mul_f32 v[16:17], v[16:17], v[192:193]\n"
"v_pk_mul_f32 v[18:19], v[18:19], v[194:195]\n"
"v_pk_mul_f32 v[20:21], v[20:21], v[196:197]\n"
"v_pk_mul_f32 v[22:23], v[22:23], v[198:199]\n"
"v_cvt_pk_bf16_f32 v40, v16, v17\n"
"v_cvt_pk_bf16_f32 v41, v18, v19\n"
"v_cvt_pk_bf16_f32 v42, v20, v21\n"
"v_cvt_pk_bf16_f32 v43, v22, v23\n"
"buffer_store_dwordx4 v[40:43], v110, s[40:43], 0 offen sc1\n"
"v_pk_mul_f32 v[24:25], v[224:225], v[224:225]\n"
"v_pk_mul_f32 v[26:27], v[226:227], v[226:227]\n"
"v_pk_mul_f32 v[28:29], v[228:229], v[228:229]\n"
"v_pk_mul_f32 v[30:31], v[230:231], v[230:231]\n"
"v_pk_fma_f32 v[24:25], v[24:25], v[106:107], v[102:103] op_sel:[0,1,1]\n"
"v_pk_fma_f32 v[26:27], v[26:27], v[106:107], v[102:103] op_sel:[0,1,1]\n"
"v_pk_fma_f32 v[28:29], v[28:29], v[106:107], v[102:103] op_sel:[0,1,1]\n"
"v_pk_fma_f32 v[30:31], v[30:31], v[106:107], v[102:103] op_sel:[0,1,1]\n"
"v_pk_mul_f32 v[24:25], v[24:25], v[224:225]\n"
"v_pk_mul_f32 v[26:27], v[26:27], v[226:227]\n"
"v_pk_mul_f32 v[28:29], v[28:29], v[228:229]\n"
"v_pk_mul_f32 v[30:31], v[30:31], v[230:231]\n"
"v_exp_f32 v24, v24\n"
"v_exp_f32 v25, v25\n"
"v_exp_f32 v26, v26\n"
"v_exp_f32 v27, v27\n"
"v_exp_f32 v28, v28\n"
"v_exp_f32 v29, v29\n"
"v_exp_f32 v30, v30\n"
"v_exp_f32 v31, v31\n"
"v_pk_fma_f32 v[24:25], v[24:25], v[78:79], v[78:79] op_sel:[0,1,1]\n"
"v_pk_fma_f32 v[26:27], v[26:27], v[78:79], v[78:79] op_sel:[0,1,1]\n"
"v_pk_fma_f32 v[28:29], v[28:29], v[78:79], v[78:79] op_sel:[0,1,1]\n"
"v_pk_fma_f32 v[30:31], v[30:31], v[78:79], v[78:79] op_sel:[0,1,1]\n"
"v_rcp_f32 v24, v24\n"
"v_rcp_f32 v25, v25\n"
"v_rcp_f32 v26, v26\n"
"v_rcp_f32 v27, v27\n"
"v_rcp_f32 v28, v28\n"
"v_rcp_f32 v29, v29\n"
"v_rcp_f32 v30, v30\n"
"v_rcp_f32 v31, v31\n"
"v_pk_mul_f32 v[24:25], v[24:25], v[224:225]\n"
"v_pk_mul_f32 v[26:27], v[26:27], v[226:227]\n"
"v_pk_mul_f32 v[28:29], v[28:29], v[228:229]\n"
"v_pk_mul_f32 v[30:31], v[30:31], v[230:231]\n"
"v_cvt_pk_bf16_f32 v32, v24, v25\n"
"v_cvt_pk_bf16_f32 v33, v26, v27\n"
"v_cvt_pk_bf16_f32 v34, v28, v29\n"
"v_cvt_pk_bf16_f32 v35, v30, v31\n"
"buffer_store_dwordx4 v[32:35], v111, s[40:43], 0 offen sc1\n"
"v_pk_mul_f32 v[16:17], v[136:137], v[136:137]\n"
"v_pk_mul_f32 v[18:19], v[138:139], v[138:139]\n"
"v_pk_mul_f32 v[20:21], v[140:141], v[140:141]\n"
"v_pk_mul_f32 v[22:23], v[142:143], v[142:143]\n"
"v_pk_fma_f32 v[16:17], v[16:17], v[104:105], v[100:101] op_sel_hi:[1,0,0]\n"
"v_pk_fma_f32 v[18:19], v[18:19], v[104:105], v[100:101] op_sel_hi:[1,0,0]\n"
"v_pk_fma_f32 v[20:21], v[20:21], v[104:105], v[100:101] op_sel_hi:[1,0,0]\n"
"v_pk_fma_f32 v[22:23], v[22:23], v[104:105], v[100:101] op_sel_hi:[1,0,0]\n"
"v_pk_mul_f32 v[16:17], v[16:17], v[136:137]\n"
"v_pk_mul_f32 v[18:19], v[18:19], v[138:139]\n"
"v_pk_mul_f32 v[20:21], v[20:21], v[140:141]\n"
"v_pk_mul_f32 v[22:23], v[22:23], v[142:143]\n"
"v_exp_f32 v16, v16\n"
"v_exp_f32 v17, v17\n"
"v_exp_f32 v18, v18\n"
"v_exp_f32 v19, v19\n"
"v_exp_f32 v20, v20\n"
"v_exp_f32 v21, v21\n"
"v_exp_f32 v22, v22\n"
"v_exp_f32 v23, v23\n"
"v_pk_fma_f32 v[16:17], v[16:17], v[76:77], v[76:77] op_sel_hi:[1,0,0]\n"
"v_pk_fma_f32 v[18:19], v[18:19], v[76:77], v[76:77] op_sel_hi:[1,0,0]\n"
"v_pk_fma_f32 v[20:21], v[20:21], v[76:77], v[76:77] op_sel_hi:[1,0,0]\n"
"v_pk_fma_f32 v[22:23], v[22:23], v[76:77], v[76:77] op_sel_hi:[1,0,0]\n"
"v_rcp_f32 v16, v16\n"
"v_rcp_f32 v17, v17\n"
"v_rcp_f32 v18, v18\n"
"v_rcp_f32 v19, v19\n"
"v_rcp_f32 v20, v20\n"
"v_rcp_f32 v21, v21\n"
"v_rcp_f32 v22, v22\n"
"v_rcp_f32 v23, v23\n"
"v_pk_mul_f32 v[16:17], v[16:17], v[136:137]\n"
"v_pk_mul_f32 v[18:19], v[18:19], v[138:139]\n"
"v_pk_mul_f32 v[20:21], v[20:21], v[140:141]\n"
"v_pk_mul_f32 v[22:23], v[22:23], v[142:143]\n"
"v_cvt_pk_bf16_f32 v36, v16, v17\n"
"v_cvt_pk_bf16_f32 v37, v18, v19\n"
"v_cvt_pk_bf16_f32 v38, v20, v21\n"
"v_cvt_pk_bf16_f32 v39, v22, v23\n"
"buffer_store_dwordx4 v[36:39], v108, s[40:43], 0 offen offset:1024 sc1\n"
"v_pk_mul_f32 v[24:25], v[168:169], v[168:169]\n"
"v_pk_mul_f32 v[26:27], v[170:171], v[170:171]\n"
"v_pk_mul_f32 v[28:29], v[172:173], v[172:173]\n"
"v_pk_mul_f32 v[30:31], v[174:175], v[174:175]\n"
"v_pk_fma_f32 v[24:25], v[24:25], v[104:105], v[100:101] op_sel:[0,1,1]\n"
"v_pk_fma_f32 v[26:27], v[26:27], v[104:105], v[100:101] op_sel:[0,1,1]\n"
"v_pk_fma_f32 v[28:29], v[28:29], v[104:105], v[100:101] op_sel:[0,1,1]\n"
"v_pk_fma_f32 v[30:31], v[30:31], v[104:105], v[100:101] op_sel:[0,1,1]\n"
"v_pk_mul_f32 v[24:25], v[24:25], v[168:169]\n"
"v_pk_mul_f32 v[26:27], v[26:27], v[170:171]\n"
"v_pk_mul_f32 v[28:29], v[28:29], v[172:173]\n"
"v_pk_mul_f32 v[30:31], v[30:31], v[174:175]\n"
"v_exp_f32 v24, v24\n"
"v_exp_f32 v25, v25\n"
"v_exp_f32 v26, v26\n"
"v_exp_f32 v27, v27\n"
"v_exp_f32 v28, v28\n"
"v_exp_f32 v29, v29\n"
"v_exp_f32 v30, v30\n"
"v_exp_f32 v31, v31\n"
"v_pk_fma_f32 v[24:25], v[24:25], v[76:77], v[76:77] op_sel:[0,1,1]\n"
"v_pk_fma_f32 v[26:27], v[26:27], v[76:77], v[76:77] op_sel:[0,1,1]\n"
"v_pk_fma_f32 v[28:29], v[28:29], v[76:77], v[76:77] op_sel:[0,1,1]\n"
"v_pk_fma_f32 v[30:31], v[30:31], v[76:77], v[76:77] op_sel:[0,1,1]\n"
"v_rcp_f32 v24, v24\n"
"v_rcp_f32 v25, v25\n"
"v_rcp_f32 v26, v26\n"
"v_rcp_f32 v27, v27\n"
"v_rcp_f32 v28, v28\n"
"v_rcp_f32 v29, v29\n"
"v_rcp_f32 v30, v30\n"
"v_rcp_f32 v31, v31\n"
"v_pk_mul_f32 v[24:25], v[24:25], v[168:169]\n"
"v_pk_mul_f32 v[26:27], v[26:27], v[170:171]\n"
"v_pk_mul_f32 v[28:29], v[28:29], v[172:173]\n"
"v_pk_mul_f32 v[30:31], v[30:31], v[174:175]\n"
"v_cvt_pk_bf16_f32 v40, v24, v25\n"
"v_cvt_pk_bf16_f32 v41, v26, v27\n"
"v_cvt_pk_bf16_f32 v42, v28, v29\n"
"v_cvt_pk_bf16_f32 v43, v30, v31\n"
"buffer_store_dwordx4 v[40:43], v109, s[40:43], 0 offen offset:1024 sc1\n"
"v_pk_mul_f32 v[16:17], v[200:201], v[200:201]\n"
"v_pk_mul_f32 v[18:19], v[202:203], v[202:203]\n"
"v_pk_mul_f32 v[20:21], v[204:205], v[204:205]\n"
"v_pk_mul_f32 v[22:23], v[206:207], v[206:207]\n"
"v_pk_fma_f32 v[16:17], v[16:17], v[106:107], v[102:103] op_sel_hi:[1,0,0]\n"
"v_pk_fma_f32 v[18:19], v[18:19], v[106:107], v[102:103] op_sel_hi:[1,0,0]\n"
"v_pk_fma_f32 v[20:21], v[20:21], v[106:107], v[102:103] op_sel_hi:[1,0,0]\n"
"v_pk_fma_f32 v[22:23], v[22:23], v[106:107], v[102:103] op_sel_hi:[1,0,0]\n"
"v_pk_mul_f32 v[16:17], v[16:17], v[200:201]\n"
"v_pk_mul_f32 v[18:19], v[18:19], v[202:203]\n"
"v_pk_mul_f32 v[20:21], v[20:21], v[204:205]\n"
"v_pk_mul_f32 v[22:23], v[22:23], v[206:207]\n"
"v_exp_f32 v16, v16\n"
"v_exp_f32 v17, v17\n"
"v_exp_f32 v18, v18\n"
"v_exp_f32 v19, v19\n"
"v_exp_f32 v20, v20\n"
"v_exp_f32 v21, v21\n"
"v_exp_f32 v22, v22\n"
"v_exp_f32 v23, v23\n"
"v_pk_fma_f32 v[16:17], v[16:17], v[78:79], v[78:79] op_sel_hi:[1,0,0]\n"
"v_pk_fma_f32 v[18:19], v[18:19], v[78:79], v[78:79] op_sel_hi:[1,0,0]\n"
"v_pk_fma_f32 v[20:21], v[20:21], v[78:79], v[78:79] op_sel_hi:[1,0,0]\n"
"v_pk_fma_f32 v[22:23], v[22:23], v[78:79], v[78:79] op_sel_hi:[1,0,0]\n"
"v_rcp_f32 v16, v16\n"
"v_rcp_f32 v17, v17\n"
"v_rcp_f32 v18, v18\n"
"v_rcp_f32 v19, v19\n"
"v_rcp_f32 v20, v20\n"
"v_rcp_f32 v21, v21\n"
"v_rcp_f32 v22, v22\n"
"v_rcp_f32 v23, v23\n"
"v_pk_mul_f32 v[16:17], v[16:17], v[200:201]\n"
"v_pk_mul_f32 v[18:19], v[18:19], v[202:203]\n"
"v_pk_mul_f32 v[20:21], v[20:21], v[204:205]\n"
"v_pk_mul_f32 v[22:23], v[22:23], v[206:207]\n"
"v_cvt_pk_bf16_f32 v32, v16, v17\n"
"v_cvt_pk_bf16_f32 v33, v18, v19\n"
"v_cvt_pk_bf16_f32 v34, v20, v21\n"
"v_cvt_pk_bf16_f32 v35, v22, v23\n"
"buffer_store_dwordx4 v[32:35], v110, s[40:43], 0 offen offset:1024 sc1\n"
"v_pk_mul_f32 v[24:25], v[232:233], v[232:233]\n"
"v_pk_mul_f32 v[26:27], v[234:235], v[234:235]\n"
"v_pk_mul_f32 v[28:29], v[236:237], v[236:237]\n"
"v_pk_mul_f32 v[30:31], v[238:239], v[238:239]\n"
"v_pk_fma_f32 v[24:25], v[24:25], v[106:107], v[102:103] op_sel:[0,1,1]\n"
"v_pk_fma_f32 v[26:27], v[26:27], v[106:107], v[102:103] op_sel:[0,1,1]\n"
"v_pk_fma_f32 v[28:29], v[28:29], v[106:107], v[102:103] op_sel:[0,1,1]\n"
"v_pk_fma_f32 v[30:31], v[30:31], v[106:107], v[102:103] op_sel:[0,1,1]\n"
"v_pk_mul_f32 v[24:25], v[24:25], v[232:233]\n"
"v_pk_mul_f32 v[26:27], v[26:27], v[234:235]\n"
"v_pk_mul_f32 v[28:29], v[28:29], v[236:237]\n"
"v_pk_mul_f32 v[30:31], v[30:31], v[238:239]\n"
"v_exp_f32 v24, v24\n"
"v_exp_f32 v25, v25\n"
"v_exp_f32 v26, v26\n"
"v_exp_f32 v27, v27\n"
"v_exp_f32 v28, v28\n"
"v_exp_f32 v29, v29\n"
"v_exp_f32 v30, v30\n"
"v_exp_f32 v31, v31\n"
"v_pk_fma_f32 v[24:25], v[24:25], v[78:79], v[78:79] op_sel:[0,1,1]\n"
"v_pk_fma_f32 v[26:27], v[26:27], v[78:79], v[78:79] op_sel:[0,1,1]\n"
"v_pk_fma_f32 v[28:29], v[28:29], v[78:79], v[78:79] op_sel:[0,1,1]\n"
"v_pk_fma_f32 v[30:31], v[30:31], v[78:79], v[78:79] op_sel:[0,1,1]\n"
"v_rcp_f32 v24, v24\n"
"v_rcp_f32 v25, v25\n"
"v_rcp_f32 v26, v26\n"
"v_rcp_f32 v27, v27\n"
"v_rcp_f32 v28, v28\n"
"v_rcp_f32 v29, v29\n"
"v_rcp_f32 v30, v30\n"
"v_rcp_f32 v31, v31\n"
"v_pk_mul_f32 v[24:25], v[24:25], v[232:233]\n"
"v_pk_mul_f32 v[26:27], v[26:27], v[234:235]\n"
"v_pk_mul_f32 v[28:29], v[28:29], v[236:237]\n"
"v_pk_mul_f32 v[30:31], v[30:31], v[238:239]\n"
"v_cvt_pk_bf16_f32 v36, v24, v25\n"
"v_cvt_pk_bf16_f32 v37, v26, v27\n"
"v_cvt_pk_bf16_f32 v38, v28, v29\n"
"v_cvt_pk_bf16_f32 v39, v30, v31\n"
"buffer_store_dwordx4 v[36:39], v111, s[40:43], 0 offen offset:1024 sc1\n"
"v_pk_mul_f32 v[16:17], v[144:145], v[144:145]\n"
"v_pk_mul_f32 v[18:19], v[146:147], v[146:147]\n"
"v_pk_mul_f32 v[20:21], v[148:149], v[148:149]\n"
"v_pk_mul_f32 v[22:23], v[150:151], v[150:151]\n"
"v_pk_fma_f32 v[16:17], v[16:17], v[104:105], v[100:101] op_sel_hi:[1,0,0]\n"
"v_pk_fma_f32 v[18:19], v[18:19], v[104:105], v[100:101] op_sel_hi:[1,0,0]\n"
"v_pk_fma_f32 v[20:21], v[20:21], v[104:105], v[100:101] op_sel_hi:[1,0,0]\n"
"v_pk_fma_f32 v[22:23], v[22:23], v[104:105], v[100:101] op_sel_hi:[1,0,0]\n"
"v_pk_mul_f32 v[16:17], v[16:17], v[144:145]\n"
"v_pk_mul_f32 v[18:19], v[18:19], v[146:147]\n"
"v_pk_mul_f32 v[20:21], v[20:21], v[148:149]\n"
"v_pk_mul_f32 v[22:23], v[22:23], v[150:151]\n"
"v_exp_f32 v16, v16\n"
"v_exp_f32 v17, v17\n"
"v_exp_f32 v18, v18\n"
"v_exp_f32 v19, v19\n"
"v_exp_f32 v20, v20\n"
"v_exp_f32 v21, v21\n"
"v_exp_f32 v22, v22\n"
"v_exp_f32 v23, v23\n"
"v_pk_fma_f32 v[16:17], v[16:17], v[76:77], v[76:77] op_sel_hi:[1,0,0]\n"
"v_pk_fma_f32 v[18:19], v[18:19], v[76:77], v[76:77] op_sel_hi:[1,0,0]\n"
"v_pk_fma_f32 v[20:21], v[20:21], v[76:77], v[76:77] op_sel_hi:[1,0,0]\n"
"v_pk_fma_f32 v[22:23], v[22:23], v[76:77], v[76:77] op_sel_hi:[1,0,0]\n"
"v_rcp_f32 v16, v16\n"
"v_rcp_f32 v17, v17\n"
"v_rcp_f32 v18, v18\n"
"v_rcp_f32 v19, v19\n"
"v_rcp_f32 v20, v20\n"
"v_rcp_f32 v21, v21\n"
"v_rcp_f32 v22, v22\n"
"v_rcp_f32 v23, v23\n"
"v_pk_mul_f32 v[16:17], v[16:17], v[144:145]\n"
"v_pk_mul_f32 v[18:19], v[18:19], v[146:147]\n"
"v_pk_mul_f32 v[20:21], v[20:21], v[148:149]\n"
"v_pk_mul_f32 v[22:23], v[22:23], v[150:151]\n"
"v_cvt_pk_bf16_f32 v40, v16, v17\n"
"v_cvt_pk_bf16_f32 v41, v18, v19\n"
"v_cvt_pk_bf16_f32 v42, v20, v21\n"
"v_cvt_pk_bf16_f32 v43, v22, v23\n"
"buffer_store_dwordx4 v[40:43], v108, s[40:43], 0 offen offset:2048 sc1\n"
"v_pk_mul_f32 v[24:25], v[176:177], v[176:177]\n"
"v_pk_mul_f32 v[26:27], v[178:179], v[178:179]\n"
"v_pk_mul_f32 v[28:29], v[180:181], v[180:181]\n"
"v_pk_mul_f32 v[30:31], v[182:183], v[182:183]\n"
"v_pk_fma_f32 v[24:25], v[24:25], v[104:105], v[100:101] op_sel:[0,1,1]\n"
"v_pk_fma_f32 v[26:27], v[26:27], v[104:105], v[100:101] op_sel:[0,1,1]\n"
"v_pk_fma_f32 v[28:29], v[28:29], v[104:105], v[100:101] op_sel:[0,1,1]\n"
"v_pk_fma_f32 v[30:31], v[30:31], v[104:105], v[100:101] op_sel:[0,1,1]\n"
"v_pk_mul_f32 v[24:25], v[24:25], v[176:177]\n"
"v_pk_mul_f32 v[26:27], v[26:27], v[178:179]\n"
"v_pk_mul_f32 v[28:29], v[28:29], v[180:181]\n"
"v_pk_mul_f32 v[30:31], v[30:31], v[182:183]\n"
"v_exp_f32 v24, v24\n"
"v_exp_f32 v25, v25\n"
"v_exp_f32 v26, v26\n"
"v_exp_f32 v27, v27\n"
"v_exp_f32 v28, v28\n"
"v_exp_f32 v29, v29\n"
"v_exp_f32 v30, v30\n"
"v_exp_f32 v31, v31\n"
"v_pk_fma_f32 v[24:25], v[24:25], v[76:77], v[76:77] op_sel:[0,1,1]\n"
"v_pk_fma_f32 v[26:27], v[26:27], v[76:77], v[76:77] op_sel:[0,1,1]\n"
"v_pk_fma_f32 v[28:29], v[28:29], v[76:77], v[76:77] op_sel:[0,1,1]\n"
"v_pk_fma_f32 v[30:31], v[30:31], v[76:77], v[76:77] op_sel:[0,1,1]\n"
"v_rcp_f32 v24, v24\n"
"v_rcp_f32 v25, v25\n"
"v_rcp_f32 v26, v26\n"
"v_rcp_f32 v27, v27\n"
"v_rcp_f32 v28, v28\n"
"v_rcp_f32 v29, v29\n"
"v_rcp_f32 v30, v30\n"
"v_rcp_f32 v31, v31\n"
"v_pk_mul_f32 v[24:25], v[24:25], v[176:177]\n"
"v_pk_mul_f32 v[26:27], v[26:27], v[178:179]\n"
"v_pk_mul_f32 v[28:29], v[28:29], v[180:181]\n"
"v_pk_mul_f32 v[30:31], v[30:31], v[182:183]\n"
"v_cvt_pk_bf16_f32 v32, v24, v25\n"
"v_cvt_pk_bf16_f32 v33, v26, v27\n"
"v_cvt_pk_bf16_f32 v34, v28, v29\n"
"v_cvt_pk_bf16_f32 v35, v30, v31\n"
"buffer_store_dwordx4 v[32:35], v109, s[40:43], 0 offen offset:2048 sc1\n"
"v_pk_mul_f32 v[16:17], v[208:209], v[208:209]\n"
"v_pk_mul_f32 v[18:19], v[210:211], v[210:211]\n"
"v_pk_mul_f32 v[20:21], v[212:213], v[212:213]\n"
"v_pk_mul_f32 v[22:23], v[214:215], v[214:215]\n"
"v_pk_fma_f32 v[16:17], v[16:17], v[106:107], v[102:103] op_sel_hi:[1,0,0]\n"
"v_pk_fma_f32 v[18:19], v[18:19], v[106:107], v[102:103] op_sel_hi:[1,0,0]\n"
"v_pk_fma_f32 v[20:21], v[20:21], v[106:107], v[102:103] op_sel_hi:[1,0,0]\n"
"v_pk_fma_f32 v[22:23], v[22:23], v[106:107], v[102:103] op_sel_hi:[1,0,0]\n"
"v_pk_mul_f32 v[16:17], v[16:17], v[208:209]\n"
"v_pk_mul_f32 v[18:19], v[18:19], v[210:211]\n"
"v_pk_mul_f32 v[20:21], v[20:21], v[212:213]\n"
"v_pk_mul_f32 v[22:23], v[22:23], v[214:215]\n"
"v_exp_f32 v16, v16\n"
"v_exp_f32 v17, v17\n"
"v_exp_f32 v18, v18\n"
"v_exp_f32 v19, v19\n"
"v_exp_f32 v20, v20\n"
"v_exp_f32 v21, v21\n"
"v_exp_f32 v22, v22\n"
"v_exp_f32 v23, v23\n"
"v_pk_fma_f32 v[16:17], v[16:17], v[78:79], v[78:79] op_sel_hi:[1,0,0]\n"
"v_pk_fma_f32 v[18:19], v[18:19], v[78:79], v[78:79] op_sel_hi:[1,0,0]\n"
"v_pk_fma_f32 v[20:21], v[20:21], v[78:79], v[78:79] op_sel_hi:[1,0,0]\n"
"v_pk_fma_f32 v[22:23], v[22:23], v[78:79], v[78:79] op_sel_hi:[1,0,0]\n"
"v_rcp_f32 v16, v16\n"
"v_rcp_f32 v17, v17\n"
"v_rcp_f32 v18, v18\n"
"v_rcp_f32 v19, v19\n"
"v_rcp_f32 v20, v20\n"
"v_rcp_f32 v21, v21\n"
"v_rcp_f32 v22, v22\n"
"v_rcp_f32 v23, v23\n"
"v_pk_mul_f32 v[16:17], v[16:17], v[208:209]\n"
"v_pk_mul_f32 v[18:19], v[18:19], v[210:211]\n"
"v_pk_mul_f32 v[20:21], v[20:21], v[212:213]\n"
"v_pk_mul_f32 v[22:23], v[22:23], v[214:215]\n"
"v_cvt_pk_bf16_f32 v36, v16, v17\n"
"v_cvt_pk_bf16_f32 v37, v18, v19\n"
"v_cvt_pk_bf16_f32 v38, v20, v21\n"
"v_cvt_pk_bf16_f32 v39, v22, v23\n"
"buffer_store_dwordx4 v[36:39], v110, s[40:43], 0 offen offset:2048 sc1\n"
"v_pk_mul_f32 v[24:25], v[240:241], v[240:241]\n"
"v_pk_mul_f32 v[26:27], v[242:243], v[242:243]\n"
"v_pk_mul_f32 v[28:29], v[244:245], v[244:245]\n"
"v_pk_mul_f32 v[30:31], v[246:247], v[246:247]\n"
"v_pk_fma_f32 v[24:25], v[24:25], v[106:107], v[102:103] op_sel:[0,1,1]\n"
"v_pk_fma_f32 v[26:27], v[26:27], v[106:107], v[102:103] op_sel:[0,1,1]\n"
"v_pk_fma_f32 v[28:29], v[28:29], v[106:107], v[102:103] op_sel:[0,1,1]\n"
"v_pk_fma_f32 v[30:31], v[30:31], v[106:107], v[102:103] op_sel:[0,1,1]\n"
"v_pk_mul_f32 v[24:25], v[24:25], v[240:241]\n"
"v_pk_mul_f32 v[26:27], v[26:27], v[242:243]\n"
"v_pk_mul_f32 v[28:29], v[28:29], v[244:245]\n"
"v_pk_mul_f32 v[30:31], v[30:31], v[246:247]\n"
"v_exp_f32 v24, v24\n"
"v_exp_f32 v25, v25\n"
"v_exp_f32 v26, v26\n"
"v_exp_f32 v27, v27\n"
"v_exp_f32 v28, v28\n"
"v_exp_f32 v29, v29\n"
"v_exp_f32 v30, v30\n"
"v_exp_f32 v31, v31\n"
"v_pk_fma_f32 v[24:25], v[24:25], v[78:79], v[78:79] op_sel:[0,1,1]\n"
"v_pk_fma_f32 v[26:27], v[26:27], v[78:79], v[78:79] op_sel:[0,1,1]\n"
"v_pk_fma_f32 v[28:29], v[28:29], v[78:79], v[78:79] op_sel:[0,1,1]\n"
"v_pk_fma_f32 v[30:31], v[30:31], v[78:79], v[78:79] op_sel:[0,1,1]\n"
"v_rcp_f32 v24, v24\n"
"v_rcp_f32 v25, v25\n"
"v_rcp_f32 v26, v26\n"
"v_rcp_f32 v27, v27\n"
"v_rcp_f32 v28, v28\n"
"v_rcp_f32 v29, v29\n"
"v_rcp_f32 v30, v30\n"
"v_rcp_f32 v31, v31\n"
"v_pk_mul_f32 v[24:25], v[24:25], v[240:241]\n"
"v_pk_mul_f32 v[26:27], v[26:27], v[242:243]\n"
"v_pk_mul_f32 v[28:29], v[28:29], v[244:245]\n"
"v_pk_mul_f32 v[30:31], v[30:31], v[246:247]\n"
"v_cvt_pk_bf16_f32 v40, v24, v25\n"
"v_cvt_pk_bf16_f32 v41, v26, v27\n"
"v_cvt_pk_bf16_f32 v42, v28, v29\n"
"v_cvt_pk_bf16_f32 v43, v30, v31\n"
"buffer_store_dwordx4 v[40:43], v111, s[40:43], 0 offen offset:2048 sc1\n"
"v_pk_mul_f32 v[16:17], v[152:153], v[152:153]\n"
"v_pk_mul_f32 v[18:19], v[154:155], v[154:155]\n"
"v_pk_mul_f32 v[20:21], v[156:157], v[156:157]\n"
"v_pk_mul_f32 v[22:23], v[158:159], v[158:159]\n"
"v_pk_fma_f32 v[16:17], v[16:17], v[104:105], v[100:101] op_sel_hi:[1,0,0]\n"
"v_pk_fma_f32 v[18:19], v[18:19], v[104:105], v[100:101] op_sel_hi:[1,0,0]\n"
"v_pk_fma_f32 v[20:21], v[20:21], v[104:105], v[100:101] op_sel_hi:[1,0,0]\n"
"v_pk_fma_f32 v[22:23], v[22:23], v[104:105], v[100:101] op_sel_hi:[1,0,0]\n"
"v_pk_mul_f32 v[16:17], v[16:17], v[152:153]\n"
"v_pk_mul_f32 v[18:19], v[18:19], v[154:155]\n"
"v_pk_mul_f32 v[20:21], v[20:21], v[156:157]\n"
"v_pk_mul_f32 v[22:23], v[22:23], v[158:159]\n"
"v_exp_f32 v16, v16\n"
"v_exp_f32 v17, v17\n"
"v_exp_f32 v18, v18\n"
"v_exp_f32 v19, v19\n"
"v_exp_f32 v20, v20\n"
"v_exp_f32 v21, v21\n"
"v_exp_f32 v22, v22\n"
"v_exp_f32 v23, v23\n"
"v_pk_fma_f32 v[16:17], v[16:17], v[76:77], v[76:77] op_sel_hi:[1,0,0]\n"
"v_pk_fma_f32 v[18:19], v[18:19], v[76:77], v[76:77] op_sel_hi:[1,0,0]\n"
"v_pk_fma_f32 v[20:21], v[20:21], v[76:77], v[76:77] op_sel_hi:[1,0,0]\n"
"v_pk_fma_f32 v[22:23], v[22:23], v[76:77], v[76:77] op_sel_hi:[1,0,0]\n"
"v_rcp_f32 v16, v16\n"
"v_rcp_f32 v17, v17\n"
"v_rcp_f32 v18, v18\n"
"v_rcp_f32 v19, v19\n"
"v_rcp_f32 v20, v20\n"
"v_rcp_f32 v21, v21\n"
"v_rcp_f32 v22, v22\n"
"v_rcp_f32 v23, v23\n"
"v_pk_mul_f32 v[16:17], v[16:17], v[152:153]\n"
"v_pk_mul_f32 v[18:19], v[18:19], v[154:155]\n"
"v_pk_mul_f32 v[20:21], v[20:21], v[156:157]\n"
"v_pk_mul_f32 v[22:23], v[22:23], v[158:159]\n"
"v_cvt_pk_bf16_f32 v32, v16, v17\n"
"v_cvt_pk_bf16_f32 v33, v18, v19\n"
"v_cvt_pk_bf16_f32 v34, v20, v21\n"
"v_cvt_pk_bf16_f32 v35, v22, v23\n"
"buffer_store_dwordx4 v[32:35], v108, s[40:43], 0 offen offset:3072 sc1\n"
"v_pk_mul_f32 v[24:25], v[184:185], v[184:185]\n"
"v_pk_mul_f32 v[26:27], v[186:187], v[186:187]\n"
"v_pk_mul_f32 v[28:29], v[188:189], v[188:189]\n"
"v_pk_mul_f32 v[30:31], v[190:191], v[190:191]\n"
"v_pk_fma_f32 v[24:25], v[24:25], v[104:105], v[100:101] op_sel:[0,1,1]\n"
"v_pk_fma_f32 v[26:27], v[26:27], v[104:105], v[100:101] op_sel:[0,1,1]\n"
"v_pk_fma_f32 v[28:29], v[28:29], v[104:105], v[100:101] op_sel:[0,1,1]\n"
"v_pk_fma_f32 v[30:31], v[30:31], v[104:105], v[100:101] op_sel:[0,1,1]\n"
"v_pk_mul_f32 v[24:25], v[24:25], v[184:185]\n"
"v_pk_mul_f32 v[26:27], v[26:27], v[186:187]\n"
"v_pk_mul_f32 v[28:29], v[28:29], v[188:189]\n"
"v_pk_mul_f32 v[30:31], v[30:31], v[190:191]\n"
"v_exp_f32 v24, v24\n"
"v_exp_f32 v25, v25\n"
"v_exp_f32 v26, v26\n"
"v_exp_f32 v27, v27\n"
"v_exp_f32 v28, v28\n"
"v_exp_f32 v29, v29\n"
"v_exp_f32 v30, v30\n"
"v_exp_f32 v31, v31\n"
"v_pk_fma_f32 v[24:25], v[24:25], v[76:77], v[76:77] op_sel:[0,1,1]\n"
"v_pk_fma_f32 v[26:27], v[26:27], v[76:77], v[76:77] op_sel:[0,1,1]\n"
"v_pk_fma_f32 v[28:29], v[28:29], v[76:77], v[76:77] op_sel:[0,1,1]\n"
"v_pk_fma_f32 v[30:31], v[30:31], v[76:77], v[76:77] op_sel:[0,1,1]\n"
"v_rcp_f32 v24, v24\n"
"v_rcp_f32 v25, v25\n"
"v_rcp_f32 v26, v26\n"
"v_rcp_f32 v27, v27\n"
"v_rcp_f32 v28, v28\n"
"v_rcp_f32 v29, v29\n"
"v_rcp_f32 v30, v30\n"
"v_rcp_f32 v31, v31\n"
"v_pk_mul_f32 v[24:25], v[24:25], v[184:185]\n"
"v_pk_mul_f32 v[26:27], v[26:27], v[186:187]\n"
"v_pk_mul_f32 v[28:29], v[28:29], v[188:189]\n"
"v_pk_mul_f32 v[30:31], v[30:31], v[190:191]\n"
"v_cvt_pk_bf16_f32 v36, v24, v25\n"
"v_cvt_pk_bf16_f32 v37, v26, v27\n"
"v_cvt_pk_bf16_f32 v38, v28, v29\n"
"v_cvt_pk_bf16_f32 v39, v30, v31\n"
"buffer_store_dwordx4 v[36:39], v109, s[40:43], 0 offen offset:3072 sc1\n"
"v_pk_mul_f32 v[16:17], v[216:217], v[216:217]\n"
"v_pk_mul_f32 v[18:19], v[218:219], v[218:219]\n"
"v_pk_mul_f32 v[20:21], v[220:221], v[220:221]\n"
"v_pk_mul_f32 v[22:23], v[222:223], v[222:223]\n"
"v_pk_fma_f32 v[16:17], v[16:17], v[106:107], v[102:103] op_sel_hi:[1,0,0]\n"
"v_pk_fma_f32 v[18:19], v[18:19], v[106:107], v[102:103] op_sel_hi:[1,0,0]\n"
"v_pk_fma_f32 v[20:21], v[20:21], v[106:107], v[102:103] op_sel_hi:[1,0,0]\n"
"v_pk_fma_f32 v[22:23], v[22:23], v[106:107], v[102:103] op_sel_hi:[1,0,0]\n"
"v_pk_mul_f32 v[16:17], v[16:17], v[216:217]\n"
"v_pk_mul_f32 v[18:19], v[18:19], v[218:219]\n"
"v_pk_mul_f32 v[20:21], v[20:21], v[220:221]\n"
"v_pk_mul_f32 v[22:23], v[22:23], v[222:223]\n"
"v_exp_f32 v16, v16\n"
"v_exp_f32 v17, v17\n"
"v_exp_f32 v18, v18\n"
"v_exp_f32 v19, v19\n"
"v_exp_f32 v20, v20\n"
"v_exp_f32 v21, v21\n"
"v_exp_f32 v22, v22\n"
"v_exp_f32 v23, v23\n"
"v_pk_fma_f32 v[16:17], v[16:17], v[78:79], v[78:79] op_sel_hi:[1,0,0]\n"
"v_pk_fma_f32 v[18:19], v[18:19], v[78:79], v[78:79] op_sel_hi:[1,0,0]\n"
"v_pk_fma_f32 v[20:21], v[20:21], v[78:79], v[78:79] op_sel_hi:[1,0,0]\n"
"v_pk_fma_f32 v[22:23], v[22:23], v[78:79], v[78:79] op_sel_hi:[1,0,0]\n"
"v_rcp_f32 v16, v16\n"
"v_rcp_f32 v17, v17\n"
"v_rcp_f32 v18, v18\n"
"v_rcp_f32 v19, v19\n"
"v_rcp_f32 v20, v20\n"
"v_rcp_f32 v21, v21\n"
"v_rcp_f32 v22, v22\n"
"v_rcp_f32 v23, v23\n"
"v_pk_mul_f32 v[16:17], v[16:17], v[216:217]\n"
"v_pk_mul_f32 v[18:19], v[18:19], v[218:219]\n"
"v_pk_mul_f32 v[20:21], v[20:21], v[220:221]\n"
"v_pk_mul_f32 v[22:23], v[22:23], v[222:223]\n"
"v_cvt_pk_bf16_f32 v40, v16, v17\n"
"v_cvt_pk_bf16_f32 v41, v18, v19\n"
"v_cvt_pk_bf16_f32 v42, v20, v21\n"
"v_cvt_pk_bf16_f32 v43, v22, v23\n"
"buffer_store_dwordx4 v[40:43], v110, s[40:43], 0 offen offset:3072 sc1\n"
"v_pk_mul_f32 v[24:25], v[248:249], v[248:249]\n"
"v_pk_mul_f32 v[26:27], v[250:251], v[250:251]\n"
"v_pk_mul_f32 v[28:29], v[252:253], v[252:253]\n"
"v_pk_mul_f32 v[30:31], v[254:255], v[254:255]\n"
"v_pk_fma_f32 v[24:25], v[24:25], v[106:107], v[102:103] op_sel:[0,1,1]\n"
"v_pk_fma_f32 v[26:27], v[26:27], v[106:107], v[102:103] op_sel:[0,1,1]\n"
"v_pk_fma_f32 v[28:29], v[28:29], v[106:107], v[102:103] op_sel:[0,1,1]\n"
"v_pk_fma_f32 v[30:31], v[30:31], v[106:107], v[102:103] op_sel:[0,1,1]\n"
"v_pk_mul_f32 v[24:25], v[24:25], v[248:249]\n"
"v_pk_mul_f32 v[26:27], v[26:27], v[250:251]\n"
"v_pk_mul_f32 v[28:29], v[28:29], v[252:253]\n"
"v_pk_mul_f32 v[30:31], v[30:31], v[254:255]\n"
"v_exp_f32 v24, v24\n"
"v_exp_f32 v25, v25\n"
"v_exp_f32 v26, v26\n"
"v_exp_f32 v27, v27\n"
"v_exp_f32 v28, v28\n"
"v_exp_f32 v29, v29\n"
"v_exp_f32 v30, v30\n"
"v_exp_f32 v31, v31\n"
"v_pk_fma_f32 v[24:25], v[24:25], v[78:79], v[78:79] op_sel:[0,1,1]\n"
"v_pk_fma_f32 v[26:27], v[26:27], v[78:79], v[78:79] op_sel:[0,1,1]\n"
"v_pk_fma_f32 v[28:29], v[28:29], v[78:79], v[78:79] op_sel:[0,1,1]\n"
"v_pk_fma_f32 v[30:31], v[30:31], v[78:79], v[78:79] op_sel:[0,1,1]\n"
"v_rcp_f32 v24, v24\n"
"v_rcp_f32 v25, v25\n"
"v_rcp_f32 v26, v26\n"
"v_rcp_f32 v27, v27\n"
"v_rcp_f32 v28, v28\n"
"v_rcp_f32 v29, v29\n"
"v_rcp_f32 v30, v30\n"
"v_rcp_f32 v31, v31\n"
"v_pk_mul_f32 v[24:25], v[24:25], v[248:249]\n"
"v_pk_mul_f32 v[26:27], v[26:27], v[250:251]\n"
"v_pk_mul_f32 v[28:29], v[28:29], v[252:253]\n"
"v_pk_mul_f32 v[30:31], v[30:31], v[254:255]\n"
"v_cvt_pk_bf16_f32 v32, v24, v25\n"
"v_cvt_pk_bf16_f32 v33, v26, v27\n"
"v_cvt_pk_bf16_f32 v34, v28, v29\n"
"v_cvt_pk_bf16_f32 v35, v30, v31\n"
"buffer_store_dwordx4 v[32:35], v111, s[40:43], 0 offen offset:3072 sc1\n"
"s_nop 1\n"
    : "+{v[128:159]}"(acc[0]), "+{v[160:191]}"(acc[1]), "+{v[192:223]}"(acc[2]), "+{v[224:255]}"(acc[3])
    : "{v[64:79]}"(bv), "{v[80:95]}"(av), "{s[40:43]}"(mksrd(hm + rb0 * (MS / 2) + pn * 2048, 3 * MS + 4096)), "{v96}"(lane * 16) : "v16", "v17", "v18", "v19", "v20", "v21", "v22", "v23", "v24", "v25", "v26", "v27", "v28", "v29", "v30", "v31", "v32", "v33", "v34", "v35", "v36", "v37", "v38", "v39", "v40", "v41", "v42", "v43", "v100", "v101", "v102", "v103", "v104", "v105", "v106", "v107", "v108", "v109", "v110", "v111", "vcc", "scc", "memory");

}

DI void phase_mlp2(char* lds, unsigned wv) {
  PAR; const u16 *w2 = P->w2, *b2 = P->b2; u16* out = P->out; const float* mod = (const float*)(ws + O_MOD); char* scr = ws + O_SCR;
  const u16* hm = (const u16*)(scr + (17 << 20)); char* xch = scr + (13 << 20);
  { const unsigned sig = bid < 224 ? 17 + (((bid & 7) >> 1) * 2 + ((bid >> 3) & 1)) * 2 + (bid & 1) : 33; edge_signal<0>(tid, ws, sig, 14, sig); }
  unsigned pm, pn, s; s2_map(bid, pm, pn, s);
  const unsigned kp = wave & 1, rb0 = pm * 8 + (wave >> 1) * 4, rbk = pm * 8 + wave * 2 + s;
  const u32x4 xr = {(2 * kp + s) * MS, (2 * kp + 1 - s) * MS, (2 - 2 * kp + s) * MS, (3 - 2 * kp - s) * MS};
  u32x16 va; u32x4 so;
  asm volatile(
"v_mbcnt_lo_u32_b32 v116, -1, 0\n"
"v_mbcnt_hi_u32_b32 v116, -1, v116\n"
"v_bfe_u32 v117, v116, 2, 2\n"
"v_lshrrev_b32 v119, 4, v116\n"
"v_and_b32 v118, 3, v116\n"
"s_mov_b32 s60, 0\n"
"s_mul_i32 s61, s70, 65536\n"
"s_lshl_b32 s62, s70, 13\n"
"v_lshlrev_b32 v122, 4, v118\n"
"v_lshl_add_u32 v122, v119, 6, v122\n"
"v_xor_b32 v123, 32, v122\n"
"v_mul_u32_u24 v120, 2048, v117\n"
"v_add_u32 v104, v120, v122\n"
"v_add_u32 v105, v120, v123\n"
"v_add_u32 v105, 7168, v105\n"
"v_add_u32 v106, v120, v122\n"
"v_add_u32 v106, 14336, v106\n"
"v_add_u32 v107, v120, v123\n"
"v_add_u32 v107, 21504, v107\n"
"v_lshl_add_u32 v100, v116, 4, s72\n"
"v_lshl_add_u32 v101, v116, 4, s73\n"
"v_lshl_add_u32 v102, v116, 4, s74\n"
"v_lshl_add_u32 v103, v116, 4, s75\n"
"v_lshlrev_b32 v120, 3, v118\n"
"v_lshl_add_u32 v120, v117, 6, v120\n"
"v_lshl_add_u32 v120, v119, 10, v120\n"
"v_add_u32 v120, s71, v120\n"
"v_and_b32 v121, 1, v119\n"
"v_lshl_add_u32 v108, v121, 5, v120\n"
"v_xor_b32 v109, 32, v108\n"
"v_lshlrev_b32 v112, 4, v116\n"
"v_add_u32 v110, 65536, v108\n"
"v_add_u32 v111, 65536, v109\n"
    : "={v[100:115]}"(va), "={s[60:63]}"(so) : "{s70}"(wave), "{s71}"(kp * 16384), "{s[72:75]}"(xr) : "scc", "v116", "v117", "v118", "v119", "v120", "v121", "v122", "v123");
  const u32x4 sx = mksrd(hm + rb0 * (MS / 2) + s * 28672 + kp * 1024, 3 * MS + 57344), sw = mksrd(w2 + s * 1792 * 1024 + pn * 128, 1791 * 2048 + 256);
  asm volatile(
"s_add_u32 s61, s61, 524288\n"
"s_add_u32 m0, s62, 0\n"
"s_nop 0\n"
"buffer_load_dwordx4 v104, s[44:47], s61 offen lds\n"
"buffer_load_dwordx4 v105, s[44:47], s61 offen offset:1024 lds\n"
"buffer_load_dwordx4 v106, s[44:47], s61 offen offset:2048 lds\n"
"buffer_load_dwordx4 v107, s[44:47], s61 offen offset:3072 lds\n"
"s_add_u32 s61, s61, 32768\n"
"s_add_u32 m0, s62, 4096\n"
"s_nop 0\n"
"buffer_load_dwordx4 v104, s[44:47], s61 offen lds\n"
"buffer_load_dwordx4 v105, s[44:47], s61 offen offset:1024 lds\n"
"buffer_load_dwordx4 v106, s[44:47], s61 offen offset:2048 lds\n"
"buffer_load_dwordx4 v107, s[44:47], s61 offen offset:3072 lds\n"
"s_add_u32 s61, s61, 229376\n"
"s_add_u32 m0, s62, 32768\n"
"s_nop 0\n"
"buffer_load_dwordx4 v104, s[44:47], s61 offen lds\n"
"buffer_load_dwordx4 v105, s[44:47], s61 offen offset:1024 lds\n"
"buffer_load_dwordx4 v106, s[44:47], s61 offen offset:2048 lds\n"
"buffer_load_dwordx4 v107, s[44:47], s61 offen offset:3072 lds\n"
"s_add_u32 s61, s61, 32768\n"
"s_add_u32 m0, s62, 36864\n"
"s_nop 0\n"
"buffer_load_dwordx4 v104, s[44:47], s61 offen lds\n"
"buffer_load_dwordx4 v105, s[44:47], s61 offen offset:1024 lds\n"
"buffer_load_dwordx4 v106, s[44:47], s61 offen offset:2048 lds\n"
"buffer_load_dwordx4 v107, s[44:47], s61 offen offset:3072 lds\n"
"s_add_u32 s61, s61, 229376\n"
    : "+{s[60:63]}"(so) : "{v[100:115]}"(va), "{s[44:47]}"(sw) : "scc", "memory");
  __builtin_amdgcn_sched_barrier(0);
  const int lane = tid & 63, i = lane & 15, g = lane >> 4;
  const u32x4 sp = mksrd(mod + 5120 + pn * 128, 512), sq = mksrd(b2 + pn * 128 - 512, 1024 + 256);
  const u32x2 po = {(unsigned)lane * 16, (unsigned)lane * 16};
  f32x32 keep; const unsigned tile = pm * 8 + pn;
  edge_wait(tid, bid, ws, 17 + (pm >> 1) * 2 + s, 1);
  asm volatile(
"buffer_load_dwordx4 a[64:67], v100, s[40:43], s60 offen sc1\n"
"buffer_load_dwordx4 a[68:71], v101, s[40:43], s60 offen sc1\n"
"buffer_load_dwordx4 a[72:75], v102, s[40:43], s60 offen sc1\n"
"buffer_load_dwordx4 a[76:79], v103, s[40:43], s60 offen sc1\n"
"s_waitcnt vmcnt(28)\n"
"s_barrier\n"
"ds_read_b64_tr_b16 a[0:1], v110 offset:0\n"
"ds_read_b64_tr_b16 a[2:3], v110 offset:4096\n"
"ds_read_b64_tr_b16 a[4:5], v111 offset:0\n"
"ds_read_b64_tr_b16 a[6:7], v111 offset:4096\n"
"ds_read_b64_tr_b16 a[8:9], v110 offset:256\n"
"ds_read_b64_tr_b16 a[10:11], v110 offset:4352\n"
"ds_read_b64_tr_b16 a[12:13], v111 offset:256\n"
"ds_read_b64_tr_b16 a[14:15], v111 offset:4352\n"
"s_waitcnt vmcnt(3) lgkmcnt(6)\n"
"v_mfma_f32_16x16x32_bf16 v[128:131], a[0:3], a[64:67], 0\n"
"ds_read_b64_tr_b16 a[16:17], v110 offset:512\n"
"s_waitcnt lgkmcnt(5)\n"
"v_mfma_f32_16x16x32_bf16 v[132:135], a[4:7], a[64:67], 0\n"
"buffer_load_dwordx4 a[80:83], v100, s[40:43], s60 offen offset:1024 sc1\n"
"s_waitcnt vmcnt(3)\n"
"v_mfma_f32_16x16x32_bf16 v[160:163], a[0:3], a[68:71], 0\n"
"ds_read_b64_tr_b16 a[18:19], v110 offset:4608\n"
"v_mfma_f32_16x16x32_bf16 v[164:167], a[4:7], a[68:71], 0\n"
"ds_read_b64_tr_b16 a[20:21], v111 offset:512\n"
"s_waitcnt vmcnt(2)\n"
"v_mfma_f32_16x16x32_bf16 v[192:195], a[0:3], a[72:75], 0\n"
"buffer_load_dwordx4 a[84:87], v101, s[40:43], s60 offen offset:1024 sc1\n"
"v_mfma_f32_16x16x32_bf16 v[196:199], a[4:7], a[72:75], 0\n"
"ds_read_b64_tr_b16 a[22:23], v111 offset:4608\n"
"s_waitcnt vmcnt(2)\n"
"v_mfma_f32_16x16x32_bf16 v[224:227], a[0:3], a[76:79], 0\n"
"v_mfma_f32_16x16x32_bf16 v[228:231], a[4:7], a[76:79], 0\n"
"buffer_load_dwordx4 a[88:91], v102, s[40:43], s60 offen offset:1024 sc1\n"
"s_waitcnt lgkmcnt(4)\n"
"v_mfma_f32_16x16x32_bf16 v[136:139], a[8:11], a[64:67], 0\n"
"ds_read_b64_tr_b16 a[24:25], v110 offset:768\n"
"v_mfma_f32_16x16x32_bf16 v[140:143], a[12:15], a[64:67], 0\n"
"ds_read_b64_tr_b16 a[26:27], v110 offset:4864\n"
"v_mfma_f32_16x16x32_bf16 v[168:171], a[8:11], a[68:71], 0\n"
"buffer_load_dwordx4 a[92:95], v103, s[40:43], s60 offen offset:1024 sc1\n"
"s_add_u32 s60, s60, 4096\n"
"v_mfma_f32_16x16x32_bf16 v[172:175], a[12:15], a[68:71], 0\n"
"ds_read_b64_tr_b16 a[28:29], v111 offset:768\n"
"v_mfma_f32_16x16x32_bf16 v[200:203], a[8:11], a[72:75], 0\n"
"ds_read_b64_tr_b16 a[30:31], v111 offset:4864\n"
"v_mfma_f32_16x16x32_bf16 v[204:207], a[12:15], a[72:75], 0\n"
"buffer_load_dwordx4 a[96:99], v100, s[40:43], s60 offen sc1\n"
"v_mfma_f32_16x16x32_bf16 v[232:235], a[8:11], a[76:79], 0\n"
"v_mfma_f32_16x16x32_bf16 v[236:239], a[12:15], a[76:79], 0\n"
"s_waitcnt lgkmcnt(4)\n"
"v_mfma_f32_16x16x32_bf16 v[144:147], a[16:19], a[64:67], 0\n"
"buffer_load_dwordx4 a[100:103], v101, s[40:43], s60 offen sc1\n"
"v_mfma_f32_16x16x32_bf16 v[148:151], a[20:23], a[64:67], 0\n"
"ds_read_b64_tr_b16 a[0:1], v110 offset:8192\n"
"v_mfma_f32_16x16x32_bf16 v[176:179], a[16:19], a[68:71], 0\n"
"ds_read_b64_tr_b16 a[2:3], v110 offset:12288\n"
"v_mfma_f32_16x16x32_bf16 v[180:183], a[20:23], a[68:71], 0\n"
"buffer_load_dwordx4 a[104:107], v102, s[40:43], s60 offen sc1\n"
"v_mfma_f32_16x16x32_bf16 v[208:211], a[16:19], a[72:75], 0\n"
"ds_read_b64_tr_b16 a[4:5], v111 offset:8192\n"
"v_mfma_f32_16x16x32_bf16 v[212:215], a[20:23], a[72:75], 0\n"
"ds_read_b64_tr_b16 a[6:7], v111 offset:12288\n"
"v_mfma_f32_16x16x32_bf16 v[240:243], a[16:19], a[76:79], 0\n"
"buffer_load_dwordx4 a[108:111], v103, s[40:43], s60 offen sc1\n"
"v_mfma_f32_16x16x32_bf16 v[244:247], a[20:23], a[76:79], 0\n"
"s_waitcnt lgkmcnt(4)\n"
"v_mfma_f32_16x16x32_bf16 v[152:155], a[24:27], a[64:67], 0\n"
"ds_read_b64_tr_b16 a[8:9], v110 offset:8448\n"
"v_mfma_f32_16x16x32_bf16 v[156:159], a[28:31], a[64:67], 0\n"
"buffer_load_dwordx4 a[112:115], v100, s[40:43], s60 offen offset:1024 sc1\n"
"v_mfma_f32_16x16x32_bf16 v[184:187], a[24:27], a[68:71], 0\n"
"ds_read_b64_tr_b16 a[10:11], v110 offset:12544\n"
"v_mfma_f32_16x16x32_bf16 v[188:191], a[28:31], a[68:71], 0\n"
"ds_read_b64_tr_b16 a[12:13], v111 offset:8448\n"
"v_mfma_f32_16x16x32_bf16 v[216:219], a[24:27], a[72:75], 0\n"
"buffer_load_dwordx4 a[116:119], v101, s[40:43], s60 offen offset:1024 sc1\n"
"v_mfma_f32_16x16x32_bf16 v[220:223], a[28:31], a[72:75], 0\n"
"ds_read_b64_tr_b16 a[14:15], v111 offset:12544\n"
"v_mfma_f32_16x16x32_bf16 v[248:251], a[24:27], a[76:79], 0\n"
"v_mfma_f32_16x16x32_bf16 v[252:255], a[28:31], a[76:79], 0\n"
"buffer_load_dwordx4 a[120:123], v102, s[40:43], s60 offen offset:1024 sc1\n"
"s_waitcnt vmcnt(10) lgkmcnt(4)\n"
"v_mfma_f32_16x16x32_bf16 v[128:131], a[0:3], a[80:83], v[128:131]\n"
"ds_read_b64_tr_b16 a[16:17], v110 offset:8704\n"
"v_mfma_f32_16x16x32_bf16 v[132:135], a[4:7], a[80:83], v[132:135]\n"
"ds_read_b64_tr_b16 a[18:19], v110 offset:12800\n"
"s_waitcnt vmcnt(9)\n"
"v_mfma_f32_16x16x32_bf16 v[160:163], a[0:3], a[84:87], v[160:163]\n"
"buffer_load_dwordx4 a[124:127], v103, s[40:43], s60 offen offset:1024 sc1\n"
"s_add_u32 s60, s60, 4096\n"
"v_mfma_f32_16x16x32_bf16 v[164:167], a[4:7], a[84:87], v[164:167]\n"
"ds_read_b64_tr_b16 a[20:21], v111 offset:8704\n"
"s_waitcnt vmcnt(9)\n"
"v_mfma_f32_16x16x32_bf16 v[192:195], a[0:3], a[88:91], v[192:195]\n"
"ds_read_b64_tr_b16 a[22:23], v111 offset:12800\n"
"v_mfma_f32_16x16x32_bf16 v[196:199], a[4:7], a[88:91], v[196:199]\n"
"buffer_load_dwordx4 a[128:131], v100, s[40:43], s60 offen sc1\n"
"s_waitcnt vmcnt(9)\n"
"v_mfma_f32_16x16x32_bf16 v[224:227], a[0:3], a[92:95], v[224:227]\n"
"v_mfma_f32_16x16x32_bf16 v[228:231], a[4:7], a[92:95], v[228:231]\n"
"s_waitcnt lgkmcnt(4)\n"
"v_mfma_f32_16x16x32_bf16 v[136:139], a[8:11], a[80:83], v[136:139]\n"
"buffer_load_dwordx4 a[132:135], v101, s[40:43], s60 offen sc1\n"
"v_mfma_f32_16x16x32_bf16 v[140:143], a[12:15], a[80:83], v[140:143]\n"
"ds_read_b64_tr_b16 a[24:25], v110 offset:8960\n"
"v_mfma_f32_16x16x32_bf16 v[168:171], a[8:11], a[84:87], v[168:171]\n"
"ds_read_b64_tr_b16 a[26:27], v110 offset:13056\n"
"v_mfma_f32_16x16x32_bf16 v[172:175], a[12:15], a[84:87], v[172:175]\n"
"buffer_load_dwordx4 a[136:139], v102, s[40:43], s60 offen sc1\n"
"v_mfma_f32_16x16x32_bf16 v[200:203], a[8:11], a[88:91], v[200:203]\n"
"ds_read_b64_tr_b16 a[28:29], v111 offset:8960\n"
"v_mfma_f32_16x16x32_bf16 v[204:207], a[12:15], a[88:91], v[204:207]\n"
"ds_read_b64_tr_b16 a[30:31], v111 offset:13056\n"
"v_mfma_f32_16x16x32_bf16 v[232:235], a[8:11], a[92:95], v[232:235]\n"
"buffer_load_dwordx4 a[140:143], v103, s[40:43], s60 offen sc1\n"
"v_mfma_f32_16x16x32_bf16 v[236:239], a[12:15], a[92:95], v[236:239]\n"
"s_barrier\n"
"s_waitcnt lgkmcnt(4)\n"
"v_mfma_f32_16x16x32_bf16 v[144:147], a[16:19], a[80:83], v[144:147]\n"
"ds_read_b64_tr_b16 a[0:1], v110 offset:32768\n"
"v_mfma_f32_16x16x32_bf16 v[148:151], a[20:23], a[80:83], v[148:151]\n"
"buffer_load_dwordx4 a[144:147], v100, s[40:43], s60 offen offset:1024 sc1\n"
"v_mfma_f32_16x16x32_bf16 v[176:179], a[16:19], a[84:87], v[176:179]\n"
"ds_read_b64_tr_b16 a[2:3], v110 offset:36864\n"
"v_mfma_f32_16x16x32_bf16 v[180:183], a[20:23], a[84:87], v[180:183]\n"
"ds_read_b64_tr_b16 a[4:5], v110 offset:40960\n"
"v_mfma_f32_16x16x32_bf16 v[208:211], a[16:19], a[88:91], v[208:211]\n"
"buffer_load_dwordx4 a[148:151], v101, s[40:43], s60 offen offset:1024 sc1\n"
"v_mfma_f32_16x16x32_bf16 v[212:215], a[20:23], a[88:91], v[212:215]\n"
"ds_read_b64_tr_b16 a[6:7], v110 offset:45056\n"
"v_mfma_f32_16x16x32_bf16 v[240:243], a[16:19], a[92:95], v[240:243]\n"
"v_mfma_f32_16x16x32_bf16 v[244:247], a[20:23], a[92:95], v[244:247]\n"
"buffer_load_dwordx4 a[152:155], v102, s[40:43], s60 offen offset:1024 sc1\n"
"s_waitcnt lgkmcnt(4)\n"
"v_mfma_f32_16x16x32_bf16 v[152:155], a[24:27], a[80:83], v[152:155]\n"
"ds_read_b64_tr_b16 a[8:9], v111 offset:32768\n"
"v_mfma_f32_16x16x32_bf16 v[156:159], a[28:31], a[80:83], v[156:159]\n"
"ds_read_b64_tr_b16 a[10:11], v111 offset:36864\n"
"v_mfma_f32_16x16x32_bf16 v[184:187], a[24:27], a[84:87], v[184:187]\n"
"buffer_load_dwordx4 a[156:159], v103, s[40:43], s60 offen offset:1024 sc1\n"
"s_add_u32 s60, s60, 4096\n"
"v_mfma_f32_16x16x32_bf16 v[188:191], a[28:31], a[84:87], v[188:191]\n"
"ds_read_b64_tr_b16 a[12:13], v111 offset:40960\n"
"v_mfma_f32_16x16x32_bf16 v[216:219], a[24:27], a[88:91], v[216:219]\n"
"ds_read_b64_tr_b16 a[14:15], v111 offset:45056\n"
"v_mfma_f32_16x16x32_bf16 v[220:223], a[28:31], a[88:91], v[220:223]\n"
"buffer_load_dwordx4 v[124:127], v112, s[64:67], 0 offen\n"
"v_mfma_f32_16x16x32_bf16 v[248:251], a[24:27], a[92:95], v[248:251]\n"
"v_mfma_f32_16x16x32_bf16 v[252:255], a[28:31], a[92:95], v[252:255]\n"
"s_waitcnt vmcnt(13) lgkmcnt(4)\n"
"v_mfma_f32_16x16x32_bf16 v[128:131], a[0:3], a[96:99], v[128:131]\n"
"buffer_load_dwordx4 a[160:163], v100, s[40:43], s60 offen sc1\n"
"s_waitcnt vmcnt(13)\n"
"v_mfma_f32_16x16x32_bf16 v[128:131], a[4:7], a[112:115], v[128:131]\n"
"ds_read_b64_tr_b16 a[16:17], v110 offset:33024\n"
"v_mfma_f32_16x16x32_bf16 v[160:163], a[0:3], a[100:103], v[160:163]\n"
"ds_read_b64_tr_b16 a[18:19], v110 offset:37120\n"
"s_waitcnt vmcnt(12)\n"
"v_mfma_f32_16x16x32_bf16 v[160:163], a[4:7], a[116:119], v[160:163]\n"
"buffer_load_dwordx4 a[164:167], v101, s[40:43], s60 offen sc1\n"
"v_mfma_f32_16x16x32_bf16 v[192:195], a[0:3], a[104:107], v[192:195]\n"
"ds_read_b64_tr_b16 a[20:21], v110 offset:41216\n"
"s_waitcnt vmcnt(12)\n"
"v_mfma_f32_16x16x32_bf16 v[192:195], a[4:7], a[120:123], v[192:195]\n"
"ds_read_b64_tr_b16 a[22:23], v110 offset:45312\n"
"v_mfma_f32_16x16x32_bf16 v[224:227], a[0:3], a[108:111], v[224:227]\n"
"buffer_load_dwordx4 a[168:171], v102, s[40:43], s60 offen sc1\n"
"s_waitcnt vmcnt(12)\n"
"v_mfma_f32_16x16x32_bf16 v[224:227], a[4:7], a[124:127], v[224:227]\n"
"s_waitcnt lgkmcnt(4)\n"
"v_mfma_f32_16x16x32_bf16 v[132:135], a[8:11], a[96:99], v[132:135]\n"
"ds_read_b64_tr_b16 a[24:25], v111 offset:33024\n"
"v_mfma_f32_16x16x32_bf16 v[132:135], a[12:15], a[112:115], v[132:135]\n"
"buffer_load_dwordx4 a[172:175], v103, s[40:43], s60 offen sc1\n"
"v_mfma_f32_16x16x32_bf16 v[164:167], a[8:11], a[100:103], v[164:167]\n"
"ds_read_b64_tr_b16 a[26:27], v111 offset:37120\n"
"v_mfma_f32_16x16x32_bf16 v[164:167], a[12:15], a[116:119], v[164:167]\n"
"ds_read_b64_tr_b16 a[28:29], v111 offset:41216\n"
"v_mfma_f32_16x16x32_bf16 v[196:199], a[8:11], a[104:107], v[196:199]\n"
"buffer_load_dwordx4 a[176:179], v100, s[40:43], s60 offen offset:1024 sc1\n"
"v_mfma_f32_16x16x32_bf16 v[196:199], a[12:15], a[120:123], v[196:199]\n"
"ds_read_b64_tr_b16 a[30:31], v111 offset:45312\n"
"v_mfma_f32_16x16x32_bf16 v[228:231], a[8:11], a[108:111], v[228:231]\n"
"v_mfma_f32_16x16x32_bf16 v[228:231], a[12:15], a[124:127], v[228:231]\n"
"buffer_load_dwordx4 a[180:183], v101, s[40:43], s60 offen offset:1024 sc1\n"
"s_waitcnt lgkmcnt(4)\n"
"v_mfma_f32_16x16x32_bf16 v[136:139], a[16:19], a[96:99], v[136:139]\n"
"ds_read_b64_tr_b16 a[0:1], v110 offset:33280\n"
"v_mfma_f32_16x16x32_bf16 v[136:139], a[20:23], a[112:115], v[136:139]\n"
"ds_read_b64_tr_b16 a[2:3], v110 offset:37376\n"
"v_mfma_f32_16x16x32_bf16 v[168:171], a[16:19], a[100:103], v[168:171]\n"
"buffer_load_dwordx4 a[184:187], v102, s[40:43], s60 offen offset:1024 sc1\n"
"v_mfma_f32_16x16x32_bf16 v[168:171], a[20:23], a[116:119], v[168:171]\n"
"ds_read_b64_tr_b16 a[4:5], v110 offset:41472\n"
"v_mfma_f32_16x16x32_bf16 v[200:203], a[16:19], a[104:107], v[200:203]\n"
"ds_read_b64_tr_b16 a[6:7], v110 offset:45568\n"
"v_mfma_f32_16x16x32_bf16 v[200:203], a[20:23], a[120:123], v[200:203]\n"
"buffer_load_dwordx4 a[188:191], v103, s[40:43], s60 offen offset:1024 sc1\n"
"s_add_u32 s60, s60, 4096\n"
"v_mfma_f32_16x16x32_bf16 v[232:235], a[16:19], a[108:111], v[232:235]\n"
"v_mfma_f32_16x16x32_bf16 v[232:235], a[20:23], a[124:127], v[232:235]\n"
"s_waitcnt lgkmcnt(4)\n"
"v_mfma_f32_16x16x32_bf16 v[140:143], a[24:27], a[96:99], v[140:143]\n"
"ds_read_b64_tr_b16 a[8:9], v111 offset:33280\n"
"v_mfma_f32_16x16x32_bf16 v[140:143], a[28:31], a[112:115], v[140:143]\n"
"ds_read_b64_tr_b16 a[10:11], v111 offset:37376\n"
"v_mfma_f32_16x16x32_bf16 v[172:175], a[24:27], a[100:103], v[172:175]\n"
"ds_read_b64_tr_b16 a[12:13], v111 offset:41472\n"
"v_mfma_f32_16x16x32_bf16 v[172:175], a[28:31], a[116:119], v[172:175]\n"
"ds_read_b64_tr_b16 a[14:15], v111 offset:45568\n"
"v_mfma_f32_16x16x32_bf16 v[204:207], a[24:27], a[104:107], v[204:207]\n"
"v_mfma_f32_16x16x32_bf16 v[204:207], a[28:31], a[120:123], v[204:207]\n"
"v_mfma_f32_16x16x32_bf16 v[236:239], a[24:27], a[108:111], v[236:239]\n"
"v_mfma_f32_16x16x32_bf16 v[236:239], a[28:31], a[124:127], v[236:239]\n"
"s_waitcnt lgkmcnt(4)\n"
"v_mfma_f32_16x16x32_bf16 v[144:147], a[0:3], a[96:99], v[144:147]\n"
"ds_read_b64_tr_b16 a[16:17], v110 offset:33536\n"
"v_mfma_f32_16x16x32_bf16 v[144:147], a[4:7], a[112:115], v[144:147]\n"
"ds_read_b64_tr_b16 a[18:19], v110 offset:37632\n"
"v_mfma_f32_16x16x32_bf16 v[176:179], a[0:3], a[100:103], v[176:179]\n"
"ds_read_b64_tr_b16 a[20:21], v110 offset:41728\n"
"v_mfma_f32_16x16x32_bf16 v[176:179], a[4:7], a[116:119], v[176:179]\n"
"ds_read_b64_tr_b16 a[22:23], v110 offset:45824\n"
"v_mfma_f32_16x16x32_bf16 v[208:211], a[0:3], a[104:107], v[208:211]\n"
"v_mfma_f32_16x16x32_bf16 v[208:211], a[4:7], a[120:123], v[208:211]\n"
"v_mfma_f32_16x16x32_bf16 v[240:243], a[0:3], a[108:111], v[240:243]\n"
"v_mfma_f32_16x16x32_bf16 v[240:243], a[4:7], a[124:127], v[240:243]\n"
"s_waitcnt lgkmcnt(4)\n"
"v_mfma_f32_16x16x32_bf16 v[148:151], a[8:11], a[96:99], v[148:151]\n"
"ds_read_b64_tr_b16 a[24:25], v111 offset:33536\n"
"v_mfma_f32_16x16x32_bf16 v[148:151], a[12:15], a[112:115], v[148:151]\n"
"ds_read_b64_tr_b16 a[26:27], v111 offset:37632\n"
"v_mfma_f32_16x16x32_bf16 v[180:183], a[8:11], a[100:103], v[180:183]\n"
"ds_read_b64_tr_b16 a[28:29], v111 offset:41728\n"
"v_mfma_f32_16x16x32_bf16 v[180:183], a[12:15], a[116:119], v[180:183]\n"
"ds_read_b64_tr_b16 a[30:31], v111 offset:45824\n"
"v_mfma_f32_16x16x32_bf16 v[212:215], a[8:11], a[104:107], v[212:215]\n"
"v_mfma_f32_16x16x32_bf16 v[212:215], a[12:15], a[120:123], v[212:215]\n"
"v_mfma_f32_16x16x32_bf16 v[244:247], a[8:11], a[108:111], v[244:247]\n"
"v_mfma_f32_16x16x32_bf16 v[244:247], a[12:15], a[124:127], v[244:247]\n"
"s_barrier\n"
"s_waitcnt lgkmcnt(4)\n"
"v_mfma_f32_16x16x32_bf16 v[152:155], a[16:19], a[96:99], v[152:155]\n"
"ds_read_b64_tr_b16 a[0:1], v108 offset:0\n"
"v_mfma_f32_16x16x32_bf16 v[152:155], a[20:23], a[112:115], v[152:155]\n"
"buffer_load_dwordx4 a[64:67], v100, s[40:43], s60 offen sc1\n"
"v_mfma_f32_16x16x32_bf16 v[184:187], a[16:19], a[100:103], v[184:187]\n"
"ds_read_b64_tr_b16 a[2:3], v108 offset:4096\n"
"v_mfma_f32_16x16x32_bf16 v[184:187], a[20:23], a[116:119], v[184:187]\n"
"ds_read_b64_tr_b16 a[4:5], v108 offset:8192\n"
"v_mfma_f32_16x16x32_bf16 v[216:219], a[16:19], a[104:107], v[216:219]\n"
"ds_read_b64_tr_b16 a[6:7], v108 offset:12288\n"
"s_add_u32 m0, s62, 65536\n"
"v_mfma_f32_16x16x32_bf16 v[216:219], a[20:23], a[120:123], v[216:219]\n"
"buffer_load_dwordx4 v104, s[44:47], s61 offen lds\n"
"v_mfma_f32_16x16x32_bf16 v[248:251], a[16:19], a[108:111], v[248:251]\n"
"v_mfma_f32_16x16x32_bf16 v[248:251], a[20:23], a[124:127], v[248:251]\n"
"s_waitcnt lgkmcnt(4)\n"
"v_mfma_f32_16x16x32_bf16 v[156:159], a[24:27], a[96:99], v[156:159]\n"
"ds_read_b64_tr_b16 a[8:9], v109 offset:0\n"
"v_mfma_f32_16x16x32_bf16 v[156:159], a[28:31], a[112:115], v[156:159]\n"
"buffer_load_dwordx4 a[68:71], v101, s[40:43], s60 offen sc1\n"
"v_mfma_f32_16x16x32_bf16 v[188:191], a[24:27], a[100:103], v[188:191]\n"
"ds_read_b64_tr_b16 a[10:11], v109 offset:4096\n"
"v_mfma_f32_16x16x32_bf16 v[188:191], a[28:31], a[116:119], v[188:191]\n"
"ds_read_b64_tr_b16 a[12:13], v109 offset:8192\n"
"v_mfma_f32_16x16x32_bf16 v[220:223], a[24:27], a[104:107], v[220:223]\n"
"ds_read_b64_tr_b16 a[14:15], v109 offset:12288\n"
"v_mfma_f32_16x16x32_bf16 v[220:223], a[28:31], a[120:123], v[220:223]\n"
"buffer_load_dwordx4 v105, s[44:47], s61 offen offset:1024 lds\n"
"v_mfma_f32_16x16x32_bf16 v[252:255], a[24:27], a[108:111], v[252:255]\n"
"v_mfma_f32_16x16x32_bf16 v[252:255], a[28:31], a[124:127], v[252:255]\n"
"s_waitcnt vmcnt(4) lgkmcnt(4)\n"
"v_mfma_f32_16x16x32_bf16 v[128:131], a[0:3], a[128:131], v[128:131]\n"
"ds_read_b64_tr_b16 a[16:17], v108 offset:256\n"
"v_mfma_f32_16x16x32_bf16 v[128:131], a[4:7], a[144:147], v[128:131]\n"
"buffer_load_dwordx4 a[72:75], v102, s[40:43], s60 offen sc1\n"
"v_mfma_f32_16x16x32_bf16 v[160:163], a[0:3], a[132:135], v[160:163]\n"
"ds_read_b64_tr_b16 a[18:19], v108 offset:4352\n"
"v_mfma_f32_16x16x32_bf16 v[160:163], a[4:7], a[148:151], v[160:163]\n"
"ds_read_b64_tr_b16 a[20:21], v108 offset:8448\n"
"v_mfma_f32_16x16x32_bf16 v[192:195], a[0:3], a[136:139], v[192:195]\n"
"ds_read_b64_tr_b16 a[22:23], v108 offset:12544\n"
"v_mfma_f32_16x16x32_bf16 v[192:195], a[4:7], a[152:155], v[192:195]\n"
"buffer_load_dwordx4 v106, s[44:47], s61 offen offset:2048 lds\n"
"v_mfma_f32_16x16x32_bf16 v[224:227], a[0:3], a[140:143], v[224:227]\n"
"v_mfma_f32_16x16x32_bf16 v[224:227], a[4:7], a[156:159], v[224:227]\n"
"s_waitcnt lgkmcnt(4)\n"
"v_mfma_f32_16x16x32_bf16 v[132:135], a[8:11], a[128:131], v[132:135]\n"
"ds_read_b64_tr_b16 a[24:25], v109 offset:256\n"
"v_mfma_f32_16x16x32_bf16 v[132:135], a[12:15], a[144:147], v[132:135]\n"
"buffer_load_dwordx4 a[76:79], v103, s[40:43], s60 offen sc1\n"
"v_mfma_f32_16x16x32_bf16 v[164:167], a[8:11], a[132:135], v[164:167]\n"
"ds_read_b64_tr_b16 a[26:27], v109 offset:4352\n"
"v_mfma_f32_16x16x32_bf16 v[164:167], a[12:15], a[148:151], v[164:167]\n"
"ds_read_b64_tr_b16 a[28:29], v109 offset:8448\n"
"v_mfma_f32_16x16x32_bf16 v[196:199], a[8:11], a[136:139], v[196:199]\n"
"ds_read_b64_tr_b16 a[30:31], v109 offset:12544\n"
"v_mfma_f32_16x16x32_bf16 v[196:199], a[12:15], a[152:155], v[196:199]\n"
"buffer_load_dwordx4 v107, s[44:47], s61 offen offset:3072 lds\n"
"s_add_u32 s61, s61, 32768\n"
"v_mfma_f32_16x16x32_bf16 v[228:231], a[8:11], a[140:143], v[228:231]\n"
"v_mfma_f32_16x16x32_bf16 v[228:231], a[12:15], a[156:159], v[228:231]\n"
"s_waitcnt lgkmcnt(4)\n"
"v_mfma_f32_16x16x32_bf16 v[136:139], a[16:19], a[128:131], v[136:139]\n"
"buffer_load_dwordx4 a[80:83], v100, s[40:43], s60 offen offset:1024 sc1\n"
"v_mfma_f32_16x16x32_bf16 v[136:139], a[20:23], a[144:147], v[136:139]\n"
"ds_read_b64_tr_b16 a[0:1], v108 offset:512\n"
"v_mfma_f32_16x16x32_bf16 v[168:171], a[16:19], a[132:135], v[168:171]\n"
"ds_read_b64_tr_b16 a[2:3], v108 offset:4608\n"
"v_mfma_f32_16x16x32_bf16 v[168:171], a[20:23], a[148:151], v[168:171]\n"
"ds_read_b64_tr_b16 a[4:5], v108 offset:8704\n"
"s_add_u32 m0, s62, 69632\n"
"v_mfma_f32_16x16x32_bf16 v[200:203], a[16:19], a[136:139], v[200:203]\n"
"buffer_load_dwordx4 v104, s[44:47], s61 offen lds\n"
"v_mfma_f32_16x16x32_bf16 v[200:203], a[20:23], a[152:155], v[200:203]\n"
"ds_read_b64_tr_b16 a[6:7], v108 offset:12800\n"
"v_mfma_f32_16x16x32_bf16 v[232:235], a[16:19], a[140:143], v[232:235]\n"
"v_mfma_f32_16x16x32_bf16 v[232:235], a[20:23], a[156:159], v[232:235]\n"
"s_waitcnt lgkmcnt(4)\n"
"v_mfma_f32_16x16x32_bf16 v[140:143], a[24:27], a[128:131], v[140:143]\n"
"buffer_load_dwordx4 a[84:87], v101, s[40:43], s60 offen offset:1024 sc1\n"
"v_mfma_f32_16x16x32_bf16 v[140:143], a[28:31], a[144:147], v[140:143]\n"
"ds_read_b64_tr_b16 a[8:9], v109 offset:512\n"
"v_mfma_f32_16x16x32_bf16 v[172:175], a[24:27], a[132:135], v[172:175]\n"
"ds_read_b64_tr_b16 a[10:11], v109 offset:4608\n"
"v_mfma_f32_16x16x32_bf16 v[172:175], a[28:31], a[148:151], v[172:175]\n"
"ds_read_b64_tr_b16 a[12:13], v109 offset:8704\n"
"v_mfma_f32_16x16x32_bf16 v[204:207], a[24:27], a[136:139], v[204:207]\n"
"buffer_load_dwordx4 v105, s[44:47], s61 offen offset:1024 lds\n"
"v_mfma_f32_16x16x32_bf16 v[204:207], a[28:31], a[152:155], v[204:207]\n"
"ds_read_b64_tr_b16 a[14:15], v109 offset:12800\n"
"v_mfma_f32_16x16x32_bf16 v[236:239], a[24:27], a[140:143], v[236:239]\n"
"v_mfma_f32_16x16x32_bf16 v[236:239], a[28:31], a[156:159], v[236:239]\n"
"s_waitcnt lgkmcnt(4)\n"
"v_mfma_f32_16x16x32_bf16 v[144:147], a[0:3], a[128:131], v[144:147]\n"
"buffer_load_dwordx4 a[88:91], v102, s[40:43], s60 offen offset:1024 sc1\n"
"v_mfma_f32_16x16x32_bf16 v[144:147], a[4:7], a[144:147], v[144:147]\n"
"ds_read_b64_tr_b16 a[16:17], v108 offset:768\n"
"v_mfma_f32_16x16x32_bf16 v[176:179], a[0:3], a[132:135], v[176:179]\n"
"ds_read_b64_tr_b16 a[18:19], v108 offset:4864\n"
"v_mfma_f32_16x16x32_bf16 v[176:179], a[4:7], a[148:151], v[176:179]\n"
"ds_read_b64_tr_b16 a[20:21], v108 offset:8960\n"
"v_mfma_f32_16x16x32_bf16 v[208:211], a[0:3], a[136:139], v[208:211]\n"
"buffer_load_dwordx4 v106, s[44:47], s61 offen offset:2048 lds\n"
"v_mfma_f32_16x16x32_bf16 v[208:211], a[4:7], a[152:155], v[208:211]\n"
"ds_read_b64_tr_b16 a[22:23], v108 offset:13056\n"
"v_mfma_f32_16x16x32_bf16 v[240:243], a[0:3], a[140:143], v[240:243]\n"
"v_mfma_f32_16x16x32_bf16 v[240:243], a[4:7], a[156:159], v[240:243]\n"
"s_waitcnt lgkmcnt(4)\n"
"v_mfma_f32_16x16x32_bf16 v[148:151], a[8:11], a[128:131], v[148:151]\n"
"buffer_load_dwordx4 a[92:95], v103, s[40:43], s60 offen offset:1024 sc1\n"
"s_add_u32 s60, s60, 4096\n"
"v_mfma_f32_16x16x32_bf16 v[148:151], a[12:15], a[144:147], v[148:151]\n"
"ds_read_b64_tr_b16 a[24:25], v109 offset:768\n"
"v_mfma_f32_16x16x32_bf16 v[180:183], a[8:11], a[132:135], v[180:183]\n"
"ds_read_b64_tr_b16 a[26:27], v109 offset:4864\n"
"v_mfma_f32_16x16x32_bf16 v[180:183], a[12:15], a[148:151], v[180:183]\n"
"ds_read_b64_tr_b16 a[28:29], v109 offset:8960\n"
"v_mfma_f32_16x16x32_bf16 v[212:215], a[8:11], a[136:139], v[212:215]\n"
"buffer_load_dwordx4 v107, s[44:47], s61 offen offset:3072 lds\n"
"s_add_u32 s61, s61, 229376\n"
"v_mfma_f32_16x16x32_bf16 v[212:215], a[12:15], a[152:155], v[212:215]\n"
"ds_read_b64_tr_b16 a[30:31], v109 offset:13056\n"
"v_mfma_f32_16x16x32_bf16 v[244:247], a[8:11], a[140:143], v[244:247]\n"
"v_mfma_f32_16x16x32_bf16 v[244:247], a[12:15], a[156:159], v[244:247]\n"
"s_barrier\n"
"s_waitcnt lgkmcnt(4)\n"
"s_mov_b32 s63, 1\n"
"LG_%=:\n"
"v_mfma_f32_16x16x32_bf16 v[152:155], a[16:19], a[128:131], v[152:155]\n"
"ds_read_b64_tr_b16 a[0:1], v108 offset:32768\n"
"v_mfma_f32_16x16x32_bf16 v[152:155], a[20:23], a[144:147], v[152:155]\n"
"buffer_load_dwordx4 a[96:99], v100, s[40:43], s60 offen sc1\n"
"v_mfma_f32_16x16x32_bf16 v[184:187], a[16:19], a[132:135], v[184:187]\n"
"ds_read_b64_tr_b16 a[2:3], v108 offset:36864\n"
"v_mfma_f32_16x16x32_bf16 v[184:187], a[20:23], a[148:151], v[184:187]\n"
"ds_read_b64_tr_b16 a[4:5], v108 offset:40960\n"
"v_mfma_f32_16x16x32_bf16 v[216:219], a[16:19], a[136:139], v[216:219]\n"
"ds_read_b64_tr_b16 a[6:7], v108 offset:45056\n"
"s_add_u32 m0, s62, 98304\n"
"v_mfma_f32_16x16x32_bf16 v[216:219], a[20:23], a[152:155], v[216:219]\n"
"buffer_load_dwordx4 v104, s[44:47], s61 offen lds\n"
"v_mfma_f32_16x16x32_bf16 v[248:251], a[16:19], a[140:143], v[248:251]\n"
"v_mfma_f32_16x16x32_bf16 v[248:251], a[20:23], a[156:159], v[248:251]\n"
"s_waitcnt lgkmcnt(4)\n"
"v_mfma_f32_16x16x32_bf16 v[156:159], a[24:27], a[128:131], v[156:159]\n"
"ds_read_b64_tr_b16 a[8:9], v109 offset:32768\n"
"v_mfma_f32_16x16x32_bf16 v[156:159], a[28:31], a[144:147], v[156:159]\n"
"buffer_load_dwordx4 a[100:103], v101, s[40:43], s60 offen sc1\n"
"v_mfma_f32_16x16x32_bf16 v[188:191], a[24:27], a[132:135], v[188:191]\n"
"ds_read_b64_tr_b16 a[10:11], v109 offset:36864\n"
"v_mfma_f32_16x16x32_bf16 v[188:191], a[28:31], a[148:151], v[188:191]\n"
"ds_read_b64_tr_b16 a[12:13], v109 offset:40960\n"
"v_mfma_f32_16x16x32_bf16 v[220:223], a[24:27], a[136:139], v[220:223]\n"
"ds_read_b64_tr_b16 a[14:15], v109 offset:45056\n"
"v_mfma_f32_16x16x32_bf16 v[220:223], a[28:31], a[152:155], v[220:223]\n"
"buffer_load_dwordx4 v105, s[44:47], s61 offen offset:1024 lds\n"
"v_mfma_f32_16x16x32_bf16 v[252:255], a[24:27], a[140:143], v[252:255]\n"
"v_mfma_f32_16x16x32_bf16 v[252:255], a[28:31], a[156:159], v[252:255]\n"
"s_waitcnt lgkmcnt(4)\n"
"v_mfma_f32_16x16x32_bf16 v[128:131], a[0:3], a[160:163], v[128:131]\n"
"ds_read_b64_tr_b16 a[16:17], v108 offset:33024\n"
"v_mfma_f32_16x16x32_bf16 v[128:131], a[4:7], a[176:179], v[128:131]\n"
"buffer_load_dwordx4 a[104:107], v102, s[40:43], s60 offen sc1\n"
"v_mfma_f32_16x16x32_bf16 v[160:163], a[0:3], a[164:167], v[160:163]\n"
"ds_read_b64_tr_b16 a[18:19], v108 offset:37120\n"
"v_mfma_f32_16x16x32_bf16 v[160:163], a[4:7], a[180:183], v[160:163]\n"
"ds_read_b64_tr_b16 a[20:21], v108 offset:41216\n"
"v_mfma_f32_16x16x32_bf16 v[192:195], a[0:3], a[168:171], v[192:195]\n"
"ds_read_b64_tr_b16 a[22:23], v108 offset:45312\n"
"v_mfma_f32_16x16x32_bf16 v[192:195], a[4:7], a[184:187], v[192:195]\n"
"buffer_load_dwordx4 v106, s[44:47], s61 offen offset:2048 lds\n"
"v_mfma_f32_16x16x32_bf16 v[224:227], a[0:3], a[172:175], v[224:227]\n"
"v_mfma_f32_16x16x32_bf16 v[224:227], a[4:7], a[188:191], v[224:227]\n"
"s_waitcnt lgkmcnt(4)\n"
"v_mfma_f32_16x16x32_bf16 v[132:135], a[8:11], a[160:163], v[132:135]\n"
"ds_read_b64_tr_b16 a[24:25], v109 offset:33024\n"
"v_mfma_f32_16x16x32_bf16 v[132:135], a[12:15], a[176:179], v[132:135]\n"
"buffer_load_dwordx4 a[108:111], v103, s[40:43], s60 offen sc1\n"
"v_mfma_f32_16x16x32_bf16 v[164:167], a[8:11], a[164:167], v[164:167]\n"
"ds_read_b64_tr_b16 a[26:27], v109 offset:37120\n"
"v_mfma_f32_16x16x32_bf16 v[164:167], a[12:15], a[180:183], v[164:167]\n"
"ds_read_b64_tr_b16 a[28:29], v109 offset:41216\n"
"v_mfma_f32_16x16x32_bf16 v[196:199], a[8:11], a[168:171], v[196:199]\n"
"ds_read_b64_tr_b16 a[30:31], v109 offset:45312\n"
"v_mfma_f32_16x16x32_bf16 v[196:199], a[12:15], a[184:187], v[196:199]\n"
"buffer_load_dwordx4 v107, s[44:47], s61 offen offset:3072 lds\n"
"s_add_u32 s61, s61, 32768\n"
"v_mfma_f32_16x16x32_bf16 v[228:231], a[8:11], a[172:175], v[228:231]\n"
"v_mfma_f32_16x16x32_bf16 v[228:231], a[12:15], a[188:191], v[228:231]\n"
"s_waitcnt lgkmcnt(4)\n"
"v_mfma_f32_16x16x32_bf16 v[136:139], a[16:19], a[160:163], v[136:139]\n"
"buffer_load_dwordx4 a[112:115], v100, s[40:43], s60 offen offset:1024 sc1\n"
"v_mfma_f32_16x16x32_bf16 v[136:139], a[20:23], a[176:179], v[136:139]\n"
"ds_read_b64_tr_b16 a[0:1], v108 offset:33280\n"
"v_mfma_f32_16x16x32_bf16 v[168:171], a[16:19], a[164:167], v[168:171]\n"
"ds_read_b64_tr_b16 a[2:3], v108 offset:37376\n"
"v_mfma_f32_16x16x32_bf16 v[168:171], a[20:23], a[180:183], v[168:171]\n"
"ds_read_b64_tr_b16 a[4:5], v108 offset:41472\n"
"s_add_u32 m0, s62, 102400\n"
"v_mfma_f32_16x16x32_bf16 v[200:203], a[16:19], a[168:171], v[200:203]\n"
"buffer_load_dwordx4 v104, s[44:47], s61 offen lds\n"
"v_mfma_f32_16x16x32_bf16 v[200:203], a[20:23], a[184:187], v[200:203]\n"
"ds_read_b64_tr_b16 a[6:7], v108 offset:45568\n"
"v_mfma_f32_16x16x32_bf16 v[232:235], a[16:19], a[172:175], v[232:235]\n"
"v_mfma_f32_16x16x32_bf16 v[232:235], a[20:23], a[188:191], v[232:235]\n"
"s_waitcnt lgkmcnt(4)\n"
"v_mfma_f32_16x16x32_bf16 v[140:143], a[24:27], a[160:163], v[140:143]\n"
"buffer_load_dwordx4 a[116:119], v101, s[40:43], s60 offen offset:1024 sc1\n"
"v_mfma_f32_16x16x32_bf16 v[140:143], a[28:31], a[176:179], v[140:143]\n"
"ds_read_b64_tr_b16 a[8:9], v109 offset:33280\n"
"v_mfma_f32_16x16x32_bf16 v[172:175], a[24:27], a[164:167], v[172:175]\n"
"ds_read_b64_tr_b16 a[10:11], v109 offset:37376\n"
"v_mfma_f32_16x16x32_bf16 v[172:175], a[28:31], a[180:183], v[172:175]\n"
"ds_read_b64_tr_b16 a[12:13], v109 offset:41472\n"
"v_mfma_f32_16x16x32_bf16 v[204:207], a[24:27], a[168:171], v[204:207]\n"
"buffer_load_dwordx4 v105, s[44:47], s61 offen offset:1024 lds\n"
"v_mfma_f32_16x16x32_bf16 v[204:207], a[28:31], a[184:187], v[204:207]\n"
"ds_read_b64_tr_b16 a[14:15], v109 offset:45568\n"
"v_mfma_f32_16x16x32_bf16 v[236:239], a[24:27], a[172:175], v[236:239]\n"
"v_mfma_f32_16x16x32_bf16 v[236:239], a[28:31], a[188:191], v[236:239]\n"
"s_waitcnt lgkmcnt(4)\n"
"v_mfma_f32_16x16x32_bf16 v[144:147], a[0:3], a[160:163], v[144:147]\n"
"buffer_load_dwordx4 a[120:123], v102, s[40:43], s60 offen offset:1024 sc1\n"
"v_mfma_f32_16x16x32_bf16 v[144:147], a[4:7], a[176:179], v[144:147]\n"
"ds_read_b64_tr_b16 a[16:17], v108 offset:33536\n"
"v_mfma_f32_16x16x32_bf16 v[176:179], a[0:3], a[164:167], v[176:179]\n"
"ds_read_b64_tr_b16 a[18:19], v108 offset:37632\n"
"v_mfma_f32_16x16x32_bf16 v[176:179], a[4:7], a[180:183], v[176:179]\n"
"ds_read_b64_tr_b16 a[20:21], v108 offset:41728\n"
"v_mfma_f32_16x16x32_bf16 v[208:211], a[0:3], a[168:171], v[208:211]\n"
"buffer_load_dwordx4 v106, s[44:47], s61 offen offset:2048 lds\n"
"v_mfma_f32_16x16x32_bf16 v[208:211], a[4:7], a[184:187], v[208:211]\n"
"ds_read_b64_tr_b16 a[22:23], v108 offset:45824\n"
"v_mfma_f32_16x16x32_bf16 v[240:243], a[0:3], a[172:175], v[240:243]\n"
"v_mfma_f32_16x16x32_bf16 v[240:243], a[4:7], a[188:191], v[240:243]\n"
"s_waitcnt lgkmcnt(4)\n"
"v_mfma_f32_16x16x32_bf16 v[148:151], a[8:11], a[160:163], v[148:151]\n"
"buffer_load_dwordx4 a[124:127], v103, s[40:43], s60 offen offset:1024 sc1\n"
"s_add_u32 s60, s60, 4096\n"
"v_mfma_f32_16x16x32_bf16 v[148:151], a[12:15], a[176:179], v[148:151]\n"
"ds_read_b64_tr_b16 a[24:25], v109 offset:33536\n"
"v_mfma_f32_16x16x32_bf16 v[180:183], a[8:11], a[164:167], v[180:183]\n"
"ds_read_b64_tr_b16 a[26:27], v109 offset:37632\n"
"v_mfma_f32_16x16x32_bf16 v[180:183], a[12:15], a[180:183], v[180:183]\n"
"ds_read_b64_tr_b16 a[28:29], v109 offset:41728\n"
"v_mfma_f32_16x16x32_bf16 v[212:215], a[8:11], a[168:171], v[212:215]\n"
"buffer_load_dwordx4 v107, s[44:47], s61 offen offset:3072 lds\n"
"s_add_u32 s61, s61, 229376\n"
"v_mfma_f32_16x16x32_bf16 v[212:215], a[12:15], a[184:187], v[212:215]\n"
"ds_read_b64_tr_b16 a[30:31], v109 offset:45824\n"
"v_mfma_f32_16x16x32_bf16 v[244:247], a[8:11], a[172:175], v[244:247]\n"
"v_mfma_f32_16x16x32_bf16 v[244:247], a[12:15], a[188:191], v[244:247]\n"
"s_waitcnt vmcnt(16) lgkmcnt(4)\n"
"s_barrier\n"
"v_mfma_f32_16x16x32_bf16 v[152:155], a[16:19], a[160:163], v[152:155]\n"
"ds_read_b64_tr_b16 a[0:1], v110 offset:0\n"
"v_mfma_f32_16x16x32_bf16 v[152:155], a[20:23], a[176:179], v[152:155]\n"
"buffer_load_dwordx4 a[128:131], v100, s[40:43], s60 offen sc1\n"
"v_mfma_f32_16x16x32_bf16 v[184:187], a[16:19], a[164:167], v[184:187]\n"
"ds_read_b64_tr_b16 a[2:3], v110 offset:4096\n"
"v_mfma_f32_16x16x32_bf16 v[184:187], a[20:23], a[180:183], v[184:187]\n"
"ds_read_b64_tr_b16 a[4:5], v110 offset:8192\n"
"v_mfma_f32_16x16x32_bf16 v[216:219], a[16:19], a[168:171], v[216:219]\n"
"ds_read_b64_tr_b16 a[6:7], v110 offset:12288\n"
"s_add_u32 m0, s62, 0\n"
"v_mfma_f32_16x16x32_bf16 v[216:219], a[20:23], a[184:187], v[216:219]\n"
"buffer_load_dwordx4 v104, s[44:47], s61 offen lds\n"
"v_mfma_f32_16x16x32_bf16 v[248:251], a[16:19], a[172:175], v[248:251]\n"
"v_mfma_f32_16x16x32_bf16 v[248:251], a[20:23], a[188:191], v[248:251]\n"
"s_waitcnt lgkmcnt(4)\n"
"v_mfma_f32_16x16x32_bf16 v[156:159], a[24:27], a[160:163], v[156:159]\n"
"ds_read_b64_tr_b16 a[8:9], v111 offset:0\n"
"v_mfma_f32_16x16x32_bf16 v[156:159], a[28:31], a[176:179], v[156:159]\n"
"buffer_load_dwordx4 a[132:135], v101, s[40:43], s60 offen sc1\n"
"v_mfma_f32_16x16x32_bf16 v[188:191], a[24:27], a[164:167], v[188:191]\n"
"ds_read_b64_tr_b16 a[10:11], v111 offset:4096\n"
"v_mfma_f32_16x16x32_bf16 v[188:191], a[28:31], a[180:183], v[188:191]\n"
"ds_read_b64_tr_b16 a[12:13], v111 offset:8192\n"
"v_mfma_f32_16x16x32_bf16 v[220:223], a[24:27], a[168:171], v[220:223]\n"
"ds_read_b64_tr_b16 a[14:15], v111 offset:12288\n"
"v_mfma_f32_16x16x32_bf16 v[220:223], a[28:31], a[184:187], v[220:223]\n"
"buffer_load_dwordx4 v105, s[44:47], s61 offen offset:1024 lds\n"
"v_mfma_f32_16x16x32_bf16 v[252:255], a[24:27], a[172:175], v[252:255]\n"
"v_mfma_f32_16x16x32_bf16 v[252:255], a[28:31], a[188:191], v[252:255]\n"
"s_waitcnt lgkmcnt(4)\n"
"v_mfma_f32_16x16x32_bf16 v[128:131], a[0:3], a[64:67], v[128:131]\n"
"ds_read_b64_tr_b16 a[16:17], v110 offset:256\n"
"v_mfma_f32_16x16x32_bf16 v[128:131], a[4:7], a[80:83], v[128:131]\n"
"buffer_load_dwordx4 a[136:139], v102, s[40:43], s60 offen sc1\n"
"v_mfma_f32_16x16x32_bf16 v[160:163], a[0:3], a[68:71], v[160:163]\n"
"ds_read_b64_tr_b16 a[18:19], v110 offset:4352\n"
"v_mfma_f32_16x16x32_bf16 v[160:163], a[4:7], a[84:87], v[160:163]\n"
"ds_read_b64_tr_b16 a[20:21], v110 offset:8448\n"
"v_mfma_f32_16x16x32_bf16 v[192:195], a[0:3], a[72:75], v[192:195]\n"
"ds_read_b64_tr_b16 a[22:23], v110 offset:12544\n"
"v_mfma_f32_16x16x32_bf16 v[192:195], a[4:7], a[88:91], v[192:195]\n"
"buffer_load_dwordx4 v106, s[44:47], s61 offen offset:2048 lds\n"
"v_mfma_f32_16x16x32_bf16 v[224:227], a[0:3], a[76:79], v[224:227]\n"
"v_mfma_f32_16x16x32_bf16 v[224:227], a[4:7], a[92:95], v[224:227]\n"
"s_waitcnt lgkmcnt(4)\n"
"v_mfma_f32_16x16x32_bf16 v[132:135], a[8:11], a[64:67], v[132:135]\n"
"ds_read_b64_tr_b16 a[24:25], v111 offset:256\n"
"v_mfma_f32_16x16x32_bf16 v[132:135], a[12:15], a[80:83], v[132:135]\n"
"buffer_load_dwordx4 a[140:143], v103, s[40:43], s60 offen sc1\n"
"v_mfma_f32_16x16x32_bf16 v[164:167], a[8:11], a[68:71], v[164:167]\n"
"ds_read_b64_tr_b16 a[26:27], v111 offset:4352\n"
"v_mfma_f32_16x16x32_bf16 v[164:167], a[12:15], a[84:87], v[164:167]\n"
"ds_read_b64_tr_b16 a[28:29], v111 offset:8448\n"
"v_mfma_f32_16x16x32_bf16 v[196:199], a[8:11], a[72:75], v[196:199]\n"
"ds_read_b64_tr_b16 a[30:31], v111 offset:12544\n"
"v_mfma_f32_16x16x32_bf16 v[196:199], a[12:15], a[88:91], v[196:199]\n"
"buffer_load_dwordx4 v107, s[44:47], s61 offen offset:3072 lds\n"
"s_add_u32 s61, s61, 32768\n"
"v_mfma_f32_16x16x32_bf16 v[228:231], a[8:11], a[76:79], v[228:231]\n"
"v_mfma_f32_16x16x32_bf16 v[228:231], a[12:15], a[92:95], v[228:231]\n"
"s_waitcnt lgkmcnt(4)\n"
"v_mfma_f32_16x16x32_bf16 v[136:139], a[16:19], a[64:67], v[136:139]\n"
"buffer_load_dwordx4 a[144:147], v100, s[40:43], s60 offen offset:1024 sc1\n"
"v_mfma_f32_16x16x32_bf16 v[136:139], a[20:23], a[80:83], v[136:139]\n"
"ds_read_b64_tr_b16 a[0:1], v110 offset:512\n"
"v_mfma_f32_16x16x32_bf16 v[168:171], a[16:19], a[68:71], v[168:171]\n"
"ds_read_b64_tr_b16 a[2:3], v110 offset:4608\n"
"v_mfma_f32_16x16x32_bf16 v[168:171], a[20:23], a[84:87], v[168:171]\n"
"ds_read_b64_tr_b16 a[4:5], v110 offset:8704\n"
"s_add_u32 m0, s62, 4096\n"
"v_mfma_f32_16x16x32_bf16 v[200:203], a[16:19], a[72:75], v[200:203]\n"
"buffer_load_dwordx4 v104, s[44:47], s61 offen lds\n"
"v_mfma_f32_16x16x32_bf16 v[200:203], a[20:23], a[88:91], v[200:203]\n"
"ds_read_b64_tr_b16 a[6:7], v110 offset:12800\n"
"v_mfma_f32_16x16x32_bf16 v[232:235], a[16:19], a[76:79], v[232:235]\n"
"v_mfma_f32_16x16x32_bf16 v[232:235], a[20:23], a[92:95], v[232:235]\n"
"s_waitcnt lgkmcnt(4)\n"
"v_mfma_f32_16x16x32_bf16 v[140:143], a[24:27], a[64:67], v[140:143]\n"
"buffer_load_dwordx4 a[148:151], v101, s[40:43], s60 offen offset:1024 sc1\n"
"v_mfma_f32_16x16x32_bf16 v[140:143], a[28:31], a[80:83], v[140:143]\n"
"ds_read_b64_tr_b16 a[8:9], v111 offset:512\n"
"v_mfma_f32_16x16x32_bf16 v[172:175], a[24:27], a[68:71], v[172:175]\n"
"ds_read_b64_tr_b16 a[10:11], v111 offset:4608\n"
"v_mfma_f32_16x16x32_bf16 v[172:175], a[28:31], a[84:87], v[172:175]\n"
"ds_read_b64_tr_b16 a[12:13], v111 offset:8704\n"
"v_mfma_f32_16x16x32_bf16 v[204:207], a[24:27], a[72:75], v[204:207]\n"
"buffer_load_dwordx4 v105, s[44:47], s61 offen offset:1024 lds\n"
"v_mfma_f32_16x16x32_bf16 v[204:207], a[28:31], a[88:91], v[204:207]\n"
"ds_read_b64_tr_b16 a[14:15], v111 offset:12800\n"
"v_mfma_f32_16x16x32_bf16 v[236:239], a[24:27], a[76:79], v[236:239]\n"
"v_mfma_f32_16x16x32_bf16 v[236:239], a[28:31], a[92:95], v[236:239]\n"
"s_waitcnt lgkmcnt(4)\n"
"v_mfma_f32_16x16x32_bf16 v[144:147], a[0:3], a[64:67], v[144:147]\n"
"buffer_load_dwordx4 a[152:155], v102, s[40:43], s60 offen offset:1024 sc1\n"
"v_mfma_f32_16x16x32_bf16 v[144:147], a[4:7], a[80:83], v[144:147]\n"
"ds_read_b64_tr_b16 a[16:17], v110 offset:768\n"
"v_mfma_f32_16x16x32_bf16 v[176:179], a[0:3], a[68:71], v[176:179]\n"
"ds_read_b64_tr_b16 a[18:19], v110 offset:4864\n"
"v_mfma_f32_16x16x32_bf16 v[176:179], a[4:7], a[84:87], v[176:179]\n"
"ds_read_b64_tr_b16 a[20:21], v110 offset:8960\n"
"v_mfma_f32_16x16x32_bf16 v[208:211], a[0:3], a[72:75], v[208:211]\n"
"buffer_load_dwordx4 v106, s[44:47], s61 offen offset:2048 lds\n"
"v_mfma_f32_16x16x32_bf16 v[208:211], a[4:7], a[88:91], v[208:211]\n"
"ds_read_b64_tr_b16 a[22:23], v110 offset:13056\n"
"v_mfma_f32_16x16x32_bf16 v[240:243], a[0:3], a[76:79], v[240:243]\n"
"v_mfma_f32_16x16x32_bf16 v[240:243], a[4:7], a[92:95], v[240:243]\n"
"s_waitcnt lgkmcnt(4)\n"
"v_mfma_f32_16x16x32_bf16 v[148:151], a[8:11], a[64:67], v[148:151]\n"
"buffer_load_dwordx4 a[156:159], v103, s[40:43], s60 offen offset:1024 sc1\n"
"s_add_u32 s60, s60, 4096\n"
"v_mfma_f32_16x16x32_bf16 v[148:151], a[12:15], a[80:83], v[148:151]\n"
"ds_read_b64_tr_b16 a[24:25], v111 offset:768\n"
"v_mfma_f32_16x16x32_bf16 v[180:183], a[8:11], a[68:71], v[180:183]\n"
"ds_read_b64_tr_b16 a[26:27], v111 offset:4864\n"
"v_mfma_f32_16x16x32_bf16 v[180:183], a[12:15], a[84:87], v[180:183]\n"
"ds_read_b64_tr_b16 a[28:29], v111 offset:8960\n"
"v_mfma_f32_16x16x32_bf16 v[212:215], a[8:11], a[72:75], v[212:215]\n"
"buffer_load_dwordx4 v107, s[44:47], s61 offen offset:3072 lds\n"
"s_add_u32 s61, s61, 229376\n"
"v_mfma_f32_16x16x32_bf16 v[212:215], a[12:15], a[88:91], v[212:215]\n"
"ds_read_b64_tr_b16 a[30:31], v111 offset:13056\n"
"v_mfma_f32_16x16x32_bf16 v[244:247], a[8:11], a[76:79], v[244:247]\n"
"v_mfma_f32_16x16x32_bf16 v[244:247], a[12:15], a[92:95], v[244:247]\n"
"s_waitcnt vmcnt(16) lgkmcnt(4)\n"
"s_barrier\n"
"v_mfma_f32_16x16x32_bf16 v[152:155], a[16:19], a[64:67], v[152:155]\n"
"ds_read_b64_tr_b16 a[0:1], v110 offset:32768\n"
"v_mfma_f32_16x16x32_bf16 v[152:155], a[20:23], a[80:83], v[152:155]\n"
"buffer_load_dwordx4 a[160:163], v100, s[40:43], s60 offen sc1\n"
"v_mfma_f32_16x16x32_bf16 v[184:187], a[16:19], a[68:71], v[184:187]\n"
"ds_read_b64_tr_b16 a[2:3], v110 offset:36864\n"
"v_mfma_f32_16x16x32_bf16 v[184:187], a[20:23], a[84:87], v[184:187]\n"
"ds_read_b64_tr_b16 a[4:5], v110 offset:40960\n"
"v_mfma_f32_16x16x32_bf16 v[216:219], a[16:19], a[72:75], v[216:219]\n"
"ds_read_b64_tr_b16 a[6:7], v110 offset:45056\n"
"s_add_u32 m0, s62, 32768\n"
"v_mfma_f32_16x16x32_bf16 v[216:219], a[20:23], a[88:91], v[216:219]\n"
"buffer_load_dwordx4 v104, s[44:47], s61 offen lds\n"
"v_mfma_f32_16x16x32_bf16 v[248:251], a[16:19], a[76:79], v[248:251]\n"
"v_mfma_f32_16x16x32_bf16 v[248:251], a[20:23], a[92:95], v[248:251]\n"
"s_waitcnt lgkmcnt(4)\n"
"v_mfma_f32_16x16x32_bf16 v[156:159], a[24:27], a[64:67], v[156:159]\n"
"ds_read_b64_tr_b16 a[8:9], v111 offset:32768\n"
"v_mfma_f32_16x16x32_bf16 v[156:159], a[28:31], a[80:83], v[156:159]\n"
"buffer_load_dwordx4 a[164:167], v101, s[40:43], s60 offen sc1\n"
"v_mfma_f32_16x16x32_bf16 v[188:191], a[24:27], a[68:71], v[188:191]\n"
"ds_read_b64_tr_b16 a[10:11], v111 offset:36864\n"
"v_mfma_f32_16x16x32_bf16 v[188:191], a[28:31], a[84:87], v[188:191]\n"
"ds_read_b64_tr_b16 a[12:13], v111 offset:40960\n"
"v_mfma_f32_16x16x32_bf16 v[220:223], a[24:27], a[72:75], v[220:223]\n"
"ds_read_b64_tr_b16 a[14:15], v111 offset:45056\n"
"v_mfma_f32_16x16x32_bf16 v[220:223], a[28:31], a[88:91], v[220:223]\n"
"buffer_load_dwordx4 v105, s[44:47], s61 offen offset:1024 lds\n"
"v_mfma_f32_16x16x32_bf16 v[252:255], a[24:27], a[76:79], v[252:255]\n"
"v_mfma_f32_16x16x32_bf16 v[252:255], a[28:31], a[92:95], v[252:255]\n"
"s_waitcnt lgkmcnt(4)\n"
"v_mfma_f32_16x16x32_bf16 v[128:131], a[0:3], a[96:99], v[128:131]\n"
"ds_read_b64_tr_b16 a[16:17], v110 offset:33024\n"
"v_mfma_f32_16x16x32_bf16 v[128:131], a[4:7], a[112:115], v[128:131]\n"
"buffer_load_dwordx4 a[168:171], v102, s[40:43], s60 offen sc1\n"
"v_mfma_f32_16x16x32_bf16 v[160:163], a[0:3], a[100:103], v[160:163]\n"
"ds_read_b64_tr_b16 a[18:19], v110 offset:37120\n"
"v_mfma_f32_16x16x32_bf16 v[160:163], a[4:7], a[116:119], v[160:163]\n"
"ds_read_b64_tr_b16 a[20:21], v110 offset:41216\n"
"v_mfma_f32_16x16x32_bf16 v[192:195], a[0:3], a[104:107], v[192:195]\n"
"ds_read_b64_tr_b16 a[22:23], v110 offset:45312\n"
"v_mfma_f32_16x16x32_bf16 v[192:195], a[4:7], a[120:123], v[192:195]\n"
"buffer_load_dwordx4 v106, s[44:47], s61 offen offset:2048 lds\n"
"v_mfma_f32_16x16x32_bf16 v[224:227], a[0:3], a[108:111], v[224:227]\n"
"v_mfma_f32_16x16x32_bf16 v[224:227], a[4:7], a[124:127], v[224:227]\n"
"s_waitcnt lgkmcnt(4)\n"
"v_mfma_f32_16x16x32_bf16 v[132:135], a[8:11], a[96:99], v[132:135]\n"
"ds_read_b64_tr_b16 a[24:25], v111 offset:33024\n"
"v_mfma_f32_16x16x32_bf16 v[132:135], a[12:15], a[112:115], v[132:135]\n"
"buffer_load_dwordx4 a[172:175], v103, s[40:43], s60 offen sc1\n"
"v_mfma_f32_16x16x32_bf16 v[164:167], a[8:11], a[100:103], v[164:167]\n"
"ds_read_b64_tr_b16 a[26:27], v111 offset:37120\n"
"v_mfma_f32_16x16x32_bf16 v[164:167], a[12:15], a[116:119], v[164:167]\n"
"ds_read_b64_tr_b16 a[28:29], v111 offset:41216\n"
"v_mfma_f32_16x16x32_bf16 v[196:199], a[8:11], a[104:107], v[196:199]\n"
"ds_read_b64_tr_b16 a[30:31], v111 offset:45312\n"
"v_mfma_f32_16x16x32_bf16 v[196:199], a[12:15], a[120:123], v[196:199]\n"
"buffer_load_dwordx4 v107, s[44:47], s61 offen offset:3072 lds\n"
"s_add_u32 s61, s61, 32768\n"
"v_mfma_f32_16x16x32_bf16 v[228:231], a[8:11], a[108:111], v[228:231]\n"
"v_mfma_f32_16x16x32_bf16 v[228:231], a[12:15], a[124:127], v[228:231]\n"
"s_waitcnt lgkmcnt(4)\n"
"v_mfma_f32_16x16x32_bf16 v[136:139], a[16:19], a[96:99], v[136:139]\n"
"buffer_load_dwordx4 a[176:179], v100, s[40:43], s60 offen offset:1024 sc1\n"
"v_mfma_f32_16x16x32_bf16 v[136:139], a[20:23], a[112:115], v[136:139]\n"
"ds_read_b64_tr_b16 a[0:1], v110 offset:33280\n"
"v_mfma_f32_16x16x32_bf16 v[168:171], a[16:19], a[100:103], v[168:171]\n"
"ds_read_b64_tr_b16 a[2:3], v110 offset:37376\n"
"v_mfma_f32_16x16x32_bf16 v[168:171], a[20:23], a[116:119], v[168:171]\n"
"ds_read_b64_tr_b16 a[4:5], v110 offset:41472\n"
"s_add_u32 m0, s62, 36864\n"
"v_mfma_f32_16x16x32_bf16 v[200:203], a[16:19], a[104:107], v[200:203]\n"
"buffer_load_dwordx4 v104, s[44:47], s61 offen lds\n"
"v_mfma_f32_16x16x32_bf16 v[200:203], a[20:23], a[120:123], v[200:203]\n"
"ds_read_b64_tr_b16 a[6:7], v110 offset:45568\n"
"v_mfma_f32_16x16x32_bf16 v[232:235], a[16:19], a[108:111], v[232:235]\n"
"v_mfma_f32_16x16x32_bf16 v[232:235], a[20:23], a[124:127], v[232:235]\n"
"s_waitcnt lgkmcnt(4)\n"
"v_mfma_f32_16x16x32_bf16 v[140:143], a[24:27], a[96:99], v[140:143]\n"
"buffer_load_dwordx4 a[180:183], v101, s[40:43], s60 offen offset:1024 sc1\n"
"v_mfma_f32_16x16x32_bf16 v[140:143], a[28:31], a[112:115], v[140:143]\n"
"ds_read_b64_tr_b16 a[8:9], v111 offset:33280\n"
"v_mfma_f32_16x16x32_bf16 v[172:175], a[24:27], a[100:103], v[172:175]\n"
"ds_read_b64_tr_b16 a[10:11], v111 offset:37376\n"
"v_mfma_f32_16x16x32_bf16 v[172:175], a[28:31], a[116:119], v[172:175]\n"
"ds_read_b64_tr_b16 a[12:13], v111 offset:41472\n"
"v_mfma_f32_16x16x32_bf16 v[204:207], a[24:27], a[104:107], v[204:207]\n"
"buffer_load_dwordx4 v105, s[44:47], s61 offen offset:1024 lds\n"
"v_mfma_f32_16x16x32_bf16 v[204:207], a[28:31], a[120:123], v[204:207]\n"
"ds_read_b64_tr_b16 a[14:15], v111 offset:45568\n"
"v_mfma_f32_16x16x32_bf16 v[236:239], a[24:27], a[108:111], v[236:239]\n"
"v_mfma_f32_16x16x32_bf16 v[236:239], a[28:31], a[124:127], v[236:239]\n"
"s_waitcnt lgkmcnt(4)\n"
"v_mfma_f32_16x16x32_bf16 v[144:147], a[0:3], a[96:99], v[144:147]\n"
"buffer_load_dwordx4 a[184:187], v102, s[40:43], s60 offen offset:1024 sc1\n"
"v_mfma_f32_16x16x32_bf16 v[144:147], a[4:7], a[112:115], v[144:147]\n"
"ds_read_b64_tr_b16 a[16:17], v110 offset:33536\n"
"v_mfma_f32_16x16x32_bf16 v[176:179], a[0:3], a[100:103], v[176:179]\n"
"ds_read_b64_tr_b16 a[18:19], v110 offset:37632\n"
"v_mfma_f32_16x16x32_bf16 v[176:179], a[4:7], a[116:119], v[176:179]\n"
"ds_read_b64_tr_b16 a[20:21], v110 offset:41728\n"
"v_mfma_f32_16x16x32_bf16 v[208:211], a[0:3], a[104:107], v[208:211]\n"
"buffer_load_dwordx4 v106, s[44:47], s61 offen offset:2048 lds\n"
"v_mfma_f32_16x16x32_bf16 v[208:211], a[4:7], a[120:123], v[208:211]\n"
"ds_read_b64_tr_b16 a[22:23], v110 offset:45824\n"
"v_mfma_f32_16x16x32_bf16 v[240:243], a[0:3], a[108:111], v[240:243]\n"
"v_mfma_f32_16x16x32_bf16 v[240:243], a[4:7], a[124:127], v[240:243]\n"
"s_waitcnt lgkmcnt(4)\n"
"v_mfma_f32_16x16x32_bf16 v[148:151], a[8:11], a[96:99], v[148:151]\n"
"buffer_load_dwordx4 a[188:191], v103, s[40:43], s60 offen offset:1024 sc1\n"
"s_add_u32 s60, s60, 4096\n"
"v_mfma_f32_16x16x32_bf16 v[148:151], a[12:15], a[112:115], v[148:151]\n"
"ds_read_b64_tr_b16 a[24:25], v111 offset:33536\n"
"v_mfma_f32_16x16x32_bf16 v[180:183], a[8:11], a[100:103], v[180:183]\n"
"ds_read_b64_tr_b16 a[26:27], v111 offset:37632\n"
"v_mfma_f32_16x16x32_bf16 v[180:183], a[12:15], a[116:119], v[180:183]\n"
"ds_read_b64_tr_b16 a[28:29], v111 offset:41728\n"
"v_mfma_f32_16x16x32_bf16 v[212:215], a[8:11], a[104:107], v[212:215]\n"
"buffer_load_dwordx4 v107, s[44:47], s61 offen offset:3072 lds\n"
"s_add_u32 s61, s61, 229376\n"
"v_mfma_f32_16x16x32_bf16 v[212:215], a[12:15], a[120:123], v[212:215]\n"
"ds_read_b64_tr_b16 a[30:31], v111 offset:45824\n"
"v_mfma_f32_16x16x32_bf16 v[244:247], a[8:11], a[108:111], v[244:247]\n"
"v_mfma_f32_16x16x32_bf16 v[244:247], a[12:15], a[124:127], v[244:247]\n"
"s_waitcnt vmcnt(16) lgkmcnt(4)\n"
"s_barrier\n"
"v_mfma_f32_16x16x32_bf16 v[152:155], a[16:19], a[96:99], v[152:155]\n"
"ds_read_b64_tr_b16 a[0:1], v108 offset:0\n"
"v_mfma_f32_16x16x32_bf16 v[152:155], a[20:23], a[112:115], v[152:155]\n"
"buffer_load_dwordx4 a[64:67], v100, s[40:43], s60 offen sc1\n"
"v_mfma_f32_16x16x32_bf16 v[184:187], a[16:19], a[100:103], v[184:187]\n"
"ds_read_b64_tr_b16 a[2:3], v108 offset:4096\n"
"v_mfma_f32_16x16x32_bf16 v[184:187], a[20:23], a[116:119], v[184:187]\n"
"ds_read_b64_tr_b16 a[4:5], v108 offset:8192\n"
"v_mfma_f32_16x16x32_bf16 v[216:219], a[16:19], a[104:107], v[216:219]\n"
"ds_read_b64_tr_b16 a[6:7], v108 offset:12288\n"
"s_add_u32 m0, s62, 65536\n"
"v_mfma_f32_16x16x32_bf16 v[216:219], a[20:23], a[120:123], v[216:219]\n"
"buffer_load_dwordx4 v104, s[44:47], s61 offen lds\n"
"v_mfma_f32_16x16x32_bf16 v[248:251], a[16:19], a[108:111], v[248:251]\n"
"v_mfma_f32_16x16x32_bf16 v[248:251], a[20:23], a[124:127], v[248:251]\n"
"s_waitcnt lgkmcnt(4)\n"
"v_mfma_f32_16x16x32_bf16 v[156:159], a[24:27], a[96:99], v[156:159]\n"
"ds_read_b64_tr_b16 a[8:9], v109 offset:0\n"
"v_mfma_f32_16x16x32_bf16 v[156:159], a[28:31], a[112:115], v[156:159]\n"
"buffer_load_dwordx4 a[68:71], v101, s[40:43], s60 offen sc1\n"
"v_mfma_f32_16x16x32_bf16 v[188:191], a[24:27], a[100:103], v[188:191]\n"
"ds_read_b64_tr_b16 a[10:11], v109 offset:4096\n"
"v_mfma_f32_16x16x32_bf16 v[188:191], a[28:31], a[116:119], v[188:191]\n"
"ds_read_b64_tr_b16 a[12:13], v109 offset:8192\n"
"v_mfma_f32_16x16x32_bf16 v[220:223], a[24:27], a[104:107], v[220:223]\n"
"ds_read_b64_tr_b16 a[14:15], v109 offset:12288\n"
"v_mfma_f32_16x16x32_bf16 v[220:223], a[28:31], a[120:123], v[220:223]\n"
"buffer_load_dwordx4 v105, s[44:47], s61 offen offset:1024 lds\n"
"v_mfma_f32_16x16x32_bf16 v[252:255], a[24:27], a[108:111], v[252:255]\n"
"v_mfma_f32_16x16x32_bf16 v[252:255], a[28:31], a[124:127], v[252:255]\n"
"s_waitcnt lgkmcnt(4)\n"
"v_mfma_f32_16x16x32_bf16 v[128:131], a[0:3], a[128:131], v[128:131]\n"
"ds_read_b64_tr_b16 a[16:17], v108 offset:256\n"
"v_mfma_f32_16x16x32_bf16 v[128:131], a[4:7], a[144:147], v[128:131]\n"
"buffer_load_dwordx4 a[72:75], v102, s[40:43], s60 offen sc1\n"
"v_mfma_f32_16x16x32_bf16 v[160:163], a[0:3], a[132:135], v[160:163]\n"
"ds_read_b64_tr_b16 a[18:19], v108 offset:4352\n"
"v_mfma_f32_16x16x32_bf16 v[160:163], a[4:7], a[148:151], v[160:163]\n"
"ds_read_b64_tr_b16 a[20:21], v108 offset:8448\n"
"v_mfma_f32_16x16x32_bf16 v[192:195], a[0:3], a[136:139], v[192:195]\n"
"ds_read_b64_tr_b16 a[22:23], v108 offset:12544\n"
"v_mfma_f32_16x16x32_bf16 v[192:195], a[4:7], a[152:155], v[192:195]\n"
"buffer_load_dwordx4 v106, s[44:47], s61 offen offset:2048 lds\n"
"v_mfma_f32_16x16x32_bf16 v[224:227], a[0:3], a[140:143], v[224:227]\n"
"v_mfma_f32_16x16x32_bf16 v[224:227], a[4:7], a[156:159], v[224:227]\n"
"s_waitcnt lgkmcnt(4)\n"
"v_mfma_f32_16x16x32_bf16 v[132:135], a[8:11], a[128:131], v[132:135]\n"
"ds_read_b64_tr_b16 a[24:25], v109 offset:256\n"
"v_mfma_f32_16x16x32_bf16 v[132:135], a[12:15], a[144:147], v[132:135]\n"
"buffer_load_dwordx4 a[76:79], v103, s[40:43], s60 offen sc1\n"
"v_mfma_f32_16x16x32_bf16 v[164:167], a[8:11], a[132:135], v[164:167]\n"
"ds_read_b64_tr_b16 a[26:27], v109 offset:4352\n"
"v_mfma_f32_16x16x32_bf16 v[164:167], a[12:15], a[148:151], v[164:167]\n"
"ds_read_b64_tr_b16 a[28:29], v109 offset:8448\n"
"v_mfma_f32_16x16x32_bf16 v[196:199], a[8:11], a[136:139], v[196:199]\n"
"ds_read_b64_tr_b16 a[30:31], v109 offset:12544\n"
"v_mfma_f32_16x16x32_bf16 v[196:199], a[12:15], a[152:155], v[196:199]\n"
"buffer_load_dwordx4 v107, s[44:47], s61 offen offset:3072 lds\n"
"s_add_u32 s61, s61, 32768\n"
"v_mfma_f32_16x16x32_bf16 v[228:231], a[8:11], a[140:143], v[228:231]\n"
"v_mfma_f32_16x16x32_bf16 v[228:231], a[12:15], a[156:159], v[228:231]\n"
"s_waitcnt lgkmcnt(4)\n"
"v_mfma_f32_16x16x32_bf16 v[136:139], a[16:19], a[128:131], v[136:139]\n"
"buffer_load_dwordx4 a[80:83], v100, s[40:43], s60 offen offset:1024 sc1\n"
"v_mfma_f32_16x16x32_bf16 v[136:139], a[20:23], a[144:147], v[136:139]\n"
"ds_read_b64_tr_b16 a[0:1], v108 offset:512\n"
"v_mfma_f32_16x16x32_bf16 v[168:171], a[16:19], a[132:135], v[168:171]\n"
"ds_read_b64_tr_b16 a[2:3], v108 offset:4608\n"
"v_mfma_f32_16x16x32_bf16 v[168:171], a[20:23], a[148:151], v[168:171]\n"
"ds_read_b64_tr_b16 a[4:5], v108 offset:8704\n"
"s_add_u32 m0, s62, 69632\n"
"v_mfma_f32_16x16x32_bf16 v[200:203], a[16:19], a[136:139], v[200:203]\n"
"buffer_load_dwordx4 v104, s[44:47], s61 offen lds\n"
"v_mfma_f32_16x16x32_bf16 v[200:203], a[20:23], a[152:155], v[200:203]\n"
"ds_read_b64_tr_b16 a[6:7], v108 offset:12800\n"
"v_mfma_f32_16x16x32_bf16 v[232:235], a[16:19], a[140:143], v[232:235]\n"
"v_mfma_f32_16x16x32_bf16 v[232:235], a[20:23], a[156:159], v[232:235]\n"
"s_waitcnt lgkmcnt(4)\n"
"v_mfma_f32_16x16x32_bf16 v[140:143], a[24:27], a[128:131], v[140:143]\n"
"buffer_load_dwordx4 a[84:87], v101, s[40:43], s60 offen offset:1024 sc1\n"
"v_mfma_f32_16x16x32_bf16 v[140:143], a[28:31], a[144:147], v[140:143]\n"
"ds_read_b64_tr_b16 a[8:9], v109 offset:512\n"
"v_mfma_f32_16x16x32_bf16 v[172:175], a[24:27], a[132:135], v[172:175]\n"
"ds_read_b64_tr_b16 a[10:11], v109 offset:4608\n"
"v_mfma_f32_16x16x32_bf16 v[172:175], a[28:31], a[148:151], v[172:175]\n"
"ds_read_b64_tr_b16 a[12:13], v109 offset:8704\n"
"v_mfma_f32_16x16x32_bf16 v[204:207], a[24:27], a[136:139], v[204:207]\n"
"buffer_load_dwordx4 v105, s[44:47], s61 offen offset:1024 lds\n"
"v_mfma_f32_16x16x32_bf16 v[204:207], a[28:31], a[152:155], v[204:207]\n"
"ds_read_b64_tr_b16 a[14:15], v109 offset:12800\n"
"v_mfma_f32_16x16x32_bf16 v[236:239], a[24:27], a[140:143], v[236:239]\n"
"v_mfma_f32_16x16x32_bf16 v[236:239], a[28:31], a[156:159], v[236:239]\n"
"s_waitcnt lgkmcnt(4)\n"
"v_mfma_f32_16x16x32_bf16 v[144:147], a[0:3], a[128:131], v[144:147]\n"
"buffer_load_dwordx4 a[88:91], v102, s[40:43], s60 offen offset:1024 sc1\n"
"v_mfma_f32_16x16x32_bf16 v[144:147], a[4:7], a[144:147], v[144:147]\n"
"ds_read_b64_tr_b16 a[16:17], v108 offset:768\n"
"v_mfma_f32_16x16x32_bf16 v[176:179], a[0:3], a[132:135], v[176:179]\n"
"ds_read_b64_tr_b16 a[18:19], v108 offset:4864\n"
"v_mfma_f32_16x16x32_bf16 v[176:179], a[4:7], a[148:151], v[176:179]\n"
"ds_read_b64_tr_b16 a[20:21], v108 offset:8960\n"
"v_mfma_f32_16x16x32_bf16 v[208:211], a[0:3], a[136:139], v[208:211]\n"
"buffer_load_dwordx4 v106, s[44:47], s61 offen offset:2048 lds\n"
"v_mfma_f32_16x16x32_bf16 v[208:211], a[4:7], a[152:155], v[208:211]\n"
"ds_read_b64_tr_b16 a[22:23], v108 offset:13056\n"
"v_mfma_f32_16x16x32_bf16 v[240:243], a[0:3], a[140:143], v[240:243]\n"
"v_mfma_f32_16x16x32_bf16 v[240:243], a[4:7], a[156:159], v[240:243]\n"
"s_waitcnt lgkmcnt(4)\n"
"v_mfma_f32_16x16x32_bf16 v[148:151], a[8:11], a[128:131], v[148:151]\n"
"buffer_load_dwordx4 a[92:95], v103, s[40:43], s60 offen offset:1024 sc1\n"
"s_add_u32 s60, s60, 4096\n"
"v_mfma_f32_16x16x32_bf16 v[148:151], a[12:15], a[144:147], v[148:151]\n"
"ds_read_b64_tr_b16 a[24:25], v109 offset:768\n"
"v_mfma_f32_16x16x32_bf16 v[180:183], a[8:11], a[132:135], v[180:183]\n"
"ds_read_b64_tr_b16 a[26:27], v109 offset:4864\n"
"v_mfma_f32_16x16x32_bf16 v[180:183], a[12:15], a[148:151], v[180:183]\n"
"ds_read_b64_tr_b16 a[28:29], v109 offset:8960\n"
"v_mfma_f32_16x16x32_bf16 v[212:215], a[8:11], a[136:139], v[212:215]\n"
"buffer_load_dwordx4 v107, s[44:47], s61 offen offset:3072 lds\n"
"s_add_u32 s61, s61, 229376\n"
"v_mfma_f32_16x16x32_bf16 v[212:215], a[12:15], a[152:155], v[212:215]\n"
"ds_read_b64_tr_b16 a[30:31], v109 offset:13056\n"
"v_mfma_f32_16x16x32_bf16 v[244:247], a[8:11], a[140:143], v[244:247]\n"
"s_sub_u32 s63, s63, 1\n"
"v_mfma_f32_16x16x32_bf16 v[244:247], a[12:15], a[156:159], v[244:247]\n"
"s_waitcnt vmcnt(16) lgkmcnt(4)\n"
"s_barrier\n"
"s_cbranch_scc0 LG_%=\n"
"v_mfma_f32_16x16x32_bf16 v[152:155], a[16:19], a[128:131], v[152:155]\n"
"ds_read_b64_tr_b16 a[0:1], v108 offset:32768\n"
"v_mfma_f32_16x16x32_bf16 v[152:155], a[20:23], a[144:147], v[152:155]\n"
"buffer_load_dwordx4 a[96:99], v100, s[40:43], s60 offen sc1\n"
"v_mfma_f32_16x16x32_bf16 v[184:187], a[16:19], a[132:135], v[184:187]\n"
"ds_read_b64_tr_b16 a[2:3], v108 offset:36864\n"
"v_mfma_f32_16x16x32_bf16 v[184:187], a[20:23], a[148:151], v[184:187]\n"
"ds_read_b64_tr_b16 a[4:5], v108 offset:40960\n"
"v_mfma_f32_16x16x32_bf16 v[216:219], a[16:19], a[136:139], v[216:219]\n"
"ds_read_b64_tr_b16 a[6:7], v108 offset:45056\n"
"s_add_u32 m0, s62, 98304\n"
"v_mfma_f32_16x16x32_bf16 v[216:219], a[20:23], a[152:155], v[216:219]\n"
"buffer_load_dwordx4 v104, s[44:47], s61 offen lds\n"
"v_mfma_f32_16x16x32_bf16 v[248:251], a[16:19], a[140:143], v[248:251]\n"
"v_mfma_f32_16x16x32_bf16 v[248:251], a[20:23], a[156:159], v[248:251]\n"
"s_waitcnt lgkmcnt(4)\n"
"v_mfma_f32_16x16x32_bf16 v[156:159], a[24:27], a[128:131], v[156:159]\n"
"ds_read_b64_tr_b16 a[8:9], v109 offset:32768\n"
"v_mfma_f32_16x16x32_bf16 v[156:159], a[28:31], a[144:147], v[156:159]\n"
"buffer_load_dwordx4 a[100:103], v101, s[40:43], s60 offen sc1\n"
"v_mfma_f32_16x16x32_bf16 v[188:191], a[24:27], a[132:135], v[188:191]\n"
"ds_read_b64_tr_b16 a[10:11], v109 offset:36864\n"
"v_mfma_f32_16x16x32_bf16 v[188:191], a[28:31], a[148:151], v[188:191]\n"
"ds_read_b64_tr_b16 a[12:13], v109 offset:40960\n"
"v_mfma_f32_16x16x32_bf16 v[220:223], a[24:27], a[136:139], v[220:223]\n"
"ds_read_b64_tr_b16 a[14:15], v109 offset:45056\n"
"v_mfma_f32_16x16x32_bf16 v[220:223], a[28:31], a[152:155], v[220:223]\n"
"buffer_load_dwordx4 v105, s[44:47], s61 offen offset:1024 lds\n"
"v_mfma_f32_16x16x32_bf16 v[252:255], a[24:27], a[140:143], v[252:255]\n"
"v_mfma_f32_16x16x32_bf16 v[252:255], a[28:31], a[156:159], v[252:255]\n"
"s_waitcnt lgkmcnt(4)\n"
"v_mfma_f32_16x16x32_bf16 v[128:131], a[0:3], a[160:163], v[128:131]\n"
"ds_read_b64_tr_b16 a[16:17], v108 offset:33024\n"
"v_mfma_f32_16x16x32_bf16 v[128:131], a[4:7], a[176:179], v[128:131]\n"
"buffer_load_dwordx4 a[104:107], v102, s[40:43], s60 offen sc1\n"
"v_mfma_f32_16x16x32_bf16 v[160:163], a[0:3], a[164:167], v[160:163]\n"
"ds_read_b64_tr_b16 a[18:19], v108 offset:37120\n"
"v_mfma_f32_16x16x32_bf16 v[160:163], a[4:7], a[180:183], v[160:163]\n"
"ds_read_b64_tr_b16 a[20:21], v108 offset:41216\n"
"v_mfma_f32_16x16x32_bf16 v[192:195], a[0:3], a[168:171], v[192:195]\n"
"ds_read_b64_tr_b16 a[22:23], v108 offset:45312\n"
"v_mfma_f32_16x16x32_bf16 v[192:195], a[4:7], a[184:187], v[192:195]\n"
"buffer_load_dwordx4 v106, s[44:47], s61 offen offset:2048 lds\n"
"v_mfma_f32_16x16x32_bf16 v[224:227], a[0:3], a[172:175], v[224:227]\n"
"v_mfma_f32_16x16x32_bf16 v[224:227], a[4:7], a[188:191], v[224:227]\n"
"s_waitcnt lgkmcnt(4)\n"
"v_mfma_f32_16x16x32_bf16 v[132:135], a[8:11], a[160:163], v[132:135]\n"
"ds_read_b64_tr_b16 a[24:25], v109 offset:33024\n"
"v_mfma_f32_16x16x32_bf16 v[132:135], a[12:15], a[176:179], v[132:135]\n"
"buffer_load_dwordx4 a[108:111], v103, s[40:43], s60 offen sc1\n"
"v_mfma_f32_16x16x32_bf16 v[164:167], a[8:11], a[164:167], v[164:167]\n"
"ds_read_b64_tr_b16 a[26:27], v109 offset:37120\n"
"v_mfma_f32_16x16x32_bf16 v[164:167], a[12:15], a[180:183], v[164:167]\n"
"ds_read_b64_tr_b16 a[28:29], v109 offset:41216\n"
"v_mfma_f32_16x16x32_bf16 v[196:199], a[8:11], a[168:171], v[196:199]\n"
"ds_read_b64_tr_b16 a[30:31], v109 offset:45312\n"
"v_mfma_f32_16x16x32_bf16 v[196:199], a[12:15], a[184:187], v[196:199]\n"
"buffer_load_dwordx4 v107, s[44:47], s61 offen offset:3072 lds\n"
"s_add_u32 s61, s61, 32768\n"
"v_mfma_f32_16x16x32_bf16 v[228:231], a[8:11], a[172:175], v[228:231]\n"
"v_mfma_f32_16x16x32_bf16 v[228:231], a[12:15], a[188:191], v[228:231]\n"
"s_waitcnt lgkmcnt(4)\n"
"v_mfma_f32_16x16x32_bf16 v[136:139], a[16:19], a[160:163], v[136:139]\n"
"buffer_load_dwordx4 a[112:115], v100, s[40:43], s60 offen offset:1024 sc1\n"
"v_mfma_f32_16x16x32_bf16 v[136:139], a[20:23], a[176:179], v[136:139]\n"
"ds_read_b64_tr_b16 a[0:1], v108 offset:33280\n"
"v_mfma_f32_16x16x32_bf16 v[168:171], a[16:19], a[164:167], v[168:171]\n"
"ds_read_b64_tr_b16 a[2:3], v108 offset:37376\n"
"v_mfma_f32_16x16x32_bf16 v[168:171], a[20:23], a[180:183], v[168:171]\n"
"ds_read_b64_tr_b16 a[4:5], v108 offset:41472\n"
"s_add_u32 m0, s62, 102400\n"
"v_mfma_f32_16x16x32_bf16 v[200:203], a[16:19], a[168:171], v[200:203]\n"
"buffer_load_dwordx4 v104, s[44:47], s61 offen lds\n"
"v_mfma_f32_16x16x32_bf16 v[200:203], a[20:23], a[184:187], v[200:203]\n"
"ds_read_b64_tr_b16 a[6:7], v108 offset:45568\n"
"v_mfma_f32_16x16x32_bf16 v[232:235], a[16:19], a[172:175], v[232:235]\n"
"v_mfma_f32_16x16x32_bf16 v[232:235], a[20:23], a[188:191], v[232:235]\n"
"s_waitcnt lgkmcnt(4)\n"
"v_mfma_f32_16x16x32_bf16 v[140:143], a[24:27], a[160:163], v[140:143]\n"
"buffer_load_dwordx4 a[116:119], v101, s[40:43], s60 offen offset:1024 sc1\n"
"v_mfma_f32_16x16x32_bf16 v[140:143], a[28:31], a[176:179], v[140:143]\n"
"ds_read_b64_tr_b16 a[8:9], v109 offset:33280\n"
"v_mfma_f32_16x16x32_bf16 v[172:175], a[24:27], a[164:167], v[172:175]\n"
"ds_read_b64_tr_b16 a[10:11], v109 offset:37376\n"
"v_mfma_f32_16x16x32_bf16 v[172:175], a[28:31], a[180:183], v[172:175]\n"
"ds_read_b64_tr_b16 a[12:13], v109 offset:41472\n"
"v_mfma_f32_16x16x32_bf16 v[204:207], a[24:27], a[168:171], v[204:207]\n"
"buffer_load_dwordx4 v105, s[44:47], s61 offen offset:1024 lds\n"
"v_mfma_f32_16x16x32_bf16 v[204:207], a[28:31], a[184:187], v[204:207]\n"
"ds_read_b64_tr_b16 a[14:15], v109 offset:45568\n"
"v_mfma_f32_16x16x32_bf16 v[236:239], a[24:27], a[172:175], v[236:239]\n"
"v_mfma_f32_16x16x32_bf16 v[236:239], a[28:31], a[188:191], v[236:239]\n"
"s_waitcnt lgkmcnt(4)\n"
"v_mfma_f32_16x16x32_bf16 v[144:147], a[0:3], a[160:163], v[144:147]\n"
"buffer_load_dwordx4 a[120:123], v102, s[40:43], s60 offen offset:1024 sc1\n"
"v_mfma_f32_16x16x32_bf16 v[144:147], a[4:7], a[176:179], v[144:147]\n"
"ds_read_b64_tr_b16 a[16:17], v108 offset:33536\n"
"v_mfma_f32_16x16x32_bf16 v[176:179], a[0:3], a[164:167], v[176:179]\n"
"ds_read_b64_tr_b16 a[18:19], v108 offset:37632\n"
"v_mfma_f32_16x16x32_bf16 v[176:179], a[4:7], a[180:183], v[176:179]\n"
"ds_read_b64_tr_b16 a[20:21], v108 offset:41728\n"
"v_mfma_f32_16x16x32_bf16 v[208:211], a[0:3], a[168:171], v[208:211]\n"
"buffer_load_dwordx4 v106, s[44:47], s61 offen offset:2048 lds\n"
"v_mfma_f32_16x16x32_bf16 v[208:211], a[4:7], a[184:187], v[208:211]\n"
"ds_read_b64_tr_b16 a[22:23], v108 offset:45824\n"
"v_mfma_f32_16x16x32_bf16 v[240:243], a[0:3], a[172:175], v[240:243]\n"
"v_mfma_f32_16x16x32_bf16 v[240:243], a[4:7], a[188:191], v[240:243]\n"
"s_waitcnt lgkmcnt(4)\n"
"v_mfma_f32_16x16x32_bf16 v[148:151], a[8:11], a[160:163], v[148:151]\n"
"buffer_load_dwordx4 a[124:127], v103, s[40:43], s60 offen offset:1024 sc1\n"
"s_add_u32 s60, s60, 4096\n"
"v_mfma_f32_16x16x32_bf16 v[148:151], a[12:15], a[176:179], v[148:151]\n"
"ds_read_b64_tr_b16 a[24:25], v109 offset:33536\n"
"v_mfma_f32_16x16x32_bf16 v[180:183], a[8:11], a[164:167], v[180:183]\n"
"ds_read_b64_tr_b16 a[26:27], v109 offset:37632\n"
"v_mfma_f32_16x16x32_bf16 v[180:183], a[12:15], a[180:183], v[180:183]\n"
"ds_read_b64_tr_b16 a[28:29], v109 offset:41728\n"
"v_mfma_f32_16x16x32_bf16 v[212:215], a[8:11], a[168:171], v[212:215]\n"
"buffer_load_dwordx4 v107, s[44:47], s61 offen offset:3072 lds\n"
"s_add_u32 s61, s61, 229376\n"
"v_mfma_f32_16x16x32_bf16 v[212:215], a[12:15], a[184:187], v[212:215]\n"
"ds_read_b64_tr_b16 a[30:31], v109 offset:45824\n"
"v_mfma_f32_16x16x32_bf16 v[244:247], a[8:11], a[172:175], v[244:247]\n"
"v_mfma_f32_16x16x32_bf16 v[244:247], a[12:15], a[188:191], v[244:247]\n"
"s_waitcnt vmcnt(16) lgkmcnt(4)\n"
"s_barrier\n"
"v_mfma_f32_16x16x32_bf16 v[152:155], a[16:19], a[160:163], v[152:155]\n"
"ds_read_b64_tr_b16 a[0:1], v110 offset:0\n"
"s_add_u32 m0, s62, 135168\n"
"v_mfma_f32_16x16x32_bf16 v[152:155], a[20:23], a[176:179], v[152:155]\n"
"buffer_load_dwordx4 v94, s[52:55], 0 offen sc1 lds\n"
"v_mfma_f32_16x16x32_bf16 v[184:187], a[16:19], a[164:167], v[184:187]\n"
"ds_read_b64_tr_b16 a[2:3], v110 offset:4096\n"
"v_mfma_f32_16x16x32_bf16 v[184:187], a[20:23], a[180:183], v[184:187]\n"
"ds_read_b64_tr_b16 a[4:5], v110 offset:8192\n"
"v_mfma_f32_16x16x32_bf16 v[216:219], a[16:19], a[168:171], v[216:219]\n"
"buffer_load_dwordx4 v95, s[56:59], 0 offen offset:1024 sc1 lds\n"
"v_mfma_f32_16x16x32_bf16 v[216:219], a[20:23], a[184:187], v[216:219]\n"
"ds_read_b64_tr_b16 a[6:7], v110 offset:12288\n"
"v_mfma_f32_16x16x32_bf16 v[248:251], a[16:19], a[172:175], v[248:251]\n"
"v_mfma_f32_16x16x32_bf16 v[248:251], a[20:23], a[188:191], v[248:251]\n"
"s_waitcnt lgkmcnt(4)\n"
"v_mfma_f32_16x16x32_bf16 v[156:159], a[24:27], a[160:163], v[156:159]\n"
"ds_read_b64_tr_b16 a[8:9], v111 offset:0\n"
"v_mfma_f32_16x16x32_bf16 v[156:159], a[28:31], a[176:179], v[156:159]\n"
"ds_read_b64_tr_b16 a[10:11], v111 offset:4096\n"
"v_mfma_f32_16x16x32_bf16 v[188:191], a[24:27], a[164:167], v[188:191]\n"
"ds_read_b64_tr_b16 a[12:13], v111 offset:8192\n"
"v_mfma_f32_16x16x32_bf16 v[188:191], a[28:31], a[180:183], v[188:191]\n"
"ds_read_b64_tr_b16 a[14:15], v111 offset:12288\n"
"v_mfma_f32_16x16x32_bf16 v[220:223], a[24:27], a[168:171], v[220:223]\n"
"v_mfma_f32_16x16x32_bf16 v[220:223], a[28:31], a[184:187], v[220:223]\n"
"v_mfma_f32_16x16x32_bf16 v[252:255], a[24:27], a[172:175], v[252:255]\n"
"v_mfma_f32_16x16x32_bf16 v[252:255], a[28:31], a[188:191], v[252:255]\n"
"s_waitcnt lgkmcnt(4)\n"
"v_mfma_f32_16x16x32_bf16 v[128:131], a[0:3], a[64:67], v[128:131]\n"
"ds_read_b64_tr_b16 a[16:17], v110 offset:256\n"
"v_mfma_f32_16x16x32_bf16 v[128:131], a[4:7], a[80:83], v[128:131]\n"
"ds_read_b64_tr_b16 a[18:19], v110 offset:4352\n"
"v_mfma_f32_16x16x32_bf16 v[160:163], a[0:3], a[68:71], v[160:163]\n"
"ds_read_b64_tr_b16 a[20:21], v110 offset:8448\n"
"v_mfma_f32_16x16x32_bf16 v[160:163], a[4:7], a[84:87], v[160:163]\n"
"ds_read_b64_tr_b16 a[22:23], v110 offset:12544\n"
"v_mfma_f32_16x16x32_bf16 v[192:195], a[0:3], a[72:75], v[192:195]\n"
"v_mfma_f32_16x16x32_bf16 v[192:195], a[4:7], a[88:91], v[192:195]\n"
"v_mfma_f32_16x16x32_bf16 v[224:227], a[0:3], a[76:79], v[224:227]\n"
"v_mfma_f32_16x16x32_bf16 v[224:227], a[4:7], a[92:95], v[224:227]\n"
"s_waitcnt lgkmcnt(4)\n"
"v_mfma_f32_16x16x32_bf16 v[132:135], a[8:11], a[64:67], v[132:135]\n"
"ds_read_b64_tr_b16 a[24:25], v111 offset:256\n"
"v_mfma_f32_16x16x32_bf16 v[132:135], a[12:15], a[80:83], v[132:135]\n"
"ds_read_b64_tr_b16 a[26:27], v111 offset:4352\n"
"v_mfma_f32_16x16x32_bf16 v[164:167], a[8:11], a[68:71], v[164:167]\n"
"ds_read_b64_tr_b16 a[28:29], v111 offset:8448\n"
"v_mfma_f32_16x16x32_bf16 v[164:167], a[12:15], a[84:87], v[164:167]\n"
"ds_read_b64_tr_b16 a[30:31], v111 offset:12544\n"
"v_mfma_f32_16x16x32_bf16 v[196:199], a[8:11], a[72:75], v[196:199]\n"
"v_mfma_f32_16x16x32_bf16 v[196:199], a[12:15], a[88:91], v[196:199]\n"
"v_mfma_f32_16x16x32_bf16 v[228:231], a[8:11], a[76:79], v[228:231]\n"
"v_mfma_f32_16x16x32_bf16 v[228:231], a[12:15], a[92:95], v[228:231]\n"
"s_waitcnt lgkmcnt(4)\n"
"v_mfma_f32_16x16x32_bf16 v[136:139], a[16:19], a[64:67], v[136:139]\n"
"ds_read_b64_tr_b16 a[0:1], v110 offset:512\n"
"v_mfma_f32_16x16x32_bf16 v[136:139], a[20:23], a[80:83], v[136:139]\n"
"ds_read_b64_tr_b16 a[2:3], v110 offset:4608\n"
"v_mfma_f32_16x16x32_bf16 v[168:171], a[16:19], a[68:71], v[168:171]\n"
"ds_read_b64_tr_b16 a[4:5], v110 offset:8704\n"
"v_mfma_f32_16x16x32_bf16 v[168:171], a[20:23], a[84:87], v[168:171]\n"
"ds_read_b64_tr_b16 a[6:7], v110 offset:12800\n"
"v_mfma_f32_16x16x32_bf16 v[200:203], a[16:19], a[72:75], v[200:203]\n"
"v_mfma_f32_16x16x32_bf16 v[200:203], a[20:23], a[88:91], v[200:203]\n"
"v_mfma_f32_16x16x32_bf16 v[232:235], a[16:19], a[76:79], v[232:235]\n"
"v_mfma_f32_16x16x32_bf16 v[232:235], a[20:23], a[92:95], v[232:235]\n"
"s_waitcnt lgkmcnt(4)\n"
"v_mfma_f32_16x16x32_bf16 v[140:143], a[24:27], a[64:67], v[140:143]\n"
"ds_read_b64_tr_b16 a[8:9], v111 offset:512\n"
"v_mfma_f32_16x16x32_bf16 v[140:143], a[28:31], a[80:83], v[140:143]\n"
"ds_read_b64_tr_b16 a[10:11], v111 offset:4608\n"
"v_mfma_f32_16x16x32_bf16 v[172:175], a[24:27], a[68:71], v[172:175]\n"
"ds_read_b64_tr_b16 a[12:13], v111 offset:8704\n"
"v_mfma_f32_16x16x32_bf16 v[172:175], a[28:31], a[84:87], v[172:175]\n"
"ds_read_b64_tr_b16 a[14:15], v111 offset:12800\n"
"v_mfma_f32_16x16x32_bf16 v[204:207], a[24:27], a[72:75], v[204:207]\n"
"v_mfma_f32_16x16x32_bf16 v[204:207], a[28:31], a[88:91], v[204:207]\n"
"v_mfma_f32_16x16x32_bf16 v[236:239], a[24:27], a[76:79], v[236:239]\n"
"v_mfma_f32_16x16x32_bf16 v[236:239], a[28:31], a[92:95], v[236:239]\n"
"s_waitcnt vmcnt(2) lgkmcnt(4)\n"
"s_barrier\n"
"v_mfma_f32_16x16x32_bf16 v[144:147], a[0:3], a[64:67], v[144:147]\n"
"ds_read_b64_tr_b16 a[16:17], v110 offset:768\n"
"ds_read_b64_tr_b16 a[252:253], v111 offset:41728\n"
"v_mfma_f32_16x16x32_bf16 v[144:147], a[4:7], a[80:83], v[144:147]\n"
"ds_read_b64_tr_b16 a[18:19], v110 offset:4864\n"
"ds_read_b64_tr_b16 a[254:255], v111 offset:45824\n"
"v_mfma_f32_16x16x32_bf16 v[176:179], a[0:3], a[68:71], v[176:179]\n"
"buffer_load_dwordx4 v[124:127], v112, s[64:67], 0 offen\n"
"v_add_u32 v116, s62, v112\n"
"v_mfma_f32_16x16x32_bf16 v[176:179], a[4:7], a[84:87], v[176:179]\n"
"ds_read_b64_tr_b16 a[20:21], v110 offset:8960\n"
"v_add_u32 v113, 0, v116\n"
"v_mfma_f32_16x16x32_bf16 v[208:211], a[0:3], a[72:75], v[208:211]\n"
"ds_read_b64_tr_b16 a[22:23], v110 offset:13056\n"
"v_xor_b32 v114, 8192, v113\n"
"v_mfma_f32_16x16x32_bf16 v[208:211], a[4:7], a[88:91], v[208:211]\n"
"ds_read_b64_tr_b16 a[192:193], v110 offset:32768\n"
"v_add_u32 v116, 32768, v116\n"
"v_mfma_f32_16x16x32_bf16 v[240:243], a[0:3], a[76:79], v[240:243]\n"
"ds_read_b64_tr_b16 a[194:195], v110 offset:36864\n"
"v_xor_b32 v117, 8192, v116\n"
"v_mfma_f32_16x16x32_bf16 v[240:243], a[4:7], a[92:95], v[240:243]\n"
"ds_read_b64_tr_b16 a[196:197], v110 offset:40960\n"
"s_waitcnt lgkmcnt(9)\n"
"v_mfma_f32_16x16x32_bf16 v[148:151], a[8:11], a[64:67], v[148:151]\n"
"ds_read_b64_tr_b16 a[24:25], v111 offset:768\n"
"v_mfma_f32_16x16x32_bf16 v[148:151], a[12:15], a[80:83], v[148:151]\n"
"ds_read_b64_tr_b16 a[26:27], v111 offset:4864\n"
"v_mfma_f32_16x16x32_bf16 v[180:183], a[8:11], a[68:71], v[180:183]\n"
"ds_read_b64_tr_b16 a[28:29], v111 offset:8960\n"
"v_mfma_f32_16x16x32_bf16 v[180:183], a[12:15], a[84:87], v[180:183]\n"
"ds_read_b64_tr_b16 a[30:31], v111 offset:13056\n"
"v_mfma_f32_16x16x32_bf16 v[212:215], a[8:11], a[72:75], v[212:215]\n"
"ds_read_b64_tr_b16 a[198:199], v110 offset:45056\n"
"v_mfma_f32_16x16x32_bf16 v[212:215], a[12:15], a[88:91], v[212:215]\n"
"ds_read_b64_tr_b16 a[200:201], v111 offset:32768\n"
"v_mfma_f32_16x16x32_bf16 v[244:247], a[8:11], a[76:79], v[244:247]\n"
"ds_read_b64_tr_b16 a[202:203], v111 offset:36864\n"
"v_mfma_f32_16x16x32_bf16 v[244:247], a[12:15], a[92:95], v[244:247]\n"
"ds_read_b64_tr_b16 a[204:205], v111 offset:40960\n"
"s_waitcnt lgkmcnt(8)\n"
"v_mfma_f32_16x16x32_bf16 v[152:155], a[16:19], a[64:67], v[152:155]\n"
"ds_read_b64_tr_b16 a[206:207], v111 offset:45056\n"
"v_mfma_f32_16x16x32_bf16 v[152:155], a[20:23], a[80:83], v[152:155]\n"
"ds_read_b64_tr_b16 a[208:209], v110 offset:33024\n"
"v_mfma_f32_16x16x32_bf16 v[184:187], a[16:19], a[68:71], v[184:187]\n"
"ds_read_b64_tr_b16 a[210:211], v110 offset:37120\n"
"v_mfma_f32_16x16x32_bf16 v[184:187], a[20:23], a[84:87], v[184:187]\n"
"ds_read_b64_tr_b16 a[212:213], v110 offset:41216\n"
"v_mfma_f32_16x16x32_bf16 v[216:219], a[16:19], a[72:75], v[216:219]\n"
"ds_read_b64_tr_b16 a[214:215], v110 offset:45312\n"
"v_mfma_f32_16x16x32_bf16 v[216:219], a[20:23], a[88:91], v[216:219]\n"
"ds_read_b64_tr_b16 a[216:217], v111 offset:33024\n"
"v_mfma_f32_16x16x32_bf16 v[248:251], a[16:19], a[76:79], v[248:251]\n"
"ds_read_b64_tr_b16 a[218:219], v111 offset:37120\n"
"v_mfma_f32_16x16x32_bf16 v[248:251], a[20:23], a[92:95], v[248:251]\n"
"ds_read_b64_tr_b16 a[220:221], v111 offset:41216\n"
"s_waitcnt lgkmcnt(8)\n"
"v_mfma_f32_16x16x32_bf16 v[156:159], a[24:27], a[64:67], v[156:159]\n"
"ds_read_b64_tr_b16 a[222:223], v111 offset:45312\n"
"v_mfma_f32_16x16x32_bf16 v[156:159], a[28:31], a[80:83], v[156:159]\n"
"ds_read_b64_tr_b16 a[224:225], v110 offset:33280\n"
"v_mfma_f32_16x16x32_bf16 v[188:191], a[24:27], a[68:71], v[188:191]\n"
"ds_read_b64_tr_b16 a[226:227], v110 offset:37376\n"
"v_mfma_f32_16x16x32_bf16 v[188:191], a[28:31], a[84:87], v[188:191]\n"
"ds_read_b64_tr_b16 a[228:229], v110 offset:41472\n"
"v_mfma_f32_16x16x32_bf16 v[220:223], a[24:27], a[72:75], v[220:223]\n"
"ds_read_b64_tr_b16 a[230:231], v110 offset:45568\n"
"v_mfma_f32_16x16x32_bf16 v[220:223], a[28:31], a[88:91], v[220:223]\n"
"ds_read_b64_tr_b16 a[232:233], v111 offset:33280\n"
"v_mfma_f32_16x16x32_bf16 v[252:255], a[24:27], a[76:79], v[252:255]\n"
"ds_read_b64_tr_b16 a[234:235], v111 offset:37376\n"
"v_mfma_f32_16x16x32_bf16 v[252:255], a[28:31], a[92:95], v[252:255]\n"
"ds_read_b64_tr_b16 a[236:237], v111 offset:41472\n"
"v_mfma_f32_16x16x32_bf16 v[224:227], a[192:195], a[108:111], v[224:227]\n"
"ds_read_b64_tr_b16 a[238:239], v111 offset:45568\n"
"v_mfma_f32_16x16x32_bf16 v[224:227], a[196:199], a[124:127], v[224:227]\n"
"ds_read_b64_tr_b16 a[240:241], v110 offset:33536\n"
"v_mfma_f32_16x16x32_bf16 v[228:231], a[200:203], a[108:111], v[228:231]\n"
"ds_read_b64_tr_b16 a[242:243], v110 offset:37632\n"
"s_waitcnt lgkmcnt(8)\n"
"v_mfma_f32_16x16x32_bf16 v[228:231], a[204:207], a[124:127], v[228:231]\n"
"ds_read_b64_tr_b16 a[244:245], v110 offset:41728\n"
"v_mfma_f32_16x16x32_bf16 v[232:235], a[208:211], a[108:111], v[232:235]\n"
"ds_read_b64_tr_b16 a[246:247], v110 offset:45824\n"
"v_mfma_f32_16x16x32_bf16 v[232:235], a[212:215], a[124:127], v[232:235]\n"
"ds_read_b64_tr_b16 a[248:249], v111 offset:33536\n"
"v_mfma_f32_16x16x32_bf16 v[236:239], a[216:219], a[108:111], v[236:239]\n"
"ds_read_b64_tr_b16 a[250:251], v111 offset:37632\n"
"v_mfma_f32_16x16x32_bf16 v[236:239], a[220:223], a[124:127], v[236:239]\n"
"ds_write_b128 v113, v[224:227] offset:0\n"
"v_mfma_f32_16x16x32_bf16 v[240:243], a[224:227], a[108:111], v[240:243]\n"
"ds_write_b128 v113, v[228:231] offset:1024\n"
"s_waitcnt lgkmcnt(8)\n"
"v_mfma_f32_16x16x32_bf16 v[240:243], a[228:231], a[124:127], v[240:243]\n"
"v_mfma_f32_16x16x32_bf16 v[244:247], a[232:235], a[108:111], v[244:247]\n"
"ds_write_b128 v113, v[232:235] offset:2048\n"
"v_mfma_f32_16x16x32_bf16 v[244:247], a[236:239], a[124:127], v[244:247]\n"
"s_waitcnt lgkmcnt(6)\n"
"v_mfma_f32_16x16x32_bf16 v[248:251], a[240:243], a[108:111], v[248:251]\n"
"ds_write_b128 v113, v[236:239] offset:3072\n"
"s_waitcnt lgkmcnt(6)\n"
"v_mfma_f32_16x16x32_bf16 v[248:251], a[244:247], a[124:127], v[248:251]\n"
"s_waitcnt lgkmcnt(4)\n"
"v_mfma_f32_16x16x32_bf16 v[252:255], a[248:251], a[108:111], v[252:255]\n"
"ds_write_b128 v113, v[240:243] offset:4096\n"
"v_mfma_f32_16x16x32_bf16 v[252:255], a[252:255], a[124:127], v[252:255]\n"
"v_mfma_f32_16x16x32_bf16 v[160:163], a[192:195], a[100:103], v[160:163]\n"
"ds_write_b128 v113, v[244:247] offset:5120\n"
"v_mfma_f32_16x16x32_bf16 v[160:163], a[196:199], a[116:119], v[160:163]\n"
"v_mfma_f32_16x16x32_bf16 v[164:167], a[200:203], a[100:103], v[164:167]\n"
"ds_write_b128 v113, v[248:251] offset:6144\n"
"v_mfma_f32_16x16x32_bf16 v[164:167], a[204:207], a[116:119], v[164:167]\n"
"v_mfma_f32_16x16x32_bf16 v[168:171], a[208:211], a[100:103], v[168:171]\n"
"s_nop 0\n"
"ds_write_b128 v113, v[252:255] offset:7168\n"
"v_mfma_f32_16x16x32_bf16 v[168:171], a[212:215], a[116:119], v[168:171]\n"
"v_mfma_f32_16x16x32_bf16 v[172:175], a[216:219], a[100:103], v[172:175]\n"
"v_mfma_f32_16x16x32_bf16 v[172:175], a[220:223], a[116:119], v[172:175]\n"
"v_mfma_f32_16x16x32_bf16 v[176:179], a[224:227], a[100:103], v[176:179]\n"
"v_mfma_f32_16x16x32_bf16 v[176:179], a[228:231], a[116:119], v[176:179]\n"
"v_mfma_f32_16x16x32_bf16 v[180:183], a[232:235], a[100:103], v[180:183]\n"
"s_waitcnt lgkmcnt(0)\n"
"s_barrier\n"
"v_mfma_f32_16x16x32_bf16 v[180:183], a[236:239], a[116:119], v[180:183]\n"
"ds_read_b128 v[16:19], v114 offset:0\n"
"v_mfma_f32_16x16x32_bf16 v[184:187], a[240:243], a[100:103], v[184:187]\n"
"ds_read_b128 v[20:23], v114 offset:1024\n"
"v_mfma_f32_16x16x32_bf16 v[184:187], a[244:247], a[116:119], v[184:187]\n"
"ds_read_b128 v[24:27], v114 offset:2048\n"
"v_mfma_f32_16x16x32_bf16 v[188:191], a[248:251], a[100:103], v[188:191]\n"
"ds_read_b128 v[28:31], v114 offset:3072\n"
"v_mfma_f32_16x16x32_bf16 v[188:191], a[252:255], a[116:119], v[188:191]\n"
"ds_read_b128 v[32:35], v114 offset:4096\n"
"v_mfma_f32_16x16x32_bf16 v[192:195], a[192:195], a[104:107], v[192:195]\n"
"ds_read_b128 v[36:39], v114 offset:5120\n"
"v_mfma_f32_16x16x32_bf16 v[192:195], a[196:199], a[120:123], v[192:195]\n"
"ds_read_b128 v[40:43], v114 offset:6144\n"
"v_mfma_f32_16x16x32_bf16 v[196:199], a[200:203], a[104:107], v[196:199]\n"
"ds_read_b128 v[44:47], v114 offset:7168\n"
"v_mfma_f32_16x16x32_bf16 v[196:199], a[204:207], a[120:123], v[196:199]\n"
"s_waitcnt lgkmcnt(7)\n"
"v_add_f32 v160, v160, v16\n"
"v_add_f32 v161, v161, v17\n"
"v_add_f32 v162, v162, v18\n"
"v_mfma_f32_16x16x32_bf16 v[200:203], a[208:211], a[104:107], v[200:203]\n"
"v_add_f32 v163, v163, v19\n"
"s_waitcnt lgkmcnt(6)\n"
"v_add_f32 v164, v164, v20\n"
"v_add_f32 v165, v165, v21\n"
"v_mfma_f32_16x16x32_bf16 v[200:203], a[212:215], a[120:123], v[200:203]\n"
"v_add_f32 v166, v166, v22\n"
"v_add_f32 v167, v167, v23\n"
"v_cvt_pk_bf16_f32 v48, v160, v161\n"
"v_mfma_f32_16x16x32_bf16 v[204:207], a[216:219], a[104:107], v[204:207]\n"
"ds_write_b128 v116, v[192:195] offset:0\n"
"v_cvt_pk_bf16_f32 v49, v162, v163\n"
"v_cvt_pk_bf16_f32 v50, v164, v165\n"
"v_mfma_f32_16x16x32_bf16 v[204:207], a[220:223], a[120:123], v[204:207]\n"
"v_cvt_pk_bf16_f32 v51, v166, v167\n"
"v_min_u32 v48, -2, v48\n"
"v_min_u32 v49, -2, v49\n"
"v_mfma_f32_16x16x32_bf16 v[208:211], a[224:227], a[104:107], v[208:211]\n"
"ds_write_b128 v116, v[196:199] offset:1024\n"
"v_min_u32 v50, -2, v50\n"
"v_min_u32 v51, -2, v51\n"
"v_mfma_f32_16x16x32_bf16 v[208:211], a[228:231], a[120:123], v[208:211]\n"
"buffer_store_dwordx4 v[48:51], v93, s[76:79], 0 offen sc1\n"
"s_waitcnt lgkmcnt(4)\n"
"v_add_f32 v168, v168, v24\n"
"v_add_f32 v169, v169, v25\n"
"v_mfma_f32_16x16x32_bf16 v[212:215], a[232:235], a[104:107], v[212:215]\n"
"ds_write_b128 v116, v[200:203] offset:2048\n"
"v_add_f32 v170, v170, v26\n"
"v_add_f32 v171, v171, v27\n"
"v_mfma_f32_16x16x32_bf16 v[212:215], a[236:239], a[120:123], v[212:215]\n"
"v_add_f32 v172, v172, v28\n"
"v_add_f32 v173, v173, v29\n"
"v_add_f32 v174, v174, v30\n"
"v_mfma_f32_16x16x32_bf16 v[216:219], a[240:243], a[104:107], v[216:219]\n"
"ds_write_b128 v116, v[204:207] offset:3072\n"
"v_add_f32 v175, v175, v31\n"
"v_cvt_pk_bf16_f32 v52, v168, v169\n"
"v_mfma_f32_16x16x32_bf16 v[216:219], a[244:247], a[120:123], v[216:219]\n"
"v_cvt_pk_bf16_f32 v53, v170, v171\n"
"v_cvt_pk_bf16_f32 v54, v172, v173\n"
"v_cvt_pk_bf16_f32 v55, v174, v175\n"
"v_mfma_f32_16x16x32_bf16 v[220:223], a[248:251], a[104:107], v[220:223]\n"
"ds_write_b128 v116, v[208:211] offset:4096\n"
"v_min_u32 v52, -2, v52\n"
"v_min_u32 v53, -2, v53\n"
"v_mfma_f32_16x16x32_bf16 v[220:223], a[252:255], a[120:123], v[220:223]\n"
"v_min_u32 v54, -2, v54\n"
"v_min_u32 v55, -2, v55\n"
"v_mfma_f32_16x16x32_bf16 v[128:131], a[192:195], a[96:99], v[128:131]\n"
"ds_write_b128 v116, v[212:215] offset:5120\n"
"buffer_store_dwordx4 v[52:55], v93, s[76:79], 0 offen offset:1024 sc1\n"
"v_add_f32 v176, v176, v32\n"
"v_mfma_f32_16x16x32_bf16 v[128:131], a[196:199], a[112:115], v[128:131]\n"
"v_add_f32 v177, v177, v33\n"
"v_add_f32 v178, v178, v34\n"
"v_add_f32 v179, v179, v35\n"
"v_mfma_f32_16x16x32_bf16 v[132:135], a[200:203], a[96:99], v[132:135]\n"
"ds_write_b128 v116, v[216:219] offset:6144\n"
"v_add_f32 v180, v180, v36\n"
"v_add_f32 v181, v181, v37\n"
"v_mfma_f32_16x16x32_bf16 v[132:135], a[204:207], a[112:115], v[132:135]\n"
"v_add_f32 v182, v182, v38\n"
"v_add_f32 v183, v183, v39\n"
"v_cvt_pk_bf16_f32 v56, v176, v177\n"
"v_mfma_f32_16x16x32_bf16 v[136:139], a[208:211], a[96:99], v[136:139]\n"
"ds_write_b128 v116, v[220:223] offset:7168\n"
"v_cvt_pk_bf16_f32 v57, v178, v179\n"
"v_cvt_pk_bf16_f32 v58, v180, v181\n"
"v_mfma_f32_16x16x32_bf16 v[136:139], a[212:215], a[112:115], v[136:139]\n"
"v_cvt_pk_bf16_f32 v59, v182, v183\n"
"v_min_u32 v56, -2, v56\n"
"v_min_u32 v57, -2, v57\n"
"v_mfma_f32_16x16x32_bf16 v[140:143], a[216:219], a[96:99], v[140:143]\n"
"v_min_u32 v58, -2, v58\n"
"v_min_u32 v59, -2, v59\n"
"v_mfma_f32_16x16x32_bf16 v[140:143], a[220:223], a[112:115], v[140:143]\n"
"buffer_store_dwordx4 v[56:59], v93, s[76:79], 0 offen offset:2048 sc1\n"
"s_waitcnt lgkmcnt(3)\n"
"v_add_f32 v184, v184, v40\n"
"v_add_f32 v185, v185, v41\n"
"v_mfma_f32_16x16x32_bf16 v[144:147], a[224:227], a[96:99], v[144:147]\n"
"v_add_f32 v186, v186, v42\n"
"v_add_f32 v187, v187, v43\n"
"v_add_f32 v188, v188, v44\n"
"v_mfma_f32_16x16x32_bf16 v[144:147], a[228:231], a[112:115], v[144:147]\n"
"v_add_f32 v189, v189, v45\n"
"v_add_f32 v190, v190, v46\n"
"v_add_f32 v191, v191, v47\n"
"v_mfma_f32_16x16x32_bf16 v[148:151], a[232:235], a[96:99], v[148:151]\n"
"v_cvt_pk_bf16_f32 v60, v184, v185\n"
"v_cvt_pk_bf16_f32 v61, v186, v187\n"
"v_cvt_pk_bf16_f32 v62, v188, v189\n"
"v_mfma_f32_16x16x32_bf16 v[148:151], a[236:239], a[112:115], v[148:151]\n"
"v_cvt_pk_bf16_f32 v63, v190, v191\n"
"v_min_u32 v60, -2, v60\n"
"v_min_u32 v61, -2, v61\n"
"v_mfma_f32_16x16x32_bf16 v[152:155], a[240:243], a[96:99], v[152:155]\n"
"v_min_u32 v62, -2, v62\n"
"v_min_u32 v63, -2, v63\n"
"v_mfma_f32_16x16x32_bf16 v[152:155], a[244:247], a[112:115], v[152:155]\n"
"buffer_store_dwordx4 v[60:63], v93, s[76:79], 0 offen offset:3072 sc1\n"
"s_waitcnt lgkmcnt(0)\n"
"s_barrier\n"
"v_mfma_f32_16x16x32_bf16 v[156:159], a[248:251], a[96:99], v[156:159]\n"
"ds_read_b128 v[16:19], v117 offset:0\n"
"ds_read_b128 v[20:23], v117 offset:1024\n"
"ds_read_b128 v[24:27], v117 offset:2048\n"
"ds_read_b128 v[28:31], v117 offset:3072\n"
"v_mfma_f32_16x16x32_bf16 v[156:159], a[252:255], a[112:115], v[156:159]\n"
"ds_read_b128 v[32:35], v117 offset:4096\n"
"ds_read_b128 v[36:39], v117 offset:5120\n"
"ds_read_b128 v[40:43], v117 offset:6144\n"
"ds_read_b128 v[44:47], v117 offset:7168\n"
"s_waitcnt vmcnt(4)\n"
"s_nop 7\n"
"s_waitcnt lgkmcnt(0)\n"
"v_pk_add_f32 v[128:129], v[128:129], v[16:17]\n"
"v_pk_add_f32 v[130:131], v[130:131], v[18:19]\n"
"v_pk_add_f32 v[132:133], v[132:133], v[20:21]\n"
"v_pk_add_f32 v[134:135], v[134:135], v[22:23]\n"
"v_pk_add_f32 v[136:137], v[136:137], v[24:25]\n"
"v_pk_add_f32 v[138:139], v[138:139], v[26:27]\n"
"v_pk_add_f32 v[140:141], v[140:141], v[28:29]\n"
"v_pk_add_f32 v[142:143], v[142:143], v[30:31]\n"
"v_pk_add_f32 v[144:145], v[144:145], v[32:33]\n"
"v_pk_add_f32 v[146:147], v[146:147], v[34:35]\n"
"v_pk_add_f32 v[148:149], v[148:149], v[36:37]\n"
"v_pk_add_f32 v[150:151], v[150:151], v[38:39]\n"
"v_pk_add_f32 v[152:153], v[152:153], v[40:41]\n"
"v_pk_add_f32 v[154:155], v[154:155], v[42:43]\n"
"v_pk_add_f32 v[156:157], v[156:157], v[44:45]\n"
"v_pk_add_f32 v[158:159], v[158:159], v[46:47]\n"
"s_nop 1\n"
    : "={v[128:159]}"(keep), "+{s[60:63]}"(so), "+{v[100:115]}"(va)
    : "{s[40:43]}"(sx), "{s[44:47]}"(sw), "{s[52:55]}"(sp), "{s[56:59]}"(sq), "{v[94:95]}"(po), "{s[64:67]}"(code_srd(bid, wave)),
      "{s[76:79]}"(mksrd(xch + ((tile * 2 + (1 - s)) * 4 + wave) * 4096, 4096)), "{v93}"(lane * 16), "s"((unsigned)(size_t)lds) : "v16", "v17", "v18", "v19", "v20", "v21", "v22", "v23", "v24", "v25", "v26", "v27", "v28", "v29", "v30", "v31", "v32", "v33", "v34", "v35", "v36", "v37", "v38", "v39", "v40", "v41", "v42", "v43", "v44", "v45", "v46", "v47", "v48", "v49", "v50", "v51", "v52", "v53", "v54", "v55", "v56", "v57", "v58", "v59", "v60", "v61", "v62", "v63", "v116", "v117", "v118", "v119", "v120", "v121", "v122", "v123", "v124", "v125", "v126", "v127", "v160", "v161", "v162", "v163", "v164", "v165", "v166", "v167", "v168", "v169", "v170", "v171", "v172", "v173", "v174", "v175", "v176", "v177", "v178", "v179", "v180", "v181", "v182", "v183", "v184", "v185", "v186", "v187", "v188", "v189", "v190", "v191", "v192", "v193", "v194", "v195", "v196", "v197", "v198", "v199", "v200", "v201", "v202", "v203", "v204", "v205", "v206", "v207", "v208", "v209", "v210", "v211", "v212", "v213", "v214", "v215", "v216", "v217", "v218", "v219", "v220", "v221", "v222", "v223", "v224", "v225", "v226", "v227", "v228", "v229", "v230", "v231", "v232", "v233", "v234", "v235", "v236", "v237", "v238", "v239", "v240", "v241", "v242", "v243", "v244", "v245", "v246", "v247", "v248", "v249", "v250", "v251", "v252", "v253", "v254", "v255", "a0", "a1", "a2", "a3", "a4", "a5", "a6", "a7", "a8", "a9", "a10", "a11", "a12", "a13", "a14", "a15", "a16", "a17", "a18", "a19", "a20", "a21", "a22", "a23", "a24", "a25", "a26", "a27", "a28", "a29", "a30", "a31", "a32", "a33", "a34", "a35", "a36", "a37", "a38", "a39", "a40", "a41", "a42", "a43", "a44", "a45", "a46", "a47", "a48", "a49", "a50", "a51", "a52", "a53", "a54", "a55", "a56", "a57", "a58", "a59", "a60", "a61", "a62", "a63", "a64", "a65", "a66", "a67", "a68", "a69", "a70", "a71", "a72", "a73", "a74", "a75", "a76", "a77", "a78", "a79", "a80", "a81", "a82", "a83", "a84", "a85", "a86", "a87", "a88", "a89", "a90", "a91", "a92", "a93", "a94", "a95", "a96", "a97", "a98", "a99", "a100", "a101", "a102", "a103", "a104", "a105", "a106", "a107", "a108", "a109", "a110", "a111", "a112", "a113", "a114", "a115", "a116", "a117", "a118", "a119", "a120", "a121", "a122", "a123", "a124", "a125", "a126", "a127", "a128", "a129", "a130", "a131", "a132", "a133", "a134", "a135", "a136", "a137", "a138", "a139", "a140", "a141", "a142", "a143", "a144", "a145", "a146", "a147", "a148", "a149", "a150", "a151", "a152", "a153", "a154", "a155", "a156", "a157", "a158", "a159", "a160", "a161", "a162", "a163", "a164", "a165", "a166", "a167", "a168", "a169", "a170", "a171", "a172", "a173", "a174", "a175", "a176", "a177", "a178", "a179", "a180", "a181", "a182", "a183", "a184", "a185", "a186", "a187", "a188", "a189", "a190", "a191", "a192", "a193", "a194", "a195", "a196", "a197", "a198", "a199", "a200", "a201", "a202", "a203", "a204", "a205", "a206", "a207", "a208", "a209", "a210", "a211", "a212", "a213", "a214", "a215", "a216", "a217", "a218", "a219", "a220", "a221", "a222", "a223", "a224", "a225", "a226", "a227", "a228", "a229", "a230", "a231", "a232", "a233", "a234", "a235", "a236", "a237", "a238", "a239", "a240", "a241", "a242", "a243", "a244", "a245", "a246", "a247", "a248", "a249", "a250", "a251", "a252", "a253", "a254", "a255", "s68", "s69", "vcc", "scc", "memory");
  u32x4 pu[4];
  char* stg = lds + 131072 + wave * 8192;
  f32x4 gb[8];
#pragma unroll
  for (int nb = 0; nb < 8; ++nb) { gb[nb] = *(const f32x4*)(stg + 4096 + nb * 64 + g * 16);
    const u32x2 xw = *(const u32x2*)(stg + (nb >> 1) * 1024 + lane * 16 + (nb & 1) * 8), bw = *(const u32x2*)(stg + 5120 + nb * 32 + g * 8);
    const f32x4 v = (f32x4){lo16(xw[0]), hi16(xw[0]), lo16(xw[1]), hi16(xw[1])} + gb[nb] * ((f32x4){keep[4 * nb], keep[4 * nb + 1], keep[4 * nb + 2], keep[4 * nb + 3]} + (f32x4){lo16(bw[0]), hi16(bw[0]), lo16(bw[1]), hi16(bw[1])});
    keep[4 * nb] = v[0]; keep[4 * nb + 1] = v[1]; keep[4 * nb + 2] = v[2]; keep[4 * nb + 3] = v[3]; }
  asm volatile("" : "+v"(keep));
  poll_load4(pu, xch, ((tile * 2 + s) * 4 + wave) * 4096 + lane * 16);
#pragma unroll
  for (int nb = 0; nb < 8; ++nb) { const unsigned p0 = pu[nb >> 1][(nb & 1) * 2], p1 = pu[nb >> 1][(nb & 1) * 2 + 1];
    const f32x4 v = (f32x4){keep[4 * nb], keep[4 * nb + 1], keep[4 * nb + 2], keep[4 * nb + 3]} + gb[nb] * (f32x4){lo16(p0), hi16(p0), lo16(p1), hi16(p1)};
    const u32x2 o = {pk2(v[0], v[1]), pk2(v[2], v[3])};
    *(u32x2*)(stg + i * 256 + (((2 * nb + (g >> 1)) ^ i) << 4) + (g & 1) * 8) = o; }
  const auto ro = __builtin_amdgcn_make_buffer_rsrc(out + rbk * 16 * 1024 + pn * 128, 0, 15 * 2048 + 256, 0x20000);
#pragma unroll
  for (int p = 0; p < 4; ++p) { const int row = p * 4 + g; const u32x4 o = *(const u32x4*)(stg + row * 256 + ((i ^ row) << 4));
    __builtin_amdgcn_raw_buffer_store_b128(o, ro, row * 2048 + i * 16, 0, 16); }
}

__global__ __launch_bounds__(256, 1) void k_block(Par) {
  __shared__ __attribute__((aligned(16))) char lds[163840];
  const unsigned wv = __builtin_amdgcn_readfirstlane(threadIdx.x >> 6);
  asm volatile("code_first:");
  phase_front(lds, wv); phase_attn(lds, wv); phase_oproj(lds, wv); phase_mlp1(lds, wv); phase_mlp2(lds, wv);
  asm volatile("code_last:");
}

extern "C" void kernel_launch(void* const* d_in, const int* in_sizes, int n_in, void* d_out, int out_size, void* d_ws, size_t ws_size, hipStream_t stream) {
  Par p; p.x = (const u16*)d_in[0]; p.kc = (const u16*)d_in[1]; p.vc = (const u16*)d_in[2]; p.wq = (const u16*)d_in[3]; p.wo = (const u16*)d_in[4];
  p.w1 = (const u16*)d_in[6]; p.b1 = (const u16*)d_in[7]; p.w2 = (const u16*)d_in[8]; p.b2 = (const u16*)d_in[9]; p.wmod = (const u16*)d_in[11];
  p.c = (const u16*)d_in[10]; p.ws = (char*)d_ws; p.out = (u16*)d_out;
  k_block<<<256, 256, 0, stream>>>(p);
}
```
